```python
import torch

def _softmax(x, axis=-1):
    return torch.softmax(x, dim=axis)
def _gelu(x):
    return torch.nn.functional.gelu(x, approximate="tanh")
def _silu(x):
    return torch.nn.functional.silu(x)
def _sigmoid(x):
    return torch.sigmoid(x)
def _layernorm(x, axis=-1, eps=1e-5):
    m = x.mean(dim=axis, keepdim=True)
    v = x.var(dim=axis, keepdim=True, unbiased=False)
    return (x - m) / torch.sqrt(v + eps)
def _scatter_add(tgt, idx, src, axis=0):
    return tgt.clone().index_add_(axis, idx, src)
def _segment_sum(src, seg, n):
    out = torch.zeros((n,) + src.shape[1:], dtype=src.dtype)
    return out.index_add_(0, seg, src)
def _pad(x, pw):
    flat = tuple(v for pair in reversed(pw) for v in pair)
    return torch.nn.functional.pad(x, flat)

kernel_name = 'gated_mix__ssm_conv_h3__compressed_kv_cla__prompt'

def setup_inputs():

    _w = (lambda a: (lambda t: t.to(torch.bfloat16) if t.is_floating_point() else t)(torch.as_tensor(a)))
    def _A_setup():
        B, S, D = 1, 2048, 512
        ks = 4
        return {
            "q": _w((lambda *s: torch.randn(*s, dtype=torch.bfloat16) * 0.05)(B, S, D)),
            "k": _w((lambda *s: torch.randn(*s, dtype=torch.bfloat16) * 0.4)(B, S, D)),
            "v": _w((lambda *s: torch.randn(*s, dtype=torch.bfloat16) * 0.4)(B, S, D)),
            "shift_k": _w((lambda *s: torch.randn(*s, dtype=torch.bfloat16) * 0.4)(D, ks) * 0.5),
            "ssm_k": _w((lambda *s: torch.randn(*s, dtype=torch.bfloat16) * 0.8)(D, S) * (1.0 / S ** 0.5)),
            "ssm_D": _w((lambda *s: torch.randn(*s, dtype=torch.bfloat16) * 0.4)(D)),
        }
    _A_in = _A_setup()

    def _B_setup():
        B, H, Sq, Sk, D, M = 1, 32, 2048, 2048, 128, 512
        Dc = 4 * D
        return {
            "x": _w((lambda *s: torch.randn(*s, dtype=torch.bfloat16) * 0.05)(B, Sq, M)),
            "c_kv": _w((lambda *s: torch.randn(*s, dtype=torch.bfloat16) * 0.4)(B, Sk, Dc)),
            "w_q": _w((lambda *s: torch.randn(*s, dtype=torch.bfloat16) * 6.4)(M, H, D) * (M ** -0.5)),
            "w_uk": _w((lambda *s: torch.randn(*s, dtype=torch.bfloat16) * 6.4)(Dc, H, D) * (Dc ** -0.5)),
            "w_uv": _w((lambda *s: torch.randn(*s, dtype=torch.bfloat16) * 0.4)(Dc, H, D) * (Dc ** -0.5)),
            "w_o": _w((lambda *s: torch.randn(*s, dtype=torch.bfloat16) * 0.4)(H, D, M) * ((H * D) ** -0.5)),
        }
    _B_in = _B_setup()

    return {
        "A_q": _A_in["q"],
        "A_k": _A_in["k"],
        "A_v": _A_in["v"],
        "A_shift_k": _A_in["shift_k"],
        "A_ssm_k": _A_in["ssm_k"],
        "A_ssm_D": _A_in["ssm_D"],
        "B_x": _B_in["x"],
        "B_c_kv": _B_in["c_kv"],
        "B_w_q": _B_in["w_q"],
        "B_w_uk": _B_in["w_uk"],
        "B_w_uv": _B_in["w_uv"],
        "B_w_o": _B_in["w_o"],
        "gate_w": _w((lambda *s: torch.randn(*s, dtype=torch.bfloat16) * 0.05)(512) * 16.0),
    }

def reference(A_q, A_k, A_v, A_shift_k, A_ssm_k, A_ssm_D, B_x, B_c_kv, B_w_q, B_w_uk, B_w_uv, B_w_o, gate_w):
    def _A_ref(q, k, v, shift_k, ssm_k, ssm_D):
        B, S, D = q.shape
        ks = shift_k.shape[-1]
        k_pad = _pad(k, ((0, 0), (ks - 1, 0), (0, 0)))
        k_shift = torch.zeros_like(k)
        for t in range(ks):
            k_shift = k_shift + k_pad[:, t:t + S, :] * shift_k[:, ks - 1 - t][None, None, :]
        kv = k_shift * v
        L = 2 * S
        kv_pad = _pad(kv, ((0, 0), (0, S), (0, 0)))
        kf_pad = _pad(ssm_k, ((0, 0), (0, S)))
        kv_f = (lambda x, axes=(-2,-1): torch.fft.rfft2(x.float(), dim=axes))(kv_pad[:, None, :, :], axes=(1, 2))
        kf_f = (lambda x, axes=(-2,-1): torch.fft.rfft2(x.float(), dim=axes))(kf_pad[:, None, :], axes=(1, 2))
        kf_f = (lambda x, *d: x.permute(*d))(kf_f, 1, 2, 0)
        y_f = kv_f * kf_f[None, :, :, :]
        y = (lambda x, s, axes=(-2,-1): torch.fft.irfft2(x, s=s, dim=axes).to(torch.bfloat16))(y_f, (1, L), axes=(1, 2))[:, 0, :S, :]
        y = y + kv * ssm_D[None, None, :]
        return q * y

    def _B_ref(x, c_kv, w_q, w_uk, w_uv, w_o):
        B, Sq, M = x.shape[0], x.shape[1], x.shape[2]
        Sk = c_kv.shape[1]
        H, D = w_q.shape[1], w_q.shape[2]
        q = torch.einsum("bqm,mhd->bhqd", x, w_q)
        k = torch.einsum("bkc,chd->bhkd", c_kv, w_uk)
        v = torch.einsum("bkc,chd->bhkd", c_kv, w_uv)
        scores = torch.einsum("bhqd,bhkd->bhqk", q, k) * (D ** -0.5)
        off = Sk - Sq
        qi = torch.arange(Sq)[:, None]
        kj = torch.arange(Sk)[None, :]
        cmask = kj <= (qi + off)
        scores = torch.where(cmask[None, None], scores, -1e9)
        p = _softmax(scores, axis=-1)
        ctx = torch.einsum("bhqk,bhkd->bhqd", p, v)
        return torch.einsum("bhqd,hdm->bqm", ctx, w_o)

    x = A_q
    g = _sigmoid((x * gate_w).sum(-1, keepdims=True))
    a = _A_ref(x, A_k, A_v, A_shift_k, A_ssm_k, A_ssm_D)
    b = _B_ref(x, B_c_kv, B_w_q, B_w_uk, B_w_uv, B_w_o)
    return g * a + (1 - g) * b
```

```cpp
#include <hip/hip_runtime.h>
#include <hip/hip_bf16.h>
#include <stdint.h>

typedef float f32x4 __attribute__((ext_vector_type(4)));
typedef short bf16x8 __attribute__((ext_vector_type(8)));
typedef unsigned u32x4 __attribute__((ext_vector_type(4)));
typedef unsigned u32x2 __attribute__((ext_vector_type(2)));
typedef unsigned short u16;
#define DI __device__ __forceinline__
#define MFMA16(a, b, c) __builtin_amdgcn_mfma_f32_16x16x32_bf16((a), (b), (c), 0, 0, 0)

constexpr int S = 2048, DM = 512, H = 32, D = 128;
constexpr size_t WS_KV  = 1u << 20;
constexpr size_t WS_Q   = WS_KV + (size_t)H * 32 * 32768;
constexpr size_t WS_CTX = WS_Q + (size_t)H * 128 * 4096;
constexpr size_t WS_KVT = WS_CTX + (size_t)128 * H * 4096;
constexpr size_t WS_YT  = WS_KVT + (size_t)DM * S * 2;
constexpr size_t WS_G   = WS_YT + (size_t)DM * S * 2;

DI float bf2f(u16 v) { return __uint_as_float((unsigned)v << 16); }
DI float bflo(unsigned v) { return __uint_as_float(v << 16); }
DI float bfhi(unsigned v) { return __uint_as_float(v & 0xffff0000u); }
DI unsigned cvtpk(float lo, float hi) {
  typedef float f32x2 __attribute__((ext_vector_type(2)));
  typedef __bf16 bf16x2 __attribute__((ext_vector_type(2)));
  f32x2 v = {lo, hi}; bf16x2 b = __builtin_convertvector(v, bf16x2); return __builtin_bit_cast(unsigned, b);
}
DI float rbf(float x) { return bflo(cvtpk(x, 0.f)); }

DI void phase_a0(int b, const u16* __restrict__ Aq, const u16* __restrict__ Ak, const u16* __restrict__ Av, const u16* __restrict__ shk,
                 const u16* __restrict__ gw, u16* __restrict__ kvT, float* __restrict__ gate, char* lds) {
  const int tid = threadIdx.x;
  const int rb = b >> 3, cg = b & 7;
  const int r = tid >> 3, c8 = tid & 7;
  const int ch0 = cg * 64 + c8 * 8, t0 = rb * 64 + 2 * r;
  u32x4 kr[5], vr[2];
#pragma unroll
  for (int i = 0; i < 5; ++i) { const int t = t0 - 3 + i; kr[i] = t >= 0 ? *(const u32x4*)(Ak + (size_t)t * DM + ch0) : (u32x4){0, 0, 0, 0}; }
#pragma unroll
  for (int i = 0; i < 2; ++i) vr[i] = *(const u32x4*)(Av + (size_t)(t0 + i) * DM + ch0);
  u32x4 wv[4];
#pragma unroll
  for (int i = 0; i < 4; ++i) wv[i] = *(const u32x4*)(shk + (size_t)ch0 * 4 + i * 8);
  u16* tile = (u16*)lds;
#pragma unroll
  for (int c = 0; c < 8; ++c) {
    const unsigned w01 = wv[c >> 1][(c & 1) * 2], w23 = wv[c >> 1][(c & 1) * 2 + 1];
    const float w0 = bflo(w01), w1 = bfhi(w01), w2 = bflo(w23), w3 = bfhi(w23);
    float kk[5];
#pragma unroll
    for (int i = 0; i < 5; ++i) kk[i] = (c & 1) ? bfhi(kr[i][c >> 1]) : bflo(kr[i][c >> 1]);
    float o[2];
#pragma unroll
    for (int e = 0; e < 2; ++e) {
      float acc = rbf(kk[e] * w3);
      acc = rbf(acc + rbf(kk[e + 1] * w2));
      acc = rbf(acc + rbf(kk[e + 2] * w1));
      acc = rbf(acc + rbf(kk[e + 3] * w0));
      const float vv = (c & 1) ? bfhi(vr[e][c >> 1]) : bflo(vr[e][c >> 1]);
      o[e] = acc * vv;
    }
    *(unsigned*)(tile + (c8 * 8 + c) * 72 + 2 * r) = cvtpk(o[0], o[1]);
  }
  {
    const int row = b * 8 + (tid >> 5), l32 = tid & 31;
    const u32x4 x0 = *(const u32x4*)(Aq + (size_t)row * DM + l32 * 16), x1 = *(const u32x4*)(Aq + (size_t)row * DM + l32 * 16 + 8);
    const u32x4 g0 = *(const u32x4*)(gw + l32 * 16), g1 = *(const u32x4*)(gw + l32 * 16 + 8);
    float s = 0.f;
#pragma unroll
    for (int e = 0; e < 4; ++e) { s += rbf(bflo(x0[e]) * bflo(g0[e])) + rbf(bfhi(x0[e]) * bfhi(g0[e])); s += rbf(bflo(x1[e]) * bflo(g1[e])) + rbf(bfhi(x1[e]) * bfhi(g1[e])); }
#pragma unroll
    for (int o = 16; o >= 1; o >>= 1) s += __shfl_xor(s, o);
    const float z = rbf(s);
    if (l32 == 0) gate[row] = rbf(1.f / (1.f + __expf(-z)));
  }
  __syncthreads();
#pragma unroll
  for (int i = 0; i < 2; ++i) {
    const int idx = tid + 256 * i, ch = idx >> 3, part = idx & 7;
    const u32x4 v = *(const u32x4*)(tile + ch * 72 + part * 8);
    *(u32x4*)(kvT + (size_t)(cg * 64 + ch) * S + rb * 64 + part * 8) = v;
  }
  __syncthreads();
}

constexpr int CV_COPY = 2048 + 48 + 64 + 8;
constexpr int CV_CH_BYTES = 8 * CV_COPY * 2 + (2048 + 1024) * 2;
DI void phase_conv(int b, const u16* __restrict__ ssmk, const u16* __restrict__ ssmD, const u16* __restrict__ kvT, u16* __restrict__ yT, char* lds) {
  const int tid = threadIdx.x, lane = tid & 63, w = tid >> 6, i = lane & 15, g = lane >> 4;
  const int cg = b & 7, p = b >> 3;
  const int chl = w >> 1, half = w & 1, ch = cg * 64 + 2 * p + chl;
  char* base = lds + chl * CV_CH_BYTES;
  u16* cop = (u16*)base;
  u16* ub = (u16*)(base + 8 * CV_COPY * 2);
  constexpr int XO = 2048 + 48;
  const int t128 = tid & 127;
  for (int y = t128; y < CV_COPY * 8; y += 128) {
    const int a = y / CV_COPY, yy = y - a * CV_COPY, hx = XO - (yy + a);
    cop[y] = (hx >= 0 && hx < 2048) ? ssmk[(size_t)ch * S + hx] : (u16)0;
  }
  for (int y = t128; y < 128; y += 128) *(u32x4*)(ub + y * 8) = (u32x4){0, 0, 0, 0};
  for (int y = t128; y < 256; y += 128) *(u32x4*)(ub + 1024 + y * 8) = *(const u32x4*)(kvT + (size_t)ch * S + y * 8);
  __syncthreads();
  const int ii = i & 7, a = (8 - ii) & 7;
  const u16* ap = cop + a * CV_COPY + (XO - i + 8 * g - a);
  f32x4 acc[4][2];
#pragma unroll
  for (int rt = 0; rt < 4; ++rt)
#pragma unroll
    for (int ct = 0; ct < 2; ++ct) acc[rt][ct] = (f32x4){0.f, 0.f, 0.f, 0.f};
  const int m0 = half ? 12 : 0, m1 = half ? 32 : 12;
  for (int m = m0; m < m1; ++m) {
    bf16x8 af[6];
#pragma unroll
    for (int n = 0; n < 6; ++n) af[n] = *(const bf16x8*)(ap - 16 * (4 * m - 2 + n));
#pragma unroll
    for (int ct = 0; ct < 2; ++ct) {
      if (ct == 0 && m >= 16) continue;
#pragma unroll
      for (int ks = 0; ks < 2; ++ks) {
        const bf16x8 bf = *(const bf16x8*)(ub + 1024 + 64 * (16 * ct + i - m) + 32 * ks + 8 * g);
#pragma unroll
        for (int rt = 0; rt < 4; ++rt) acc[rt][ct] = MFMA16(af[rt - 2 * ks + 2], bf, acc[rt][ct]);
      }
    }
  }
  __syncthreads();
  float* red = (float*)base;
  if (half == 1) {
#pragma unroll
    for (int rt = 0; rt < 4; ++rt)
#pragma unroll
      for (int ct = 0; ct < 2; ++ct) *(f32x4*)(red + 64 * (16 * ct + i) + 16 * rt + 4 * g) = acc[rt][ct];
  }
  __syncthreads();
  if (half == 0) {
    const float Dv = bf2f(ssmD[ch]);
#pragma unroll
    for (int rt = 0; rt < 4; ++rt)
#pragma unroll
      for (int ct = 0; ct < 2; ++ct) {
        const int t = 64 * (16 * ct + i) + 16 * rt + 4 * g;
        const f32x4 o = *(const f32x4*)(red + t);
        const u32x2 uu = *(const u32x2*)(ub + 1024 + t);
        float y[4];
#pragma unroll
        for (int r = 0; r < 4; ++r) {
          const float u = (r & 1) ? bfhi(uu[r >> 1]) : bflo(uu[r >> 1]);
          y[r] = rbf(acc[rt][ct][r] + o[r]) + rbf(u * Dv);
        }
        u32x2 pk = {cvtpk(y[0], y[1]), cvtpk(y[2], y[3])};
        *(u32x2*)(yT + (size_t)ch * S + t) = pk;
      }
  }
  __syncthreads();
}

__global__ __launch_bounds__(256) void k_proj(const u16* __restrict__ x, const u16* __restrict__ ckv, const u16* __restrict__ wq, const u16* __restrict__ wuk,
                                              const u16* __restrict__ wuv, char* __restrict__ ws) {
  const int b = blockIdx.x, which = b / 256, h = (b >> 3) & 31, rb = b & 7;
  const int lane = threadIdx.x & 63, w = threadIdx.x >> 6, i = lane & 15, g = lane >> 4;
  const u16* X = which == 0 ? x : ckv;
  const u16* W = which == 0 ? wq : which == 1 ? wuk : wuv;
  const int row0 = rb * 256 + w * 64;
  f32x4 acc[8][4];
#pragma unroll
  for (int a = 0; a < 8; ++a)
#pragma unroll
    for (int c = 0; c < 4; ++c) acc[a][c] = (f32x4){0.f, 0.f, 0.f, 0.f};
  for (int ks = 0; ks < 16; ++ks) {
    bf16x8 xf[4];
#pragma unroll
    for (int c = 0; c < 4; ++c) xf[c] = *(const bf16x8*)(X + (size_t)(row0 + 16 * c + i) * DM + 32 * ks + 8 * g);
#pragma unroll
    for (int dt = 0; dt < 8; ++dt) {
      bf16x8 wf;
#pragma unroll
      for (int j = 0; j < 8; ++j) wf[j] = (short)W[(size_t)(32 * ks + 8 * g + j) * (H * D) + h * D + 16 * dt + i];
#pragma unroll
      for (int c = 0; c < 4; ++c) acc[dt][c] = which == 2 ? MFMA16(xf[c], wf, acc[dt][c]) : MFMA16(wf, xf[c], acc[dt][c]);
    }
  }
  if (which == 2) {
#pragma unroll
    for (int k2 = 0; k2 < 2; ++k2)
#pragma unroll
      for (int db = 0; db < 8; ++db) {
        const f32x4 lo = acc[db][2 * k2], hi = acc[db][2 * k2 + 1];
        u32x4 fr = {cvtpk(lo[0], lo[1]), cvtpk(lo[2], lo[3]), cvtpk(hi[0], hi[1]), cvtpk(hi[2], hi[3])};
        const int T = row0 >> 6;
        *(u32x4*)(ws + WS_KV + ((size_t)h * 32 + T) * 32768 + 16384 + (k2 * 8 + db) * 1024 + lane * 16) = fr;
      }
  } else {
    const float sc = which == 0 ? 0.12751743082f : 1.f;
#pragma unroll
    for (int c = 0; c < 4; ++c)
#pragma unroll
      for (int s = 0; s < 4; ++s) {
        const f32x4 lo = acc[2 * s][c] * sc, hi = acc[2 * s + 1][c] * sc;
        u32x4 fr = {cvtpk(lo[0], lo[1]), cvtpk(lo[2], lo[3]), cvtpk(hi[0], hi[1]), cvtpk(hi[2], hi[3])};
        if (which == 0) *(u32x4*)(ws + WS_Q + (((size_t)h * 128 + (row0 >> 4) + c) * 4 + s) * 1024 + lane * 16) = fr;
        else            *(u32x4*)(ws + WS_KV + ((size_t)h * 32 + (row0 >> 6)) * 32768 + (c * 4 + s) * 1024 + lane * 16) = fr;
      }
  }
}

DI void attn_qtile_generic(int h, int qt, const char* __restrict__ kvh  , const bf16x8 (&qf)[4], char* __restrict__ ctx) {
  const int lane = threadIdx.x & 63, i = lane & 15, g = lane >> 4;
  const int row = 16 * qt + i;
  f32x4 o[8];
#pragma unroll
  for (int d = 0; d < 8; ++d) o[d] = (f32x4){0.f, 0.f, 0.f, 0.f};
  float m = -1e30f, l = 0.f;
  const int nT = (16 * qt + 15) / 64 + 1;
  for (int T = 0; T < nT; ++T) {
    const char* kp = kvh + (size_t)T * 32768 + lane * 16;
    f32x4 s[4];
#pragma unroll
    for (int kb = 0; kb < 4; ++kb) {
      s[kb] = (f32x4){0.f, 0.f, 0.f, 0.f};
#pragma unroll
      for (int ss = 0; ss < 4; ++ss) s[kb] = MFMA16(*(const bf16x8*)(kp + (kb * 4 + ss) * 1024), qf[ss], s[kb]);
    }
    float mx = -__builtin_inff();
#pragma unroll
    for (int kb = 0; kb < 4; ++kb)
#pragma unroll
      for (int r = 0; r < 4; ++r) { const int key = 64 * T + 16 * kb + 4 * g + r; if (key > row) s[kb][r] = -__builtin_inff(); mx = fmaxf(mx, s[kb][r]); }
    mx = fmaxf(mx, __shfl_xor(mx, 16)); mx = fmaxf(mx, __shfl_xor(mx, 32));
    const float mn = fmaxf(m, mx), al = __builtin_amdgcn_exp2f(m - mn);
    m = mn;
    float ps = 0.f;
#pragma unroll
    for (int kb = 0; kb < 4; ++kb)
#pragma unroll
      for (int r = 0; r < 4; ++r) { s[kb][r] = __builtin_amdgcn_exp2f(s[kb][r] - mn); }
    bf16x8 pf[2];
#pragma unroll
    for (int ks = 0; ks < 2; ++ks) {
      u32x4 pw = {cvtpk(s[2 * ks][0], s[2 * ks][1]), cvtpk(s[2 * ks][2], s[2 * ks][3]), cvtpk(s[2 * ks + 1][0], s[2 * ks + 1][1]), cvtpk(s[2 * ks + 1][2], s[2 * ks + 1][3])};
#pragma unroll
      for (int e = 0; e < 4; ++e) ps += bflo(pw[e]) + bfhi(pw[e]);
      pf[ks] = __builtin_bit_cast(bf16x8, pw);
    }
    l = l * al + ps;
#pragma unroll
    for (int d = 0; d < 8; ++d) {
      o[d] *= al;
#pragma unroll
      for (int ks = 0; ks < 2; ++ks) o[d] = MFMA16(*(const bf16x8*)(kp + 16384 + (ks * 8 + d) * 1024), pf[ks], o[d]);
    }
  }
  l += __shfl_xor(l, 16); l += __shfl_xor(l, 32);
  const float il = 1.f / l;
#pragma unroll
  for (int s2 = 0; s2 < 4; ++s2) {
    const f32x4 lo = o[2 * s2] * il, hi = o[2 * s2 + 1] * il;
    u32x4 fr = {cvtpk(lo[0], lo[1]), cvtpk(lo[2], lo[3]), cvtpk(hi[0], hi[1]), cvtpk(hi[2], hi[3])};
    *(u32x4*)(ctx + (((size_t)qt * H + h) * 4 + s2) * 1024 + lane * 16) = fr;
  }
}
__global__ __launch_bounds__(256) void k_attn(char* __restrict__ ws) {
  const int b = blockIdx.x, h = b >> 3, j = b & 7;
  const int lane = threadIdx.x & 63, w = threadIdx.x >> 6;
  for (int q4 = 0; q4 < 4; ++q4) {
    const int blk = q4 < 2 ? j : 15 - j;
    const int qt = blk * 8 + 2 * w + (q4 & 1);
    bf16x8 qf[4];
#pragma unroll
    for (int s = 0; s < 4; ++s) qf[s] = *(const bf16x8*)(ws + WS_Q + (((size_t)h * 128 + qt) * 4 + s) * 1024 + lane * 16);
    attn_qtile_generic(h, qt, ws + WS_KV + (size_t)h * 32 * 32768, qf, ws + WS_CTX);
  }
}

__global__ __launch_bounds__(256) void k_out(const u16* __restrict__ Aq, const u16* __restrict__ wo, const char* __restrict__ ws, u16* __restrict__ out) {
  __shared__ float red[4][16][64 * 4];
  const int b = blockIdx.x, rt = (b & 7) + 8 * ((b >> 3) & 3), ct = b >> 5;
  const int lane = threadIdx.x & 63, w = threadIdx.x >> 6, i = lane & 15, g = lane >> 4;
  f32x4 acc[4][4];
#pragma unroll
  for (int a = 0; a < 4; ++a)
#pragma unroll
    for (int c = 0; c < 4; ++c) acc[a][c] = (f32x4){0.f, 0.f, 0.f, 0.f};
  for (int hh = 0; hh < 8; ++hh) {
    const int h = 8 * w + hh;
    for (int s = 0; s < 4; ++s) {
      bf16x8 cf[4];
#pragma unroll
      for (int c = 0; c < 4; ++c) cf[c] = *(const bf16x8*)(ws + WS_CTX + (((size_t)(4 * rt + c) * H + h) * 4 + s) * 1024 + lane * 16);
#pragma unroll
      for (int mt = 0; mt < 4; ++mt) {
        const int m = 64 * ct + 32 * (mt >> 1) + 8 * (i >> 2) + 4 * (mt & 1) + (i & 3);
        bf16x8 wf;
#pragma unroll
        for (int j = 0; j < 8; ++j) wf[j] = (short)wo[(size_t)(h * D + 32 * s + 16 * (j >> 2) + 4 * g + (j & 3)) * DM + m];
#pragma unroll
        for (int c = 0; c < 4; ++c) acc[mt][c] = MFMA16(wf, cf[c], acc[mt][c]);
      }
    }
  }
#pragma unroll
  for (int mt = 0; mt < 4; ++mt)
#pragma unroll
    for (int c = 0; c < 4; ++c) *(f32x4*)&red[w][mt * 4 + c][lane * 4] = acc[mt][c];
  __syncthreads();
  const u16* yT = (const u16*)(ws + WS_YT);
  const float* gate = (const float*)(ws + WS_G);
  const int t = 64 * rt + 16 * w + i;
  const float gt = gate[t], g1 = rbf(1.f - gt);
#pragma unroll
  for (int u = 0; u < 2; ++u) {
    const int m0 = 64 * ct + 32 * u + 8 * g;
    const u32x4 qv = *(const u32x4*)(Aq + (size_t)t * DM + m0);
    float res[8];
#pragma unroll
    for (int e = 0; e < 8; ++e) {
      const int mt = 2 * u + (e >> 2), r = e & 3;
      float bsum = 0.f;
#pragma unroll
      for (int ww = 0; ww < 4; ++ww) bsum += red[ww][mt * 4 + w][lane * 4 + r];
      const float y = bf2f(yT[(size_t)(m0 + e) * S + t]);
      const float q = (e & 1) ? bfhi(qv[e >> 1]) : bflo(qv[e >> 1]);
      const float a = rbf(q * y);
      res[e] = rbf(gt * a) + rbf(g1 * rbf(bsum));
    }
    u32x4 o = {cvtpk(res[0], res[1]), cvtpk(res[2], res[3]), cvtpk(res[4], res[5]), cvtpk(res[6], res[7])};
    *(u32x4*)(out + (size_t)t * DM + m0) = o;
  }
}

__global__ __launch_bounds__(256) void k_a0(const u16* Aq, const u16* Ak, const u16* Av, const u16* shk, const u16* gw, char* ws) {
  __shared__ __attribute__((aligned(16))) char lds[64 * 72 * 2];
  phase_a0(blockIdx.x, Aq, Ak, Av, shk, gw, (u16*)(ws + WS_KVT), (float*)(ws + WS_G), lds);
}
__global__ __launch_bounds__(256) void k_conv(const u16* ssmk, const u16* ssmD, char* ws) {
  extern __shared__ __attribute__((aligned(16))) char lds[];
  phase_conv(blockIdx.x, ssmk, ssmD, (const u16*)(ws + WS_KVT), (u16*)(ws + WS_YT), lds);
}

extern "C" void kernel_launch(void* const* d_in, const int* in_sizes, int n_in, void* d_out, int out_size, void* d_ws, size_t ws_size, hipStream_t stream) {
  const u16 *Aq = (const u16*)d_in[0], *Ak = (const u16*)d_in[1], *Av = (const u16*)d_in[2], *shk = (const u16*)d_in[3], *ssmk = (const u16*)d_in[4], *ssmD = (const u16*)d_in[5];
  const u16 *Bx = (const u16*)d_in[6], *ckv = (const u16*)d_in[7], *wq = (const u16*)d_in[8], *wuk = (const u16*)d_in[9], *wuv = (const u16*)d_in[10], *wo = (const u16*)d_in[11];
  const u16* gw = (const u16*)d_in[12];
  char* ws = (char*)d_ws;
  (void)hipFuncSetAttribute((const void*)k_conv, hipFuncAttributeMaxDynamicSharedMemorySize, 2 * CV_CH_BYTES);
  k_a0<<<256, 256, 0, stream>>>(Aq, Ak, Av, shk, gw, ws);
  k_conv<<<256, 256, 2 * CV_CH_BYTES, stream>>>(ssmk, ssmD, ws);
  k_proj<<<768, 256, 0, stream>>>(Aq, ckv, wq, wuk, wuv, ws);
  k_attn<<<256, 256, 0, stream>>>(ws);
  k_out<<<256, 256, 0, stream>>>(Aq, wo, ws, (u16*)d_out);
}
```

```cpp
#include <hip/hip_runtime.h>
#include <hip/hip_bf16.h>
#include <stdint.h>

typedef float f32x4 __attribute__((ext_vector_type(4)));
typedef short bf16x8 __attribute__((ext_vector_type(8)));
typedef unsigned u32x4 __attribute__((ext_vector_type(4)));
typedef unsigned u32x2 __attribute__((ext_vector_type(2)));
typedef unsigned short u16;
#define DI __device__ __forceinline__
#define MFMA16(a, b, c) __builtin_amdgcn_mfma_f32_16x16x32_bf16((a), (b), (c), 0, 0, 0)

constexpr int S = 2048, DM = 512, H = 32, D = 128;
constexpr size_t WS_KV  = 1u << 20;
constexpr size_t WS_Q   = WS_KV + (size_t)H * 32 * 32768;
constexpr size_t WS_CTX = WS_Q + (size_t)H * 128 * 4096;
constexpr size_t WS_KVT = WS_CTX + (size_t)128 * H * 4096;
constexpr size_t WS_YT  = WS_KVT + (size_t)DM * S * 2;
constexpr size_t WS_G   = WS_YT + (size_t)DM * S * 2;
constexpr size_t WS_WOF = WS_G + 65536;

DI __amdgpu_buffer_rsrc_t ws_rsrc(char* ws) { return __builtin_amdgcn_make_buffer_rsrc((void*)ws, (short)0, (int)(128u << 20), 0x00020000); }
DI void st16_wt(char* ws, size_t off, u32x4 v) { __builtin_amdgcn_raw_buffer_store_b128(v, ws_rsrc(ws), (unsigned)off, 0, 16); }
DI bf16x8 ld16(char* ws, unsigned off) { return __builtin_bit_cast(bf16x8, __builtin_amdgcn_raw_buffer_load_b128(ws_rsrc(ws), off, 0, 16)); }
DI float bflo(unsigned v) { return __uint_as_float(v << 16); }
DI float bfhi(unsigned v) { return __uint_as_float(v & 0xffff0000u); }
DI unsigned cvtpk(float lo, float hi) {
  typedef float f32x2 __attribute__((ext_vector_type(2)));
  typedef __bf16 bf16x2 __attribute__((ext_vector_type(2)));
  f32x2 v = {lo, hi}; bf16x2 b = __builtin_convertvector(v, bf16x2); return __builtin_bit_cast(unsigned, b);
}
DI float shx(float v, int lane, int mask) { return __int_as_float(__builtin_amdgcn_ds_bpermute((lane ^ mask) << 2, __float_as_int(v))); }

DI void attn_qtile_generic(int lane, int h, int qt, unsigned kvh  , const bf16x8 (&qf)[4], char* ws) {
  const int i = lane & 15, g = lane >> 4;
  const int row = 16 * qt + i;
  f32x4 o[8];
#pragma unroll
  for (int d = 0; d < 8; ++d) o[d] = (f32x4){0.f, 0.f, 0.f, 0.f};
  float m = -1e30f, l = 0.f;
  const int nT = (16 * qt + 15) / 64 + 1;
  for (int T = 0; T < nT; ++T) {
    const unsigned kp = kvh + T * 32768 + lane * 16;
    f32x4 s[4];
#pragma unroll
    for (int kb = 0; kb < 4; ++kb) {
      s[kb] = (f32x4){0.f, 0.f, 0.f, 0.f};
#pragma unroll
      for (int ss = 0; ss < 4; ++ss) s[kb] = MFMA16(ld16(ws, kp + (kb * 4 + ss) * 1024), qf[ss], s[kb]);
    }
    float mx = -__builtin_inff();
#pragma unroll
    for (int kb = 0; kb < 4; ++kb)
#pragma unroll
      for (int r = 0; r < 4; ++r) { const int key = 64 * T + 16 * kb + 4 * g + r; if (key > row) s[kb][r] = -__builtin_inff(); mx = fmaxf(mx, s[kb][r]); }
    mx = fmaxf(mx, shx(mx, lane, 16)); mx = fmaxf(mx, shx(mx, lane, 32));
    const float mn = fmaxf(m, mx), al = __builtin_amdgcn_exp2f(m - mn);
    m = mn;
    float ps = 0.f;
#pragma unroll
    for (int kb = 0; kb < 4; ++kb)
#pragma unroll
      for (int r = 0; r < 4; ++r) { s[kb][r] = __builtin_amdgcn_exp2f(s[kb][r] - mn); }
    bf16x8 pf[2];
#pragma unroll
    for (int ks = 0; ks < 2; ++ks) {
      u32x4 pw = {cvtpk(s[2 * ks][0], s[2 * ks][1]), cvtpk(s[2 * ks][2], s[2 * ks][3]), cvtpk(s[2 * ks + 1][0], s[2 * ks + 1][1]), cvtpk(s[2 * ks + 1][2], s[2 * ks + 1][3])};
#pragma unroll
      for (int e = 0; e < 4; ++e) ps += bflo(pw[e]) + bfhi(pw[e]);
      pf[ks] = __builtin_bit_cast(bf16x8, pw);
    }
    l = l * al + ps;
#pragma unroll
    for (int d = 0; d < 8; ++d) {
      o[d] *= al;
#pragma unroll
      for (int ks = 0; ks < 2; ++ks) o[d] = MFMA16(ld16(ws, kp + 16384 + (ks * 8 + d) * 1024), pf[ks], o[d]);
    }
  }
  l += shx(l, lane, 16); l += shx(l, lane, 32);
  const float il = 1.f / l;
#pragma unroll
  for (int s2 = 0; s2 < 4; ++s2) {
    const f32x4 lo = o[2 * s2] * il, hi = o[2 * s2 + 1] * il;
    u32x4 fr = {cvtpk(lo[0], lo[1]), cvtpk(lo[2], lo[3]), cvtpk(hi[0], hi[1]), cvtpk(hi[2], hi[3])};
    st16_wt(ws, WS_CTX + (((((size_t)(qt >> 2) * H + h) * 4 + s2) * 4 + (qt & 3)) * 1024) + lane * 16, fr);
  }
}
typedef __attribute__((address_space(1))) unsigned gu32;
struct Args { const u16 *Aq, *Ak, *Av, *shk, *ssmk, *ssmD, *ckv, *wq, *wuk, *wuv, *wo, *gw; u16* out; char* ws; };
typedef const __attribute__((address_space(4))) Args* ArgsP;
DI ArgsP fresh_args(ArgsP p) { asm volatile("" : "+s"(p)); return p; }
DI int fresh_tid(unsigned w) { int l; asm volatile("v_mbcnt_lo_u32_b32 %0, -1, 0\n\tv_mbcnt_hi_u32_b32 %0, -1, %0" : "=v"(l)); return (int)(w << 6) + l; }

__global__ __launch_bounds__(256, 1) void fused_kernel(const u16* Aq_, const u16* Ak_, const u16* Av_, const u16* shk_, const u16* ssmk_, const u16* ssmD_, const u16* ckv_,
    const u16* wq_, const u16* wuk_, const u16* wuv_, const u16* wo_, const u16* gw_, u16* out_, char* ws_) {
  extern __shared__ __attribute__((aligned(16))) char lds[];
  int b = blockIdx.x;
  unsigned w = __builtin_amdgcn_readfirstlane(threadIdx.x >> 6);
  const unsigned ldsb = (unsigned)(size_t)lds;
  ArgsP ka = (ArgsP)__builtin_amdgcn_kernarg_segment_ptr();
  unsigned tag = 0;
  unsigned mode = 0;
#pragma clang loop unroll(disable)
  for (;;) {
    unsigned generic;
    asm volatile(
R"ASM(
FUSED_M_BEG:
s_mov_b32 s98, %1
s_cmp_lg_u32 %6, 0
s_cbranch_scc1 N_resume_%=
s_mov_b64 s[20:21], %2
s_mov_b32 s22, %3
s_mov_b32 s23, %4
s_mov_b32 s24, %5
s_and_b32 s27, s22, 7
s_lshl_b32 s27, s27, 2
s_lshr_b32 s28, s22, 3
s_and_b32 s28, s28, 3
s_add_u32 s25, s27, s28
s_lshr_b32 s26, s22, 5
s_getpc_b64 s[78:79]
FUSED_PC:
s_sub_u32 s78, s78, FUSED_PC-FUSED_M_BEG
s_subb_u32 s79, s79, 0
v_mbcnt_lo_u32_b32 v24, -1, 0
v_mbcnt_hi_u32_b32 v24, -1, v24
v_lshlrev_b32 v24, 7, v24
s_lshr_b32 s27, s22, 3
s_lshl_b32 s27, s27, 2
s_add_u32 s27, s27, s23
s_lshl_b32 s27, s27, 10
v_add_u32 v25, s27, v24
s_mov_b32 s27, FUSED_N_END-FUSED_M_BEG-4
v_min_u32 v25, s27, v25
v_and_b32 v25, 0xfffffffc, v25
s_mov_b32 exec_lo, 0xff
s_mov_b32 exec_hi, 0
global_load_dword v5, v25, s[78:79] offset:0
s_mov_b64 exec, -1
s_load_dwordx2 s[60:61], s[20:21], 0x30
s_load_dwordx2 s[62:63], s[20:21], 0x40
s_load_dwordx2 s[64:65], s[20:21], 0x48
s_load_dwordx2 s[66:67], s[20:21], 0x68
s_load_dwordx2 s[92:93], s[20:21], 0x0
s_load_dwordx2 s[94:95], s[20:21], 0x38
v_mbcnt_lo_u32_b32 v0, -1, 0
v_mbcnt_hi_u32_b32 v0, -1, v0
v_lshlrev_b32 v4, 4, v0
v_lshrrev_b32 v1, 3, v0
v_and_b32 v2, 7, v0
v_lshlrev_b32 v2, 7, v2
v_and_b32 v1, 3, v1
v_lshl_add_u32 v23, v1, 10, v2
v_lshl_add_u32 v7, v1, 13, v2
s_lshr_b32 s27, s22, 3
s_lshl_b32 s28, s27, 16
s_lshl_b32 s29, s23, 14
s_add_u32 s59, s28, s29
s_lshl_b32 s28, s27, 17
s_and_b32 s29, s22, 7
s_lshl_b32 s29, s29, 10
s_add_u32 s75, s28, s29
s_lshl_b32 s29, s23, 15
s_add_u32 s75, s75, s29
v_lshrrev_b32 v1, 4, v0
v_and_b32 v2, 7, v0
v_xor_b32 v2, v2, v1
v_lshlrev_b32 v2, 4, v2
v_lshrrev_b32 v3, 3, v0
v_lshl_add_u32 v2, v3, 10, v2
v_mov_b32 v8, v2
v_xor_b32 v9, 64, v2
v_add_u32 v9, 0x1c00, v9
v_mov_b32 v10, v2
v_add_u32 v10, 0x3800, v10
v_xor_b32 v11, 64, v2
v_add_u32 v11, 0x5400, v11
s_and_b32 s30, s23, 1
s_lshl_b32 s30, s30, 1
v_bfe_u32 v1, v0, 4, 1
v_or_b32 v1, s30, v1
v_and_b32 v2, 3, v0
v_xor_b32 v1, v1, v2
v_lshrrev_b32 v2, 5, v0
v_lshl_add_u32 v1, v2, 2, v1
v_lshlrev_b32 v1, 4, v1
v_bfe_u32 v2, v0, 2, 3
v_lshl_add_u32 v1, v2, 13, v1
v_add_u32 v12, 0x400, v1
v_add_u32 v13, 0x80, v1
s_waitcnt lgkmcnt(0)
s_mov_b32 s36, s60
s_and_b32 s37, s61, 0xffff
s_mov_b32 s38, 0x200000
s_mov_b32 s39, 0x20000
s_sub_u32 s40, s62, 0x400
s_subb_u32 s41, s63, 0
s_and_b32 s41, s41, 0xffff
s_mov_b32 s42, 0x400400
s_mov_b32 s43, 0x20000
s_sub_u32 s44, s64, 0x400
s_subb_u32 s45, s65, 0
s_and_b32 s45, s45, 0xffff
s_mov_b32 s46, 0x400400
s_mov_b32 s47, 0x20000
s_lshl_b32 s27, s25, 20
s_add_u32 s27, s27, 0x100000
s_add_u32 s66, s66, s27
s_addc_u32 s67, s67, 0
s_mov_b32 s48, s66
s_and_b32 s49, s67, 0xffff
s_mov_b32 s50, 0x100000
s_mov_b32 s51, 0x20000
s_lshl_b32 s27, s23, 13
s_add_u32 s52, s24, s27
s_lshl_b32 s27, s23, 11
s_add_u32 s53, s24, s27
s_add_u32 s53, s53, 0x8000
s_add_u32 s69, s53, 0x2000
s_lshl_b32 s27, s26, 18
s_lshl_b32 s28, s23, 16
s_add_u32 s54, s27, s28
s_add_u32 s55, s54, 0x8000
s_lshl_b32 s27, s23, 16
s_lshl_b32 s28, s25, 8
s_add_u32 s56, s27, s28
s_add_u32 s68, s56, 0x40000
s_add_u32 m0, s52, 0
s_nop 0
buffer_load_dwordx4 v8, s[36:39], s54 offen offset:0 lds
buffer_load_dwordx4 v9, s[36:39], s54 offen offset:1024 lds
buffer_load_dwordx4 v10, s[36:39], s54 offen offset:2048 lds
buffer_load_dwordx4 v11, s[36:39], s54 offen offset:3072 lds
s_add_u32 m0, s52, 0x1000
s_nop 0
buffer_load_dwordx4 v8, s[36:39], s55 offen offset:0 lds
buffer_load_dwordx4 v9, s[36:39], s55 offen offset:1024 lds
buffer_load_dwordx4 v10, s[36:39], s55 offen offset:2048 lds
buffer_load_dwordx4 v11, s[36:39], s55 offen offset:3072 lds
s_xor_b32 s52, s52, 0x10000
s_add_u32 s54, s54, 0x80
s_add_u32 s55, s55, 0x80
s_add_u32 m0, s53, 0
s_nop 0
buffer_load_dwordx4 v12, s[40:43], s56 offen offset:0 lds
buffer_load_dwordx4 v13, s[40:43], s56 offen offset:1024 lds
s_add_u32 m0, s53, 0x4000
s_nop 0
buffer_load_dwordx4 v12, s[44:47], s56 offen offset:0 lds
buffer_load_dwordx4 v13, s[44:47], s56 offen offset:1024 lds
s_xor_b32 s53, s53, 0x10000
s_add_u32 s56, s56, 0x80000
s_add_u32 m0, s53, 0
s_nop 0
buffer_load_dwordx4 v12, s[40:43], s56 offen offset:0 lds
buffer_load_dwordx4 v13, s[40:43], s56 offen offset:1024 lds
s_add_u32 m0, s53, 0x4000
s_nop 0
buffer_load_dwordx4 v12, s[44:47], s56 offen offset:0 lds
buffer_load_dwordx4 v13, s[44:47], s56 offen offset:1024 lds
s_xor_b32 s53, s53, 0x10000
s_add_u32 s56, s56, 0x80000
s_add_u32 m0, s69, 0
s_nop 0
buffer_load_dwordx4 v12, s[40:43], s68 offen offset:0 lds
buffer_load_dwordx4 v13, s[40:43], s68 offen offset:1024 lds
s_add_u32 m0, s69, 0x4000
s_nop 0
buffer_load_dwordx4 v12, s[44:47], s68 offen offset:0 lds
buffer_load_dwordx4 v13, s[44:47], s68 offen offset:1024 lds
s_xor_b32 s69, s69, 0x10000
s_add_u32 s68, s68, 0x80000
s_add_u32 m0, s52, 0
s_nop 0
buffer_load_dwordx4 v8, s[36:39], s54 offen offset:0 lds
buffer_load_dwordx4 v9, s[36:39], s54 offen offset:1024 lds
buffer_load_dwordx4 v10, s[36:39], s54 offen offset:2048 lds
buffer_load_dwordx4 v11, s[36:39], s54 offen offset:3072 lds
s_add_u32 m0, s52, 0x1000
s_nop 0
buffer_load_dwordx4 v8, s[36:39], s55 offen offset:0 lds
buffer_load_dwordx4 v9, s[36:39], s55 offen offset:1024 lds
buffer_load_dwordx4 v10, s[36:39], s55 offen offset:2048 lds
buffer_load_dwordx4 v11, s[36:39], s55 offen offset:3072 lds
s_xor_b32 s52, s52, 0x10000
s_add_u32 s54, s54, 0x80
s_add_u32 s55, s55, 0x80
s_lshl_b32 s27, s26, 16
s_and_b32 s28, s23, 1
s_lshl_b32 s28, s28, 15
s_add_u32 s27, s27, s28
s_lshl_b32 s28, s25, 8
s_add_u32 s27, s27, s28
s_add_u32 s74, s27, 0x180400
s_lshr_b32 s27, s22, 3
s_lshl_b32 s27, s27, 16
s_lshl_b32 s28, s23, 14
s_add_u32 s31, s27, s28
s_load_dwordx2 s[78:79], s[20:21], 0x68
v_mov_b32 v60, 0
s_waitcnt lgkmcnt(0)
global_load_dword v61, v60, s[78:79] sc1
s_load_dwordx2 s[60:61], s[20:21], 0x8
s_load_dwordx2 s[62:63], s[20:21], 0x10
s_load_dwordx2 s[64:65], s[20:21], 0x18
s_load_dwordx2 s[66:67], s[20:21], 0x58
s_load_dwordx2 s[96:97], s[20:21], 0x50
s_waitcnt lgkmcnt(0)
s_mov_b32 s76, s60
s_and_b32 s77, s61, 0xffff
s_mov_b32 s78, 0x200000
s_mov_b32 s79, 0x20000
s_mov_b32 s80, s62
s_and_b32 s81, s63, 0xffff
s_mov_b32 s82, 0x200000
s_mov_b32 s83, 0x20000
s_mov_b32 s84, s64
s_and_b32 s85, s65, 0xffff
s_mov_b32 s86, 0x1000
s_mov_b32 s87, 0x20000
s_mov_b32 s88, s66
s_and_b32 s89, s67, 0xffff
s_mov_b32 s90, 0x400
s_mov_b32 s91, 0x20000
s_and_b32 s27, s23, 1
s_lshl_b32 s27, s27, 14
s_add_u32 s27, s27, s24
v_lshrrev_b32 v1, 4, v0
v_bfe_u32 v2, v0, 1, 3
v_xor_b32 v1, v1, v2
v_lshlrev_b32 v1, 4, v1
v_and_b32 v2, 15, v0
v_lshl_add_u32 v1, v2, 7, v1
v_add_u32 v16, s27, v1
v_xor_b32 v17, 64, v16
s_lshr_b32 s28, s23, 1
s_lshl_b32 s28, s28, 14
s_add_u32 s28, s28, s24
v_lshrrev_b32 v1, 4, v0
v_lshlrev_b32 v2, 11, v1
v_bfe_u32 v3, v0, 2, 2
v_lshl_add_u32 v2, v3, 6, v2
v_and_b32 v3, 1, v0
v_lshl_add_u32 v2, v3, 3, v2
v_and_b32 v1, 1, v1
v_bfe_u32 v3, v0, 1, 1
v_lshl_or_b32 v1, v1, 1, v3
v_lshl_add_u32 v2, v1, 4, v2
v_add_u32 v2, s28, v2
v_mov_b32 v18, v2
v_add_u32 v18, 0x8000, v18
v_xor_b32 v19, 16, v2
v_add_u32 v19, 0x8100, v19
v_xor_b32 v20, 32, v2
v_add_u32 v20, 0x8000, v20
v_xor_b32 v21, 48, v2
v_add_u32 v21, 0x8100, v21
s_lshl_b32 s27, s26, 17
s_and_b32 s28, s23, 1
s_lshl_b32 s28, s28, 16
s_add_u32 s58, s27, s28
s_waitcnt vmcnt(17)
s_barrier
s_cmp_ge_u32 s23, 2
s_cbranch_scc1 A_V_%=
ds_read_b128 a[32:35], v16 offset:0
ds_read_b64_tr_b16 a[0:1], v18 offset:0
ds_read_b64_tr_b16 a[2:3], v19 offset:0
ds_read_b128 a[36:39], v16 offset:2048
ds_read_b64_tr_b16 a[4:5], v20 offset:0
ds_read_b64_tr_b16 a[6:7], v21 offset:0
ds_read_b128 a[40:43], v16 offset:4096
ds_read_b64_tr_b16 a[8:9], v18 offset:512
ds_read_b64_tr_b16 a[10:11], v19 offset:512
ds_read_b128 a[44:47], v16 offset:6144
ds_read_b64_tr_b16 a[12:13], v20 offset:512
ds_read_b64_tr_b16 a[14:15], v21 offset:512
ds_read_b128 a[48:51], v16 offset:8192
ds_read_b64_tr_b16 a[16:17], v18 offset:1024
ds_read_b64_tr_b16 a[18:19], v19 offset:1024
ds_read_b128 a[52:55], v16 offset:10240
ds_read_b64_tr_b16 a[20:21], v20 offset:1024
ds_read_b64_tr_b16 a[22:23], v21 offset:1024
ds_read_b128 a[56:59], v16 offset:12288
ds_read_b64_tr_b16 a[24:25], v18 offset:1536
ds_read_b64_tr_b16 a[26:27], v19 offset:1536
ds_read_b128 a[60:63], v16 offset:14336
ds_read_b64_tr_b16 a[28:29], v20 offset:1536
ds_read_b64_tr_b16 a[30:31], v21 offset:1536
s_waitcnt vmcnt(9) lgkmcnt(0)
s_barrier
v_mfma_f32_16x16x32_bf16 v[64:67], a[0:3], a[32:35], 0
ds_read_b128 a[96:99], v17 offset:0
v_mfma_f32_16x16x32_bf16 v[68:71], a[0:3], a[36:39], 0
s_add_u32 m0, s53, 0
v_mfma_f32_16x16x32_bf16 v[72:75], a[0:3], a[40:43], 0
ds_read_b64_tr_b16 a[64:65], v18 offset:8192
v_mfma_f32_16x16x32_bf16 v[76:79], a[0:3], a[44:47], 0
v_mfma_f32_16x16x32_bf16 v[80:83], a[0:3], a[48:51], 0
ds_read_b64_tr_b16 a[66:67], v19 offset:8192
v_mfma_f32_16x16x32_bf16 v[84:87], a[0:3], a[52:55], 0
buffer_load_dwordx4 v12, s[40:43], s56 offen offset:0 lds
v_mfma_f32_16x16x32_bf16 v[88:91], a[0:3], a[56:59], 0
ds_read_b128 a[100:103], v17 offset:2048
v_mfma_f32_16x16x32_bf16 v[92:95], a[0:3], a[60:63], 0
v_mfma_f32_16x16x32_bf16 v[96:99], a[4:7], a[32:35], 0
ds_read_b64_tr_b16 a[68:69], v20 offset:8192
v_mfma_f32_16x16x32_bf16 v[100:103], a[4:7], a[36:39], 0
buffer_load_dwordx4 v13, s[40:43], s56 offen offset:1024 lds
v_mfma_f32_16x16x32_bf16 v[104:107], a[4:7], a[40:43], 0
ds_read_b64_tr_b16 a[70:71], v21 offset:8192
v_mfma_f32_16x16x32_bf16 v[108:111], a[4:7], a[44:47], 0
v_mfma_f32_16x16x32_bf16 v[112:115], a[4:7], a[48:51], 0
ds_read_b128 a[104:107], v17 offset:4096
v_mfma_f32_16x16x32_bf16 v[116:119], a[4:7], a[52:55], 0
v_mfma_f32_16x16x32_bf16 v[120:123], a[4:7], a[56:59], 0
ds_read_b64_tr_b16 a[72:73], v18 offset:8704
v_mfma_f32_16x16x32_bf16 v[124:127], a[4:7], a[60:63], 0
s_add_u32 m0, s53, 0x4000
v_mfma_f32_16x16x32_bf16 v[128:131], a[8:11], a[32:35], 0
ds_read_b64_tr_b16 a[74:75], v19 offset:8704
v_mfma_f32_16x16x32_bf16 v[132:135], a[8:11], a[36:39], 0
v_mfma_f32_16x16x32_bf16 v[136:139], a[8:11], a[40:43], 0
ds_read_b128 a[108:111], v17 offset:6144
v_mfma_f32_16x16x32_bf16 v[140:143], a[8:11], a[44:47], 0
buffer_load_dwordx4 v12, s[44:47], s56 offen offset:0 lds
v_mfma_f32_16x16x32_bf16 v[144:147], a[8:11], a[48:51], 0
ds_read_b64_tr_b16 a[76:77], v20 offset:8704
v_mfma_f32_16x16x32_bf16 v[148:151], a[8:11], a[52:55], 0
v_mfma_f32_16x16x32_bf16 v[152:155], a[8:11], a[56:59], 0
ds_read_b64_tr_b16 a[78:79], v21 offset:8704
v_mfma_f32_16x16x32_bf16 v[156:159], a[8:11], a[60:63], 0
buffer_load_dwordx4 v13, s[44:47], s56 offen offset:1024 lds
v_mfma_f32_16x16x32_bf16 v[160:163], a[12:15], a[32:35], 0
ds_read_b128 a[112:115], v17 offset:8192
v_mfma_f32_16x16x32_bf16 v[164:167], a[12:15], a[36:39], 0
v_mfma_f32_16x16x32_bf16 v[168:171], a[12:15], a[40:43], 0
ds_read_b64_tr_b16 a[80:81], v18 offset:9216
v_mfma_f32_16x16x32_bf16 v[172:175], a[12:15], a[44:47], 0
s_xor_b32 s53, s53, 0x10000
s_add_u32 s56, s56, 0x80000
v_mfma_f32_16x16x32_bf16 v[176:179], a[12:15], a[48:51], 0
ds_read_b64_tr_b16 a[82:83], v19 offset:9216
v_mfma_f32_16x16x32_bf16 v[180:183], a[12:15], a[52:55], 0
v_mfma_f32_16x16x32_bf16 v[184:187], a[12:15], a[56:59], 0
ds_read_b128 a[116:119], v17 offset:10240
v_mfma_f32_16x16x32_bf16 v[188:191], a[12:15], a[60:63], 0
s_add_u32 m0, s69, 0
v_mfma_f32_16x16x32_bf16 v[192:195], a[16:19], a[32:35], 0
ds_read_b64_tr_b16 a[84:85], v20 offset:9216
v_mfma_f32_16x16x32_bf16 v[196:199], a[16:19], a[36:39], 0
v_mfma_f32_16x16x32_bf16 v[200:203], a[16:19], a[40:43], 0
ds_read_b64_tr_b16 a[86:87], v21 offset:9216
v_mfma_f32_16x16x32_bf16 v[204:207], a[16:19], a[44:47], 0
buffer_load_dwordx4 v12, s[40:43], s68 offen offset:0 lds
v_mfma_f32_16x16x32_bf16 v[208:211], a[16:19], a[48:51], 0
ds_read_b128 a[120:123], v17 offset:12288
v_mfma_f32_16x16x32_bf16 v[212:215], a[16:19], a[52:55], 0
v_mfma_f32_16x16x32_bf16 v[216:219], a[16:19], a[56:59], 0
ds_read_b64_tr_b16 a[88:89], v18 offset:9728
v_mfma_f32_16x16x32_bf16 v[220:223], a[16:19], a[60:63], 0
v_mfma_f32_16x16x32_bf16 v[224:227], a[20:23], a[32:35], 0
ds_read_b64_tr_b16 a[90:91], v19 offset:9728
v_mfma_f32_16x16x32_bf16 v[228:231], a[20:23], a[36:39], 0
buffer_load_dwordx4 v13, s[40:43], s68 offen offset:1024 lds
v_mfma_f32_16x16x32_bf16 v[232:235], a[20:23], a[40:43], 0
ds_read_b128 a[124:127], v17 offset:14336
v_mfma_f32_16x16x32_bf16 v[236:239], a[20:23], a[44:47], 0
v_mfma_f32_16x16x32_bf16 v[240:243], a[20:23], a[48:51], 0
ds_read_b64_tr_b16 a[92:93], v20 offset:9728
v_mfma_f32_16x16x32_bf16 v[244:247], a[20:23], a[52:55], 0
s_add_u32 m0, s69, 0x4000
v_mfma_f32_16x16x32_bf16 v[248:251], a[20:23], a[56:59], 0
ds_read_b64_tr_b16 a[94:95], v21 offset:9728
v_mfma_f32_16x16x32_bf16 v[252:255], a[20:23], a[60:63], 0
v_mfma_f32_16x16x32_bf16 a[128:131], a[24:27], a[32:35], 0
buffer_load_dwordx4 v12, s[44:47], s68 offen offset:0 lds
v_mfma_f32_16x16x32_bf16 a[132:135], a[24:27], a[36:39], 0
v_mfma_f32_16x16x32_bf16 a[136:139], a[24:27], a[40:43], 0
v_mfma_f32_16x16x32_bf16 a[140:143], a[24:27], a[44:47], 0
v_mfma_f32_16x16x32_bf16 a[144:147], a[24:27], a[48:51], 0
v_mfma_f32_16x16x32_bf16 a[148:151], a[24:27], a[52:55], 0
buffer_load_dwordx4 v13, s[44:47], s68 offen offset:1024 lds
v_mfma_f32_16x16x32_bf16 a[152:155], a[24:27], a[56:59], 0
v_mfma_f32_16x16x32_bf16 a[156:159], a[24:27], a[60:63], 0
v_mfma_f32_16x16x32_bf16 a[160:163], a[28:31], a[32:35], 0
v_mfma_f32_16x16x32_bf16 a[164:167], a[28:31], a[36:39], 0
s_xor_b32 s69, s69, 0x10000
s_add_u32 s68, s68, 0x80000
v_mfma_f32_16x16x32_bf16 a[168:171], a[28:31], a[40:43], 0
v_mfma_f32_16x16x32_bf16 a[172:175], a[28:31], a[44:47], 0
v_mfma_f32_16x16x32_bf16 a[176:179], a[28:31], a[48:51], 0
v_mfma_f32_16x16x32_bf16 a[180:183], a[28:31], a[52:55], 0
v_mfma_f32_16x16x32_bf16 a[184:187], a[28:31], a[56:59], 0
v_mfma_f32_16x16x32_bf16 a[188:191], a[28:31], a[60:63], 0
s_waitcnt vmcnt(9) lgkmcnt(0)
s_barrier
v_xor_b32 v16, 0x10000, v16
v_xor_b32 v17, 0x10000, v17
v_xor_b32 v18, 0x10000, v18
v_xor_b32 v19, 0x10000, v19
v_xor_b32 v20, 0x10000, v20
v_xor_b32 v21, 0x10000, v21
v_mfma_f32_16x16x32_bf16 v[64:67], a[64:67], a[96:99], v[64:67]
ds_read_b128 a[32:35], v16 offset:0
v_mfma_f32_16x16x32_bf16 v[68:71], a[64:67], a[100:103], v[68:71]
s_add_u32 m0, s52, 0
v_mfma_f32_16x16x32_bf16 v[72:75], a[64:67], a[104:107], v[72:75]
ds_read_b64_tr_b16 a[0:1], v18 offset:0
v_mfma_f32_16x16x32_bf16 v[76:79], a[64:67], a[108:111], v[76:79]
v_mfma_f32_16x16x32_bf16 v[80:83], a[64:67], a[112:115], v[80:83]
ds_read_b64_tr_b16 a[2:3], v19 offset:0
v_mfma_f32_16x16x32_bf16 v[84:87], a[64:67], a[116:119], v[84:87]
v_mfma_f32_16x16x32_bf16 v[88:91], a[64:67], a[120:123], v[88:91]
ds_read_b128 a[36:39], v16 offset:2048
v_mfma_f32_16x16x32_bf16 v[92:95], a[64:67], a[124:127], v[92:95]
buffer_load_dwordx4 v8, s[36:39], s54 offen offset:0 lds
v_mfma_f32_16x16x32_bf16 v[96:99], a[68:71], a[96:99], v[96:99]
ds_read_b64_tr_b16 a[4:5], v20 offset:0
v_mfma_f32_16x16x32_bf16 v[100:103], a[68:71], a[100:103], v[100:103]
v_mfma_f32_16x16x32_bf16 v[104:107], a[68:71], a[104:107], v[104:107]
ds_read_b64_tr_b16 a[6:7], v21 offset:0
v_mfma_f32_16x16x32_bf16 v[108:111], a[68:71], a[108:111], v[108:111]
v_mfma_f32_16x16x32_bf16 v[112:115], a[68:71], a[112:115], v[112:115]
ds_read_b128 a[40:43], v16 offset:4096
v_mfma_f32_16x16x32_bf16 v[116:119], a[68:71], a[116:119], v[116:119]
buffer_load_dwordx4 v9, s[36:39], s54 offen offset:1024 lds
v_mfma_f32_16x16x32_bf16 v[120:123], a[68:71], a[120:123], v[120:123]
ds_read_b64_tr_b16 a[8:9], v18 offset:512
v_mfma_f32_16x16x32_bf16 v[124:127], a[68:71], a[124:127], v[124:127]
v_mfma_f32_16x16x32_bf16 v[128:131], a[72:75], a[96:99], v[128:131]
ds_read_b64_tr_b16 a[10:11], v19 offset:512
v_mfma_f32_16x16x32_bf16 v[132:135], a[72:75], a[100:103], v[132:135]
buffer_load_dwordx4 v10, s[36:39], s54 offen offset:2048 lds
v_mfma_f32_16x16x32_bf16 v[136:139], a[72:75], a[104:107], v[136:139]
ds_read_b128 a[44:47], v16 offset:6144
v_mfma_f32_16x16x32_bf16 v[140:143], a[72:75], a[108:111], v[140:143]
v_mfma_f32_16x16x32_bf16 v[144:147], a[72:75], a[112:115], v[144:147]
ds_read_b64_tr_b16 a[12:13], v20 offset:512
v_mfma_f32_16x16x32_bf16 v[148:151], a[72:75], a[116:119], v[148:151]
v_mfma_f32_16x16x32_bf16 v[152:155], a[72:75], a[120:123], v[152:155]
ds_read_b64_tr_b16 a[14:15], v21 offset:512
v_mfma_f32_16x16x32_bf16 v[156:159], a[72:75], a[124:127], v[156:159]
buffer_load_dwordx4 v11, s[36:39], s54 offen offset:3072 lds
v_mfma_f32_16x16x32_bf16 v[160:163], a[76:79], a[96:99], v[160:163]
ds_read_b128 a[48:51], v16 offset:8192
v_mfma_f32_16x16x32_bf16 v[164:167], a[76:79], a[100:103], v[164:167]
v_mfma_f32_16x16x32_bf16 v[168:171], a[76:79], a[104:107], v[168:171]
ds_read_b64_tr_b16 a[16:17], v18 offset:1024
v_mfma_f32_16x16x32_bf16 v[172:175], a[76:79], a[108:111], v[172:175]
v_mfma_f32_16x16x32_bf16 v[176:179], a[76:79], a[112:115], v[176:179]
ds_read_b64_tr_b16 a[18:19], v19 offset:1024
v_mfma_f32_16x16x32_bf16 v[180:183], a[76:79], a[116:119], v[180:183]
s_add_u32 m0, s52, 0x1000
v_mfma_f32_16x16x32_bf16 v[184:187], a[76:79], a[120:123], v[184:187]
ds_read_b128 a[52:55], v16 offset:10240
v_mfma_f32_16x16x32_bf16 v[188:191], a[76:79], a[124:127], v[188:191]
v_mfma_f32_16x16x32_bf16 v[192:195], a[80:83], a[96:99], v[192:195]
ds_read_b64_tr_b16 a[20:21], v20 offset:1024
v_mfma_f32_16x16x32_bf16 v[196:199], a[80:83], a[100:103], v[196:199]
v_mfma_f32_16x16x32_bf16 v[200:203], a[80:83], a[104:107], v[200:203]
ds_read_b64_tr_b16 a[22:23], v21 offset:1024
v_mfma_f32_16x16x32_bf16 v[204:207], a[80:83], a[108:111], v[204:207]
buffer_load_dwordx4 v8, s[36:39], s55 offen offset:0 lds
v_mfma_f32_16x16x32_bf16 v[208:211], a[80:83], a[112:115], v[208:211]
ds_read_b128 a[56:59], v16 offset:12288
v_mfma_f32_16x16x32_bf16 v[212:215], a[80:83], a[116:119], v[212:215]
v_mfma_f32_16x16x32_bf16 v[216:219], a[80:83], a[120:123], v[216:219]
ds_read_b64_tr_b16 a[24:25], v18 offset:1536
v_mfma_f32_16x16x32_bf16 v[220:223], a[80:83], a[124:127], v[220:223]
buffer_load_dwordx4 v9, s[36:39], s55 offen offset:1024 lds
v_mfma_f32_16x16x32_bf16 v[224:227], a[84:87], a[96:99], v[224:227]
ds_read_b64_tr_b16 a[26:27], v19 offset:1536
v_mfma_f32_16x16x32_bf16 v[228:231], a[84:87], a[100:103], v[228:231]
v_mfma_f32_16x16x32_bf16 v[232:235], a[84:87], a[104:107], v[232:235]
ds_read_b128 a[60:63], v16 offset:14336
v_mfma_f32_16x16x32_bf16 v[236:239], a[84:87], a[108:111], v[236:239]
v_mfma_f32_16x16x32_bf16 v[240:243], a[84:87], a[112:115], v[240:243]
ds_read_b64_tr_b16 a[28:29], v20 offset:1536
v_mfma_f32_16x16x32_bf16 v[244:247], a[84:87], a[116:119], v[244:247]
buffer_load_dwordx4 v10, s[36:39], s55 offen offset:2048 lds
v_mfma_f32_16x16x32_bf16 v[248:251], a[84:87], a[120:123], v[248:251]
ds_read_b64_tr_b16 a[30:31], v21 offset:1536
v_mfma_f32_16x16x32_bf16 v[252:255], a[84:87], a[124:127], v[252:255]
v_mfma_f32_16x16x32_bf16 a[128:131], a[88:91], a[96:99], a[128:131]
v_mfma_f32_16x16x32_bf16 a[132:135], a[88:91], a[100:103], a[132:135]
v_mfma_f32_16x16x32_bf16 a[136:139], a[88:91], a[104:107], a[136:139]
buffer_load_dwordx4 v11, s[36:39], s55 offen offset:3072 lds
v_mfma_f32_16x16x32_bf16 a[140:143], a[88:91], a[108:111], a[140:143]
v_mfma_f32_16x16x32_bf16 a[144:147], a[88:91], a[112:115], a[144:147]
v_mfma_f32_16x16x32_bf16 a[148:151], a[88:91], a[116:119], a[148:151]
v_mfma_f32_16x16x32_bf16 a[152:155], a[88:91], a[120:123], a[152:155]
v_mfma_f32_16x16x32_bf16 a[156:159], a[88:91], a[124:127], a[156:159]
v_mfma_f32_16x16x32_bf16 a[160:163], a[92:95], a[96:99], a[160:163]
s_xor_b32 s52, s52, 0x10000
s_add_u32 s54, s54, 0x80
s_add_u32 s55, s55, 0x80
v_mfma_f32_16x16x32_bf16 a[164:167], a[92:95], a[100:103], a[164:167]
v_mfma_f32_16x16x32_bf16 a[168:171], a[92:95], a[104:107], a[168:171]
v_mfma_f32_16x16x32_bf16 a[172:175], a[92:95], a[108:111], a[172:175]
v_mfma_f32_16x16x32_bf16 a[176:179], a[92:95], a[112:115], a[176:179]
v_mfma_f32_16x16x32_bf16 a[180:183], a[92:95], a[116:119], a[180:183]
v_mfma_f32_16x16x32_bf16 a[184:187], a[92:95], a[120:123], a[184:187]
v_mfma_f32_16x16x32_bf16 a[188:191], a[92:95], a[124:127], a[188:191]
s_waitcnt vmcnt(8) lgkmcnt(0)
s_barrier
v_mfma_f32_16x16x32_bf16 v[64:67], a[0:3], a[32:35], v[64:67]
ds_read_b128 a[96:99], v17 offset:0
v_mfma_f32_16x16x32_bf16 v[68:71], a[0:3], a[36:39], v[68:71]
s_add_u32 m0, s53, 0
v_mfma_f32_16x16x32_bf16 v[72:75], a[0:3], a[40:43], v[72:75]
ds_read_b64_tr_b16 a[64:65], v18 offset:8192
v_mfma_f32_16x16x32_bf16 v[76:79], a[0:3], a[44:47], v[76:79]
v_mfma_f32_16x16x32_bf16 v[80:83], a[0:3], a[48:51], v[80:83]
ds_read_b64_tr_b16 a[66:67], v19 offset:8192
v_mfma_f32_16x16x32_bf16 v[84:87], a[0:3], a[52:55], v[84:87]
buffer_load_dwordx4 v12, s[40:43], s56 offen offset:0 lds
v_mfma_f32_16x16x32_bf16 v[88:91], a[0:3], a[56:59], v[88:91]
ds_read_b128 a[100:103], v17 offset:2048
v_mfma_f32_16x16x32_bf16 v[92:95], a[0:3], a[60:63], v[92:95]
v_mfma_f32_16x16x32_bf16 v[96:99], a[4:7], a[32:35], v[96:99]
ds_read_b64_tr_b16 a[68:69], v20 offset:8192
v_mfma_f32_16x16x32_bf16 v[100:103], a[4:7], a[36:39], v[100:103]
buffer_load_dwordx4 v13, s[40:43], s56 offen offset:1024 lds
v_mfma_f32_16x16x32_bf16 v[104:107], a[4:7], a[40:43], v[104:107]
ds_read_b64_tr_b16 a[70:71], v21 offset:8192
v_mfma_f32_16x16x32_bf16 v[108:111], a[4:7], a[44:47], v[108:111]
v_mfma_f32_16x16x32_bf16 v[112:115], a[4:7], a[48:51], v[112:115]
ds_read_b128 a[104:107], v17 offset:4096
v_mfma_f32_16x16x32_bf16 v[116:119], a[4:7], a[52:55], v[116:119]
v_mfma_f32_16x16x32_bf16 v[120:123], a[4:7], a[56:59], v[120:123]
ds_read_b64_tr_b16 a[72:73], v18 offset:8704
v_mfma_f32_16x16x32_bf16 v[124:127], a[4:7], a[60:63], v[124:127]
s_add_u32 m0, s53, 0x4000
v_mfma_f32_16x16x32_bf16 v[128:131], a[8:11], a[32:35], v[128:131]
ds_read_b64_tr_b16 a[74:75], v19 offset:8704
v_mfma_f32_16x16x32_bf16 v[132:135], a[8:11], a[36:39], v[132:135]
v_mfma_f32_16x16x32_bf16 v[136:139], a[8:11], a[40:43], v[136:139]
ds_read_b128 a[108:111], v17 offset:6144
v_mfma_f32_16x16x32_bf16 v[140:143], a[8:11], a[44:47], v[140:143]
buffer_load_dwordx4 v12, s[44:47], s56 offen offset:0 lds
v_mfma_f32_16x16x32_bf16 v[144:147], a[8:11], a[48:51], v[144:147]
ds_read_b64_tr_b16 a[76:77], v20 offset:8704
v_mfma_f32_16x16x32_bf16 v[148:151], a[8:11], a[52:55], v[148:151]
v_mfma_f32_16x16x32_bf16 v[152:155], a[8:11], a[56:59], v[152:155]
ds_read_b64_tr_b16 a[78:79], v21 offset:8704
v_mfma_f32_16x16x32_bf16 v[156:159], a[8:11], a[60:63], v[156:159]
buffer_load_dwordx4 v13, s[44:47], s56 offen offset:1024 lds
v_mfma_f32_16x16x32_bf16 v[160:163], a[12:15], a[32:35], v[160:163]
ds_read_b128 a[112:115], v17 offset:8192
v_mfma_f32_16x16x32_bf16 v[164:167], a[12:15], a[36:39], v[164:167]
v_mfma_f32_16x16x32_bf16 v[168:171], a[12:15], a[40:43], v[168:171]
ds_read_b64_tr_b16 a[80:81], v18 offset:9216
v_mfma_f32_16x16x32_bf16 v[172:175], a[12:15], a[44:47], v[172:175]
s_xor_b32 s53, s53, 0x10000
s_add_u32 s56, s56, 0x80000
v_mfma_f32_16x16x32_bf16 v[176:179], a[12:15], a[48:51], v[176:179]
ds_read_b64_tr_b16 a[82:83], v19 offset:9216
v_mfma_f32_16x16x32_bf16 v[180:183], a[12:15], a[52:55], v[180:183]
v_mfma_f32_16x16x32_bf16 v[184:187], a[12:15], a[56:59], v[184:187]
ds_read_b128 a[116:119], v17 offset:10240
v_mfma_f32_16x16x32_bf16 v[188:191], a[12:15], a[60:63], v[188:191]
s_add_u32 m0, s69, 0
v_mfma_f32_16x16x32_bf16 v[192:195], a[16:19], a[32:35], v[192:195]
ds_read_b64_tr_b16 a[84:85], v20 offset:9216
v_mfma_f32_16x16x32_bf16 v[196:199], a[16:19], a[36:39], v[196:199]
v_mfma_f32_16x16x32_bf16 v[200:203], a[16:19], a[40:43], v[200:203]
ds_read_b64_tr_b16 a[86:87], v21 offset:9216
v_mfma_f32_16x16x32_bf16 v[204:207], a[16:19], a[44:47], v[204:207]
buffer_load_dwordx4 v12, s[40:43], s68 offen offset:0 lds
v_mfma_f32_16x16x32_bf16 v[208:211], a[16:19], a[48:51], v[208:211]
ds_read_b128 a[120:123], v17 offset:12288
v_mfma_f32_16x16x32_bf16 v[212:215], a[16:19], a[52:55], v[212:215]
v_mfma_f32_16x16x32_bf16 v[216:219], a[16:19], a[56:59], v[216:219]
ds_read_b64_tr_b16 a[88:89], v18 offset:9728
v_mfma_f32_16x16x32_bf16 v[220:223], a[16:19], a[60:63], v[220:223]
v_mfma_f32_16x16x32_bf16 v[224:227], a[20:23], a[32:35], v[224:227]
ds_read_b64_tr_b16 a[90:91], v19 offset:9728
v_mfma_f32_16x16x32_bf16 v[228:231], a[20:23], a[36:39], v[228:231]
buffer_load_dwordx4 v13, s[40:43], s68 offen offset:1024 lds
v_mfma_f32_16x16x32_bf16 v[232:235], a[20:23], a[40:43], v[232:235]
ds_read_b128 a[124:127], v17 offset:14336
v_mfma_f32_16x16x32_bf16 v[236:239], a[20:23], a[44:47], v[236:239]
v_mfma_f32_16x16x32_bf16 v[240:243], a[20:23], a[48:51], v[240:243]
ds_read_b64_tr_b16 a[92:93], v20 offset:9728
v_mfma_f32_16x16x32_bf16 v[244:247], a[20:23], a[52:55], v[244:247]
s_add_u32 m0, s69, 0x4000
v_mfma_f32_16x16x32_bf16 v[248:251], a[20:23], a[56:59], v[248:251]
ds_read_b64_tr_b16 a[94:95], v21 offset:9728
v_mfma_f32_16x16x32_bf16 v[252:255], a[20:23], a[60:63], v[252:255]
v_mfma_f32_16x16x32_bf16 a[128:131], a[24:27], a[32:35], a[128:131]
buffer_load_dwordx4 v12, s[44:47], s68 offen offset:0 lds
v_mfma_f32_16x16x32_bf16 a[132:135], a[24:27], a[36:39], a[132:135]
v_mfma_f32_16x16x32_bf16 a[136:139], a[24:27], a[40:43], a[136:139]
v_mfma_f32_16x16x32_bf16 a[140:143], a[24:27], a[44:47], a[140:143]
v_mfma_f32_16x16x32_bf16 a[144:147], a[24:27], a[48:51], a[144:147]
v_mfma_f32_16x16x32_bf16 a[148:151], a[24:27], a[52:55], a[148:151]
buffer_load_dwordx4 v13, s[44:47], s68 offen offset:1024 lds
v_mfma_f32_16x16x32_bf16 a[152:155], a[24:27], a[56:59], a[152:155]
v_mfma_f32_16x16x32_bf16 a[156:159], a[24:27], a[60:63], a[156:159]
v_mfma_f32_16x16x32_bf16 a[160:163], a[28:31], a[32:35], a[160:163]
v_mfma_f32_16x16x32_bf16 a[164:167], a[28:31], a[36:39], a[164:167]
s_xor_b32 s69, s69, 0x10000
s_add_u32 s68, s68, 0x80000
v_mfma_f32_16x16x32_bf16 a[168:171], a[28:31], a[40:43], a[168:171]
v_mfma_f32_16x16x32_bf16 a[172:175], a[28:31], a[44:47], a[172:175]
v_mfma_f32_16x16x32_bf16 a[176:179], a[28:31], a[48:51], a[176:179]
v_mfma_f32_16x16x32_bf16 a[180:183], a[28:31], a[52:55], a[180:183]
v_mfma_f32_16x16x32_bf16 a[184:187], a[28:31], a[56:59], a[184:187]
v_mfma_f32_16x16x32_bf16 a[188:191], a[28:31], a[60:63], a[188:191]
s_waitcnt vmcnt(8) lgkmcnt(0)
s_barrier
v_xor_b32 v16, 0x10000, v16
v_xor_b32 v17, 0x10000, v17
v_xor_b32 v18, 0x10000, v18
v_xor_b32 v19, 0x10000, v19
v_xor_b32 v20, 0x10000, v20
v_xor_b32 v21, 0x10000, v21
v_mfma_f32_16x16x32_bf16 v[64:67], a[64:67], a[96:99], v[64:67]
ds_read_b128 a[32:35], v16 offset:0
v_mfma_f32_16x16x32_bf16 v[68:71], a[64:67], a[100:103], v[68:71]
s_add_u32 m0, s52, 0
v_mfma_f32_16x16x32_bf16 v[72:75], a[64:67], a[104:107], v[72:75]
ds_read_b64_tr_b16 a[0:1], v18 offset:0
v_mfma_f32_16x16x32_bf16 v[76:79], a[64:67], a[108:111], v[76:79]
v_mfma_f32_16x16x32_bf16 v[80:83], a[64:67], a[112:115], v[80:83]
ds_read_b64_tr_b16 a[2:3], v19 offset:0
v_mfma_f32_16x16x32_bf16 v[84:87], a[64:67], a[116:119], v[84:87]
v_mfma_f32_16x16x32_bf16 v[88:91], a[64:67], a[120:123], v[88:91]
ds_read_b128 a[36:39], v16 offset:2048
v_mfma_f32_16x16x32_bf16 v[92:95], a[64:67], a[124:127], v[92:95]
buffer_load_dwordx4 v8, s[36:39], s54 offen offset:0 lds
v_mfma_f32_16x16x32_bf16 v[96:99], a[68:71], a[96:99], v[96:99]
ds_read_b64_tr_b16 a[4:5], v20 offset:0
v_mfma_f32_16x16x32_bf16 v[100:103], a[68:71], a[100:103], v[100:103]
v_mfma_f32_16x16x32_bf16 v[104:107], a[68:71], a[104:107], v[104:107]
ds_read_b64_tr_b16 a[6:7], v21 offset:0
v_mfma_f32_16x16x32_bf16 v[108:111], a[68:71], a[108:111], v[108:111]
v_mfma_f32_16x16x32_bf16 v[112:115], a[68:71], a[112:115], v[112:115]
ds_read_b128 a[40:43], v16 offset:4096
v_mfma_f32_16x16x32_bf16 v[116:119], a[68:71], a[116:119], v[116:119]
buffer_load_dwordx4 v9, s[36:39], s54 offen offset:1024 lds
v_mfma_f32_16x16x32_bf16 v[120:123], a[68:71], a[120:123], v[120:123]
ds_read_b64_tr_b16 a[8:9], v18 offset:512
v_mfma_f32_16x16x32_bf16 v[124:127], a[68:71], a[124:127], v[124:127]
v_mfma_f32_16x16x32_bf16 v[128:131], a[72:75], a[96:99], v[128:131]
ds_read_b64_tr_b16 a[10:11], v19 offset:512
v_mfma_f32_16x16x32_bf16 v[132:135], a[72:75], a[100:103], v[132:135]
buffer_load_dwordx4 v10, s[36:39], s54 offen offset:2048 lds
v_mfma_f32_16x16x32_bf16 v[136:139], a[72:75], a[104:107], v[136:139]
ds_read_b128 a[44:47], v16 offset:6144
v_mfma_f32_16x16x32_bf16 v[140:143], a[72:75], a[108:111], v[140:143]
v_mfma_f32_16x16x32_bf16 v[144:147], a[72:75], a[112:115], v[144:147]
ds_read_b64_tr_b16 a[12:13], v20 offset:512
v_mfma_f32_16x16x32_bf16 v[148:151], a[72:75], a[116:119], v[148:151]
v_mfma_f32_16x16x32_bf16 v[152:155], a[72:75], a[120:123], v[152:155]
ds_read_b64_tr_b16 a[14:15], v21 offset:512
v_mfma_f32_16x16x32_bf16 v[156:159], a[72:75], a[124:127], v[156:159]
buffer_load_dwordx4 v11, s[36:39], s54 offen offset:3072 lds
v_mfma_f32_16x16x32_bf16 v[160:163], a[76:79], a[96:99], v[160:163]
ds_read_b128 a[48:51], v16 offset:8192
v_mfma_f32_16x16x32_bf16 v[164:167], a[76:79], a[100:103], v[164:167]
v_mfma_f32_16x16x32_bf16 v[168:171], a[76:79], a[104:107], v[168:171]
ds_read_b64_tr_b16 a[16:17], v18 offset:1024
v_mfma_f32_16x16x32_bf16 v[172:175], a[76:79], a[108:111], v[172:175]
v_mfma_f32_16x16x32_bf16 v[176:179], a[76:79], a[112:115], v[176:179]
ds_read_b64_tr_b16 a[18:19], v19 offset:1024
v_mfma_f32_16x16x32_bf16 v[180:183], a[76:79], a[116:119], v[180:183]
s_add_u32 m0, s52, 0x1000
v_mfma_f32_16x16x32_bf16 v[184:187], a[76:79], a[120:123], v[184:187]
ds_read_b128 a[52:55], v16 offset:10240
v_mfma_f32_16x16x32_bf16 v[188:191], a[76:79], a[124:127], v[188:191]
v_mfma_f32_16x16x32_bf16 v[192:195], a[80:83], a[96:99], v[192:195]
ds_read_b64_tr_b16 a[20:21], v20 offset:1024
v_mfma_f32_16x16x32_bf16 v[196:199], a[80:83], a[100:103], v[196:199]
v_mfma_f32_16x16x32_bf16 v[200:203], a[80:83], a[104:107], v[200:203]
ds_read_b64_tr_b16 a[22:23], v21 offset:1024
v_mfma_f32_16x16x32_bf16 v[204:207], a[80:83], a[108:111], v[204:207]
buffer_load_dwordx4 v8, s[36:39], s55 offen offset:0 lds
v_mfma_f32_16x16x32_bf16 v[208:211], a[80:83], a[112:115], v[208:211]
ds_read_b128 a[56:59], v16 offset:12288
v_mfma_f32_16x16x32_bf16 v[212:215], a[80:83], a[116:119], v[212:215]
v_mfma_f32_16x16x32_bf16 v[216:219], a[80:83], a[120:123], v[216:219]
ds_read_b64_tr_b16 a[24:25], v18 offset:1536
v_mfma_f32_16x16x32_bf16 v[220:223], a[80:83], a[124:127], v[220:223]
buffer_load_dwordx4 v9, s[36:39], s55 offen offset:1024 lds
v_mfma_f32_16x16x32_bf16 v[224:227], a[84:87], a[96:99], v[224:227]
ds_read_b64_tr_b16 a[26:27], v19 offset:1536
v_mfma_f32_16x16x32_bf16 v[228:231], a[84:87], a[100:103], v[228:231]
v_mfma_f32_16x16x32_bf16 v[232:235], a[84:87], a[104:107], v[232:235]
ds_read_b128 a[60:63], v16 offset:14336
v_mfma_f32_16x16x32_bf16 v[236:239], a[84:87], a[108:111], v[236:239]
v_mfma_f32_16x16x32_bf16 v[240:243], a[84:87], a[112:115], v[240:243]
ds_read_b64_tr_b16 a[28:29], v20 offset:1536
v_mfma_f32_16x16x32_bf16 v[244:247], a[84:87], a[116:119], v[244:247]
buffer_load_dwordx4 v10, s[36:39], s55 offen offset:2048 lds
v_mfma_f32_16x16x32_bf16 v[248:251], a[84:87], a[120:123], v[248:251]
ds_read_b64_tr_b16 a[30:31], v21 offset:1536
v_mfma_f32_16x16x32_bf16 v[252:255], a[84:87], a[124:127], v[252:255]
v_mfma_f32_16x16x32_bf16 a[128:131], a[88:91], a[96:99], a[128:131]
v_mfma_f32_16x16x32_bf16 a[132:135], a[88:91], a[100:103], a[132:135]
v_mfma_f32_16x16x32_bf16 a[136:139], a[88:91], a[104:107], a[136:139]
buffer_load_dwordx4 v11, s[36:39], s55 offen offset:3072 lds
v_mfma_f32_16x16x32_bf16 a[140:143], a[88:91], a[108:111], a[140:143]
v_mfma_f32_16x16x32_bf16 a[144:147], a[88:91], a[112:115], a[144:147]
v_mfma_f32_16x16x32_bf16 a[148:151], a[88:91], a[116:119], a[148:151]
v_mfma_f32_16x16x32_bf16 a[152:155], a[88:91], a[120:123], a[152:155]
v_mfma_f32_16x16x32_bf16 a[156:159], a[88:91], a[124:127], a[156:159]
v_mfma_f32_16x16x32_bf16 a[160:163], a[92:95], a[96:99], a[160:163]
s_xor_b32 s52, s52, 0x10000
s_add_u32 s54, s54, 0x80
s_add_u32 s55, s55, 0x80
v_mfma_f32_16x16x32_bf16 a[164:167], a[92:95], a[100:103], a[164:167]
v_mfma_f32_16x16x32_bf16 a[168:171], a[92:95], a[104:107], a[168:171]
v_mfma_f32_16x16x32_bf16 a[172:175], a[92:95], a[108:111], a[172:175]
v_mfma_f32_16x16x32_bf16 a[176:179], a[92:95], a[112:115], a[176:179]
v_mfma_f32_16x16x32_bf16 a[180:183], a[92:95], a[116:119], a[180:183]
v_mfma_f32_16x16x32_bf16 a[184:187], a[92:95], a[120:123], a[184:187]
v_mfma_f32_16x16x32_bf16 a[188:191], a[92:95], a[124:127], a[188:191]
s_waitcnt vmcnt(8) lgkmcnt(0)
s_barrier
v_mfma_f32_16x16x32_bf16 v[64:67], a[0:3], a[32:35], v[64:67]
ds_read_b128 a[96:99], v17 offset:0
v_mfma_f32_16x16x32_bf16 v[68:71], a[0:3], a[36:39], v[68:71]
s_add_u32 m0, s53, 0
v_mfma_f32_16x16x32_bf16 v[72:75], a[0:3], a[40:43], v[72:75]
ds_read_b64_tr_b16 a[64:65], v18 offset:8192
v_mfma_f32_16x16x32_bf16 v[76:79], a[0:3], a[44:47], v[76:79]
v_mfma_f32_16x16x32_bf16 v[80:83], a[0:3], a[48:51], v[80:83]
ds_read_b64_tr_b16 a[66:67], v19 offset:8192
v_mfma_f32_16x16x32_bf16 v[84:87], a[0:3], a[52:55], v[84:87]
buffer_load_dwordx4 v12, s[40:43], s56 offen offset:0 lds
v_mfma_f32_16x16x32_bf16 v[88:91], a[0:3], a[56:59], v[88:91]
ds_read_b128 a[100:103], v17 offset:2048
v_mfma_f32_16x16x32_bf16 v[92:95], a[0:3], a[60:63], v[92:95]
v_mfma_f32_16x16x32_bf16 v[96:99], a[4:7], a[32:35], v[96:99]
ds_read_b64_tr_b16 a[68:69], v20 offset:8192
v_mfma_f32_16x16x32_bf16 v[100:103], a[4:7], a[36:39], v[100:103]
buffer_load_dwordx4 v13, s[40:43], s56 offen offset:1024 lds
v_mfma_f32_16x16x32_bf16 v[104:107], a[4:7], a[40:43], v[104:107]
ds_read_b64_tr_b16 a[70:71], v21 offset:8192
v_mfma_f32_16x16x32_bf16 v[108:111], a[4:7], a[44:47], v[108:111]
v_mfma_f32_16x16x32_bf16 v[112:115], a[4:7], a[48:51], v[112:115]
ds_read_b128 a[104:107], v17 offset:4096
v_mfma_f32_16x16x32_bf16 v[116:119], a[4:7], a[52:55], v[116:119]
s_add_u32 m0, s53, 0x4000
v_mfma_f32_16x16x32_bf16 v[120:123], a[4:7], a[56:59], v[120:123]
ds_read_b64_tr_b16 a[72:73], v18 offset:8704
v_mfma_f32_16x16x32_bf16 v[124:127], a[4:7], a[60:63], v[124:127]
v_mfma_f32_16x16x32_bf16 v[128:131], a[8:11], a[32:35], v[128:131]
ds_read_b64_tr_b16 a[74:75], v19 offset:8704
v_mfma_f32_16x16x32_bf16 v[132:135], a[8:11], a[36:39], v[132:135]
buffer_load_dwordx4 v12, s[44:47], s56 offen offset:0 lds
v_mfma_f32_16x16x32_bf16 v[136:139], a[8:11], a[40:43], v[136:139]
ds_read_b128 a[108:111], v17 offset:6144
v_mfma_f32_16x16x32_bf16 v[140:143], a[8:11], a[44:47], v[140:143]
v_mfma_f32_16x16x32_bf16 v[144:147], a[8:11], a[48:51], v[144:147]
ds_read_b64_tr_b16 a[76:77], v20 offset:8704
v_mfma_f32_16x16x32_bf16 v[148:151], a[8:11], a[52:55], v[148:151]
buffer_load_dwordx4 v13, s[44:47], s56 offen offset:1024 lds
v_mfma_f32_16x16x32_bf16 v[152:155], a[8:11], a[56:59], v[152:155]
ds_read_b64_tr_b16 a[78:79], v21 offset:8704
v_mfma_f32_16x16x32_bf16 v[156:159], a[8:11], a[60:63], v[156:159]
v_mfma_f32_16x16x32_bf16 v[160:163], a[12:15], a[32:35], v[160:163]
ds_read_b128 a[112:115], v17 offset:8192
v_mfma_f32_16x16x32_bf16 v[164:167], a[12:15], a[36:39], v[164:167]
s_xor_b32 s53, s53, 0x10000
s_add_u32 s56, s56, 0x80000
v_mfma_f32_16x16x32_bf16 v[168:171], a[12:15], a[40:43], v[168:171]
ds_read_b64_tr_b16 a[80:81], v18 offset:9216
v_mfma_f32_16x16x32_bf16 v[172:175], a[12:15], a[44:47], v[172:175]
v_mfma_f32_16x16x32_bf16 v[176:179], a[12:15], a[48:51], v[176:179]
ds_read_b64_tr_b16 a[82:83], v19 offset:9216
v_mfma_f32_16x16x32_bf16 v[180:183], a[12:15], a[52:55], v[180:183]
s_add_u32 m0, s69, 0
v_mfma_f32_16x16x32_bf16 v[184:187], a[12:15], a[56:59], v[184:187]
ds_read_b128 a[116:119], v17 offset:10240
v_mfma_f32_16x16x32_bf16 v[188:191], a[12:15], a[60:63], v[188:191]
v_mfma_f32_16x16x32_bf16 v[192:195], a[16:19], a[32:35], v[192:195]
ds_read_b64_tr_b16 a[84:85], v20 offset:9216
v_mfma_f32_16x16x32_bf16 v[196:199], a[16:19], a[36:39], v[196:199]
buffer_load_dwordx4 v12, s[40:43], s68 offen offset:0 lds
v_mfma_f32_16x16x32_bf16 v[200:203], a[16:19], a[40:43], v[200:203]
ds_read_b64_tr_b16 a[86:87], v21 offset:9216
v_mfma_f32_16x16x32_bf16 v[204:207], a[16:19], a[44:47], v[204:207]
v_mfma_f32_16x16x32_bf16 v[208:211], a[16:19], a[48:51], v[208:211]
ds_read_b128 a[120:123], v17 offset:12288
v_mfma_f32_16x16x32_bf16 v[212:215], a[16:19], a[52:55], v[212:215]
buffer_load_dwordx4 v13, s[40:43], s68 offen offset:1024 lds
v_mfma_f32_16x16x32_bf16 v[216:219], a[16:19], a[56:59], v[216:219]
ds_read_b64_tr_b16 a[88:89], v18 offset:9728
v_mfma_f32_16x16x32_bf16 v[220:223], a[16:19], a[60:63], v[220:223]
v_mfma_f32_16x16x32_bf16 v[224:227], a[20:23], a[32:35], v[224:227]
ds_read_b64_tr_b16 a[90:91], v19 offset:9728
v_mfma_f32_16x16x32_bf16 v[228:231], a[20:23], a[36:39], v[228:231]
s_add_u32 m0, s69, 0x4000
v_mfma_f32_16x16x32_bf16 v[232:235], a[20:23], a[40:43], v[232:235]
ds_read_b128 a[124:127], v17 offset:14336
v_mfma_f32_16x16x32_bf16 v[236:239], a[20:23], a[44:47], v[236:239]
v_mfma_f32_16x16x32_bf16 v[240:243], a[20:23], a[48:51], v[240:243]
ds_read_b64_tr_b16 a[92:93], v20 offset:9728
v_mfma_f32_16x16x32_bf16 v[244:247], a[20:23], a[52:55], v[244:247]
buffer_load_dwordx4 v12, s[44:47], s68 offen offset:0 lds
v_mfma_f32_16x16x32_bf16 v[248:251], a[20:23], a[56:59], v[248:251]
ds_read_b64_tr_b16 a[94:95], v21 offset:9728
v_mfma_f32_16x16x32_bf16 v[252:255], a[20:23], a[60:63], v[252:255]
v_mfma_f32_16x16x32_bf16 a[128:131], a[24:27], a[32:35], a[128:131]
v_mfma_f32_16x16x32_bf16 a[132:135], a[24:27], a[36:39], a[132:135]
buffer_load_dwordx4 v13, s[44:47], s68 offen offset:1024 lds
v_mfma_f32_16x16x32_bf16 a[136:139], a[24:27], a[40:43], a[136:139]
v_mfma_f32_16x16x32_bf16 a[140:143], a[24:27], a[44:47], a[140:143]
v_mfma_f32_16x16x32_bf16 a[144:147], a[24:27], a[48:51], a[144:147]
v_mfma_f32_16x16x32_bf16 a[148:151], a[24:27], a[52:55], a[148:151]
s_xor_b32 s69, s69, 0x10000
s_add_u32 s68, s68, 0x80000
v_mfma_f32_16x16x32_bf16 a[152:155], a[24:27], a[56:59], a[152:155]
v_mfma_f32_16x16x32_bf16 a[156:159], a[24:27], a[60:63], a[156:159]
v_mfma_f32_16x16x32_bf16 a[160:163], a[28:31], a[32:35], a[160:163]
v_mfma_f32_16x16x32_bf16 a[164:167], a[28:31], a[36:39], a[164:167]
v_lshrrev_b32 v0, 4, v4
v_bfe_u32 v1, v0, 3, 1
v_lshlrev_b32 v1, 13, v1
v_and_b32 v2, 7, v0
v_lshl_add_u32 v1, v2, 7, v1
v_add_u32 v1, s75, v1
s_add_u32 s75, s75, 0x4000
s_mov_b32 exec_lo, 0xffff
s_mov_b32 exec_hi, 0
global_load_dword v5, v1, s[94:95] offset:0
s_mov_b64 exec, -1
v_mfma_f32_16x16x32_bf16 a[168:171], a[28:31], a[40:43], a[168:171]
v_mfma_f32_16x16x32_bf16 a[172:175], a[28:31], a[44:47], a[172:175]
v_mfma_f32_16x16x32_bf16 a[176:179], a[28:31], a[48:51], a[176:179]
v_mfma_f32_16x16x32_bf16 a[180:183], a[28:31], a[52:55], a[180:183]
v_mfma_f32_16x16x32_bf16 a[184:187], a[28:31], a[56:59], a[184:187]
v_mfma_f32_16x16x32_bf16 a[188:191], a[28:31], a[60:63], a[188:191]
s_waitcnt vmcnt(9) lgkmcnt(0)
s_barrier
v_xor_b32 v16, 0x10000, v16
v_xor_b32 v17, 0x10000, v17
v_xor_b32 v18, 0x10000, v18
v_xor_b32 v19, 0x10000, v19
v_xor_b32 v20, 0x10000, v20
v_xor_b32 v21, 0x10000, v21
v_mfma_f32_16x16x32_bf16 v[64:67], a[64:67], a[96:99], v[64:67]
ds_read_b128 a[32:35], v16 offset:0
v_mfma_f32_16x16x32_bf16 v[68:71], a[64:67], a[100:103], v[68:71]
s_add_u32 m0, s52, 0
v_mfma_f32_16x16x32_bf16 v[72:75], a[64:67], a[104:107], v[72:75]
ds_read_b64_tr_b16 a[0:1], v18 offset:0
v_mfma_f32_16x16x32_bf16 v[76:79], a[64:67], a[108:111], v[76:79]
v_mfma_f32_16x16x32_bf16 v[80:83], a[64:67], a[112:115], v[80:83]
ds_read_b64_tr_b16 a[2:3], v19 offset:0
v_mfma_f32_16x16x32_bf16 v[84:87], a[64:67], a[116:119], v[84:87]
v_mfma_f32_16x16x32_bf16 v[88:91], a[64:67], a[120:123], v[88:91]
ds_read_b128 a[36:39], v16 offset:2048
v_mfma_f32_16x16x32_bf16 v[92:95], a[64:67], a[124:127], v[92:95]
buffer_load_dwordx4 v8, s[36:39], s54 offen offset:0 lds
v_mfma_f32_16x16x32_bf16 v[96:99], a[68:71], a[96:99], v[96:99]
ds_read_b64_tr_b16 a[4:5], v20 offset:0
v_mfma_f32_16x16x32_bf16 v[100:103], a[68:71], a[100:103], v[100:103]
v_mfma_f32_16x16x32_bf16 v[104:107], a[68:71], a[104:107], v[104:107]
ds_read_b64_tr_b16 a[6:7], v21 offset:0
v_mfma_f32_16x16x32_bf16 v[108:111], a[68:71], a[108:111], v[108:111]
buffer_load_dwordx4 v9, s[36:39], s54 offen offset:1024 lds
v_mfma_f32_16x16x32_bf16 v[112:115], a[68:71], a[112:115], v[112:115]
ds_read_b128 a[40:43], v16 offset:4096
v_mfma_f32_16x16x32_bf16 v[116:119], a[68:71], a[116:119], v[116:119]
v_mfma_f32_16x16x32_bf16 v[120:123], a[68:71], a[120:123], v[120:123]
ds_read_b64_tr_b16 a[8:9], v18 offset:512
v_mfma_f32_16x16x32_bf16 v[124:127], a[68:71], a[124:127], v[124:127]
v_mfma_f32_16x16x32_bf16 v[128:131], a[72:75], a[96:99], v[128:131]
ds_read_b64_tr_b16 a[10:11], v19 offset:512
v_mfma_f32_16x16x32_bf16 v[132:135], a[72:75], a[100:103], v[132:135]
buffer_load_dwordx4 v10, s[36:39], s54 offen offset:2048 lds
v_mfma_f32_16x16x32_bf16 v[136:139], a[72:75], a[104:107], v[136:139]
ds_read_b128 a[44:47], v16 offset:6144
v_mfma_f32_16x16x32_bf16 v[140:143], a[72:75], a[108:111], v[140:143]
v_mfma_f32_16x16x32_bf16 v[144:147], a[72:75], a[112:115], v[144:147]
ds_read_b64_tr_b16 a[12:13], v20 offset:512
v_mfma_f32_16x16x32_bf16 v[148:151], a[72:75], a[116:119], v[148:151]
buffer_load_dwordx4 v11, s[36:39], s54 offen offset:3072 lds
v_mfma_f32_16x16x32_bf16 v[152:155], a[72:75], a[120:123], v[152:155]
ds_read_b64_tr_b16 a[14:15], v21 offset:512
v_mfma_f32_16x16x32_bf16 v[156:159], a[72:75], a[124:127], v[156:159]
v_mfma_f32_16x16x32_bf16 v[160:163], a[76:79], a[96:99], v[160:163]
ds_read_b128 a[48:51], v16 offset:8192
v_mfma_f32_16x16x32_bf16 v[164:167], a[76:79], a[100:103], v[164:167]
v_mfma_f32_16x16x32_bf16 v[168:171], a[76:79], a[104:107], v[168:171]
ds_read_b64_tr_b16 a[16:17], v18 offset:1024
v_mfma_f32_16x16x32_bf16 v[172:175], a[76:79], a[108:111], v[172:175]
s_add_u32 m0, s52, 0x1000
v_mfma_f32_16x16x32_bf16 v[176:179], a[76:79], a[112:115], v[176:179]
ds_read_b64_tr_b16 a[18:19], v19 offset:1024
v_mfma_f32_16x16x32_bf16 v[180:183], a[76:79], a[116:119], v[180:183]
v_mfma_f32_16x16x32_bf16 v[184:187], a[76:79], a[120:123], v[184:187]
ds_read_b128 a[52:55], v16 offset:10240
v_mfma_f32_16x16x32_bf16 v[188:191], a[76:79], a[124:127], v[188:191]
buffer_load_dwordx4 v8, s[36:39], s55 offen offset:0 lds
v_mfma_f32_16x16x32_bf16 v[192:195], a[80:83], a[96:99], v[192:195]
ds_read_b64_tr_b16 a[20:21], v20 offset:1024
v_mfma_f32_16x16x32_bf16 v[196:199], a[80:83], a[100:103], v[196:199]
v_mfma_f32_16x16x32_bf16 v[200:203], a[80:83], a[104:107], v[200:203]
ds_read_b64_tr_b16 a[22:23], v21 offset:1024
v_mfma_f32_16x16x32_bf16 v[204:207], a[80:83], a[108:111], v[204:207]
v_mfma_f32_16x16x32_bf16 v[208:211], a[80:83], a[112:115], v[208:211]
ds_read_b128 a[56:59], v16 offset:12288
v_mfma_f32_16x16x32_bf16 v[212:215], a[80:83], a[116:119], v[212:215]
buffer_load_dwordx4 v9, s[36:39], s55 offen offset:1024 lds
v_mfma_f32_16x16x32_bf16 v[216:219], a[80:83], a[120:123], v[216:219]
ds_read_b64_tr_b16 a[24:25], v18 offset:1536
v_mfma_f32_16x16x32_bf16 v[220:223], a[80:83], a[124:127], v[220:223]
v_mfma_f32_16x16x32_bf16 v[224:227], a[84:87], a[96:99], v[224:227]
ds_read_b64_tr_b16 a[26:27], v19 offset:1536
v_mfma_f32_16x16x32_bf16 v[228:231], a[84:87], a[100:103], v[228:231]
buffer_load_dwordx4 v10, s[36:39], s55 offen offset:2048 lds
v_mfma_f32_16x16x32_bf16 v[232:235], a[84:87], a[104:107], v[232:235]
ds_read_b128 a[60:63], v16 offset:14336
v_mfma_f32_16x16x32_bf16 v[236:239], a[84:87], a[108:111], v[236:239]
v_mfma_f32_16x16x32_bf16 v[240:243], a[84:87], a[112:115], v[240:243]
ds_read_b64_tr_b16 a[28:29], v20 offset:1536
v_mfma_f32_16x16x32_bf16 v[244:247], a[84:87], a[116:119], v[244:247]
v_mfma_f32_16x16x32_bf16 v[248:251], a[84:87], a[120:123], v[248:251]
ds_read_b64_tr_b16 a[30:31], v21 offset:1536
v_mfma_f32_16x16x32_bf16 v[252:255], a[84:87], a[124:127], v[252:255]
buffer_load_dwordx4 v11, s[36:39], s55 offen offset:3072 lds
v_mfma_f32_16x16x32_bf16 a[128:131], a[88:91], a[96:99], a[128:131]
v_mfma_f32_16x16x32_bf16 a[132:135], a[88:91], a[100:103], a[132:135]
v_mfma_f32_16x16x32_bf16 a[136:139], a[88:91], a[104:107], a[136:139]
v_mfma_f32_16x16x32_bf16 a[140:143], a[88:91], a[108:111], a[140:143]
s_xor_b32 s52, s52, 0x10000
s_add_u32 s54, s54, 0x80
s_add_u32 s55, s55, 0x80
v_mfma_f32_16x16x32_bf16 a[144:147], a[88:91], a[112:115], a[144:147]
v_mfma_f32_16x16x32_bf16 a[148:151], a[88:91], a[116:119], a[148:151]
v_mfma_f32_16x16x32_bf16 a[152:155], a[88:91], a[120:123], a[152:155]
v_mfma_f32_16x16x32_bf16 a[156:159], a[88:91], a[124:127], a[156:159]
v_mfma_f32_16x16x32_bf16 a[160:163], a[92:95], a[96:99], a[160:163]
v_lshrrev_b32 v0, 4, v4
v_bfe_u32 v1, v0, 3, 1
v_lshlrev_b32 v1, 13, v1
v_and_b32 v2, 7, v0
v_lshl_add_u32 v1, v2, 7, v1
v_add_u32 v1, s75, v1
s_add_u32 s75, s75, 0x4000
s_mov_b32 exec_lo, 0xffff
s_mov_b32 exec_hi, 0
global_load_dword v5, v1, s[94:95] offset:0
s_mov_b64 exec, -1
v_mfma_f32_16x16x32_bf16 a[164:167], a[92:95], a[100:103], a[164:167]
v_mfma_f32_16x16x32_bf16 a[168:171], a[92:95], a[104:107], a[168:171]
v_mfma_f32_16x16x32_bf16 a[172:175], a[92:95], a[108:111], a[172:175]
v_mfma_f32_16x16x32_bf16 a[176:179], a[92:95], a[112:115], a[176:179]
v_mfma_f32_16x16x32_bf16 a[180:183], a[92:95], a[116:119], a[180:183]
v_mfma_f32_16x16x32_bf16 a[184:187], a[92:95], a[120:123], a[184:187]
v_mfma_f32_16x16x32_bf16 a[188:191], a[92:95], a[124:127], a[188:191]
s_waitcnt vmcnt(10) lgkmcnt(0)
s_barrier
v_mfma_f32_16x16x32_bf16 v[64:67], a[0:3], a[32:35], v[64:67]
ds_read_b128 a[96:99], v17 offset:0
v_mfma_f32_16x16x32_bf16 v[68:71], a[0:3], a[36:39], v[68:71]
s_add_u32 m0, s53, 0
v_mfma_f32_16x16x32_bf16 v[72:75], a[0:3], a[40:43], v[72:75]
ds_read_b64_tr_b16 a[64:65], v18 offset:8192
v_mfma_f32_16x16x32_bf16 v[76:79], a[0:3], a[44:47], v[76:79]
v_mfma_f32_16x16x32_bf16 v[80:83], a[0:3], a[48:51], v[80:83]
ds_read_b64_tr_b16 a[66:67], v19 offset:8192
v_mfma_f32_16x16x32_bf16 v[84:87], a[0:3], a[52:55], v[84:87]
buffer_load_dwordx4 v12, s[40:43], s56 offen offset:0 lds
v_mfma_f32_16x16x32_bf16 v[88:91], a[0:3], a[56:59], v[88:91]
ds_read_b128 a[100:103], v17 offset:2048
v_mfma_f32_16x16x32_bf16 v[92:95], a[0:3], a[60:63], v[92:95]
v_mfma_f32_16x16x32_bf16 v[96:99], a[4:7], a[32:35], v[96:99]
ds_read_b64_tr_b16 a[68:69], v20 offset:8192
v_mfma_f32_16x16x32_bf16 v[100:103], a[4:7], a[36:39], v[100:103]
buffer_load_dwordx4 v13, s[40:43], s56 offen offset:1024 lds
v_mfma_f32_16x16x32_bf16 v[104:107], a[4:7], a[40:43], v[104:107]
ds_read_b64_tr_b16 a[70:71], v21 offset:8192
v_mfma_f32_16x16x32_bf16 v[108:111], a[4:7], a[44:47], v[108:111]
v_mfma_f32_16x16x32_bf16 v[112:115], a[4:7], a[48:51], v[112:115]
ds_read_b128 a[104:107], v17 offset:4096
v_mfma_f32_16x16x32_bf16 v[116:119], a[4:7], a[52:55], v[116:119]
s_add_u32 m0, s53, 0x4000
v_mfma_f32_16x16x32_bf16 v[120:123], a[4:7], a[56:59], v[120:123]
ds_read_b64_tr_b16 a[72:73], v18 offset:8704
v_mfma_f32_16x16x32_bf16 v[124:127], a[4:7], a[60:63], v[124:127]
v_mfma_f32_16x16x32_bf16 v[128:131], a[8:11], a[32:35], v[128:131]
ds_read_b64_tr_b16 a[74:75], v19 offset:8704
v_mfma_f32_16x16x32_bf16 v[132:135], a[8:11], a[36:39], v[132:135]
buffer_load_dwordx4 v12, s[44:47], s56 offen offset:0 lds
v_mfma_f32_16x16x32_bf16 v[136:139], a[8:11], a[40:43], v[136:139]
ds_read_b128 a[108:111], v17 offset:6144
v_mfma_f32_16x16x32_bf16 v[140:143], a[8:11], a[44:47], v[140:143]
v_mfma_f32_16x16x32_bf16 v[144:147], a[8:11], a[48:51], v[144:147]
ds_read_b64_tr_b16 a[76:77], v20 offset:8704
v_mfma_f32_16x16x32_bf16 v[148:151], a[8:11], a[52:55], v[148:151]
buffer_load_dwordx4 v13, s[44:47], s56 offen offset:1024 lds
v_mfma_f32_16x16x32_bf16 v[152:155], a[8:11], a[56:59], v[152:155]
ds_read_b64_tr_b16 a[78:79], v21 offset:8704
v_mfma_f32_16x16x32_bf16 v[156:159], a[8:11], a[60:63], v[156:159]
v_mfma_f32_16x16x32_bf16 v[160:163], a[12:15], a[32:35], v[160:163]
ds_read_b128 a[112:115], v17 offset:8192
v_mfma_f32_16x16x32_bf16 v[164:167], a[12:15], a[36:39], v[164:167]
s_xor_b32 s53, s53, 0x10000
s_add_u32 s56, s56, 0x80000
v_mfma_f32_16x16x32_bf16 v[168:171], a[12:15], a[40:43], v[168:171]
ds_read_b64_tr_b16 a[80:81], v18 offset:9216
v_mfma_f32_16x16x32_bf16 v[172:175], a[12:15], a[44:47], v[172:175]
v_mfma_f32_16x16x32_bf16 v[176:179], a[12:15], a[48:51], v[176:179]
ds_read_b64_tr_b16 a[82:83], v19 offset:9216
v_mfma_f32_16x16x32_bf16 v[180:183], a[12:15], a[52:55], v[180:183]
s_add_u32 m0, s69, 0
v_mfma_f32_16x16x32_bf16 v[184:187], a[12:15], a[56:59], v[184:187]
ds_read_b128 a[116:119], v17 offset:10240
v_mfma_f32_16x16x32_bf16 v[188:191], a[12:15], a[60:63], v[188:191]
v_mfma_f32_16x16x32_bf16 v[192:195], a[16:19], a[32:35], v[192:195]
ds_read_b64_tr_b16 a[84:85], v20 offset:9216
v_mfma_f32_16x16x32_bf16 v[196:199], a[16:19], a[36:39], v[196:199]
buffer_load_dwordx4 v12, s[40:43], s68 offen offset:0 lds
v_mfma_f32_16x16x32_bf16 v[200:203], a[16:19], a[40:43], v[200:203]
ds_read_b64_tr_b16 a[86:87], v21 offset:9216
v_mfma_f32_16x16x32_bf16 v[204:207], a[16:19], a[44:47], v[204:207]
v_mfma_f32_16x16x32_bf16 v[208:211], a[16:19], a[48:51], v[208:211]
ds_read_b128 a[120:123], v17 offset:12288
v_mfma_f32_16x16x32_bf16 v[212:215], a[16:19], a[52:55], v[212:215]
buffer_load_dwordx4 v13, s[40:43], s68 offen offset:1024 lds
v_mfma_f32_16x16x32_bf16 v[216:219], a[16:19], a[56:59], v[216:219]
ds_read_b64_tr_b16 a[88:89], v18 offset:9728
v_mfma_f32_16x16x32_bf16 v[220:223], a[16:19], a[60:63], v[220:223]
v_mfma_f32_16x16x32_bf16 v[224:227], a[20:23], a[32:35], v[224:227]
ds_read_b64_tr_b16 a[90:91], v19 offset:9728
v_mfma_f32_16x16x32_bf16 v[228:231], a[20:23], a[36:39], v[228:231]
s_add_u32 m0, s69, 0x4000
v_mfma_f32_16x16x32_bf16 v[232:235], a[20:23], a[40:43], v[232:235]
ds_read_b128 a[124:127], v17 offset:14336
v_mfma_f32_16x16x32_bf16 v[236:239], a[20:23], a[44:47], v[236:239]
v_mfma_f32_16x16x32_bf16 v[240:243], a[20:23], a[48:51], v[240:243]
ds_read_b64_tr_b16 a[92:93], v20 offset:9728
v_mfma_f32_16x16x32_bf16 v[244:247], a[20:23], a[52:55], v[244:247]
buffer_load_dwordx4 v12, s[44:47], s68 offen offset:0 lds
v_mfma_f32_16x16x32_bf16 v[248:251], a[20:23], a[56:59], v[248:251]
ds_read_b64_tr_b16 a[94:95], v21 offset:9728
v_mfma_f32_16x16x32_bf16 v[252:255], a[20:23], a[60:63], v[252:255]
v_mfma_f32_16x16x32_bf16 a[128:131], a[24:27], a[32:35], a[128:131]
v_mfma_f32_16x16x32_bf16 a[132:135], a[24:27], a[36:39], a[132:135]
buffer_load_dwordx4 v13, s[44:47], s68 offen offset:1024 lds
v_mfma_f32_16x16x32_bf16 a[136:139], a[24:27], a[40:43], a[136:139]
v_mfma_f32_16x16x32_bf16 a[140:143], a[24:27], a[44:47], a[140:143]
v_mfma_f32_16x16x32_bf16 a[144:147], a[24:27], a[48:51], a[144:147]
v_mfma_f32_16x16x32_bf16 a[148:151], a[24:27], a[52:55], a[148:151]
s_xor_b32 s69, s69, 0x10000
s_add_u32 s68, s68, 0x80000
v_mfma_f32_16x16x32_bf16 a[152:155], a[24:27], a[56:59], a[152:155]
v_mfma_f32_16x16x32_bf16 a[156:159], a[24:27], a[60:63], a[156:159]
v_mfma_f32_16x16x32_bf16 a[160:163], a[28:31], a[32:35], a[160:163]
v_mfma_f32_16x16x32_bf16 a[164:167], a[28:31], a[36:39], a[164:167]
v_lshrrev_b32 v0, 4, v4
v_bfe_u32 v1, v0, 3, 1
v_lshlrev_b32 v1, 10, v1
v_and_b32 v2, 7, v0
v_lshl_add_u32 v1, v2, 7, v1
v_add_u32 v1, s59, v1
s_add_u32 s59, s59, 0x800
s_mov_b32 exec_lo, 0xffff
s_mov_b32 exec_hi, 0
global_load_dword v5, v1, s[92:93] offset:0
s_mov_b64 exec, -1
v_mfma_f32_16x16x32_bf16 a[168:171], a[28:31], a[40:43], a[168:171]
v_mfma_f32_16x16x32_bf16 a[172:175], a[28:31], a[44:47], a[172:175]
v_mfma_f32_16x16x32_bf16 a[176:179], a[28:31], a[48:51], a[176:179]
v_mfma_f32_16x16x32_bf16 a[180:183], a[28:31], a[52:55], a[180:183]
v_mfma_f32_16x16x32_bf16 a[184:187], a[28:31], a[56:59], a[184:187]
v_mfma_f32_16x16x32_bf16 a[188:191], a[28:31], a[60:63], a[188:191]
s_waitcnt vmcnt(10) lgkmcnt(0)
s_barrier
v_xor_b32 v16, 0x10000, v16
v_xor_b32 v17, 0x10000, v17
v_xor_b32 v18, 0x10000, v18
v_xor_b32 v19, 0x10000, v19
v_xor_b32 v20, 0x10000, v20
v_xor_b32 v21, 0x10000, v21
v_mfma_f32_16x16x32_bf16 v[64:67], a[64:67], a[96:99], v[64:67]
ds_read_b128 a[32:35], v16 offset:0
v_mfma_f32_16x16x32_bf16 v[68:71], a[64:67], a[100:103], v[68:71]
s_add_u32 m0, s52, 0
v_mfma_f32_16x16x32_bf16 v[72:75], a[64:67], a[104:107], v[72:75]
ds_read_b64_tr_b16 a[0:1], v18 offset:0
v_mfma_f32_16x16x32_bf16 v[76:79], a[64:67], a[108:111], v[76:79]
v_mfma_f32_16x16x32_bf16 v[80:83], a[64:67], a[112:115], v[80:83]
ds_read_b64_tr_b16 a[2:3], v19 offset:0
v_mfma_f32_16x16x32_bf16 v[84:87], a[64:67], a[116:119], v[84:87]
v_mfma_f32_16x16x32_bf16 v[88:91], a[64:67], a[120:123], v[88:91]
ds_read_b128 a[36:39], v16 offset:2048
v_mfma_f32_16x16x32_bf16 v[92:95], a[64:67], a[124:127], v[92:95]
buffer_load_dwordx4 v8, s[36:39], s54 offen offset:0 lds
v_mfma_f32_16x16x32_bf16 v[96:99], a[68:71], a[96:99], v[96:99]
ds_read_b64_tr_b16 a[4:5], v20 offset:0
v_mfma_f32_16x16x32_bf16 v[100:103], a[68:71], a[100:103], v[100:103]
v_mfma_f32_16x16x32_bf16 v[104:107], a[68:71], a[104:107], v[104:107]
ds_read_b64_tr_b16 a[6:7], v21 offset:0
v_mfma_f32_16x16x32_bf16 v[108:111], a[68:71], a[108:111], v[108:111]
buffer_load_dwordx4 v9, s[36:39], s54 offen offset:1024 lds
v_mfma_f32_16x16x32_bf16 v[112:115], a[68:71], a[112:115], v[112:115]
ds_read_b128 a[40:43], v16 offset:4096
v_mfma_f32_16x16x32_bf16 v[116:119], a[68:71], a[116:119], v[116:119]
v_mfma_f32_16x16x32_bf16 v[120:123], a[68:71], a[120:123], v[120:123]
ds_read_b64_tr_b16 a[8:9], v18 offset:512
v_mfma_f32_16x16x32_bf16 v[124:127], a[68:71], a[124:127], v[124:127]
v_mfma_f32_16x16x32_bf16 v[128:131], a[72:75], a[96:99], v[128:131]
ds_read_b64_tr_b16 a[10:11], v19 offset:512
v_mfma_f32_16x16x32_bf16 v[132:135], a[72:75], a[100:103], v[132:135]
buffer_load_dwordx4 v10, s[36:39], s54 offen offset:2048 lds
v_mfma_f32_16x16x32_bf16 v[136:139], a[72:75], a[104:107], v[136:139]
ds_read_b128 a[44:47], v16 offset:6144
v_mfma_f32_16x16x32_bf16 v[140:143], a[72:75], a[108:111], v[140:143]
v_mfma_f32_16x16x32_bf16 v[144:147], a[72:75], a[112:115], v[144:147]
ds_read_b64_tr_b16 a[12:13], v20 offset:512
v_mfma_f32_16x16x32_bf16 v[148:151], a[72:75], a[116:119], v[148:151]
buffer_load_dwordx4 v11, s[36:39], s54 offen offset:3072 lds
v_mfma_f32_16x16x32_bf16 v[152:155], a[72:75], a[120:123], v[152:155]
ds_read_b64_tr_b16 a[14:15], v21 offset:512
v_mfma_f32_16x16x32_bf16 v[156:159], a[72:75], a[124:127], v[156:159]
v_mfma_f32_16x16x32_bf16 v[160:163], a[76:79], a[96:99], v[160:163]
ds_read_b128 a[48:51], v16 offset:8192
v_mfma_f32_16x16x32_bf16 v[164:167], a[76:79], a[100:103], v[164:167]
v_mfma_f32_16x16x32_bf16 v[168:171], a[76:79], a[104:107], v[168:171]
ds_read_b64_tr_b16 a[16:17], v18 offset:1024
v_mfma_f32_16x16x32_bf16 v[172:175], a[76:79], a[108:111], v[172:175]
s_add_u32 m0, s52, 0x1000
v_mfma_f32_16x16x32_bf16 v[176:179], a[76:79], a[112:115], v[176:179]
ds_read_b64_tr_b16 a[18:19], v19 offset:1024
v_mfma_f32_16x16x32_bf16 v[180:183], a[76:79], a[116:119], v[180:183]
v_mfma_f32_16x16x32_bf16 v[184:187], a[76:79], a[120:123], v[184:187]
ds_read_b128 a[52:55], v16 offset:10240
v_mfma_f32_16x16x32_bf16 v[188:191], a[76:79], a[124:127], v[188:191]
buffer_load_dwordx4 v8, s[36:39], s55 offen offset:0 lds
v_mfma_f32_16x16x32_bf16 v[192:195], a[80:83], a[96:99], v[192:195]
ds_read_b64_tr_b16 a[20:21], v20 offset:1024
v_mfma_f32_16x16x32_bf16 v[196:199], a[80:83], a[100:103], v[196:199]
v_mfma_f32_16x16x32_bf16 v[200:203], a[80:83], a[104:107], v[200:203]
ds_read_b64_tr_b16 a[22:23], v21 offset:1024
v_mfma_f32_16x16x32_bf16 v[204:207], a[80:83], a[108:111], v[204:207]
v_mfma_f32_16x16x32_bf16 v[208:211], a[80:83], a[112:115], v[208:211]
ds_read_b128 a[56:59], v16 offset:12288
v_mfma_f32_16x16x32_bf16 v[212:215], a[80:83], a[116:119], v[212:215]
buffer_load_dwordx4 v9, s[36:39], s55 offen offset:1024 lds
v_mfma_f32_16x16x32_bf16 v[216:219], a[80:83], a[120:123], v[216:219]
ds_read_b64_tr_b16 a[24:25], v18 offset:1536
v_mfma_f32_16x16x32_bf16 v[220:223], a[80:83], a[124:127], v[220:223]
v_mfma_f32_16x16x32_bf16 v[224:227], a[84:87], a[96:99], v[224:227]
ds_read_b64_tr_b16 a[26:27], v19 offset:1536
v_mfma_f32_16x16x32_bf16 v[228:231], a[84:87], a[100:103], v[228:231]
buffer_load_dwordx4 v10, s[36:39], s55 offen offset:2048 lds
v_mfma_f32_16x16x32_bf16 v[232:235], a[84:87], a[104:107], v[232:235]
ds_read_b128 a[60:63], v16 offset:14336
v_mfma_f32_16x16x32_bf16 v[236:239], a[84:87], a[108:111], v[236:239]
v_mfma_f32_16x16x32_bf16 v[240:243], a[84:87], a[112:115], v[240:243]
ds_read_b64_tr_b16 a[28:29], v20 offset:1536
v_mfma_f32_16x16x32_bf16 v[244:247], a[84:87], a[116:119], v[244:247]
v_mfma_f32_16x16x32_bf16 v[248:251], a[84:87], a[120:123], v[248:251]
ds_read_b64_tr_b16 a[30:31], v21 offset:1536
v_mfma_f32_16x16x32_bf16 v[252:255], a[84:87], a[124:127], v[252:255]
buffer_load_dwordx4 v11, s[36:39], s55 offen offset:3072 lds
v_mfma_f32_16x16x32_bf16 a[128:131], a[88:91], a[96:99], a[128:131]
v_mfma_f32_16x16x32_bf16 a[132:135], a[88:91], a[100:103], a[132:135]
v_mfma_f32_16x16x32_bf16 a[136:139], a[88:91], a[104:107], a[136:139]
v_mfma_f32_16x16x32_bf16 a[140:143], a[88:91], a[108:111], a[140:143]
s_xor_b32 s52, s52, 0x10000
s_add_u32 s54, s54, 0x80
s_add_u32 s55, s55, 0x80
v_mfma_f32_16x16x32_bf16 a[144:147], a[88:91], a[112:115], a[144:147]
v_mfma_f32_16x16x32_bf16 a[148:151], a[88:91], a[116:119], a[148:151]
v_mfma_f32_16x16x32_bf16 a[152:155], a[88:91], a[120:123], a[152:155]
v_mfma_f32_16x16x32_bf16 a[156:159], a[88:91], a[124:127], a[156:159]
v_mfma_f32_16x16x32_bf16 a[160:163], a[92:95], a[96:99], a[160:163]
v_lshrrev_b32 v0, 4, v4
v_bfe_u32 v1, v0, 3, 1
v_lshlrev_b32 v1, 10, v1
v_and_b32 v2, 7, v0
v_lshl_add_u32 v1, v2, 7, v1
v_add_u32 v1, s59, v1
s_add_u32 s59, s59, 0x800
s_mov_b32 exec_lo, 0xffff
s_mov_b32 exec_hi, 0
global_load_dword v5, v1, s[92:93] offset:0
s_mov_b64 exec, -1
v_mfma_f32_16x16x32_bf16 a[164:167], a[92:95], a[100:103], a[164:167]
v_mfma_f32_16x16x32_bf16 a[168:171], a[92:95], a[104:107], a[168:171]
v_mfma_f32_16x16x32_bf16 a[172:175], a[92:95], a[108:111], a[172:175]
v_mfma_f32_16x16x32_bf16 a[176:179], a[92:95], a[112:115], a[176:179]
v_mfma_f32_16x16x32_bf16 a[180:183], a[92:95], a[116:119], a[180:183]
v_mfma_f32_16x16x32_bf16 a[184:187], a[92:95], a[120:123], a[184:187]
v_mfma_f32_16x16x32_bf16 a[188:191], a[92:95], a[124:127], a[188:191]
s_waitcnt vmcnt(10) lgkmcnt(0)
s_barrier
v_mfma_f32_16x16x32_bf16 v[64:67], a[0:3], a[32:35], v[64:67]
ds_read_b128 a[96:99], v17 offset:0
v_mfma_f32_16x16x32_bf16 v[68:71], a[0:3], a[36:39], v[68:71]
s_add_u32 m0, s53, 0
v_mfma_f32_16x16x32_bf16 v[72:75], a[0:3], a[40:43], v[72:75]
ds_read_b64_tr_b16 a[64:65], v18 offset:8192
v_mfma_f32_16x16x32_bf16 v[76:79], a[0:3], a[44:47], v[76:79]
v_mfma_f32_16x16x32_bf16 v[80:83], a[0:3], a[48:51], v[80:83]
ds_read_b64_tr_b16 a[66:67], v19 offset:8192
v_mfma_f32_16x16x32_bf16 v[84:87], a[0:3], a[52:55], v[84:87]
buffer_load_dwordx4 v12, s[40:43], s56 offen offset:0 lds
v_mfma_f32_16x16x32_bf16 v[88:91], a[0:3], a[56:59], v[88:91]
ds_read_b128 a[100:103], v17 offset:2048
v_mfma_f32_16x16x32_bf16 v[92:95], a[0:3], a[60:63], v[92:95]
v_mfma_f32_16x16x32_bf16 v[96:99], a[4:7], a[32:35], v[96:99]
ds_read_b64_tr_b16 a[68:69], v20 offset:8192
v_mfma_f32_16x16x32_bf16 v[100:103], a[4:7], a[36:39], v[100:103]
buffer_load_dwordx4 v13, s[40:43], s56 offen offset:1024 lds
v_mfma_f32_16x16x32_bf16 v[104:107], a[4:7], a[40:43], v[104:107]
ds_read_b64_tr_b16 a[70:71], v21 offset:8192
v_mfma_f32_16x16x32_bf16 v[108:111], a[4:7], a[44:47], v[108:111]
v_mfma_f32_16x16x32_bf16 v[112:115], a[4:7], a[48:51], v[112:115]
ds_read_b128 a[104:107], v17 offset:4096
v_mfma_f32_16x16x32_bf16 v[116:119], a[4:7], a[52:55], v[116:119]
s_add_u32 m0, s53, 0x4000
v_mfma_f32_16x16x32_bf16 v[120:123], a[4:7], a[56:59], v[120:123]
ds_read_b64_tr_b16 a[72:73], v18 offset:8704
v_mfma_f32_16x16x32_bf16 v[124:127], a[4:7], a[60:63], v[124:127]
v_mfma_f32_16x16x32_bf16 v[128:131], a[8:11], a[32:35], v[128:131]
ds_read_b64_tr_b16 a[74:75], v19 offset:8704
v_mfma_f32_16x16x32_bf16 v[132:135], a[8:11], a[36:39], v[132:135]
buffer_load_dwordx4 v12, s[44:47], s56 offen offset:0 lds
v_mfma_f32_16x16x32_bf16 v[136:139], a[8:11], a[40:43], v[136:139]
ds_read_b128 a[108:111], v17 offset:6144
v_mfma_f32_16x16x32_bf16 v[140:143], a[8:11], a[44:47], v[140:143]
v_mfma_f32_16x16x32_bf16 v[144:147], a[8:11], a[48:51], v[144:147]
ds_read_b64_tr_b16 a[76:77], v20 offset:8704
v_mfma_f32_16x16x32_bf16 v[148:151], a[8:11], a[52:55], v[148:151]
buffer_load_dwordx4 v13, s[44:47], s56 offen offset:1024 lds
v_mfma_f32_16x16x32_bf16 v[152:155], a[8:11], a[56:59], v[152:155]
ds_read_b64_tr_b16 a[78:79], v21 offset:8704
v_mfma_f32_16x16x32_bf16 v[156:159], a[8:11], a[60:63], v[156:159]
v_mfma_f32_16x16x32_bf16 v[160:163], a[12:15], a[32:35], v[160:163]
ds_read_b128 a[112:115], v17 offset:8192
v_mfma_f32_16x16x32_bf16 v[164:167], a[12:15], a[36:39], v[164:167]
s_xor_b32 s53, s53, 0x10000
s_add_u32 s56, s56, 0x80000
v_mfma_f32_16x16x32_bf16 v[168:171], a[12:15], a[40:43], v[168:171]
ds_read_b64_tr_b16 a[80:81], v18 offset:9216
v_mfma_f32_16x16x32_bf16 v[172:175], a[12:15], a[44:47], v[172:175]
v_mfma_f32_16x16x32_bf16 v[176:179], a[12:15], a[48:51], v[176:179]
ds_read_b64_tr_b16 a[82:83], v19 offset:9216
v_mfma_f32_16x16x32_bf16 v[180:183], a[12:15], a[52:55], v[180:183]
s_add_u32 m0, s69, 0
v_mfma_f32_16x16x32_bf16 v[184:187], a[12:15], a[56:59], v[184:187]
ds_read_b128 a[116:119], v17 offset:10240
v_mfma_f32_16x16x32_bf16 v[188:191], a[12:15], a[60:63], v[188:191]
v_mfma_f32_16x16x32_bf16 v[192:195], a[16:19], a[32:35], v[192:195]
ds_read_b64_tr_b16 a[84:85], v20 offset:9216
v_mfma_f32_16x16x32_bf16 v[196:199], a[16:19], a[36:39], v[196:199]
buffer_load_dwordx4 v12, s[40:43], s68 offen offset:0 lds
v_mfma_f32_16x16x32_bf16 v[200:203], a[16:19], a[40:43], v[200:203]
ds_read_b64_tr_b16 a[86:87], v21 offset:9216
v_mfma_f32_16x16x32_bf16 v[204:207], a[16:19], a[44:47], v[204:207]
v_mfma_f32_16x16x32_bf16 v[208:211], a[16:19], a[48:51], v[208:211]
ds_read_b128 a[120:123], v17 offset:12288
v_mfma_f32_16x16x32_bf16 v[212:215], a[16:19], a[52:55], v[212:215]
buffer_load_dwordx4 v13, s[40:43], s68 offen offset:1024 lds
v_mfma_f32_16x16x32_bf16 v[216:219], a[16:19], a[56:59], v[216:219]
ds_read_b64_tr_b16 a[88:89], v18 offset:9728
v_mfma_f32_16x16x32_bf16 v[220:223], a[16:19], a[60:63], v[220:223]
v_mfma_f32_16x16x32_bf16 v[224:227], a[20:23], a[32:35], v[224:227]
ds_read_b64_tr_b16 a[90:91], v19 offset:9728
v_mfma_f32_16x16x32_bf16 v[228:231], a[20:23], a[36:39], v[228:231]
s_add_u32 m0, s69, 0x4000
v_mfma_f32_16x16x32_bf16 v[232:235], a[20:23], a[40:43], v[232:235]
ds_read_b128 a[124:127], v17 offset:14336
v_mfma_f32_16x16x32_bf16 v[236:239], a[20:23], a[44:47], v[236:239]
v_mfma_f32_16x16x32_bf16 v[240:243], a[20:23], a[48:51], v[240:243]
ds_read_b64_tr_b16 a[92:93], v20 offset:9728
v_mfma_f32_16x16x32_bf16 v[244:247], a[20:23], a[52:55], v[244:247]
buffer_load_dwordx4 v12, s[44:47], s68 offen offset:0 lds
v_mfma_f32_16x16x32_bf16 v[248:251], a[20:23], a[56:59], v[248:251]
ds_read_b64_tr_b16 a[94:95], v21 offset:9728
v_mfma_f32_16x16x32_bf16 v[252:255], a[20:23], a[60:63], v[252:255]
v_mfma_f32_16x16x32_bf16 a[128:131], a[24:27], a[32:35], a[128:131]
v_mfma_f32_16x16x32_bf16 a[132:135], a[24:27], a[36:39], a[132:135]
buffer_load_dwordx4 v13, s[44:47], s68 offen offset:1024 lds
v_mfma_f32_16x16x32_bf16 a[136:139], a[24:27], a[40:43], a[136:139]
v_mfma_f32_16x16x32_bf16 a[140:143], a[24:27], a[44:47], a[140:143]
v_mfma_f32_16x16x32_bf16 a[144:147], a[24:27], a[48:51], a[144:147]
v_mfma_f32_16x16x32_bf16 a[148:151], a[24:27], a[52:55], a[148:151]
s_xor_b32 s69, s69, 0x10000
s_add_u32 s68, s68, 0x80000
v_mfma_f32_16x16x32_bf16 a[152:155], a[24:27], a[56:59], a[152:155]
v_mfma_f32_16x16x32_bf16 a[156:159], a[24:27], a[60:63], a[156:159]
v_mfma_f32_16x16x32_bf16 a[160:163], a[28:31], a[32:35], a[160:163]
v_mfma_f32_16x16x32_bf16 a[164:167], a[28:31], a[36:39], a[164:167]
v_lshrrev_b32 v0, 4, v4
v_bfe_u32 v1, v0, 3, 1
v_lshlrev_b32 v1, 10, v1
v_and_b32 v2, 7, v0
v_lshl_add_u32 v1, v2, 7, v1
v_add_u32 v1, s59, v1
s_add_u32 s59, s59, 0x800
s_mov_b32 exec_lo, 0xffff
s_mov_b32 exec_hi, 0
global_load_dword v5, v1, s[92:93] offset:0
s_mov_b64 exec, -1
v_mfma_f32_16x16x32_bf16 a[168:171], a[28:31], a[40:43], a[168:171]
v_mfma_f32_16x16x32_bf16 a[172:175], a[28:31], a[44:47], a[172:175]
v_mfma_f32_16x16x32_bf16 a[176:179], a[28:31], a[48:51], a[176:179]
v_mfma_f32_16x16x32_bf16 a[180:183], a[28:31], a[52:55], a[180:183]
v_mfma_f32_16x16x32_bf16 a[184:187], a[28:31], a[56:59], a[184:187]
v_mfma_f32_16x16x32_bf16 a[188:191], a[28:31], a[60:63], a[188:191]
s_waitcnt vmcnt(10) lgkmcnt(0)
s_barrier
v_xor_b32 v16, 0x10000, v16
v_xor_b32 v17, 0x10000, v17
v_xor_b32 v18, 0x10000, v18
v_xor_b32 v19, 0x10000, v19
v_xor_b32 v20, 0x10000, v20
v_xor_b32 v21, 0x10000, v21
v_mfma_f32_16x16x32_bf16 v[64:67], a[64:67], a[96:99], v[64:67]
ds_read_b128 a[32:35], v16 offset:0
v_mfma_f32_16x16x32_bf16 v[68:71], a[64:67], a[100:103], v[68:71]
s_add_u32 m0, s52, 0
v_mfma_f32_16x16x32_bf16 v[72:75], a[64:67], a[104:107], v[72:75]
ds_read_b64_tr_b16 a[0:1], v18 offset:0
v_mfma_f32_16x16x32_bf16 v[76:79], a[64:67], a[108:111], v[76:79]
v_mfma_f32_16x16x32_bf16 v[80:83], a[64:67], a[112:115], v[80:83]
ds_read_b64_tr_b16 a[2:3], v19 offset:0
v_mfma_f32_16x16x32_bf16 v[84:87], a[64:67], a[116:119], v[84:87]
v_mfma_f32_16x16x32_bf16 v[88:91], a[64:67], a[120:123], v[88:91]
ds_read_b128 a[36:39], v16 offset:2048
v_mfma_f32_16x16x32_bf16 v[92:95], a[64:67], a[124:127], v[92:95]
buffer_load_dwordx4 v8, s[36:39], s54 offen offset:0 lds
v_mfma_f32_16x16x32_bf16 v[96:99], a[68:71], a[96:99], v[96:99]
ds_read_b64_tr_b16 a[4:5], v20 offset:0
v_mfma_f32_16x16x32_bf16 v[100:103], a[68:71], a[100:103], v[100:103]
v_mfma_f32_16x16x32_bf16 v[104:107], a[68:71], a[104:107], v[104:107]
ds_read_b64_tr_b16 a[6:7], v21 offset:0
v_mfma_f32_16x16x32_bf16 v[108:111], a[68:71], a[108:111], v[108:111]
buffer_load_dwordx4 v9, s[36:39], s54 offen offset:1024 lds
v_mfma_f32_16x16x32_bf16 v[112:115], a[68:71], a[112:115], v[112:115]
ds_read_b128 a[40:43], v16 offset:4096
v_mfma_f32_16x16x32_bf16 v[116:119], a[68:71], a[116:119], v[116:119]
v_mfma_f32_16x16x32_bf16 v[120:123], a[68:71], a[120:123], v[120:123]
ds_read_b64_tr_b16 a[8:9], v18 offset:512
v_mfma_f32_16x16x32_bf16 v[124:127], a[68:71], a[124:127], v[124:127]
v_mfma_f32_16x16x32_bf16 v[128:131], a[72:75], a[96:99], v[128:131]
ds_read_b64_tr_b16 a[10:11], v19 offset:512
v_mfma_f32_16x16x32_bf16 v[132:135], a[72:75], a[100:103], v[132:135]
buffer_load_dwordx4 v10, s[36:39], s54 offen offset:2048 lds
v_mfma_f32_16x16x32_bf16 v[136:139], a[72:75], a[104:107], v[136:139]
ds_read_b128 a[44:47], v16 offset:6144
v_mfma_f32_16x16x32_bf16 v[140:143], a[72:75], a[108:111], v[140:143]
v_mfma_f32_16x16x32_bf16 v[144:147], a[72:75], a[112:115], v[144:147]
ds_read_b64_tr_b16 a[12:13], v20 offset:512
v_mfma_f32_16x16x32_bf16 v[148:151], a[72:75], a[116:119], v[148:151]
buffer_load_dwordx4 v11, s[36:39], s54 offen offset:3072 lds
v_mfma_f32_16x16x32_bf16 v[152:155], a[72:75], a[120:123], v[152:155]
ds_read_b64_tr_b16 a[14:15], v21 offset:512
v_mfma_f32_16x16x32_bf16 v[156:159], a[72:75], a[124:127], v[156:159]
v_mfma_f32_16x16x32_bf16 v[160:163], a[76:79], a[96:99], v[160:163]
ds_read_b128 a[48:51], v16 offset:8192
v_mfma_f32_16x16x32_bf16 v[164:167], a[76:79], a[100:103], v[164:167]
v_mfma_f32_16x16x32_bf16 v[168:171], a[76:79], a[104:107], v[168:171]
ds_read_b64_tr_b16 a[16:17], v18 offset:1024
v_mfma_f32_16x16x32_bf16 v[172:175], a[76:79], a[108:111], v[172:175]
s_add_u32 m0, s52, 0x1000
v_mfma_f32_16x16x32_bf16 v[176:179], a[76:79], a[112:115], v[176:179]
ds_read_b64_tr_b16 a[18:19], v19 offset:1024
v_mfma_f32_16x16x32_bf16 v[180:183], a[76:79], a[116:119], v[180:183]
v_mfma_f32_16x16x32_bf16 v[184:187], a[76:79], a[120:123], v[184:187]
ds_read_b128 a[52:55], v16 offset:10240
v_mfma_f32_16x16x32_bf16 v[188:191], a[76:79], a[124:127], v[188:191]
buffer_load_dwordx4 v8, s[36:39], s55 offen offset:0 lds
v_mfma_f32_16x16x32_bf16 v[192:195], a[80:83], a[96:99], v[192:195]
ds_read_b64_tr_b16 a[20:21], v20 offset:1024
v_mfma_f32_16x16x32_bf16 v[196:199], a[80:83], a[100:103], v[196:199]
v_mfma_f32_16x16x32_bf16 v[200:203], a[80:83], a[104:107], v[200:203]
ds_read_b64_tr_b16 a[22:23], v21 offset:1024
v_mfma_f32_16x16x32_bf16 v[204:207], a[80:83], a[108:111], v[204:207]
v_mfma_f32_16x16x32_bf16 v[208:211], a[80:83], a[112:115], v[208:211]
ds_read_b128 a[56:59], v16 offset:12288
v_mfma_f32_16x16x32_bf16 v[212:215], a[80:83], a[116:119], v[212:215]
buffer_load_dwordx4 v9, s[36:39], s55 offen offset:1024 lds
v_mfma_f32_16x16x32_bf16 v[216:219], a[80:83], a[120:123], v[216:219]
ds_read_b64_tr_b16 a[24:25], v18 offset:1536
v_mfma_f32_16x16x32_bf16 v[220:223], a[80:83], a[124:127], v[220:223]
v_mfma_f32_16x16x32_bf16 v[224:227], a[84:87], a[96:99], v[224:227]
ds_read_b64_tr_b16 a[26:27], v19 offset:1536
v_mfma_f32_16x16x32_bf16 v[228:231], a[84:87], a[100:103], v[228:231]
buffer_load_dwordx4 v10, s[36:39], s55 offen offset:2048 lds
v_mfma_f32_16x16x32_bf16 v[232:235], a[84:87], a[104:107], v[232:235]
ds_read_b128 a[60:63], v16 offset:14336
v_mfma_f32_16x16x32_bf16 v[236:239], a[84:87], a[108:111], v[236:239]
v_mfma_f32_16x16x32_bf16 v[240:243], a[84:87], a[112:115], v[240:243]
ds_read_b64_tr_b16 a[28:29], v20 offset:1536
v_mfma_f32_16x16x32_bf16 v[244:247], a[84:87], a[116:119], v[244:247]
v_mfma_f32_16x16x32_bf16 v[248:251], a[84:87], a[120:123], v[248:251]
ds_read_b64_tr_b16 a[30:31], v21 offset:1536
v_mfma_f32_16x16x32_bf16 v[252:255], a[84:87], a[124:127], v[252:255]
buffer_load_dwordx4 v11, s[36:39], s55 offen offset:3072 lds
v_mfma_f32_16x16x32_bf16 a[128:131], a[88:91], a[96:99], a[128:131]
v_mfma_f32_16x16x32_bf16 a[132:135], a[88:91], a[100:103], a[132:135]
v_mfma_f32_16x16x32_bf16 a[136:139], a[88:91], a[104:107], a[136:139]
v_mfma_f32_16x16x32_bf16 a[140:143], a[88:91], a[108:111], a[140:143]
s_xor_b32 s52, s52, 0x10000
s_add_u32 s54, s54, 0x80
s_add_u32 s55, s55, 0x80
v_mfma_f32_16x16x32_bf16 a[144:147], a[88:91], a[112:115], a[144:147]
v_mfma_f32_16x16x32_bf16 a[148:151], a[88:91], a[116:119], a[148:151]
v_mfma_f32_16x16x32_bf16 a[152:155], a[88:91], a[120:123], a[152:155]
v_mfma_f32_16x16x32_bf16 a[156:159], a[88:91], a[124:127], a[156:159]
v_mfma_f32_16x16x32_bf16 a[160:163], a[92:95], a[96:99], a[160:163]
v_lshrrev_b32 v0, 4, v4
v_bfe_u32 v1, v0, 3, 1
v_lshlrev_b32 v1, 10, v1
v_and_b32 v2, 7, v0
v_lshl_add_u32 v1, v2, 7, v1
v_add_u32 v1, s59, v1
s_add_u32 s59, s59, 0x800
s_mov_b32 exec_lo, 0xffff
s_mov_b32 exec_hi, 0
global_load_dword v5, v1, s[92:93] offset:0
s_mov_b64 exec, -1
v_mfma_f32_16x16x32_bf16 a[164:167], a[92:95], a[100:103], a[164:167]
v_mfma_f32_16x16x32_bf16 a[168:171], a[92:95], a[104:107], a[168:171]
v_mfma_f32_16x16x32_bf16 a[172:175], a[92:95], a[108:111], a[172:175]
v_mfma_f32_16x16x32_bf16 a[176:179], a[92:95], a[112:115], a[176:179]
v_mfma_f32_16x16x32_bf16 a[180:183], a[92:95], a[116:119], a[180:183]
v_mfma_f32_16x16x32_bf16 a[184:187], a[92:95], a[120:123], a[184:187]
v_mfma_f32_16x16x32_bf16 a[188:191], a[92:95], a[124:127], a[188:191]
s_waitcnt vmcnt(10) lgkmcnt(0)
s_barrier
v_mfma_f32_16x16x32_bf16 v[64:67], a[0:3], a[32:35], v[64:67]
ds_read_b128 a[96:99], v17 offset:0
v_mfma_f32_16x16x32_bf16 v[68:71], a[0:3], a[36:39], v[68:71]
s_add_u32 m0, s53, 0
v_mfma_f32_16x16x32_bf16 v[72:75], a[0:3], a[40:43], v[72:75]
ds_read_b64_tr_b16 a[64:65], v18 offset:8192
v_mfma_f32_16x16x32_bf16 v[76:79], a[0:3], a[44:47], v[76:79]
v_mfma_f32_16x16x32_bf16 v[80:83], a[0:3], a[48:51], v[80:83]
ds_read_b64_tr_b16 a[66:67], v19 offset:8192
v_mfma_f32_16x16x32_bf16 v[84:87], a[0:3], a[52:55], v[84:87]
buffer_load_dwordx4 v12, s[40:43], s56 offen offset:0 lds
v_mfma_f32_16x16x32_bf16 v[88:91], a[0:3], a[56:59], v[88:91]
ds_read_b128 a[100:103], v17 offset:2048
v_mfma_f32_16x16x32_bf16 v[92:95], a[0:3], a[60:63], v[92:95]
v_mfma_f32_16x16x32_bf16 v[96:99], a[4:7], a[32:35], v[96:99]
ds_read_b64_tr_b16 a[68:69], v20 offset:8192
v_mfma_f32_16x16x32_bf16 v[100:103], a[4:7], a[36:39], v[100:103]
buffer_load_dwordx4 v13, s[40:43], s56 offen offset:1024 lds
v_mfma_f32_16x16x32_bf16 v[104:107], a[4:7], a[40:43], v[104:107]
ds_read_b64_tr_b16 a[70:71], v21 offset:8192
v_mfma_f32_16x16x32_bf16 v[108:111], a[4:7], a[44:47], v[108:111]
v_mfma_f32_16x16x32_bf16 v[112:115], a[4:7], a[48:51], v[112:115]
ds_read_b128 a[104:107], v17 offset:4096
v_mfma_f32_16x16x32_bf16 v[116:119], a[4:7], a[52:55], v[116:119]
s_add_u32 m0, s53, 0x4000
v_mfma_f32_16x16x32_bf16 v[120:123], a[4:7], a[56:59], v[120:123]
ds_read_b64_tr_b16 a[72:73], v18 offset:8704
v_mfma_f32_16x16x32_bf16 v[124:127], a[4:7], a[60:63], v[124:127]
v_mfma_f32_16x16x32_bf16 v[128:131], a[8:11], a[32:35], v[128:131]
ds_read_b64_tr_b16 a[74:75], v19 offset:8704
v_mfma_f32_16x16x32_bf16 v[132:135], a[8:11], a[36:39], v[132:135]
buffer_load_dwordx4 v12, s[44:47], s56 offen offset:0 lds
v_mfma_f32_16x16x32_bf16 v[136:139], a[8:11], a[40:43], v[136:139]
ds_read_b128 a[108:111], v17 offset:6144
v_mfma_f32_16x16x32_bf16 v[140:143], a[8:11], a[44:47], v[140:143]
v_mfma_f32_16x16x32_bf16 v[144:147], a[8:11], a[48:51], v[144:147]
ds_read_b64_tr_b16 a[76:77], v20 offset:8704
v_mfma_f32_16x16x32_bf16 v[148:151], a[8:11], a[52:55], v[148:151]
buffer_load_dwordx4 v13, s[44:47], s56 offen offset:1024 lds
v_mfma_f32_16x16x32_bf16 v[152:155], a[8:11], a[56:59], v[152:155]
ds_read_b64_tr_b16 a[78:79], v21 offset:8704
v_mfma_f32_16x16x32_bf16 v[156:159], a[8:11], a[60:63], v[156:159]
v_mfma_f32_16x16x32_bf16 v[160:163], a[12:15], a[32:35], v[160:163]
ds_read_b128 a[112:115], v17 offset:8192
v_mfma_f32_16x16x32_bf16 v[164:167], a[12:15], a[36:39], v[164:167]
s_xor_b32 s53, s53, 0x10000
s_add_u32 s56, s56, 0x80000
v_mfma_f32_16x16x32_bf16 v[168:171], a[12:15], a[40:43], v[168:171]
ds_read_b64_tr_b16 a[80:81], v18 offset:9216
v_mfma_f32_16x16x32_bf16 v[172:175], a[12:15], a[44:47], v[172:175]
v_mfma_f32_16x16x32_bf16 v[176:179], a[12:15], a[48:51], v[176:179]
ds_read_b64_tr_b16 a[82:83], v19 offset:9216
v_mfma_f32_16x16x32_bf16 v[180:183], a[12:15], a[52:55], v[180:183]
s_add_u32 m0, s69, 0
v_mfma_f32_16x16x32_bf16 v[184:187], a[12:15], a[56:59], v[184:187]
ds_read_b128 a[116:119], v17 offset:10240
v_mfma_f32_16x16x32_bf16 v[188:191], a[12:15], a[60:63], v[188:191]
v_mfma_f32_16x16x32_bf16 v[192:195], a[16:19], a[32:35], v[192:195]
ds_read_b64_tr_b16 a[84:85], v20 offset:9216
v_mfma_f32_16x16x32_bf16 v[196:199], a[16:19], a[36:39], v[196:199]
buffer_load_dwordx4 v12, s[40:43], s68 offen offset:0 lds
v_mfma_f32_16x16x32_bf16 v[200:203], a[16:19], a[40:43], v[200:203]
ds_read_b64_tr_b16 a[86:87], v21 offset:9216
v_mfma_f32_16x16x32_bf16 v[204:207], a[16:19], a[44:47], v[204:207]
v_mfma_f32_16x16x32_bf16 v[208:211], a[16:19], a[48:51], v[208:211]
ds_read_b128 a[120:123], v17 offset:12288
v_mfma_f32_16x16x32_bf16 v[212:215], a[16:19], a[52:55], v[212:215]
buffer_load_dwordx4 v13, s[40:43], s68 offen offset:1024 lds
v_mfma_f32_16x16x32_bf16 v[216:219], a[16:19], a[56:59], v[216:219]
ds_read_b64_tr_b16 a[88:89], v18 offset:9728
v_mfma_f32_16x16x32_bf16 v[220:223], a[16:19], a[60:63], v[220:223]
v_mfma_f32_16x16x32_bf16 v[224:227], a[20:23], a[32:35], v[224:227]
ds_read_b64_tr_b16 a[90:91], v19 offset:9728
v_mfma_f32_16x16x32_bf16 v[228:231], a[20:23], a[36:39], v[228:231]
s_add_u32 m0, s69, 0x4000
v_mfma_f32_16x16x32_bf16 v[232:235], a[20:23], a[40:43], v[232:235]
ds_read_b128 a[124:127], v17 offset:14336
v_mfma_f32_16x16x32_bf16 v[236:239], a[20:23], a[44:47], v[236:239]
v_mfma_f32_16x16x32_bf16 v[240:243], a[20:23], a[48:51], v[240:243]
ds_read_b64_tr_b16 a[92:93], v20 offset:9728
v_mfma_f32_16x16x32_bf16 v[244:247], a[20:23], a[52:55], v[244:247]
buffer_load_dwordx4 v12, s[44:47], s68 offen offset:0 lds
v_mfma_f32_16x16x32_bf16 v[248:251], a[20:23], a[56:59], v[248:251]
ds_read_b64_tr_b16 a[94:95], v21 offset:9728
v_mfma_f32_16x16x32_bf16 v[252:255], a[20:23], a[60:63], v[252:255]
v_mfma_f32_16x16x32_bf16 a[128:131], a[24:27], a[32:35], a[128:131]
v_mfma_f32_16x16x32_bf16 a[132:135], a[24:27], a[36:39], a[132:135]
buffer_load_dwordx4 v13, s[44:47], s68 offen offset:1024 lds
v_mfma_f32_16x16x32_bf16 a[136:139], a[24:27], a[40:43], a[136:139]
v_mfma_f32_16x16x32_bf16 a[140:143], a[24:27], a[44:47], a[140:143]
v_mfma_f32_16x16x32_bf16 a[144:147], a[24:27], a[48:51], a[144:147]
v_mfma_f32_16x16x32_bf16 a[148:151], a[24:27], a[52:55], a[148:151]
s_xor_b32 s69, s69, 0x10000
s_add_u32 s68, s68, 0x80000
v_mfma_f32_16x16x32_bf16 a[152:155], a[24:27], a[56:59], a[152:155]
v_mfma_f32_16x16x32_bf16 a[156:159], a[24:27], a[60:63], a[156:159]
v_mfma_f32_16x16x32_bf16 a[160:163], a[28:31], a[32:35], a[160:163]
v_mfma_f32_16x16x32_bf16 a[164:167], a[28:31], a[36:39], a[164:167]
v_lshrrev_b32 v0, 4, v4
v_bfe_u32 v1, v0, 3, 1
v_lshlrev_b32 v1, 10, v1
v_and_b32 v2, 7, v0
v_lshl_add_u32 v1, v2, 7, v1
v_add_u32 v1, s59, v1
s_add_u32 s59, s59, 0x800
s_mov_b32 exec_lo, 0xffff
s_mov_b32 exec_hi, 0
global_load_dword v5, v1, s[92:93] offset:0
s_mov_b64 exec, -1
v_mfma_f32_16x16x32_bf16 a[168:171], a[28:31], a[40:43], a[168:171]
v_mfma_f32_16x16x32_bf16 a[172:175], a[28:31], a[44:47], a[172:175]
v_mfma_f32_16x16x32_bf16 a[176:179], a[28:31], a[48:51], a[176:179]
v_mfma_f32_16x16x32_bf16 a[180:183], a[28:31], a[52:55], a[180:183]
v_mfma_f32_16x16x32_bf16 a[184:187], a[28:31], a[56:59], a[184:187]
v_mfma_f32_16x16x32_bf16 a[188:191], a[28:31], a[60:63], a[188:191]
s_waitcnt vmcnt(10) lgkmcnt(0)
s_barrier
v_xor_b32 v16, 0x10000, v16
v_xor_b32 v17, 0x10000, v17
v_xor_b32 v18, 0x10000, v18
v_xor_b32 v19, 0x10000, v19
v_xor_b32 v20, 0x10000, v20
v_xor_b32 v21, 0x10000, v21
v_mfma_f32_16x16x32_bf16 v[64:67], a[64:67], a[96:99], v[64:67]
ds_read_b128 a[32:35], v16 offset:0
v_mfma_f32_16x16x32_bf16 v[68:71], a[64:67], a[100:103], v[68:71]
s_add_u32 m0, s52, 0
v_mfma_f32_16x16x32_bf16 v[72:75], a[64:67], a[104:107], v[72:75]
ds_read_b64_tr_b16 a[0:1], v18 offset:0
v_mfma_f32_16x16x32_bf16 v[76:79], a[64:67], a[108:111], v[76:79]
v_mfma_f32_16x16x32_bf16 v[80:83], a[64:67], a[112:115], v[80:83]
ds_read_b64_tr_b16 a[2:3], v19 offset:0
v_mfma_f32_16x16x32_bf16 v[84:87], a[64:67], a[116:119], v[84:87]
v_mfma_f32_16x16x32_bf16 v[88:91], a[64:67], a[120:123], v[88:91]
ds_read_b128 a[36:39], v16 offset:2048
v_mfma_f32_16x16x32_bf16 v[92:95], a[64:67], a[124:127], v[92:95]
buffer_load_dwordx4 v8, s[36:39], s54 offen offset:0 lds
v_mfma_f32_16x16x32_bf16 v[96:99], a[68:71], a[96:99], v[96:99]
ds_read_b64_tr_b16 a[4:5], v20 offset:0
v_mfma_f32_16x16x32_bf16 v[100:103], a[68:71], a[100:103], v[100:103]
v_mfma_f32_16x16x32_bf16 v[104:107], a[68:71], a[104:107], v[104:107]
ds_read_b64_tr_b16 a[6:7], v21 offset:0
v_mfma_f32_16x16x32_bf16 v[108:111], a[68:71], a[108:111], v[108:111]
buffer_load_dwordx4 v9, s[36:39], s54 offen offset:1024 lds
v_mfma_f32_16x16x32_bf16 v[112:115], a[68:71], a[112:115], v[112:115]
ds_read_b128 a[40:43], v16 offset:4096
v_mfma_f32_16x16x32_bf16 v[116:119], a[68:71], a[116:119], v[116:119]
v_mfma_f32_16x16x32_bf16 v[120:123], a[68:71], a[120:123], v[120:123]
ds_read_b64_tr_b16 a[8:9], v18 offset:512
v_mfma_f32_16x16x32_bf16 v[124:127], a[68:71], a[124:127], v[124:127]
v_mfma_f32_16x16x32_bf16 v[128:131], a[72:75], a[96:99], v[128:131]
ds_read_b64_tr_b16 a[10:11], v19 offset:512
v_mfma_f32_16x16x32_bf16 v[132:135], a[72:75], a[100:103], v[132:135]
buffer_load_dwordx4 v10, s[36:39], s54 offen offset:2048 lds
v_mfma_f32_16x16x32_bf16 v[136:139], a[72:75], a[104:107], v[136:139]
ds_read_b128 a[44:47], v16 offset:6144
v_mfma_f32_16x16x32_bf16 v[140:143], a[72:75], a[108:111], v[140:143]
v_mfma_f32_16x16x32_bf16 v[144:147], a[72:75], a[112:115], v[144:147]
ds_read_b64_tr_b16 a[12:13], v20 offset:512
v_mfma_f32_16x16x32_bf16 v[148:151], a[72:75], a[116:119], v[148:151]
buffer_load_dwordx4 v11, s[36:39], s54 offen offset:3072 lds
v_mfma_f32_16x16x32_bf16 v[152:155], a[72:75], a[120:123], v[152:155]
ds_read_b64_tr_b16 a[14:15], v21 offset:512
v_mfma_f32_16x16x32_bf16 v[156:159], a[72:75], a[124:127], v[156:159]
v_mfma_f32_16x16x32_bf16 v[160:163], a[76:79], a[96:99], v[160:163]
ds_read_b128 a[48:51], v16 offset:8192
v_mfma_f32_16x16x32_bf16 v[164:167], a[76:79], a[100:103], v[164:167]
v_mfma_f32_16x16x32_bf16 v[168:171], a[76:79], a[104:107], v[168:171]
ds_read_b64_tr_b16 a[16:17], v18 offset:1024
v_mfma_f32_16x16x32_bf16 v[172:175], a[76:79], a[108:111], v[172:175]
s_add_u32 m0, s52, 0x1000
v_mfma_f32_16x16x32_bf16 v[176:179], a[76:79], a[112:115], v[176:179]
ds_read_b64_tr_b16 a[18:19], v19 offset:1024
v_mfma_f32_16x16x32_bf16 v[180:183], a[76:79], a[116:119], v[180:183]
v_mfma_f32_16x16x32_bf16 v[184:187], a[76:79], a[120:123], v[184:187]
ds_read_b128 a[52:55], v16 offset:10240
v_mfma_f32_16x16x32_bf16 v[188:191], a[76:79], a[124:127], v[188:191]
buffer_load_dwordx4 v8, s[36:39], s55 offen offset:0 lds
v_mfma_f32_16x16x32_bf16 v[192:195], a[80:83], a[96:99], v[192:195]
ds_read_b64_tr_b16 a[20:21], v20 offset:1024
v_mfma_f32_16x16x32_bf16 v[196:199], a[80:83], a[100:103], v[196:199]
v_mfma_f32_16x16x32_bf16 v[200:203], a[80:83], a[104:107], v[200:203]
ds_read_b64_tr_b16 a[22:23], v21 offset:1024
v_mfma_f32_16x16x32_bf16 v[204:207], a[80:83], a[108:111], v[204:207]
v_mfma_f32_16x16x32_bf16 v[208:211], a[80:83], a[112:115], v[208:211]
ds_read_b128 a[56:59], v16 offset:12288
v_mfma_f32_16x16x32_bf16 v[212:215], a[80:83], a[116:119], v[212:215]
buffer_load_dwordx4 v9, s[36:39], s55 offen offset:1024 lds
v_mfma_f32_16x16x32_bf16 v[216:219], a[80:83], a[120:123], v[216:219]
ds_read_b64_tr_b16 a[24:25], v18 offset:1536
v_mfma_f32_16x16x32_bf16 v[220:223], a[80:83], a[124:127], v[220:223]
v_mfma_f32_16x16x32_bf16 v[224:227], a[84:87], a[96:99], v[224:227]
ds_read_b64_tr_b16 a[26:27], v19 offset:1536
v_mfma_f32_16x16x32_bf16 v[228:231], a[84:87], a[100:103], v[228:231]
buffer_load_dwordx4 v10, s[36:39], s55 offen offset:2048 lds
v_mfma_f32_16x16x32_bf16 v[232:235], a[84:87], a[104:107], v[232:235]
ds_read_b128 a[60:63], v16 offset:14336
v_mfma_f32_16x16x32_bf16 v[236:239], a[84:87], a[108:111], v[236:239]
v_mfma_f32_16x16x32_bf16 v[240:243], a[84:87], a[112:115], v[240:243]
ds_read_b64_tr_b16 a[28:29], v20 offset:1536
v_mfma_f32_16x16x32_bf16 v[244:247], a[84:87], a[116:119], v[244:247]
v_mfma_f32_16x16x32_bf16 v[248:251], a[84:87], a[120:123], v[248:251]
ds_read_b64_tr_b16 a[30:31], v21 offset:1536
v_mfma_f32_16x16x32_bf16 v[252:255], a[84:87], a[124:127], v[252:255]
buffer_load_dwordx4 v11, s[36:39], s55 offen offset:3072 lds
v_mfma_f32_16x16x32_bf16 a[128:131], a[88:91], a[96:99], a[128:131]
v_mfma_f32_16x16x32_bf16 a[132:135], a[88:91], a[100:103], a[132:135]
v_mfma_f32_16x16x32_bf16 a[136:139], a[88:91], a[104:107], a[136:139]
v_mfma_f32_16x16x32_bf16 a[140:143], a[88:91], a[108:111], a[140:143]
s_xor_b32 s52, s52, 0x10000
s_add_u32 s54, s54, 0x80
s_add_u32 s55, s55, 0x80
v_mfma_f32_16x16x32_bf16 a[144:147], a[88:91], a[112:115], a[144:147]
v_mfma_f32_16x16x32_bf16 a[148:151], a[88:91], a[116:119], a[148:151]
v_mfma_f32_16x16x32_bf16 a[152:155], a[88:91], a[120:123], a[152:155]
v_mfma_f32_16x16x32_bf16 a[156:159], a[88:91], a[124:127], a[156:159]
v_mfma_f32_16x16x32_bf16 a[160:163], a[92:95], a[96:99], a[160:163]
v_lshrrev_b32 v0, 4, v4
v_bfe_u32 v1, v0, 3, 1
v_lshlrev_b32 v1, 10, v1
v_and_b32 v2, 7, v0
v_lshl_add_u32 v1, v2, 7, v1
v_add_u32 v1, s59, v1
s_add_u32 s59, s59, 0x800
s_mov_b32 exec_lo, 0xffff
s_mov_b32 exec_hi, 0
global_load_dword v5, v1, s[92:93] offset:0
s_mov_b64 exec, -1
v_mfma_f32_16x16x32_bf16 a[164:167], a[92:95], a[100:103], a[164:167]
v_mfma_f32_16x16x32_bf16 a[168:171], a[92:95], a[104:107], a[168:171]
v_mfma_f32_16x16x32_bf16 a[172:175], a[92:95], a[108:111], a[172:175]
v_mfma_f32_16x16x32_bf16 a[176:179], a[92:95], a[112:115], a[176:179]
v_mfma_f32_16x16x32_bf16 a[180:183], a[92:95], a[116:119], a[180:183]
v_mfma_f32_16x16x32_bf16 a[184:187], a[92:95], a[120:123], a[184:187]
v_mfma_f32_16x16x32_bf16 a[188:191], a[92:95], a[124:127], a[188:191]
s_waitcnt vmcnt(10) lgkmcnt(0)
s_barrier
v_mfma_f32_16x16x32_bf16 v[64:67], a[0:3], a[32:35], v[64:67]
ds_read_b128 a[96:99], v17 offset:0
v_mfma_f32_16x16x32_bf16 v[68:71], a[0:3], a[36:39], v[68:71]
s_add_u32 m0, s69, 0
v_mfma_f32_16x16x32_bf16 v[72:75], a[0:3], a[40:43], v[72:75]
ds_read_b64_tr_b16 a[64:65], v18 offset:8192
v_mfma_f32_16x16x32_bf16 v[76:79], a[0:3], a[44:47], v[76:79]
buffer_load_dwordx4 v12, s[40:43], s68 offen offset:0 lds
v_mfma_f32_16x16x32_bf16 v[80:83], a[0:3], a[48:51], v[80:83]
ds_read_b64_tr_b16 a[66:67], v19 offset:8192
v_mfma_f32_16x16x32_bf16 v[84:87], a[0:3], a[52:55], v[84:87]
buffer_load_dwordx4 v13, s[40:43], s68 offen offset:1024 lds
v_mfma_f32_16x16x32_bf16 v[88:91], a[0:3], a[56:59], v[88:91]
ds_read_b128 a[100:103], v17 offset:2048
v_mfma_f32_16x16x32_bf16 v[92:95], a[0:3], a[60:63], v[92:95]
s_add_u32 m0, s69, 0x4000
v_mfma_f32_16x16x32_bf16 v[96:99], a[4:7], a[32:35], v[96:99]
ds_read_b64_tr_b16 a[68:69], v20 offset:8192
v_mfma_f32_16x16x32_bf16 v[100:103], a[4:7], a[36:39], v[100:103]
buffer_load_dwordx4 v12, s[44:47], s68 offen offset:0 lds
v_mfma_f32_16x16x32_bf16 v[104:107], a[4:7], a[40:43], v[104:107]
ds_read_b64_tr_b16 a[70:71], v21 offset:8192
v_mfma_f32_16x16x32_bf16 v[108:111], a[4:7], a[44:47], v[108:111]
buffer_load_dwordx4 v13, s[44:47], s68 offen offset:1024 lds
v_mfma_f32_16x16x32_bf16 v[112:115], a[4:7], a[48:51], v[112:115]
ds_read_b128 a[104:107], v17 offset:4096
v_mfma_f32_16x16x32_bf16 v[116:119], a[4:7], a[52:55], v[116:119]
s_xor_b32 s69, s69, 0x10000
s_add_u32 s68, s68, 0x80000
v_mfma_f32_16x16x32_bf16 v[120:123], a[4:7], a[56:59], v[120:123]
ds_read_b64_tr_b16 a[72:73], v18 offset:8704
v_mfma_f32_16x16x32_bf16 v[124:127], a[4:7], a[60:63], v[124:127]
s_mov_b32 s36, s92
v_mfma_f32_16x16x32_bf16 v[128:131], a[8:11], a[32:35], v[128:131]
ds_read_b64_tr_b16 a[74:75], v19 offset:8704
v_mfma_f32_16x16x32_bf16 v[132:135], a[8:11], a[36:39], v[132:135]
s_and_b32 s37, s93, 0xffff
v_mfma_f32_16x16x32_bf16 v[136:139], a[8:11], a[40:43], v[136:139]
ds_read_b128 a[108:111], v17 offset:6144
v_mfma_f32_16x16x32_bf16 v[140:143], a[8:11], a[44:47], v[140:143]
s_mov_b32 s38, 0x200000
v_mfma_f32_16x16x32_bf16 v[144:147], a[8:11], a[48:51], v[144:147]
ds_read_b64_tr_b16 a[76:77], v20 offset:8704
v_mfma_f32_16x16x32_bf16 v[148:151], a[8:11], a[52:55], v[148:151]
s_mov_b32 s39, 0x20000
v_mfma_f32_16x16x32_bf16 v[152:155], a[8:11], a[56:59], v[152:155]
ds_read_b64_tr_b16 a[78:79], v21 offset:8704
v_mfma_f32_16x16x32_bf16 v[156:159], a[8:11], a[60:63], v[156:159]
s_sub_u32 s40, s94, 0x400
v_mfma_f32_16x16x32_bf16 v[160:163], a[12:15], a[32:35], v[160:163]
ds_read_b128 a[112:115], v17 offset:8192
v_mfma_f32_16x16x32_bf16 v[164:167], a[12:15], a[36:39], v[164:167]
s_subb_u32 s41, s95, 0
v_mfma_f32_16x16x32_bf16 v[168:171], a[12:15], a[40:43], v[168:171]
ds_read_b64_tr_b16 a[80:81], v18 offset:9216
v_mfma_f32_16x16x32_bf16 v[172:175], a[12:15], a[44:47], v[172:175]
s_and_b32 s41, s41, 0xffff
v_mfma_f32_16x16x32_bf16 v[176:179], a[12:15], a[48:51], v[176:179]
ds_read_b64_tr_b16 a[82:83], v19 offset:9216
v_mfma_f32_16x16x32_bf16 v[180:183], a[12:15], a[52:55], v[180:183]
s_mov_b32 s42, 0x400400
v_mfma_f32_16x16x32_bf16 v[184:187], a[12:15], a[56:59], v[184:187]
ds_read_b128 a[116:119], v17 offset:10240
v_mfma_f32_16x16x32_bf16 v[188:191], a[12:15], a[60:63], v[188:191]
s_mov_b32 s43, 0x20000
v_mfma_f32_16x16x32_bf16 v[192:195], a[16:19], a[32:35], v[192:195]
ds_read_b64_tr_b16 a[84:85], v20 offset:9216
v_mfma_f32_16x16x32_bf16 v[196:199], a[16:19], a[36:39], v[196:199]
s_lshl_b32 s27, s26, 17
v_mfma_f32_16x16x32_bf16 v[200:203], a[16:19], a[40:43], v[200:203]
ds_read_b64_tr_b16 a[86:87], v21 offset:9216
v_mfma_f32_16x16x32_bf16 v[204:207], a[16:19], a[44:47], v[204:207]
s_lshl_b32 s28, s23, 15
v_mfma_f32_16x16x32_bf16 v[208:211], a[16:19], a[48:51], v[208:211]
ds_read_b128 a[120:123], v17 offset:12288
v_mfma_f32_16x16x32_bf16 v[212:215], a[16:19], a[52:55], v[212:215]
s_add_u32 s54, s27, s28
v_mfma_f32_16x16x32_bf16 v[216:219], a[16:19], a[56:59], v[216:219]
ds_read_b64_tr_b16 a[88:89], v18 offset:9728
v_mfma_f32_16x16x32_bf16 v[220:223], a[16:19], a[60:63], v[220:223]
s_sub_u32 s27, 15, s26
v_mfma_f32_16x16x32_bf16 v[224:227], a[20:23], a[32:35], v[224:227]
ds_read_b64_tr_b16 a[90:91], v19 offset:9728
v_mfma_f32_16x16x32_bf16 v[228:231], a[20:23], a[36:39], v[228:231]
s_lshl_b32 s27, s27, 17
v_mfma_f32_16x16x32_bf16 v[232:235], a[20:23], a[40:43], v[232:235]
ds_read_b128 a[124:127], v17 offset:14336
v_mfma_f32_16x16x32_bf16 v[236:239], a[20:23], a[44:47], v[236:239]
s_add_u32 s55, s27, s28
v_mfma_f32_16x16x32_bf16 v[240:243], a[20:23], a[48:51], v[240:243]
ds_read_b64_tr_b16 a[92:93], v20 offset:9728
v_mfma_f32_16x16x32_bf16 v[244:247], a[20:23], a[52:55], v[244:247]
s_lshl_b32 s27, s23, 16
v_mfma_f32_16x16x32_bf16 v[248:251], a[20:23], a[56:59], v[248:251]
ds_read_b64_tr_b16 a[94:95], v21 offset:9728
v_mfma_f32_16x16x32_bf16 v[252:255], a[20:23], a[60:63], v[252:255]
s_lshl_b32 s28, s25, 8
v_mfma_f32_16x16x32_bf16 a[128:131], a[24:27], a[32:35], a[128:131]
s_add_u32 s56, s27, s28
v_mfma_f32_16x16x32_bf16 a[132:135], a[24:27], a[36:39], a[132:135]
s_add_u32 s68, s56, 0x40000
v_mfma_f32_16x16x32_bf16 a[136:139], a[24:27], a[40:43], a[136:139]
s_lshl_b32 s27, s23, 13
v_mfma_f32_16x16x32_bf16 a[140:143], a[24:27], a[44:47], a[140:143]
v_mfma_f32_16x16x32_bf16 a[144:147], a[24:27], a[48:51], a[144:147]
s_add_u32 s52, s24, s27
v_mfma_f32_16x16x32_bf16 a[148:151], a[24:27], a[52:55], a[148:151]
v_mfma_f32_16x16x32_bf16 a[152:155], a[24:27], a[56:59], a[152:155]
s_lshl_b32 s27, s23, 11
v_mfma_f32_16x16x32_bf16 a[156:159], a[24:27], a[60:63], a[156:159]
v_mfma_f32_16x16x32_bf16 a[160:163], a[28:31], a[32:35], a[160:163]
s_add_u32 s53, s24, s27
v_mfma_f32_16x16x32_bf16 a[164:167], a[28:31], a[36:39], a[164:167]
v_mfma_f32_16x16x32_bf16 a[168:171], a[28:31], a[40:43], a[168:171]
s_add_u32 s53, s53, 0x8000
v_mfma_f32_16x16x32_bf16 a[172:175], a[28:31], a[44:47], a[172:175]
v_mfma_f32_16x16x32_bf16 a[176:179], a[28:31], a[48:51], a[176:179]
v_lshrrev_b32 v0, 4, v4
v_bfe_u32 v1, v0, 3, 1
v_lshlrev_b32 v1, 10, v1
v_and_b32 v2, 7, v0
v_lshl_add_u32 v1, v2, 7, v1
v_add_u32 v1, s59, v1
s_add_u32 s59, s59, 0x800
s_mov_b32 exec_lo, 0xffff
s_mov_b32 exec_hi, 0
global_load_dword v5, v1, s[92:93] offset:0
s_mov_b64 exec, -1
v_mfma_f32_16x16x32_bf16 a[180:183], a[28:31], a[52:55], a[180:183]
v_mfma_f32_16x16x32_bf16 a[184:187], a[28:31], a[56:59], a[184:187]
v_mfma_f32_16x16x32_bf16 a[188:191], a[28:31], a[60:63], a[188:191]
s_waitcnt vmcnt(6) lgkmcnt(0)
s_barrier
v_xor_b32 v16, 0x10000, v16
v_xor_b32 v17, 0x10000, v17
v_xor_b32 v18, 0x10000, v18
v_xor_b32 v19, 0x10000, v19
v_xor_b32 v20, 0x10000, v20
v_xor_b32 v21, 0x10000, v21
v_mfma_f32_16x16x32_bf16 v[64:67], a[64:67], a[96:99], v[64:67]
ds_read_b128 a[32:35], v16 offset:0
v_mfma_f32_16x16x32_bf16 v[68:71], a[64:67], a[100:103], v[68:71]
s_add_u32 m0, s53, 0
v_mfma_f32_16x16x32_bf16 v[72:75], a[64:67], a[104:107], v[72:75]
ds_read_b64_tr_b16 a[0:1], v18 offset:0
v_mfma_f32_16x16x32_bf16 v[76:79], a[64:67], a[108:111], v[76:79]
v_mfma_f32_16x16x32_bf16 v[80:83], a[64:67], a[112:115], v[80:83]
ds_read_b64_tr_b16 a[2:3], v19 offset:0
v_mfma_f32_16x16x32_bf16 v[84:87], a[64:67], a[116:119], v[84:87]
v_mfma_f32_16x16x32_bf16 v[88:91], a[64:67], a[120:123], v[88:91]
ds_read_b128 a[36:39], v16 offset:2048
v_mfma_f32_16x16x32_bf16 v[92:95], a[64:67], a[124:127], v[92:95]
buffer_load_dwordx4 v12, s[40:43], s56 offen offset:0 lds
v_mfma_f32_16x16x32_bf16 v[96:99], a[68:71], a[96:99], v[96:99]
ds_read_b64_tr_b16 a[4:5], v20 offset:0
v_mfma_f32_16x16x32_bf16 v[100:103], a[68:71], a[100:103], v[100:103]
v_mfma_f32_16x16x32_bf16 v[104:107], a[68:71], a[104:107], v[104:107]
ds_read_b64_tr_b16 a[6:7], v21 offset:0
v_mfma_f32_16x16x32_bf16 v[108:111], a[68:71], a[108:111], v[108:111]
v_mfma_f32_16x16x32_bf16 v[112:115], a[68:71], a[112:115], v[112:115]
ds_read_b128 a[40:43], v16 offset:4096
v_mfma_f32_16x16x32_bf16 v[116:119], a[68:71], a[116:119], v[116:119]
buffer_load_dwordx4 v13, s[40:43], s56 offen offset:1024 lds
v_mfma_f32_16x16x32_bf16 v[120:123], a[68:71], a[120:123], v[120:123]
ds_read_b64_tr_b16 a[8:9], v18 offset:512
v_mfma_f32_16x16x32_bf16 v[124:127], a[68:71], a[124:127], v[124:127]
v_mfma_f32_16x16x32_bf16 v[128:131], a[72:75], a[96:99], v[128:131]
ds_read_b64_tr_b16 a[10:11], v19 offset:512
v_mfma_f32_16x16x32_bf16 v[132:135], a[72:75], a[100:103], v[132:135]
v_mfma_f32_16x16x32_bf16 v[136:139], a[72:75], a[104:107], v[136:139]
ds_read_b128 a[44:47], v16 offset:6144
v_mfma_f32_16x16x32_bf16 v[140:143], a[72:75], a[108:111], v[140:143]
s_add_u32 m0, s53, 0x2000
v_mfma_f32_16x16x32_bf16 v[144:147], a[72:75], a[112:115], v[144:147]
ds_read_b64_tr_b16 a[12:13], v20 offset:512
v_mfma_f32_16x16x32_bf16 v[148:151], a[72:75], a[116:119], v[148:151]
v_mfma_f32_16x16x32_bf16 v[152:155], a[72:75], a[120:123], v[152:155]
ds_read_b64_tr_b16 a[14:15], v21 offset:512
v_mfma_f32_16x16x32_bf16 v[156:159], a[72:75], a[124:127], v[156:159]
v_mfma_f32_16x16x32_bf16 v[160:163], a[76:79], a[96:99], v[160:163]
ds_read_b128 a[48:51], v16 offset:8192
v_mfma_f32_16x16x32_bf16 v[164:167], a[76:79], a[100:103], v[164:167]
buffer_load_dwordx4 v12, s[40:43], s68 offen offset:0 lds
v_mfma_f32_16x16x32_bf16 v[168:171], a[76:79], a[104:107], v[168:171]
ds_read_b64_tr_b16 a[16:17], v18 offset:1024
v_mfma_f32_16x16x32_bf16 v[172:175], a[76:79], a[108:111], v[172:175]
v_mfma_f32_16x16x32_bf16 v[176:179], a[76:79], a[112:115], v[176:179]
ds_read_b64_tr_b16 a[18:19], v19 offset:1024
v_mfma_f32_16x16x32_bf16 v[180:183], a[76:79], a[116:119], v[180:183]
v_mfma_f32_16x16x32_bf16 v[184:187], a[76:79], a[120:123], v[184:187]
ds_read_b128 a[52:55], v16 offset:10240
v_mfma_f32_16x16x32_bf16 v[188:191], a[76:79], a[124:127], v[188:191]
buffer_load_dwordx4 v13, s[40:43], s68 offen offset:1024 lds
v_mfma_f32_16x16x32_bf16 v[192:195], a[80:83], a[96:99], v[192:195]
ds_read_b64_tr_b16 a[20:21], v20 offset:1024
v_mfma_f32_16x16x32_bf16 v[196:199], a[80:83], a[100:103], v[196:199]
v_mfma_f32_16x16x32_bf16 v[200:203], a[80:83], a[104:107], v[200:203]
ds_read_b64_tr_b16 a[22:23], v21 offset:1024
v_mfma_f32_16x16x32_bf16 v[204:207], a[80:83], a[108:111], v[204:207]
v_mfma_f32_16x16x32_bf16 v[208:211], a[80:83], a[112:115], v[208:211]
ds_read_b128 a[56:59], v16 offset:12288
v_mfma_f32_16x16x32_bf16 v[212:215], a[80:83], a[116:119], v[212:215]
s_add_u32 m0, s52, 0
v_mfma_f32_16x16x32_bf16 v[216:219], a[80:83], a[120:123], v[216:219]
ds_read_b64_tr_b16 a[24:25], v18 offset:1536
v_mfma_f32_16x16x32_bf16 v[220:223], a[80:83], a[124:127], v[220:223]
v_mfma_f32_16x16x32_bf16 v[224:227], a[84:87], a[96:99], v[224:227]
ds_read_b64_tr_b16 a[26:27], v19 offset:1536
v_mfma_f32_16x16x32_bf16 v[228:231], a[84:87], a[100:103], v[228:231]
v_mfma_f32_16x16x32_bf16 v[232:235], a[84:87], a[104:107], v[232:235]
ds_read_b128 a[60:63], v16 offset:14336
v_mfma_f32_16x16x32_bf16 v[236:239], a[84:87], a[108:111], v[236:239]
buffer_load_dwordx4 v8, s[36:39], s54 offen offset:0 lds
v_mfma_f32_16x16x32_bf16 v[240:243], a[84:87], a[112:115], v[240:243]
ds_read_b64_tr_b16 a[28:29], v20 offset:1536
v_mfma_f32_16x16x32_bf16 v[244:247], a[84:87], a[116:119], v[244:247]
v_mfma_f32_16x16x32_bf16 v[248:251], a[84:87], a[120:123], v[248:251]
ds_read_b64_tr_b16 a[30:31], v21 offset:1536
v_mfma_f32_16x16x32_bf16 v[252:255], a[84:87], a[124:127], v[252:255]
v_mfma_f32_16x16x32_bf16 a[128:131], a[88:91], a[96:99], a[128:131]
v_mfma_f32_16x16x32_bf16 a[132:135], a[88:91], a[100:103], a[132:135]
buffer_load_dwordx4 v9, s[36:39], s54 offen offset:1024 lds
v_mfma_f32_16x16x32_bf16 a[136:139], a[88:91], a[104:107], a[136:139]
v_mfma_f32_16x16x32_bf16 a[140:143], a[88:91], a[108:111], a[140:143]
v_mfma_f32_16x16x32_bf16 a[144:147], a[88:91], a[112:115], a[144:147]
v_mfma_f32_16x16x32_bf16 a[148:151], a[88:91], a[116:119], a[148:151]
v_mfma_f32_16x16x32_bf16 a[152:155], a[88:91], a[120:123], a[152:155]
v_mfma_f32_16x16x32_bf16 a[156:159], a[88:91], a[124:127], a[156:159]
v_lshrrev_b32 v0, 4, v4
v_bfe_u32 v1, v0, 3, 1
v_lshlrev_b32 v1, 10, v1
v_and_b32 v2, 7, v0
v_lshl_add_u32 v1, v2, 7, v1
v_add_u32 v1, s59, v1
s_add_u32 s59, s59, 0x800
s_mov_b32 exec_lo, 0xffff
s_mov_b32 exec_hi, 0
global_load_dword v5, v1, s[92:93] offset:0
s_mov_b64 exec, -1
v_mfma_f32_16x16x32_bf16 a[160:163], a[92:95], a[96:99], a[160:163]
v_mfma_f32_16x16x32_bf16 a[164:167], a[92:95], a[100:103], a[164:167]
v_mfma_f32_16x16x32_bf16 a[168:171], a[92:95], a[104:107], a[168:171]
v_mfma_f32_16x16x32_bf16 a[172:175], a[92:95], a[108:111], a[172:175]
v_mfma_f32_16x16x32_bf16 a[176:179], a[92:95], a[112:115], a[176:179]
v_mfma_f32_16x16x32_bf16 a[180:183], a[92:95], a[116:119], a[180:183]
v_mfma_f32_16x16x32_bf16 a[184:187], a[92:95], a[120:123], a[184:187]
v_mfma_f32_16x16x32_bf16 a[188:191], a[92:95], a[124:127], a[188:191]
s_waitcnt vmcnt(8) lgkmcnt(0)
s_barrier
v_mfma_f32_16x16x32_bf16 v[64:67], a[0:3], a[32:35], v[64:67]
ds_read_b128 a[96:99], v17 offset:0
v_mfma_f32_16x16x32_bf16 v[68:71], a[0:3], a[36:39], v[68:71]
buffer_load_dwordx4 v10, s[36:39], s54 offen offset:2048 lds
v_mfma_f32_16x16x32_bf16 v[72:75], a[0:3], a[40:43], v[72:75]
ds_read_b64_tr_b16 a[64:65], v18 offset:8192
v_mfma_f32_16x16x32_bf16 v[76:79], a[0:3], a[44:47], v[76:79]
v_mfma_f32_16x16x32_bf16 v[80:83], a[0:3], a[48:51], v[80:83]
ds_read_b64_tr_b16 a[66:67], v19 offset:8192
v_mfma_f32_16x16x32_bf16 v[84:87], a[0:3], a[52:55], v[84:87]
v_mfma_f32_16x16x32_bf16 v[88:91], a[0:3], a[56:59], v[88:91]
ds_read_b128 a[100:103], v17 offset:2048
v_mfma_f32_16x16x32_bf16 v[92:95], a[0:3], a[60:63], v[92:95]
buffer_load_dwordx4 v11, s[36:39], s54 offen offset:3072 lds
v_mfma_f32_16x16x32_bf16 v[96:99], a[4:7], a[32:35], v[96:99]
ds_read_b64_tr_b16 a[68:69], v20 offset:8192
v_mfma_f32_16x16x32_bf16 v[100:103], a[4:7], a[36:39], v[100:103]
v_mfma_f32_16x16x32_bf16 v[104:107], a[4:7], a[40:43], v[104:107]
ds_read_b64_tr_b16 a[70:71], v21 offset:8192
v_mfma_f32_16x16x32_bf16 v[108:111], a[4:7], a[44:47], v[108:111]
v_mfma_f32_16x16x32_bf16 v[112:115], a[4:7], a[48:51], v[112:115]
ds_read_b128 a[104:107], v17 offset:4096
v_mfma_f32_16x16x32_bf16 v[116:119], a[4:7], a[52:55], v[116:119]
v_mfma_f32_16x16x32_bf16 v[120:123], a[4:7], a[56:59], v[120:123]
ds_read_b64_tr_b16 a[72:73], v18 offset:8704
v_mfma_f32_16x16x32_bf16 v[124:127], a[4:7], a[60:63], v[124:127]
s_add_u32 m0, s52, 0x1000
v_mfma_f32_16x16x32_bf16 v[128:131], a[8:11], a[32:35], v[128:131]
ds_read_b64_tr_b16 a[74:75], v19 offset:8704
v_mfma_f32_16x16x32_bf16 v[132:135], a[8:11], a[36:39], v[132:135]
v_mfma_f32_16x16x32_bf16 v[136:139], a[8:11], a[40:43], v[136:139]
ds_read_b128 a[108:111], v17 offset:6144
v_mfma_f32_16x16x32_bf16 v[140:143], a[8:11], a[44:47], v[140:143]
v_mfma_f32_16x16x32_bf16 v[144:147], a[8:11], a[48:51], v[144:147]
ds_read_b64_tr_b16 a[76:77], v20 offset:8704
v_mfma_f32_16x16x32_bf16 v[148:151], a[8:11], a[52:55], v[148:151]
buffer_load_dwordx4 v8, s[36:39], s55 offen offset:0 lds
v_mfma_f32_16x16x32_bf16 v[152:155], a[8:11], a[56:59], v[152:155]
ds_read_b64_tr_b16 a[78:79], v21 offset:8704
v_mfma_f32_16x16x32_bf16 v[156:159], a[8:11], a[60:63], v[156:159]
v_mfma_f32_16x16x32_bf16 v[160:163], a[12:15], a[32:35], v[160:163]
ds_read_b128 a[112:115], v17 offset:8192
v_mfma_f32_16x16x32_bf16 v[164:167], a[12:15], a[36:39], v[164:167]
v_mfma_f32_16x16x32_bf16 v[168:171], a[12:15], a[40:43], v[168:171]
ds_read_b64_tr_b16 a[80:81], v18 offset:9216
v_mfma_f32_16x16x32_bf16 v[172:175], a[12:15], a[44:47], v[172:175]
v_mfma_f32_16x16x32_bf16 v[176:179], a[12:15], a[48:51], v[176:179]
ds_read_b64_tr_b16 a[82:83], v19 offset:9216
v_mfma_f32_16x16x32_bf16 v[180:183], a[12:15], a[52:55], v[180:183]
buffer_load_dwordx4 v9, s[36:39], s55 offen offset:1024 lds
v_mfma_f32_16x16x32_bf16 v[184:187], a[12:15], a[56:59], v[184:187]
ds_read_b128 a[116:119], v17 offset:10240
v_mfma_f32_16x16x32_bf16 v[188:191], a[12:15], a[60:63], v[188:191]
v_mfma_f32_16x16x32_bf16 v[192:195], a[16:19], a[32:35], v[192:195]
ds_read_b64_tr_b16 a[84:85], v20 offset:9216
v_mfma_f32_16x16x32_bf16 v[196:199], a[16:19], a[36:39], v[196:199]
v_mfma_f32_16x16x32_bf16 v[200:203], a[16:19], a[40:43], v[200:203]
ds_read_b64_tr_b16 a[86:87], v21 offset:9216
v_mfma_f32_16x16x32_bf16 v[204:207], a[16:19], a[44:47], v[204:207]
buffer_load_dwordx4 v10, s[36:39], s55 offen offset:2048 lds
v_mfma_f32_16x16x32_bf16 v[208:211], a[16:19], a[48:51], v[208:211]
ds_read_b128 a[120:123], v17 offset:12288
v_mfma_f32_16x16x32_bf16 v[212:215], a[16:19], a[52:55], v[212:215]
v_mfma_f32_16x16x32_bf16 v[216:219], a[16:19], a[56:59], v[216:219]
ds_read_b64_tr_b16 a[88:89], v18 offset:9728
v_mfma_f32_16x16x32_bf16 v[220:223], a[16:19], a[60:63], v[220:223]
v_mfma_f32_16x16x32_bf16 v[224:227], a[20:23], a[32:35], v[224:227]
ds_read_b64_tr_b16 a[90:91], v19 offset:9728
v_mfma_f32_16x16x32_bf16 v[228:231], a[20:23], a[36:39], v[228:231]
buffer_load_dwordx4 v11, s[36:39], s55 offen offset:3072 lds
v_mfma_f32_16x16x32_bf16 v[232:235], a[20:23], a[40:43], v[232:235]
ds_read_b128 a[124:127], v17 offset:14336
v_mfma_f32_16x16x32_bf16 v[236:239], a[20:23], a[44:47], v[236:239]
v_mfma_f32_16x16x32_bf16 v[240:243], a[20:23], a[48:51], v[240:243]
ds_read_b64_tr_b16 a[92:93], v20 offset:9728
v_mfma_f32_16x16x32_bf16 v[244:247], a[20:23], a[52:55], v[244:247]
v_mfma_f32_16x16x32_bf16 v[248:251], a[20:23], a[56:59], v[248:251]
ds_read_b64_tr_b16 a[94:95], v21 offset:9728
v_mfma_f32_16x16x32_bf16 v[252:255], a[20:23], a[60:63], v[252:255]
v_mfma_f32_16x16x32_bf16 a[128:131], a[24:27], a[32:35], a[128:131]
s_add_u32 s54, s54, 0x80
s_add_u32 s55, s55, 0x80
v_mfma_f32_16x16x32_bf16 a[132:135], a[24:27], a[36:39], a[132:135]
v_mfma_f32_16x16x32_bf16 a[136:139], a[24:27], a[40:43], a[136:139]
v_mfma_f32_16x16x32_bf16 a[140:143], a[24:27], a[44:47], a[140:143]
v_mfma_f32_16x16x32_bf16 a[144:147], a[24:27], a[48:51], a[144:147]
v_mfma_f32_16x16x32_bf16 a[148:151], a[24:27], a[52:55], a[148:151]
v_mfma_f32_16x16x32_bf16 a[152:155], a[24:27], a[56:59], a[152:155]
v_mfma_f32_16x16x32_bf16 a[156:159], a[24:27], a[60:63], a[156:159]
s_add_u32 s56, s56, 0x80000
s_add_u32 s68, s68, 0x80000
v_mfma_f32_16x16x32_bf16 a[160:163], a[28:31], a[32:35], a[160:163]
v_mfma_f32_16x16x32_bf16 a[164:167], a[28:31], a[36:39], a[164:167]
v_mfma_f32_16x16x32_bf16 a[168:171], a[28:31], a[40:43], a[168:171]
v_mfma_f32_16x16x32_bf16 a[172:175], a[28:31], a[44:47], a[172:175]
v_mfma_f32_16x16x32_bf16 a[176:179], a[28:31], a[48:51], a[176:179]
v_mfma_f32_16x16x32_bf16 a[180:183], a[28:31], a[52:55], a[180:183]
v_mfma_f32_16x16x32_bf16 a[184:187], a[28:31], a[56:59], a[184:187]
v_mfma_f32_16x16x32_bf16 a[188:191], a[28:31], a[60:63], a[188:191]
s_waitcnt lgkmcnt(0)
s_barrier
v_mfma_f32_16x16x32_bf16 v[64:67], a[64:67], a[96:99], v[64:67]
s_add_u32 m0, s53, 0xc000
v_mfma_f32_16x16x32_bf16 v[68:71], a[64:67], a[100:103], v[68:71]
v_mfma_f32_16x16x32_bf16 v[72:75], a[64:67], a[104:107], v[72:75]
v_mfma_f32_16x16x32_bf16 v[76:79], a[64:67], a[108:111], v[76:79]
buffer_load_dwordx4 v12, s[40:43], s56 offen offset:0 lds
v_mfma_f32_16x16x32_bf16 v[80:83], a[64:67], a[112:115], v[80:83]
v_mfma_f32_16x16x32_bf16 v[84:87], a[64:67], a[116:119], v[84:87]
v_mfma_f32_16x16x32_bf16 v[88:91], a[64:67], a[120:123], v[88:91]
buffer_load_dwordx4 v13, s[40:43], s56 offen offset:1024 lds
v_mfma_f32_16x16x32_bf16 v[92:95], a[64:67], a[124:127], v[92:95]
v_mfma_f32_16x16x32_bf16 v[96:99], a[68:71], a[96:99], v[96:99]
v_mfma_f32_16x16x32_bf16 v[100:103], a[68:71], a[100:103], v[100:103]
v_mfma_f32_16x16x32_bf16 v[104:107], a[68:71], a[104:107], v[104:107]
s_add_u32 m0, s53, 0xe000
v_mfma_f32_16x16x32_bf16 v[108:111], a[68:71], a[108:111], v[108:111]
v_mfma_f32_16x16x32_bf16 v[112:115], a[68:71], a[112:115], v[112:115]
v_mfma_f32_16x16x32_bf16 v[116:119], a[68:71], a[116:119], v[116:119]
buffer_load_dwordx4 v12, s[40:43], s68 offen offset:0 lds
v_mfma_f32_16x16x32_bf16 v[120:123], a[68:71], a[120:123], v[120:123]
v_mfma_f32_16x16x32_bf16 v[124:127], a[68:71], a[124:127], v[124:127]
v_mfma_f32_16x16x32_bf16 v[128:131], a[72:75], a[96:99], v[128:131]
v_mfma_f32_16x16x32_bf16 v[132:135], a[72:75], a[100:103], v[132:135]
buffer_load_dwordx4 v13, s[40:43], s68 offen offset:1024 lds
v_mfma_f32_16x16x32_bf16 v[136:139], a[72:75], a[104:107], v[136:139]
v_mfma_f32_16x16x32_bf16 v[140:143], a[72:75], a[108:111], v[140:143]
v_cvt_pk_bf16_f32 v64, v64, v65
v_cvt_pk_bf16_f32 v65, v66, v67
v_mfma_f32_16x16x32_bf16 v[144:147], a[72:75], a[112:115], v[144:147]
s_add_u32 m0, s52, 0xc000
v_cvt_pk_bf16_f32 v66, v96, v97
v_cvt_pk_bf16_f32 v67, v98, v99
v_mfma_f32_16x16x32_bf16 v[148:151], a[72:75], a[116:119], v[148:151]
v_cvt_pk_bf16_f32 v68, v68, v69
v_cvt_pk_bf16_f32 v69, v70, v71
v_mfma_f32_16x16x32_bf16 v[152:155], a[72:75], a[120:123], v[152:155]
v_cvt_pk_bf16_f32 v70, v100, v101
v_cvt_pk_bf16_f32 v71, v102, v103
v_mfma_f32_16x16x32_bf16 v[156:159], a[72:75], a[124:127], v[156:159]
v_cvt_pk_bf16_f32 v72, v72, v73
v_cvt_pk_bf16_f32 v73, v74, v75
v_mfma_f32_16x16x32_bf16 v[160:163], a[76:79], a[96:99], v[160:163]
buffer_load_dwordx4 v8, s[36:39], s54 offen offset:0 lds
v_cvt_pk_bf16_f32 v74, v104, v105
v_cvt_pk_bf16_f32 v75, v106, v107
v_mfma_f32_16x16x32_bf16 v[164:167], a[76:79], a[100:103], v[164:167]
v_cvt_pk_bf16_f32 v76, v76, v77
v_cvt_pk_bf16_f32 v77, v78, v79
v_mfma_f32_16x16x32_bf16 v[168:171], a[76:79], a[104:107], v[168:171]
v_cvt_pk_bf16_f32 v78, v108, v109
v_cvt_pk_bf16_f32 v79, v110, v111
v_mfma_f32_16x16x32_bf16 v[172:175], a[76:79], a[108:111], v[172:175]
buffer_load_dwordx4 v9, s[36:39], s54 offen offset:1024 lds
v_cvt_pk_bf16_f32 v80, v80, v81
v_cvt_pk_bf16_f32 v81, v82, v83
v_mfma_f32_16x16x32_bf16 v[176:179], a[76:79], a[112:115], v[176:179]
v_cvt_pk_bf16_f32 v82, v112, v113
v_cvt_pk_bf16_f32 v83, v114, v115
v_mfma_f32_16x16x32_bf16 v[180:183], a[76:79], a[116:119], v[180:183]
v_cvt_pk_bf16_f32 v84, v84, v85
v_cvt_pk_bf16_f32 v85, v86, v87
v_mfma_f32_16x16x32_bf16 v[184:187], a[76:79], a[120:123], v[184:187]
v_cvt_pk_bf16_f32 v86, v116, v117
v_cvt_pk_bf16_f32 v87, v118, v119
v_mfma_f32_16x16x32_bf16 v[188:191], a[76:79], a[124:127], v[188:191]
buffer_load_dwordx4 v10, s[36:39], s54 offen offset:2048 lds
v_cvt_pk_bf16_f32 v88, v88, v89
v_cvt_pk_bf16_f32 v89, v90, v91
v_mfma_f32_16x16x32_bf16 v[192:195], a[80:83], a[96:99], v[192:195]
v_cvt_pk_bf16_f32 v90, v120, v121
v_cvt_pk_bf16_f32 v91, v122, v123
v_mfma_f32_16x16x32_bf16 v[196:199], a[80:83], a[100:103], v[196:199]
v_cvt_pk_bf16_f32 v92, v92, v93
v_cvt_pk_bf16_f32 v93, v94, v95
v_mfma_f32_16x16x32_bf16 v[200:203], a[80:83], a[104:107], v[200:203]
buffer_load_dwordx4 v11, s[36:39], s54 offen offset:3072 lds
v_cvt_pk_bf16_f32 v94, v124, v125
v_cvt_pk_bf16_f32 v95, v126, v127
v_mfma_f32_16x16x32_bf16 v[204:207], a[80:83], a[108:111], v[204:207]
v_cvt_pk_bf16_f32 v128, v128, v129
v_cvt_pk_bf16_f32 v129, v130, v131
v_mfma_f32_16x16x32_bf16 v[208:211], a[80:83], a[112:115], v[208:211]
v_cvt_pk_bf16_f32 v130, v160, v161
v_cvt_pk_bf16_f32 v131, v162, v163
v_mfma_f32_16x16x32_bf16 v[212:215], a[80:83], a[116:119], v[212:215]
s_add_u32 m0, s52, 0xd000
v_cvt_pk_bf16_f32 v132, v132, v133
v_cvt_pk_bf16_f32 v133, v134, v135
v_mfma_f32_16x16x32_bf16 v[216:219], a[80:83], a[120:123], v[216:219]
v_cvt_pk_bf16_f32 v134, v164, v165
v_cvt_pk_bf16_f32 v135, v166, v167
v_mfma_f32_16x16x32_bf16 v[220:223], a[80:83], a[124:127], v[220:223]
v_cvt_pk_bf16_f32 v136, v136, v137
v_cvt_pk_bf16_f32 v137, v138, v139
v_mfma_f32_16x16x32_bf16 v[224:227], a[84:87], a[96:99], v[224:227]
v_cvt_pk_bf16_f32 v138, v168, v169
v_cvt_pk_bf16_f32 v139, v170, v171
v_mfma_f32_16x16x32_bf16 v[228:231], a[84:87], a[100:103], v[228:231]
buffer_load_dwordx4 v8, s[36:39], s55 offen offset:0 lds
v_cvt_pk_bf16_f32 v140, v140, v141
v_cvt_pk_bf16_f32 v141, v142, v143
v_mfma_f32_16x16x32_bf16 v[232:235], a[84:87], a[104:107], v[232:235]
v_cvt_pk_bf16_f32 v142, v172, v173
v_cvt_pk_bf16_f32 v143, v174, v175
v_mfma_f32_16x16x32_bf16 v[236:239], a[84:87], a[108:111], v[236:239]
v_cvt_pk_bf16_f32 v144, v144, v145
v_cvt_pk_bf16_f32 v145, v146, v147
v_mfma_f32_16x16x32_bf16 v[240:243], a[84:87], a[112:115], v[240:243]
buffer_load_dwordx4 v9, s[36:39], s55 offen offset:1024 lds
v_cvt_pk_bf16_f32 v146, v176, v177
v_cvt_pk_bf16_f32 v147, v178, v179
v_mfma_f32_16x16x32_bf16 v[244:247], a[84:87], a[116:119], v[244:247]
v_cvt_pk_bf16_f32 v148, v148, v149
v_cvt_pk_bf16_f32 v149, v150, v151
v_mfma_f32_16x16x32_bf16 v[248:251], a[84:87], a[120:123], v[248:251]
v_cvt_pk_bf16_f32 v150, v180, v181
v_cvt_pk_bf16_f32 v151, v182, v183
v_mfma_f32_16x16x32_bf16 v[252:255], a[84:87], a[124:127], v[252:255]
v_cvt_pk_bf16_f32 v152, v152, v153
v_cvt_pk_bf16_f32 v153, v154, v155
v_mfma_f32_16x16x32_bf16 a[128:131], a[88:91], a[96:99], a[128:131]
buffer_load_dwordx4 v10, s[36:39], s55 offen offset:2048 lds
v_cvt_pk_bf16_f32 v154, v184, v185
v_cvt_pk_bf16_f32 v155, v186, v187
v_mfma_f32_16x16x32_bf16 a[132:135], a[88:91], a[100:103], a[132:135]
v_cvt_pk_bf16_f32 v156, v156, v157
v_cvt_pk_bf16_f32 v157, v158, v159
v_mfma_f32_16x16x32_bf16 a[136:139], a[88:91], a[104:107], a[136:139]
v_cvt_pk_bf16_f32 v158, v188, v189
v_cvt_pk_bf16_f32 v159, v190, v191
v_mfma_f32_16x16x32_bf16 a[140:143], a[88:91], a[108:111], a[140:143]
buffer_load_dwordx4 v11, s[36:39], s55 offen offset:3072 lds
v_cvt_pk_bf16_f32 v192, v192, v193
v_cvt_pk_bf16_f32 v193, v194, v195
v_mfma_f32_16x16x32_bf16 a[144:147], a[88:91], a[112:115], a[144:147]
v_cvt_pk_bf16_f32 v194, v224, v225
v_cvt_pk_bf16_f32 v195, v226, v227
v_mfma_f32_16x16x32_bf16 a[148:151], a[88:91], a[116:119], a[148:151]
v_cvt_pk_bf16_f32 v196, v196, v197
v_cvt_pk_bf16_f32 v197, v198, v199
v_mfma_f32_16x16x32_bf16 a[152:155], a[88:91], a[120:123], a[152:155]
v_cvt_pk_bf16_f32 v198, v228, v229
v_cvt_pk_bf16_f32 v199, v230, v231
v_mfma_f32_16x16x32_bf16 a[156:159], a[88:91], a[124:127], a[156:159]
s_add_u32 s54, s54, 0x80
s_add_u32 s55, s55, 0x80
v_cvt_pk_bf16_f32 v200, v200, v201
v_cvt_pk_bf16_f32 v201, v202, v203
v_mfma_f32_16x16x32_bf16 a[160:163], a[92:95], a[96:99], a[160:163]
v_cvt_pk_bf16_f32 v202, v232, v233
v_cvt_pk_bf16_f32 v203, v234, v235
v_mfma_f32_16x16x32_bf16 a[164:167], a[92:95], a[100:103], a[164:167]
v_cvt_pk_bf16_f32 v204, v204, v205
v_cvt_pk_bf16_f32 v205, v206, v207
v_mfma_f32_16x16x32_bf16 a[168:171], a[92:95], a[104:107], a[168:171]
s_add_u32 s56, s56, 0x80000
s_add_u32 s68, s68, 0x80000
v_cvt_pk_bf16_f32 v206, v236, v237
v_cvt_pk_bf16_f32 v207, v238, v239
v_mfma_f32_16x16x32_bf16 a[172:175], a[92:95], a[108:111], a[172:175]
v_cvt_pk_bf16_f32 v208, v208, v209
v_cvt_pk_bf16_f32 v209, v210, v211
v_mfma_f32_16x16x32_bf16 a[176:179], a[92:95], a[112:115], a[176:179]
v_cvt_pk_bf16_f32 v210, v240, v241
v_cvt_pk_bf16_f32 v211, v242, v243
v_mfma_f32_16x16x32_bf16 a[180:183], a[92:95], a[116:119], a[180:183]
v_cvt_pk_bf16_f32 v212, v212, v213
v_cvt_pk_bf16_f32 v213, v214, v215
v_mfma_f32_16x16x32_bf16 a[184:187], a[92:95], a[120:123], a[184:187]
v_cvt_pk_bf16_f32 v214, v244, v245
v_cvt_pk_bf16_f32 v215, v246, v247
v_mfma_f32_16x16x32_bf16 a[188:191], a[92:95], a[124:127], a[188:191]
v_cvt_pk_bf16_f32 v216, v216, v217
v_cvt_pk_bf16_f32 v217, v218, v219
s_nop 7
v_cvt_pk_bf16_f32 v218, v248, v249
v_cvt_pk_bf16_f32 v219, v250, v251
v_cvt_pk_bf16_f32 v220, v220, v221
v_cvt_pk_bf16_f32 v221, v222, v223
v_cvt_pk_bf16_f32 v222, v252, v253
v_cvt_pk_bf16_f32 v223, v254, v255
v_accvgpr_read_b32 v0, a128
v_accvgpr_read_b32 v1, a129
v_accvgpr_read_b32 v2, a130
v_accvgpr_read_b32 v3, a131
v_accvgpr_read_b32 v32, a160
v_accvgpr_read_b32 v33, a161
v_accvgpr_read_b32 v34, a162
v_accvgpr_read_b32 v35, a163
v_cvt_pk_bf16_f32 v96, v0, v1
v_cvt_pk_bf16_f32 v97, v2, v3
v_cvt_pk_bf16_f32 v98, v32, v33
v_cvt_pk_bf16_f32 v99, v34, v35
s_add_u32 m0, s53, 0x18000
s_nop 0
buffer_load_dwordx4 v12, s[40:43], s56 offen offset:0 lds
v_accvgpr_read_b32 v0, a132
v_accvgpr_read_b32 v1, a133
v_accvgpr_read_b32 v2, a134
v_accvgpr_read_b32 v3, a135
v_accvgpr_read_b32 v32, a164
v_accvgpr_read_b32 v33, a165
v_accvgpr_read_b32 v34, a166
v_accvgpr_read_b32 v35, a167
v_cvt_pk_bf16_f32 v100, v0, v1
v_cvt_pk_bf16_f32 v101, v2, v3
v_cvt_pk_bf16_f32 v102, v32, v33
v_cvt_pk_bf16_f32 v103, v34, v35
buffer_load_dwordx4 v13, s[40:43], s56 offen offset:1024 lds
s_add_u32 m0, s53, 0x1a000
s_nop 0
v_accvgpr_read_b32 v0, a136
v_accvgpr_read_b32 v1, a137
v_accvgpr_read_b32 v2, a138
v_accvgpr_read_b32 v3, a139
v_accvgpr_read_b32 v32, a168
v_accvgpr_read_b32 v33, a169
v_accvgpr_read_b32 v34, a170
v_accvgpr_read_b32 v35, a171
v_cvt_pk_bf16_f32 v104, v0, v1
v_cvt_pk_bf16_f32 v105, v2, v3
v_cvt_pk_bf16_f32 v106, v32, v33
v_cvt_pk_bf16_f32 v107, v34, v35
buffer_load_dwordx4 v12, s[40:43], s68 offen offset:0 lds
buffer_load_dwordx4 v13, s[40:43], s68 offen offset:1024 lds
v_accvgpr_read_b32 v0, a140
v_accvgpr_read_b32 v1, a141
v_accvgpr_read_b32 v2, a142
v_accvgpr_read_b32 v3, a143
v_accvgpr_read_b32 v32, a172
v_accvgpr_read_b32 v33, a173
v_accvgpr_read_b32 v34, a174
v_accvgpr_read_b32 v35, a175
v_cvt_pk_bf16_f32 v108, v0, v1
v_cvt_pk_bf16_f32 v109, v2, v3
v_cvt_pk_bf16_f32 v110, v32, v33
v_cvt_pk_bf16_f32 v111, v34, v35
s_add_u32 m0, s52, 0x18000
s_nop 0
buffer_load_dwordx4 v8, s[36:39], s54 offen offset:0 lds
v_accvgpr_read_b32 v0, a144
v_accvgpr_read_b32 v1, a145
v_accvgpr_read_b32 v2, a146
v_accvgpr_read_b32 v3, a147
v_accvgpr_read_b32 v32, a176
v_accvgpr_read_b32 v33, a177
v_accvgpr_read_b32 v34, a178
v_accvgpr_read_b32 v35, a179
v_cvt_pk_bf16_f32 v112, v0, v1
v_cvt_pk_bf16_f32 v113, v2, v3
v_cvt_pk_bf16_f32 v114, v32, v33
v_cvt_pk_bf16_f32 v115, v34, v35
buffer_load_dwordx4 v9, s[36:39], s54 offen offset:1024 lds
buffer_load_dwordx4 v10, s[36:39], s54 offen offset:2048 lds
v_accvgpr_read_b32 v0, a148
v_accvgpr_read_b32 v1, a149
v_accvgpr_read_b32 v2, a150
v_accvgpr_read_b32 v3, a151
v_accvgpr_read_b32 v32, a180
v_accvgpr_read_b32 v33, a181
v_accvgpr_read_b32 v34, a182
v_accvgpr_read_b32 v35, a183
v_cvt_pk_bf16_f32 v116, v0, v1
v_cvt_pk_bf16_f32 v117, v2, v3
v_cvt_pk_bf16_f32 v118, v32, v33
v_cvt_pk_bf16_f32 v119, v34, v35
buffer_load_dwordx4 v11, s[36:39], s54 offen offset:3072 lds
s_add_u32 m0, s52, 0x19000
s_nop 0
v_accvgpr_read_b32 v0, a152
v_accvgpr_read_b32 v1, a153
v_accvgpr_read_b32 v2, a154
v_accvgpr_read_b32 v3, a155
v_accvgpr_read_b32 v32, a184
v_accvgpr_read_b32 v33, a185
v_accvgpr_read_b32 v34, a186
v_accvgpr_read_b32 v35, a187
v_cvt_pk_bf16_f32 v120, v0, v1
v_cvt_pk_bf16_f32 v121, v2, v3
v_cvt_pk_bf16_f32 v122, v32, v33
v_cvt_pk_bf16_f32 v123, v34, v35
buffer_load_dwordx4 v8, s[36:39], s55 offen offset:0 lds
buffer_load_dwordx4 v9, s[36:39], s55 offen offset:1024 lds
v_accvgpr_read_b32 v0, a156
v_accvgpr_read_b32 v1, a157
v_accvgpr_read_b32 v2, a158
v_accvgpr_read_b32 v3, a159
v_accvgpr_read_b32 v32, a188
v_accvgpr_read_b32 v33, a189
v_accvgpr_read_b32 v34, a190
v_accvgpr_read_b32 v35, a191
v_cvt_pk_bf16_f32 v124, v0, v1
v_cvt_pk_bf16_f32 v125, v2, v3
v_cvt_pk_bf16_f32 v126, v32, v33
v_cvt_pk_bf16_f32 v127, v34, v35
buffer_load_dwordx4 v10, s[36:39], s55 offen offset:2048 lds
buffer_load_dwordx4 v11, s[36:39], s55 offen offset:3072 lds
s_add_u32 s54, s54, 0x80
s_add_u32 s55, s55, 0x80
s_add_u32 s56, s56, 0x80000
s_add_u32 s68, s68, 0x80000
v_mbcnt_lo_u32_b32 v0, -1, 0
v_mbcnt_hi_u32_b32 v0, -1, v0
s_lshl_b32 s27, s23, 13
s_add_u32 s27, s27, s24
v_lshrrev_b32 v1, 4, v0
v_bfe_u32 v2, v0, 1, 3
v_xor_b32 v1, v1, v2
v_lshlrev_b32 v1, 4, v1
v_and_b32 v2, 15, v0
v_lshl_add_u32 v1, v2, 7, v1
v_add_u32 v16, s27, v1
v_xor_b32 v17, 64, v16
v_lshrrev_b32 v1, 4, v0
v_lshlrev_b32 v2, 11, v1
v_bfe_u32 v3, v0, 2, 2
v_lshl_add_u32 v2, v3, 6, v2
v_and_b32 v3, 1, v0
v_lshl_add_u32 v2, v3, 3, v2
v_and_b32 v1, 1, v1
v_bfe_u32 v3, v0, 1, 1
v_lshl_or_b32 v1, v1, 1, v3
v_lshl_add_u32 v2, v1, 4, v2
v_add_u32 v2, s24, v2
v_mov_b32 v18, v2
v_add_u32 v18, 0x8000, v18
v_xor_b32 v19, 16, v2
v_add_u32 v19, 0x8100, v19
v_xor_b32 v20, 32, v2
v_add_u32 v20, 0x8000, v20
v_xor_b32 v21, 48, v2
v_add_u32 v21, 0x8100, v21
s_waitcnt vmcnt(24)
s_barrier
ds_read_b128 a[32:35], v16 offset:0
ds_read_b128 a[36:39], v16 offset:2048
ds_read_b128 a[40:43], v16 offset:4096
ds_read_b128 a[44:47], v16 offset:6144
ds_read_b64_tr_b16 a[0:1], v18 offset:0
ds_read_b64_tr_b16 a[2:3], v19 offset:0
ds_read_b64_tr_b16 a[4:5], v20 offset:0
ds_read_b64_tr_b16 a[6:7], v21 offset:0
ds_read_b64_tr_b16 a[8:9], v18 offset:512
ds_read_b64_tr_b16 a[10:11], v19 offset:512
ds_read_b64_tr_b16 a[12:13], v20 offset:512
ds_read_b64_tr_b16 a[14:15], v21 offset:512
ds_read_b64_tr_b16 a[16:17], v18 offset:1024
ds_read_b64_tr_b16 a[18:19], v19 offset:1024
ds_read_b64_tr_b16 a[20:21], v20 offset:1024
ds_read_b64_tr_b16 a[22:23], v21 offset:1024
ds_read_b64_tr_b16 a[24:25], v18 offset:1536
ds_read_b64_tr_b16 a[26:27], v19 offset:1536
ds_read_b64_tr_b16 a[28:29], v20 offset:1536
ds_read_b64_tr_b16 a[30:31], v21 offset:1536
v_mov_b32 v22, 0
s_waitcnt lgkmcnt(14)
v_mfma_f32_16x16x32_bf16 a[64:67], a[0:3], a[32:35], 0
s_add_u32 s27, s58, 0
buffer_store_dwordx4 v[64:67], v4, s[48:51], s27 offen offset:0 sc1
v_mfma_f32_16x16x32_bf16 a[68:71], a[0:3], a[36:39], 0
ds_read_b128 a[48:51], v17 offset:0
v_mfma_f32_16x16x32_bf16 a[72:75], a[0:3], a[40:43], 0
ds_read_b128 a[52:55], v17 offset:2048
v_mfma_f32_16x16x32_bf16 a[76:79], a[0:3], a[44:47], 0
ds_read_b64_tr_b16 a[0:1], v18 offset:8192
s_waitcnt lgkmcnt(14)
v_mfma_f32_16x16x32_bf16 a[80:83], a[4:7], a[32:35], 0
ds_read_b64_tr_b16 a[2:3], v19 offset:8192
v_mfma_f32_16x16x32_bf16 a[84:87], a[4:7], a[36:39], 0
ds_read_b128 a[56:59], v17 offset:4096
v_mfma_f32_16x16x32_bf16 a[88:91], a[4:7], a[40:43], 0
ds_read_b128 a[60:63], v17 offset:6144
v_mfma_f32_16x16x32_bf16 a[92:95], a[4:7], a[44:47], 0
ds_read_b64_tr_b16 a[4:5], v20 offset:8192
s_waitcnt lgkmcnt(14)
v_mfma_f32_16x16x32_bf16 a[96:99], a[8:11], a[32:35], 0
ds_read_b64_tr_b16 a[6:7], v21 offset:8192
v_mfma_f32_16x16x32_bf16 v[24:27], v[64:67], v[64:67], 0
v_mfma_f32_16x16x32_bf16 a[100:103], a[8:11], a[36:39], 0
s_add_u32 s27, s58, 0
buffer_store_dwordx4 v[128:131], v4, s[48:51], s27 offen offset:1024 sc1
v_mfma_f32_16x16x32_bf16 a[104:107], a[8:11], a[40:43], 0
v_mfma_f32_16x16x32_bf16 a[108:111], a[8:11], a[44:47], 0
ds_read_b64_tr_b16 a[8:9], v18 offset:8704
v_mfma_f32_16x16x32_bf16 a[112:115], a[12:15], a[32:35], 0
ds_read_b64_tr_b16 a[10:11], v19 offset:8704
v_mfma_f32_16x16x32_bf16 a[116:119], a[12:15], a[36:39], 0
v_mfma_f32_16x16x32_bf16 a[120:123], a[12:15], a[40:43], 0
v_mfma_f32_16x16x32_bf16 a[124:127], a[12:15], a[44:47], 0
ds_read_b64_tr_b16 a[12:13], v20 offset:8704
s_waitcnt lgkmcnt(4)
v_mfma_f32_16x16x32_bf16 a[128:131], a[16:19], a[32:35], 0
ds_read_b64_tr_b16 a[14:15], v21 offset:8704
v_mfma_f32_16x16x32_bf16 a[132:135], a[16:19], a[36:39], 0
s_add_u32 s27, s58, 0
buffer_store_dwordx4 v[192:195], v4, s[48:51], s27 offen offset:2048 sc1
v_mfma_f32_16x16x32_bf16 a[136:139], a[16:19], a[40:43], 0
v_mfma_f32_16x16x32_bf16 a[140:143], a[16:19], a[44:47], 0
ds_read_b64_tr_b16 a[16:17], v18 offset:9216
v_mfma_f32_16x16x32_bf16 a[144:147], a[20:23], a[32:35], 0
ds_read_b64_tr_b16 a[18:19], v19 offset:9216
v_mfma_f32_16x16x32_bf16 v[24:27], v[128:131], v[128:131], v[24:27]
v_mfma_f32_16x16x32_bf16 a[148:151], a[20:23], a[36:39], 0
v_mfma_f32_16x16x32_bf16 a[152:155], a[20:23], a[40:43], 0
v_mfma_f32_16x16x32_bf16 a[156:159], a[20:23], a[44:47], 0
ds_read_b64_tr_b16 a[20:21], v20 offset:9216
v_mfma_f32_16x16x32_bf16 a[160:163], a[24:27], a[32:35], 0
ds_read_b64_tr_b16 a[22:23], v21 offset:9216
v_mfma_f32_16x16x32_bf16 a[164:167], a[24:27], a[36:39], 0
s_add_u32 s27, s58, 0
buffer_store_dwordx4 v[96:99], v4, s[48:51], s27 offen offset:3072 sc1
v_mfma_f32_16x16x32_bf16 a[168:171], a[24:27], a[40:43], 0
v_mfma_f32_16x16x32_bf16 a[172:175], a[24:27], a[44:47], 0
ds_read_b64_tr_b16 a[24:25], v18 offset:9728
v_mfma_f32_16x16x32_bf16 a[176:179], a[28:31], a[32:35], 0
ds_read_b64_tr_b16 a[26:27], v19 offset:9728
v_mfma_f32_16x16x32_bf16 a[180:183], a[28:31], a[36:39], 0
v_mfma_f32_16x16x32_bf16 a[184:187], a[28:31], a[40:43], 0
v_mfma_f32_16x16x32_bf16 a[188:191], a[28:31], a[44:47], 0
ds_read_b64_tr_b16 a[28:29], v20 offset:9728
ds_read_b64_tr_b16 a[30:31], v21 offset:9728
v_mfma_f32_16x16x32_bf16 a[64:67], a[0:3], a[48:51], a[64:67]
v_mfma_f32_16x16x32_bf16 a[68:71], a[0:3], a[52:55], a[68:71]
v_mfma_f32_16x16x32_bf16 a[72:75], a[0:3], a[56:59], a[72:75]
v_mfma_f32_16x16x32_bf16 a[76:79], a[0:3], a[60:63], a[76:79]
s_waitcnt lgkmcnt(4)
v_mfma_f32_16x16x32_bf16 a[80:83], a[4:7], a[48:51], a[80:83]
s_waitcnt vmcnt(16) lgkmcnt(0)
s_barrier
v_add_u32 v16, 0xc000, v16
v_add_u32 v17, 0xc000, v17
v_add_u32 v18, 0xc000, v18
v_add_u32 v19, 0xc000, v19
v_add_u32 v20, 0xc000, v20
v_add_u32 v21, 0xc000, v21
v_mfma_f32_16x16x32_bf16 a[84:87], a[4:7], a[52:55], a[84:87]
ds_read_b64_tr_b16 a[0:1], v18 offset:0
v_mfma_f32_16x16x32_bf16 a[88:91], a[4:7], a[56:59], a[88:91]
ds_read_b64_tr_b16 a[2:3], v19 offset:0
v_mfma_f32_16x16x32_bf16 a[92:95], a[4:7], a[60:63], a[92:95]
ds_read_b64_tr_b16 a[4:5], v20 offset:0
s_add_u32 m0, s53, 0
v_mfma_f32_16x16x32_bf16 a[96:99], a[8:11], a[48:51], a[96:99]
ds_read_b64_tr_b16 a[6:7], v21 offset:0
buffer_load_dwordx4 v12, s[40:43], s56 offen offset:0 lds
v_mfma_f32_16x16x32_bf16 v[24:27], v[192:195], v[192:195], v[24:27]
v_mfma_f32_16x16x32_bf16 a[100:103], a[8:11], a[52:55], a[100:103]
ds_read_b128 a[32:35], v16 offset:0
v_mfma_f32_16x16x32_bf16 a[104:107], a[8:11], a[56:59], a[104:107]
ds_read_b128 a[36:39], v16 offset:2048
buffer_load_dwordx4 v13, s[40:43], s56 offen offset:1024 lds
v_mfma_f32_16x16x32_bf16 a[108:111], a[8:11], a[60:63], a[108:111]
ds_read_b64_tr_b16 a[8:9], v18 offset:512
s_add_u32 m0, s53, 0x2000
v_mfma_f32_16x16x32_bf16 a[112:115], a[12:15], a[48:51], a[112:115]
ds_read_b64_tr_b16 a[10:11], v19 offset:512
v_mfma_f32_16x16x32_bf16 a[116:119], a[12:15], a[52:55], a[116:119]
ds_read_b128 a[40:43], v16 offset:4096
buffer_load_dwordx4 v12, s[40:43], s68 offen offset:0 lds
v_mfma_f32_16x16x32_bf16 a[120:123], a[12:15], a[56:59], a[120:123]
ds_read_b128 a[44:47], v16 offset:6144
buffer_load_dwordx4 v13, s[40:43], s68 offen offset:1024 lds
v_mfma_f32_16x16x32_bf16 a[124:127], a[12:15], a[60:63], a[124:127]
ds_read_b64_tr_b16 a[12:13], v20 offset:512
v_mfma_f32_16x16x32_bf16 a[128:131], a[16:19], a[48:51], a[128:131]
ds_read_b64_tr_b16 a[14:15], v21 offset:512
s_add_u32 m0, s52, 0
v_mfma_f32_16x16x32_bf16 a[132:135], a[16:19], a[52:55], a[132:135]
buffer_load_dwordx4 v8, s[36:39], s54 offen offset:0 lds
v_mfma_f32_16x16x32_bf16 a[136:139], a[16:19], a[56:59], a[136:139]
buffer_load_dwordx4 v9, s[36:39], s54 offen offset:1024 lds
v_mfma_f32_16x16x32_bf16 a[140:143], a[16:19], a[60:63], a[140:143]
ds_read_b64_tr_b16 a[16:17], v18 offset:1024
v_mfma_f32_16x16x32_bf16 a[144:147], a[20:23], a[48:51], a[144:147]
ds_read_b64_tr_b16 a[18:19], v19 offset:1024
buffer_load_dwordx4 v10, s[36:39], s54 offen offset:2048 lds
v_mfma_f32_16x16x32_bf16 v[24:27], v[96:99], v[96:99], v[24:27]
v_mfma_f32_16x16x32_bf16 a[148:151], a[20:23], a[52:55], a[148:151]
buffer_load_dwordx4 v11, s[36:39], s54 offen offset:3072 lds
v_mfma_f32_16x16x32_bf16 a[152:155], a[20:23], a[56:59], a[152:155]
s_add_u32 m0, s52, 0x1000
v_mfma_f32_16x16x32_bf16 a[156:159], a[20:23], a[60:63], a[156:159]
ds_read_b64_tr_b16 a[20:21], v20 offset:1024
v_mfma_f32_16x16x32_bf16 a[160:163], a[24:27], a[48:51], a[160:163]
ds_read_b64_tr_b16 a[22:23], v21 offset:1024
buffer_load_dwordx4 v8, s[36:39], s55 offen offset:0 lds
v_mfma_f32_16x16x32_bf16 a[164:167], a[24:27], a[52:55], a[164:167]
buffer_load_dwordx4 v9, s[36:39], s55 offen offset:1024 lds
v_mfma_f32_16x16x32_bf16 a[168:171], a[24:27], a[56:59], a[168:171]
buffer_load_dwordx4 v10, s[36:39], s55 offen offset:2048 lds
v_mfma_f32_16x16x32_bf16 a[172:175], a[24:27], a[60:63], a[172:175]
ds_read_b64_tr_b16 a[24:25], v18 offset:1536
v_mfma_f32_16x16x32_bf16 a[176:179], a[28:31], a[48:51], a[176:179]
ds_read_b64_tr_b16 a[26:27], v19 offset:1536
v_mfma_f32_16x16x32_bf16 a[180:183], a[28:31], a[52:55], a[180:183]
buffer_load_dwordx4 v11, s[36:39], s55 offen offset:3072 lds
v_mfma_f32_16x16x32_bf16 a[184:187], a[28:31], a[56:59], a[184:187]
s_add_u32 s54, s54, 0x80
s_add_u32 s55, s55, 0x80
s_add_u32 s56, s56, 0x80000
s_add_u32 s68, s68, 0x80000
v_mfma_f32_16x16x32_bf16 a[188:191], a[28:31], a[60:63], a[188:191]
ds_read_b64_tr_b16 a[28:29], v20 offset:1536
ds_read_b64_tr_b16 a[30:31], v21 offset:1536
s_waitcnt lgkmcnt(6)
v_mfma_f32_16x16x32_bf16 a[64:67], a[0:3], a[32:35], a[64:67]
s_add_u32 s27, s58, 0x1000
buffer_store_dwordx4 v[68:71], v4, s[48:51], s27 offen offset:0 sc1
v_mfma_f32_16x16x32_bf16 a[68:71], a[0:3], a[36:39], a[68:71]
ds_read_b128 a[48:51], v17 offset:0
v_mfma_f32_16x16x32_bf16 a[72:75], a[0:3], a[40:43], a[72:75]
ds_read_b128 a[52:55], v17 offset:2048
v_mfma_f32_16x16x32_bf16 a[76:79], a[0:3], a[44:47], a[76:79]
ds_read_b64_tr_b16 a[0:1], v18 offset:8192
v_mfma_f32_16x16x32_bf16 a[80:83], a[4:7], a[32:35], a[80:83]
ds_read_b64_tr_b16 a[2:3], v19 offset:8192
v_mfma_f32_16x16x32_bf16 a[84:87], a[4:7], a[36:39], a[84:87]
ds_read_b128 a[56:59], v17 offset:4096
v_mfma_f32_16x16x32_bf16 a[88:91], a[4:7], a[40:43], a[88:91]
ds_read_b128 a[60:63], v17 offset:6144
v_mfma_f32_16x16x32_bf16 a[92:95], a[4:7], a[44:47], a[92:95]
ds_read_b64_tr_b16 a[4:5], v20 offset:8192
v_mfma_f32_16x16x32_bf16 a[96:99], a[8:11], a[32:35], a[96:99]
ds_read_b64_tr_b16 a[6:7], v21 offset:8192
v_mfma_f32_16x16x32_bf16 v[28:31], v[68:71], v[68:71], 0
v_mfma_f32_16x16x32_bf16 a[100:103], a[8:11], a[36:39], a[100:103]
s_add_u32 s27, s58, 0x1000
buffer_store_dwordx4 v[132:135], v4, s[48:51], s27 offen offset:1024 sc1
v_mfma_f32_16x16x32_bf16 a[104:107], a[8:11], a[40:43], a[104:107]
v_mfma_f32_16x16x32_bf16 a[108:111], a[8:11], a[44:47], a[108:111]
ds_read_b64_tr_b16 a[8:9], v18 offset:8704
v_mfma_f32_16x16x32_bf16 a[112:115], a[12:15], a[32:35], a[112:115]
ds_read_b64_tr_b16 a[10:11], v19 offset:8704
v_max3_f32 v22, v22, v24, v25
v_max3_f32 v22, v22, v26, v27
v_mfma_f32_16x16x32_bf16 a[116:119], a[12:15], a[36:39], a[116:119]
v_mfma_f32_16x16x32_bf16 a[120:123], a[12:15], a[40:43], a[120:123]
v_mfma_f32_16x16x32_bf16 a[124:127], a[12:15], a[44:47], a[124:127]
ds_read_b64_tr_b16 a[12:13], v20 offset:8704
v_mfma_f32_16x16x32_bf16 a[128:131], a[16:19], a[32:35], a[128:131]
ds_read_b64_tr_b16 a[14:15], v21 offset:8704
v_mfma_f32_16x16x32_bf16 a[132:135], a[16:19], a[36:39], a[132:135]
s_add_u32 s27, s58, 0x1000
buffer_store_dwordx4 v[196:199], v4, s[48:51], s27 offen offset:2048 sc1
v_mfma_f32_16x16x32_bf16 a[136:139], a[16:19], a[40:43], a[136:139]
v_mfma_f32_16x16x32_bf16 a[140:143], a[16:19], a[44:47], a[140:143]
ds_read_b64_tr_b16 a[16:17], v18 offset:9216
s_waitcnt lgkmcnt(5)
v_mfma_f32_16x16x32_bf16 a[144:147], a[20:23], a[32:35], a[144:147]
ds_read_b64_tr_b16 a[18:19], v19 offset:9216
v_mfma_f32_16x16x32_bf16 v[28:31], v[132:135], v[132:135], v[28:31]
v_mfma_f32_16x16x32_bf16 a[148:151], a[20:23], a[36:39], a[148:151]
v_mfma_f32_16x16x32_bf16 a[152:155], a[20:23], a[40:43], a[152:155]
v_mfma_f32_16x16x32_bf16 a[156:159], a[20:23], a[44:47], a[156:159]
ds_read_b64_tr_b16 a[20:21], v20 offset:9216
v_mfma_f32_16x16x32_bf16 a[160:163], a[24:27], a[32:35], a[160:163]
ds_read_b64_tr_b16 a[22:23], v21 offset:9216
v_mfma_f32_16x16x32_bf16 a[164:167], a[24:27], a[36:39], a[164:167]
s_add_u32 s27, s58, 0x1000
buffer_store_dwordx4 v[100:103], v4, s[48:51], s27 offen offset:3072 sc1
v_mfma_f32_16x16x32_bf16 a[168:171], a[24:27], a[40:43], a[168:171]
v_mfma_f32_16x16x32_bf16 a[172:175], a[24:27], a[44:47], a[172:175]
ds_read_b64_tr_b16 a[24:25], v18 offset:9728
v_mfma_f32_16x16x32_bf16 a[176:179], a[28:31], a[32:35], a[176:179]
ds_read_b64_tr_b16 a[26:27], v19 offset:9728
v_mfma_f32_16x16x32_bf16 a[180:183], a[28:31], a[36:39], a[180:183]
v_mfma_f32_16x16x32_bf16 a[184:187], a[28:31], a[40:43], a[184:187]
v_mfma_f32_16x16x32_bf16 a[188:191], a[28:31], a[44:47], a[188:191]
ds_read_b64_tr_b16 a[28:29], v20 offset:9728
ds_read_b64_tr_b16 a[30:31], v21 offset:9728
v_mfma_f32_16x16x32_bf16 a[64:67], a[0:3], a[48:51], a[64:67]
v_mfma_f32_16x16x32_bf16 a[68:71], a[0:3], a[52:55], a[68:71]
v_mfma_f32_16x16x32_bf16 a[72:75], a[0:3], a[56:59], a[72:75]
v_mfma_f32_16x16x32_bf16 a[76:79], a[0:3], a[60:63], a[76:79]
v_mfma_f32_16x16x32_bf16 a[80:83], a[4:7], a[48:51], a[80:83]
s_waitcnt vmcnt(20) lgkmcnt(0)
s_barrier
v_add_u32 v16, 0xc000, v16
v_add_u32 v17, 0xc000, v17
v_add_u32 v18, 0xc000, v18
v_add_u32 v19, 0xc000, v19
v_add_u32 v20, 0xc000, v20
v_add_u32 v21, 0xc000, v21
v_mfma_f32_16x16x32_bf16 a[84:87], a[4:7], a[52:55], a[84:87]
ds_read_b64_tr_b16 a[0:1], v18 offset:0
v_mfma_f32_16x16x32_bf16 a[88:91], a[4:7], a[56:59], a[88:91]
ds_read_b64_tr_b16 a[2:3], v19 offset:0
v_mfma_f32_16x16x32_bf16 a[92:95], a[4:7], a[60:63], a[92:95]
ds_read_b64_tr_b16 a[4:5], v20 offset:0
s_add_u32 m0, s53, 0xc000
v_mfma_f32_16x16x32_bf16 a[96:99], a[8:11], a[48:51], a[96:99]
ds_read_b64_tr_b16 a[6:7], v21 offset:0
buffer_load_dwordx4 v12, s[40:43], s56 offen offset:0 lds
v_mfma_f32_16x16x32_bf16 v[28:31], v[196:199], v[196:199], v[28:31]
v_mfma_f32_16x16x32_bf16 a[100:103], a[8:11], a[52:55], a[100:103]
ds_read_b128 a[32:35], v16 offset:0
v_mfma_f32_16x16x32_bf16 a[104:107], a[8:11], a[56:59], a[104:107]
ds_read_b128 a[36:39], v16 offset:2048
buffer_load_dwordx4 v13, s[40:43], s56 offen offset:1024 lds
v_mfma_f32_16x16x32_bf16 a[108:111], a[8:11], a[60:63], a[108:111]
ds_read_b64_tr_b16 a[8:9], v18 offset:512
s_add_u32 m0, s53, 0xe000
v_mfma_f32_16x16x32_bf16 a[112:115], a[12:15], a[48:51], a[112:115]
ds_read_b64_tr_b16 a[10:11], v19 offset:512
v_mfma_f32_16x16x32_bf16 a[116:119], a[12:15], a[52:55], a[116:119]
ds_read_b128 a[40:43], v16 offset:4096
buffer_load_dwordx4 v12, s[40:43], s68 offen offset:0 lds
v_mfma_f32_16x16x32_bf16 a[120:123], a[12:15], a[56:59], a[120:123]
ds_read_b128 a[44:47], v16 offset:6144
buffer_load_dwordx4 v13, s[40:43], s68 offen offset:1024 lds
v_mfma_f32_16x16x32_bf16 a[124:127], a[12:15], a[60:63], a[124:127]
ds_read_b64_tr_b16 a[12:13], v20 offset:512
v_mfma_f32_16x16x32_bf16 a[128:131], a[16:19], a[48:51], a[128:131]
ds_read_b64_tr_b16 a[14:15], v21 offset:512
s_add_u32 m0, s52, 0xc000
v_mfma_f32_16x16x32_bf16 a[132:135], a[16:19], a[52:55], a[132:135]
buffer_load_dwordx4 v8, s[36:39], s54 offen offset:0 lds
v_mfma_f32_16x16x32_bf16 a[136:139], a[16:19], a[56:59], a[136:139]
buffer_load_dwordx4 v9, s[36:39], s54 offen offset:1024 lds
v_mfma_f32_16x16x32_bf16 a[140:143], a[16:19], a[60:63], a[140:143]
ds_read_b64_tr_b16 a[16:17], v18 offset:1024
v_mfma_f32_16x16x32_bf16 a[144:147], a[20:23], a[48:51], a[144:147]
ds_read_b64_tr_b16 a[18:19], v19 offset:1024
buffer_load_dwordx4 v10, s[36:39], s54 offen offset:2048 lds
v_mfma_f32_16x16x32_bf16 v[28:31], v[100:103], v[100:103], v[28:31]
v_mfma_f32_16x16x32_bf16 a[148:151], a[20:23], a[52:55], a[148:151]
buffer_load_dwordx4 v11, s[36:39], s54 offen offset:3072 lds
v_mfma_f32_16x16x32_bf16 a[152:155], a[20:23], a[56:59], a[152:155]
s_add_u32 m0, s52, 0xd000
v_mfma_f32_16x16x32_bf16 a[156:159], a[20:23], a[60:63], a[156:159]
ds_read_b64_tr_b16 a[20:21], v20 offset:1024
v_mfma_f32_16x16x32_bf16 a[160:163], a[24:27], a[48:51], a[160:163]
ds_read_b64_tr_b16 a[22:23], v21 offset:1024
buffer_load_dwordx4 v8, s[36:39], s55 offen offset:0 lds
v_mfma_f32_16x16x32_bf16 a[164:167], a[24:27], a[52:55], a[164:167]
buffer_load_dwordx4 v9, s[36:39], s55 offen offset:1024 lds
v_mfma_f32_16x16x32_bf16 a[168:171], a[24:27], a[56:59], a[168:171]
buffer_load_dwordx4 v10, s[36:39], s55 offen offset:2048 lds
v_mfma_f32_16x16x32_bf16 a[172:175], a[24:27], a[60:63], a[172:175]
ds_read_b64_tr_b16 a[24:25], v18 offset:1536
v_mfma_f32_16x16x32_bf16 a[176:179], a[28:31], a[48:51], a[176:179]
ds_read_b64_tr_b16 a[26:27], v19 offset:1536
v_mfma_f32_16x16x32_bf16 a[180:183], a[28:31], a[52:55], a[180:183]
buffer_load_dwordx4 v11, s[36:39], s55 offen offset:3072 lds
v_mfma_f32_16x16x32_bf16 a[184:187], a[28:31], a[56:59], a[184:187]
s_add_u32 s54, s54, 0x80
s_add_u32 s55, s55, 0x80
s_add_u32 s56, s56, 0x80000
s_add_u32 s68, s68, 0x80000
v_mfma_f32_16x16x32_bf16 a[188:191], a[28:31], a[60:63], a[188:191]
ds_read_b64_tr_b16 a[28:29], v20 offset:1536
ds_read_b64_tr_b16 a[30:31], v21 offset:1536
s_waitcnt lgkmcnt(6)
v_mfma_f32_16x16x32_bf16 a[64:67], a[0:3], a[32:35], a[64:67]
s_add_u32 s27, s58, 0x2000
buffer_store_dwordx4 v[72:75], v4, s[48:51], s27 offen offset:0 sc1
v_mfma_f32_16x16x32_bf16 a[68:71], a[0:3], a[36:39], a[68:71]
ds_read_b128 a[48:51], v17 offset:0
v_mfma_f32_16x16x32_bf16 a[72:75], a[0:3], a[40:43], a[72:75]
ds_read_b128 a[52:55], v17 offset:2048
v_mfma_f32_16x16x32_bf16 a[76:79], a[0:3], a[44:47], a[76:79]
ds_read_b64_tr_b16 a[0:1], v18 offset:8192
v_mfma_f32_16x16x32_bf16 a[80:83], a[4:7], a[32:35], a[80:83]
ds_read_b64_tr_b16 a[2:3], v19 offset:8192
v_mfma_f32_16x16x32_bf16 a[84:87], a[4:7], a[36:39], a[84:87]
ds_read_b128 a[56:59], v17 offset:4096
v_mfma_f32_16x16x32_bf16 a[88:91], a[4:7], a[40:43], a[88:91]
ds_read_b128 a[60:63], v17 offset:6144
v_mfma_f32_16x16x32_bf16 a[92:95], a[4:7], a[44:47], a[92:95]
ds_read_b64_tr_b16 a[4:5], v20 offset:8192
v_mfma_f32_16x16x32_bf16 a[96:99], a[8:11], a[32:35], a[96:99]
ds_read_b64_tr_b16 a[6:7], v21 offset:8192
v_mfma_f32_16x16x32_bf16 v[24:27], v[72:75], v[72:75], 0
v_mfma_f32_16x16x32_bf16 a[100:103], a[8:11], a[36:39], a[100:103]
s_add_u32 s27, s58, 0x2000
buffer_store_dwordx4 v[136:139], v4, s[48:51], s27 offen offset:1024 sc1
v_mfma_f32_16x16x32_bf16 a[104:107], a[8:11], a[40:43], a[104:107]
v_mfma_f32_16x16x32_bf16 a[108:111], a[8:11], a[44:47], a[108:111]
ds_read_b64_tr_b16 a[8:9], v18 offset:8704
v_mfma_f32_16x16x32_bf16 a[112:115], a[12:15], a[32:35], a[112:115]
ds_read_b64_tr_b16 a[10:11], v19 offset:8704
v_max3_f32 v22, v22, v28, v29
v_max3_f32 v22, v22, v30, v31
v_mfma_f32_16x16x32_bf16 a[116:119], a[12:15], a[36:39], a[116:119]
v_mfma_f32_16x16x32_bf16 a[120:123], a[12:15], a[40:43], a[120:123]
v_mfma_f32_16x16x32_bf16 a[124:127], a[12:15], a[44:47], a[124:127]
ds_read_b64_tr_b16 a[12:13], v20 offset:8704
v_mfma_f32_16x16x32_bf16 a[128:131], a[16:19], a[32:35], a[128:131]
ds_read_b64_tr_b16 a[14:15], v21 offset:8704
v_mfma_f32_16x16x32_bf16 a[132:135], a[16:19], a[36:39], a[132:135]
s_add_u32 s27, s58, 0x2000
buffer_store_dwordx4 v[200:203], v4, s[48:51], s27 offen offset:2048 sc1
v_mfma_f32_16x16x32_bf16 a[136:139], a[16:19], a[40:43], a[136:139]
v_mfma_f32_16x16x32_bf16 a[140:143], a[16:19], a[44:47], a[140:143]
ds_read_b64_tr_b16 a[16:17], v18 offset:9216
s_waitcnt lgkmcnt(5)
v_mfma_f32_16x16x32_bf16 a[144:147], a[20:23], a[32:35], a[144:147]
ds_read_b64_tr_b16 a[18:19], v19 offset:9216
v_mfma_f32_16x16x32_bf16 v[24:27], v[136:139], v[136:139], v[24:27]
v_mfma_f32_16x16x32_bf16 a[148:151], a[20:23], a[36:39], a[148:151]
v_mfma_f32_16x16x32_bf16 a[152:155], a[20:23], a[40:43], a[152:155]
v_mfma_f32_16x16x32_bf16 a[156:159], a[20:23], a[44:47], a[156:159]
ds_read_b64_tr_b16 a[20:21], v20 offset:9216
v_mfma_f32_16x16x32_bf16 a[160:163], a[24:27], a[32:35], a[160:163]
ds_read_b64_tr_b16 a[22:23], v21 offset:9216
v_mfma_f32_16x16x32_bf16 a[164:167], a[24:27], a[36:39], a[164:167]
s_add_u32 s27, s58, 0x2000
buffer_store_dwordx4 v[104:107], v4, s[48:51], s27 offen offset:3072 sc1
v_mfma_f32_16x16x32_bf16 a[168:171], a[24:27], a[40:43], a[168:171]
v_mfma_f32_16x16x32_bf16 a[172:175], a[24:27], a[44:47], a[172:175]
ds_read_b64_tr_b16 a[24:25], v18 offset:9728
v_mfma_f32_16x16x32_bf16 a[176:179], a[28:31], a[32:35], a[176:179]
ds_read_b64_tr_b16 a[26:27], v19 offset:9728
v_mfma_f32_16x16x32_bf16 a[180:183], a[28:31], a[36:39], a[180:183]
v_mfma_f32_16x16x32_bf16 a[184:187], a[28:31], a[40:43], a[184:187]
v_mfma_f32_16x16x32_bf16 a[188:191], a[28:31], a[44:47], a[188:191]
ds_read_b64_tr_b16 a[28:29], v20 offset:9728
ds_read_b64_tr_b16 a[30:31], v21 offset:9728
v_mfma_f32_16x16x32_bf16 a[64:67], a[0:3], a[48:51], a[64:67]
v_mfma_f32_16x16x32_bf16 a[68:71], a[0:3], a[52:55], a[68:71]
v_mfma_f32_16x16x32_bf16 a[72:75], a[0:3], a[56:59], a[72:75]
v_mfma_f32_16x16x32_bf16 a[76:79], a[0:3], a[60:63], a[76:79]
v_mfma_f32_16x16x32_bf16 a[80:83], a[4:7], a[48:51], a[80:83]
s_waitcnt vmcnt(20) lgkmcnt(0)
s_barrier
v_add_u32 v16, 0xfffe8000, v16
v_add_u32 v17, 0xfffe8000, v17
v_add_u32 v18, 0xfffe8000, v18
v_add_u32 v19, 0xfffe8000, v19
v_add_u32 v20, 0xfffe8000, v20
v_add_u32 v21, 0xfffe8000, v21
v_mfma_f32_16x16x32_bf16 a[84:87], a[4:7], a[52:55], a[84:87]
ds_read_b64_tr_b16 a[0:1], v18 offset:0
v_mfma_f32_16x16x32_bf16 a[88:91], a[4:7], a[56:59], a[88:91]
ds_read_b64_tr_b16 a[2:3], v19 offset:0
v_mfma_f32_16x16x32_bf16 a[92:95], a[4:7], a[60:63], a[92:95]
ds_read_b64_tr_b16 a[4:5], v20 offset:0
s_add_u32 m0, s53, 0x18000
v_mfma_f32_16x16x32_bf16 a[96:99], a[8:11], a[48:51], a[96:99]
ds_read_b64_tr_b16 a[6:7], v21 offset:0
buffer_load_dwordx4 v12, s[40:43], s56 offen offset:0 lds
v_mfma_f32_16x16x32_bf16 v[24:27], v[200:203], v[200:203], v[24:27]
v_mfma_f32_16x16x32_bf16 a[100:103], a[8:11], a[52:55], a[100:103]
ds_read_b128 a[32:35], v16 offset:0
v_mfma_f32_16x16x32_bf16 a[104:107], a[8:11], a[56:59], a[104:107]
ds_read_b128 a[36:39], v16 offset:2048
buffer_load_dwordx4 v13, s[40:43], s56 offen offset:1024 lds
v_mfma_f32_16x16x32_bf16 a[108:111], a[8:11], a[60:63], a[108:111]
ds_read_b64_tr_b16 a[8:9], v18 offset:512
s_add_u32 m0, s53, 0x1a000
v_mfma_f32_16x16x32_bf16 a[112:115], a[12:15], a[48:51], a[112:115]
ds_read_b64_tr_b16 a[10:11], v19 offset:512
v_mfma_f32_16x16x32_bf16 a[116:119], a[12:15], a[52:55], a[116:119]
ds_read_b128 a[40:43], v16 offset:4096
buffer_load_dwordx4 v12, s[40:43], s68 offen offset:0 lds
v_mfma_f32_16x16x32_bf16 a[120:123], a[12:15], a[56:59], a[120:123]
ds_read_b128 a[44:47], v16 offset:6144
buffer_load_dwordx4 v13, s[40:43], s68 offen offset:1024 lds
v_mfma_f32_16x16x32_bf16 a[124:127], a[12:15], a[60:63], a[124:127]
ds_read_b64_tr_b16 a[12:13], v20 offset:512
v_mfma_f32_16x16x32_bf16 a[128:131], a[16:19], a[48:51], a[128:131]
ds_read_b64_tr_b16 a[14:15], v21 offset:512
s_add_u32 m0, s52, 0x18000
v_mfma_f32_16x16x32_bf16 a[132:135], a[16:19], a[52:55], a[132:135]
buffer_load_dwordx4 v8, s[36:39], s54 offen offset:0 lds
v_mfma_f32_16x16x32_bf16 a[136:139], a[16:19], a[56:59], a[136:139]
buffer_load_dwordx4 v9, s[36:39], s54 offen offset:1024 lds
v_mfma_f32_16x16x32_bf16 a[140:143], a[16:19], a[60:63], a[140:143]
ds_read_b64_tr_b16 a[16:17], v18 offset:1024
v_mfma_f32_16x16x32_bf16 a[144:147], a[20:23], a[48:51], a[144:147]
ds_read_b64_tr_b16 a[18:19], v19 offset:1024
buffer_load_dwordx4 v10, s[36:39], s54 offen offset:2048 lds
v_mfma_f32_16x16x32_bf16 v[24:27], v[104:107], v[104:107], v[24:27]
v_mfma_f32_16x16x32_bf16 a[148:151], a[20:23], a[52:55], a[148:151]
buffer_load_dwordx4 v11, s[36:39], s54 offen offset:3072 lds
v_mfma_f32_16x16x32_bf16 a[152:155], a[20:23], a[56:59], a[152:155]
s_add_u32 m0, s52, 0x19000
v_mfma_f32_16x16x32_bf16 a[156:159], a[20:23], a[60:63], a[156:159]
ds_read_b64_tr_b16 a[20:21], v20 offset:1024
v_mfma_f32_16x16x32_bf16 a[160:163], a[24:27], a[48:51], a[160:163]
ds_read_b64_tr_b16 a[22:23], v21 offset:1024
buffer_load_dwordx4 v8, s[36:39], s55 offen offset:0 lds
v_mfma_f32_16x16x32_bf16 a[164:167], a[24:27], a[52:55], a[164:167]
buffer_load_dwordx4 v9, s[36:39], s55 offen offset:1024 lds
v_mfma_f32_16x16x32_bf16 a[168:171], a[24:27], a[56:59], a[168:171]
buffer_load_dwordx4 v10, s[36:39], s55 offen offset:2048 lds
v_mfma_f32_16x16x32_bf16 a[172:175], a[24:27], a[60:63], a[172:175]
ds_read_b64_tr_b16 a[24:25], v18 offset:1536
v_mfma_f32_16x16x32_bf16 a[176:179], a[28:31], a[48:51], a[176:179]
ds_read_b64_tr_b16 a[26:27], v19 offset:1536
v_mfma_f32_16x16x32_bf16 a[180:183], a[28:31], a[52:55], a[180:183]
buffer_load_dwordx4 v11, s[36:39], s55 offen offset:3072 lds
v_mfma_f32_16x16x32_bf16 a[184:187], a[28:31], a[56:59], a[184:187]
s_add_u32 s54, s54, 0x80
s_add_u32 s55, s55, 0x80
s_add_u32 s56, s56, 0x80000
s_add_u32 s68, s68, 0x80000
v_mfma_f32_16x16x32_bf16 a[188:191], a[28:31], a[60:63], a[188:191]
ds_read_b64_tr_b16 a[28:29], v20 offset:1536
ds_read_b64_tr_b16 a[30:31], v21 offset:1536
s_waitcnt lgkmcnt(6)
v_mfma_f32_16x16x32_bf16 a[64:67], a[0:3], a[32:35], a[64:67]
s_add_u32 s27, s58, 0x3000
buffer_store_dwordx4 v[76:79], v4, s[48:51], s27 offen offset:0 sc1
v_mfma_f32_16x16x32_bf16 a[68:71], a[0:3], a[36:39], a[68:71]
ds_read_b128 a[48:51], v17 offset:0
v_mfma_f32_16x16x32_bf16 a[72:75], a[0:3], a[40:43], a[72:75]
ds_read_b128 a[52:55], v17 offset:2048
v_mfma_f32_16x16x32_bf16 a[76:79], a[0:3], a[44:47], a[76:79]
ds_read_b64_tr_b16 a[0:1], v18 offset:8192
v_mfma_f32_16x16x32_bf16 a[80:83], a[4:7], a[32:35], a[80:83]
ds_read_b64_tr_b16 a[2:3], v19 offset:8192
v_mfma_f32_16x16x32_bf16 a[84:87], a[4:7], a[36:39], a[84:87]
ds_read_b128 a[56:59], v17 offset:4096
v_mfma_f32_16x16x32_bf16 a[88:91], a[4:7], a[40:43], a[88:91]
ds_read_b128 a[60:63], v17 offset:6144
v_mfma_f32_16x16x32_bf16 a[92:95], a[4:7], a[44:47], a[92:95]
ds_read_b64_tr_b16 a[4:5], v20 offset:8192
v_mfma_f32_16x16x32_bf16 a[96:99], a[8:11], a[32:35], a[96:99]
ds_read_b64_tr_b16 a[6:7], v21 offset:8192
v_mfma_f32_16x16x32_bf16 v[28:31], v[76:79], v[76:79], 0
v_mfma_f32_16x16x32_bf16 a[100:103], a[8:11], a[36:39], a[100:103]
s_add_u32 s27, s58, 0x3000
buffer_store_dwordx4 v[140:143], v4, s[48:51], s27 offen offset:1024 sc1
v_mfma_f32_16x16x32_bf16 a[104:107], a[8:11], a[40:43], a[104:107]
v_mfma_f32_16x16x32_bf16 a[108:111], a[8:11], a[44:47], a[108:111]
ds_read_b64_tr_b16 a[8:9], v18 offset:8704
v_mfma_f32_16x16x32_bf16 a[112:115], a[12:15], a[32:35], a[112:115]
ds_read_b64_tr_b16 a[10:11], v19 offset:8704
v_max3_f32 v22, v22, v24, v25
v_max3_f32 v22, v22, v26, v27
v_mfma_f32_16x16x32_bf16 a[116:119], a[12:15], a[36:39], a[116:119]
v_mfma_f32_16x16x32_bf16 a[120:123], a[12:15], a[40:43], a[120:123]
v_mfma_f32_16x16x32_bf16 a[124:127], a[12:15], a[44:47], a[124:127]
ds_read_b64_tr_b16 a[12:13], v20 offset:8704
v_mfma_f32_16x16x32_bf16 a[128:131], a[16:19], a[32:35], a[128:131]
ds_read_b64_tr_b16 a[14:15], v21 offset:8704
v_mfma_f32_16x16x32_bf16 a[132:135], a[16:19], a[36:39], a[132:135]
s_add_u32 s27, s58, 0x3000
buffer_store_dwordx4 v[204:207], v4, s[48:51], s27 offen offset:2048 sc1
v_mfma_f32_16x16x32_bf16 a[136:139], a[16:19], a[40:43], a[136:139]
v_mfma_f32_16x16x32_bf16 a[140:143], a[16:19], a[44:47], a[140:143]
ds_read_b64_tr_b16 a[16:17], v18 offset:9216
s_waitcnt lgkmcnt(5)
v_mfma_f32_16x16x32_bf16 a[144:147], a[20:23], a[32:35], a[144:147]
ds_read_b64_tr_b16 a[18:19], v19 offset:9216
v_mfma_f32_16x16x32_bf16 v[28:31], v[140:143], v[140:143], v[28:31]
v_mfma_f32_16x16x32_bf16 a[148:151], a[20:23], a[36:39], a[148:151]
v_mfma_f32_16x16x32_bf16 a[152:155], a[20:23], a[40:43], a[152:155]
v_mfma_f32_16x16x32_bf16 a[156:159], a[20:23], a[44:47], a[156:159]
ds_read_b64_tr_b16 a[20:21], v20 offset:9216
v_mfma_f32_16x16x32_bf16 a[160:163], a[24:27], a[32:35], a[160:163]
ds_read_b64_tr_b16 a[22:23], v21 offset:9216
v_mfma_f32_16x16x32_bf16 a[164:167], a[24:27], a[36:39], a[164:167]
s_add_u32 s27, s58, 0x3000
buffer_store_dwordx4 v[108:111], v4, s[48:51], s27 offen offset:3072 sc1
v_mfma_f32_16x16x32_bf16 a[168:171], a[24:27], a[40:43], a[168:171]
v_mfma_f32_16x16x32_bf16 a[172:175], a[24:27], a[44:47], a[172:175]
ds_read_b64_tr_b16 a[24:25], v18 offset:9728
v_mfma_f32_16x16x32_bf16 a[176:179], a[28:31], a[32:35], a[176:179]
ds_read_b64_tr_b16 a[26:27], v19 offset:9728
v_mfma_f32_16x16x32_bf16 a[180:183], a[28:31], a[36:39], a[180:183]
v_mfma_f32_16x16x32_bf16 a[184:187], a[28:31], a[40:43], a[184:187]
v_mfma_f32_16x16x32_bf16 a[188:191], a[28:31], a[44:47], a[188:191]
ds_read_b64_tr_b16 a[28:29], v20 offset:9728
ds_read_b64_tr_b16 a[30:31], v21 offset:9728
v_mfma_f32_16x16x32_bf16 a[64:67], a[0:3], a[48:51], a[64:67]
v_mfma_f32_16x16x32_bf16 a[68:71], a[0:3], a[52:55], a[68:71]
v_mfma_f32_16x16x32_bf16 a[72:75], a[0:3], a[56:59], a[72:75]
v_mfma_f32_16x16x32_bf16 a[76:79], a[0:3], a[60:63], a[76:79]
v_mfma_f32_16x16x32_bf16 a[80:83], a[4:7], a[48:51], a[80:83]
s_waitcnt vmcnt(20) lgkmcnt(0)
s_barrier
v_add_u32 v16, 0xc000, v16
v_add_u32 v17, 0xc000, v17
v_add_u32 v18, 0xc000, v18
v_add_u32 v19, 0xc000, v19
v_add_u32 v20, 0xc000, v20
v_add_u32 v21, 0xc000, v21
v_mfma_f32_16x16x32_bf16 a[84:87], a[4:7], a[52:55], a[84:87]
ds_read_b64_tr_b16 a[0:1], v18 offset:0
v_mfma_f32_16x16x32_bf16 a[88:91], a[4:7], a[56:59], a[88:91]
ds_read_b64_tr_b16 a[2:3], v19 offset:0
v_mfma_f32_16x16x32_bf16 a[92:95], a[4:7], a[60:63], a[92:95]
ds_read_b64_tr_b16 a[4:5], v20 offset:0
s_add_u32 m0, s53, 0
v_mfma_f32_16x16x32_bf16 a[96:99], a[8:11], a[48:51], a[96:99]
ds_read_b64_tr_b16 a[6:7], v21 offset:0
buffer_load_dwordx4 v12, s[40:43], s56 offen offset:0 lds
v_mfma_f32_16x16x32_bf16 v[28:31], v[204:207], v[204:207], v[28:31]
v_mfma_f32_16x16x32_bf16 a[100:103], a[8:11], a[52:55], a[100:103]
ds_read_b128 a[32:35], v16 offset:0
v_mfma_f32_16x16x32_bf16 a[104:107], a[8:11], a[56:59], a[104:107]
ds_read_b128 a[36:39], v16 offset:2048
buffer_load_dwordx4 v13, s[40:43], s56 offen offset:1024 lds
v_mfma_f32_16x16x32_bf16 a[108:111], a[8:11], a[60:63], a[108:111]
ds_read_b64_tr_b16 a[8:9], v18 offset:512
s_add_u32 m0, s53, 0x2000
v_mfma_f32_16x16x32_bf16 a[112:115], a[12:15], a[48:51], a[112:115]
ds_read_b64_tr_b16 a[10:11], v19 offset:512
v_mfma_f32_16x16x32_bf16 a[116:119], a[12:15], a[52:55], a[116:119]
ds_read_b128 a[40:43], v16 offset:4096
buffer_load_dwordx4 v12, s[40:43], s68 offen offset:0 lds
v_mfma_f32_16x16x32_bf16 a[120:123], a[12:15], a[56:59], a[120:123]
ds_read_b128 a[44:47], v16 offset:6144
buffer_load_dwordx4 v13, s[40:43], s68 offen offset:1024 lds
v_mfma_f32_16x16x32_bf16 a[124:127], a[12:15], a[60:63], a[124:127]
ds_read_b64_tr_b16 a[12:13], v20 offset:512
v_mfma_f32_16x16x32_bf16 a[128:131], a[16:19], a[48:51], a[128:131]
ds_read_b64_tr_b16 a[14:15], v21 offset:512
s_add_u32 m0, s52, 0
v_mfma_f32_16x16x32_bf16 a[132:135], a[16:19], a[52:55], a[132:135]
buffer_load_dwordx4 v8, s[36:39], s54 offen offset:0 lds
v_mfma_f32_16x16x32_bf16 a[136:139], a[16:19], a[56:59], a[136:139]
buffer_load_dwordx4 v9, s[36:39], s54 offen offset:1024 lds
v_mfma_f32_16x16x32_bf16 a[140:143], a[16:19], a[60:63], a[140:143]
ds_read_b64_tr_b16 a[16:17], v18 offset:1024
v_mfma_f32_16x16x32_bf16 a[144:147], a[20:23], a[48:51], a[144:147]
ds_read_b64_tr_b16 a[18:19], v19 offset:1024
buffer_load_dwordx4 v10, s[36:39], s54 offen offset:2048 lds
v_mfma_f32_16x16x32_bf16 v[28:31], v[108:111], v[108:111], v[28:31]
v_mfma_f32_16x16x32_bf16 a[148:151], a[20:23], a[52:55], a[148:151]
buffer_load_dwordx4 v11, s[36:39], s54 offen offset:3072 lds
v_mfma_f32_16x16x32_bf16 a[152:155], a[20:23], a[56:59], a[152:155]
s_add_u32 m0, s52, 0x1000
v_mfma_f32_16x16x32_bf16 a[156:159], a[20:23], a[60:63], a[156:159]
ds_read_b64_tr_b16 a[20:21], v20 offset:1024
v_mfma_f32_16x16x32_bf16 a[160:163], a[24:27], a[48:51], a[160:163]
ds_read_b64_tr_b16 a[22:23], v21 offset:1024
buffer_load_dwordx4 v8, s[36:39], s55 offen offset:0 lds
v_mfma_f32_16x16x32_bf16 a[164:167], a[24:27], a[52:55], a[164:167]
buffer_load_dwordx4 v9, s[36:39], s55 offen offset:1024 lds
v_mfma_f32_16x16x32_bf16 a[168:171], a[24:27], a[56:59], a[168:171]
buffer_load_dwordx4 v10, s[36:39], s55 offen offset:2048 lds
v_mfma_f32_16x16x32_bf16 a[172:175], a[24:27], a[60:63], a[172:175]
ds_read_b64_tr_b16 a[24:25], v18 offset:1536
v_mfma_f32_16x16x32_bf16 a[176:179], a[28:31], a[48:51], a[176:179]
ds_read_b64_tr_b16 a[26:27], v19 offset:1536
v_mfma_f32_16x16x32_bf16 a[180:183], a[28:31], a[52:55], a[180:183]
buffer_load_dwordx4 v11, s[36:39], s55 offen offset:3072 lds
v_mfma_f32_16x16x32_bf16 a[184:187], a[28:31], a[56:59], a[184:187]
s_add_u32 s54, s54, 0x80
s_add_u32 s55, s55, 0x80
s_add_u32 s56, s56, 0x80000
s_add_u32 s68, s68, 0x80000
v_mfma_f32_16x16x32_bf16 a[188:191], a[28:31], a[60:63], a[188:191]
ds_read_b64_tr_b16 a[28:29], v20 offset:1536
ds_read_b64_tr_b16 a[30:31], v21 offset:1536
s_waitcnt lgkmcnt(6)
v_mfma_f32_16x16x32_bf16 a[64:67], a[0:3], a[32:35], a[64:67]
s_add_u32 s27, s58, 0x8000
buffer_store_dwordx4 v[80:83], v4, s[48:51], s27 offen offset:0 sc1
v_mfma_f32_16x16x32_bf16 a[68:71], a[0:3], a[36:39], a[68:71]
ds_read_b128 a[48:51], v17 offset:0
v_mfma_f32_16x16x32_bf16 a[72:75], a[0:3], a[40:43], a[72:75]
ds_read_b128 a[52:55], v17 offset:2048
v_mfma_f32_16x16x32_bf16 a[76:79], a[0:3], a[44:47], a[76:79]
ds_read_b64_tr_b16 a[0:1], v18 offset:8192
v_mfma_f32_16x16x32_bf16 a[80:83], a[4:7], a[32:35], a[80:83]
ds_read_b64_tr_b16 a[2:3], v19 offset:8192
v_mfma_f32_16x16x32_bf16 a[84:87], a[4:7], a[36:39], a[84:87]
ds_read_b128 a[56:59], v17 offset:4096
v_mfma_f32_16x16x32_bf16 a[88:91], a[4:7], a[40:43], a[88:91]
ds_read_b128 a[60:63], v17 offset:6144
v_mfma_f32_16x16x32_bf16 a[92:95], a[4:7], a[44:47], a[92:95]
ds_read_b64_tr_b16 a[4:5], v20 offset:8192
v_mfma_f32_16x16x32_bf16 a[96:99], a[8:11], a[32:35], a[96:99]
ds_read_b64_tr_b16 a[6:7], v21 offset:8192
v_mfma_f32_16x16x32_bf16 v[24:27], v[80:83], v[80:83], 0
v_mfma_f32_16x16x32_bf16 a[100:103], a[8:11], a[36:39], a[100:103]
s_add_u32 s27, s58, 0x8000
buffer_store_dwordx4 v[144:147], v4, s[48:51], s27 offen offset:1024 sc1
v_mfma_f32_16x16x32_bf16 a[104:107], a[8:11], a[40:43], a[104:107]
v_mfma_f32_16x16x32_bf16 a[108:111], a[8:11], a[44:47], a[108:111]
ds_read_b64_tr_b16 a[8:9], v18 offset:8704
v_mfma_f32_16x16x32_bf16 a[112:115], a[12:15], a[32:35], a[112:115]
ds_read_b64_tr_b16 a[10:11], v19 offset:8704
v_max3_f32 v22, v22, v28, v29
v_max3_f32 v22, v22, v30, v31
v_mfma_f32_16x16x32_bf16 a[116:119], a[12:15], a[36:39], a[116:119]
v_mfma_f32_16x16x32_bf16 a[120:123], a[12:15], a[40:43], a[120:123]
v_mfma_f32_16x16x32_bf16 a[124:127], a[12:15], a[44:47], a[124:127]
ds_read_b64_tr_b16 a[12:13], v20 offset:8704
v_mfma_f32_16x16x32_bf16 a[128:131], a[16:19], a[32:35], a[128:131]
ds_read_b64_tr_b16 a[14:15], v21 offset:8704
v_mfma_f32_16x16x32_bf16 a[132:135], a[16:19], a[36:39], a[132:135]
s_add_u32 s27, s58, 0x8000
buffer_store_dwordx4 v[208:211], v4, s[48:51], s27 offen offset:2048 sc1
v_mfma_f32_16x16x32_bf16 a[136:139], a[16:19], a[40:43], a[136:139]
v_mfma_f32_16x16x32_bf16 a[140:143], a[16:19], a[44:47], a[140:143]
ds_read_b64_tr_b16 a[16:17], v18 offset:9216
s_waitcnt lgkmcnt(5)
v_mfma_f32_16x16x32_bf16 a[144:147], a[20:23], a[32:35], a[144:147]
ds_read_b64_tr_b16 a[18:19], v19 offset:9216
v_mfma_f32_16x16x32_bf16 v[24:27], v[144:147], v[144:147], v[24:27]
v_mfma_f32_16x16x32_bf16 a[148:151], a[20:23], a[36:39], a[148:151]
v_mfma_f32_16x16x32_bf16 a[152:155], a[20:23], a[40:43], a[152:155]
v_mfma_f32_16x16x32_bf16 a[156:159], a[20:23], a[44:47], a[156:159]
ds_read_b64_tr_b16 a[20:21], v20 offset:9216
v_mfma_f32_16x16x32_bf16 a[160:163], a[24:27], a[32:35], a[160:163]
ds_read_b64_tr_b16 a[22:23], v21 offset:9216
v_mfma_f32_16x16x32_bf16 a[164:167], a[24:27], a[36:39], a[164:167]
s_add_u32 s27, s58, 0x8000
buffer_store_dwordx4 v[112:115], v4, s[48:51], s27 offen offset:3072 sc1
v_mfma_f32_16x16x32_bf16 a[168:171], a[24:27], a[40:43], a[168:171]
v_mfma_f32_16x16x32_bf16 a[172:175], a[24:27], a[44:47], a[172:175]
ds_read_b64_tr_b16 a[24:25], v18 offset:9728
v_mfma_f32_16x16x32_bf16 a[176:179], a[28:31], a[32:35], a[176:179]
ds_read_b64_tr_b16 a[26:27], v19 offset:9728
v_mfma_f32_16x16x32_bf16 a[180:183], a[28:31], a[36:39], a[180:183]
v_mfma_f32_16x16x32_bf16 a[184:187], a[28:31], a[40:43], a[184:187]
v_mfma_f32_16x16x32_bf16 a[188:191], a[28:31], a[44:47], a[188:191]
ds_read_b64_tr_b16 a[28:29], v20 offset:9728
ds_read_b64_tr_b16 a[30:31], v21 offset:9728
v_mfma_f32_16x16x32_bf16 a[64:67], a[0:3], a[48:51], a[64:67]
v_mfma_f32_16x16x32_bf16 a[68:71], a[0:3], a[52:55], a[68:71]
v_mfma_f32_16x16x32_bf16 a[72:75], a[0:3], a[56:59], a[72:75]
v_mfma_f32_16x16x32_bf16 a[76:79], a[0:3], a[60:63], a[76:79]
v_mfma_f32_16x16x32_bf16 a[80:83], a[4:7], a[48:51], a[80:83]
s_waitcnt vmcnt(20) lgkmcnt(0)
s_barrier
v_add_u32 v16, 0xc000, v16
v_add_u32 v17, 0xc000, v17
v_add_u32 v18, 0xc000, v18
v_add_u32 v19, 0xc000, v19
v_add_u32 v20, 0xc000, v20
v_add_u32 v21, 0xc000, v21
v_mfma_f32_16x16x32_bf16 a[84:87], a[4:7], a[52:55], a[84:87]
ds_read_b64_tr_b16 a[0:1], v18 offset:0
v_mfma_f32_16x16x32_bf16 a[88:91], a[4:7], a[56:59], a[88:91]
ds_read_b64_tr_b16 a[2:3], v19 offset:0
v_mfma_f32_16x16x32_bf16 a[92:95], a[4:7], a[60:63], a[92:95]
ds_read_b64_tr_b16 a[4:5], v20 offset:0
s_add_u32 m0, s53, 0xc000
v_mfma_f32_16x16x32_bf16 a[96:99], a[8:11], a[48:51], a[96:99]
ds_read_b64_tr_b16 a[6:7], v21 offset:0
buffer_load_dwordx4 v12, s[40:43], s56 offen offset:0 lds
v_mfma_f32_16x16x32_bf16 v[24:27], v[208:211], v[208:211], v[24:27]
v_mfma_f32_16x16x32_bf16 a[100:103], a[8:11], a[52:55], a[100:103]
ds_read_b128 a[32:35], v16 offset:0
v_mfma_f32_16x16x32_bf16 a[104:107], a[8:11], a[56:59], a[104:107]
ds_read_b128 a[36:39], v16 offset:2048
buffer_load_dwordx4 v13, s[40:43], s56 offen offset:1024 lds
v_mfma_f32_16x16x32_bf16 a[108:111], a[8:11], a[60:63], a[108:111]
ds_read_b64_tr_b16 a[8:9], v18 offset:512
s_add_u32 m0, s53, 0xe000
v_mfma_f32_16x16x32_bf16 a[112:115], a[12:15], a[48:51], a[112:115]
ds_read_b64_tr_b16 a[10:11], v19 offset:512
v_mfma_f32_16x16x32_bf16 a[116:119], a[12:15], a[52:55], a[116:119]
ds_read_b128 a[40:43], v16 offset:4096
buffer_load_dwordx4 v12, s[40:43], s68 offen offset:0 lds
v_mfma_f32_16x16x32_bf16 a[120:123], a[12:15], a[56:59], a[120:123]
ds_read_b128 a[44:47], v16 offset:6144
buffer_load_dwordx4 v13, s[40:43], s68 offen offset:1024 lds
v_mfma_f32_16x16x32_bf16 a[124:127], a[12:15], a[60:63], a[124:127]
ds_read_b64_tr_b16 a[12:13], v20 offset:512
v_mfma_f32_16x16x32_bf16 a[128:131], a[16:19], a[48:51], a[128:131]
ds_read_b64_tr_b16 a[14:15], v21 offset:512
s_add_u32 m0, s52, 0xc000
v_mfma_f32_16x16x32_bf16 a[132:135], a[16:19], a[52:55], a[132:135]
buffer_load_dwordx4 v8, s[36:39], s54 offen offset:0 lds
v_mfma_f32_16x16x32_bf16 a[136:139], a[16:19], a[56:59], a[136:139]
buffer_load_dwordx4 v9, s[36:39], s54 offen offset:1024 lds
v_mfma_f32_16x16x32_bf16 a[140:143], a[16:19], a[60:63], a[140:143]
ds_read_b64_tr_b16 a[16:17], v18 offset:1024
v_mfma_f32_16x16x32_bf16 a[144:147], a[20:23], a[48:51], a[144:147]
ds_read_b64_tr_b16 a[18:19], v19 offset:1024
buffer_load_dwordx4 v10, s[36:39], s54 offen offset:2048 lds
v_mfma_f32_16x16x32_bf16 v[24:27], v[112:115], v[112:115], v[24:27]
v_mfma_f32_16x16x32_bf16 a[148:151], a[20:23], a[52:55], a[148:151]
buffer_load_dwordx4 v11, s[36:39], s54 offen offset:3072 lds
v_mfma_f32_16x16x32_bf16 a[152:155], a[20:23], a[56:59], a[152:155]
s_add_u32 m0, s52, 0xd000
v_mfma_f32_16x16x32_bf16 a[156:159], a[20:23], a[60:63], a[156:159]
ds_read_b64_tr_b16 a[20:21], v20 offset:1024
v_mfma_f32_16x16x32_bf16 a[160:163], a[24:27], a[48:51], a[160:163]
ds_read_b64_tr_b16 a[22:23], v21 offset:1024
buffer_load_dwordx4 v8, s[36:39], s55 offen offset:0 lds
v_mfma_f32_16x16x32_bf16 a[164:167], a[24:27], a[52:55], a[164:167]
buffer_load_dwordx4 v9, s[36:39], s55 offen offset:1024 lds
v_mfma_f32_16x16x32_bf16 a[168:171], a[24:27], a[56:59], a[168:171]
buffer_load_dwordx4 v10, s[36:39], s55 offen offset:2048 lds
v_mfma_f32_16x16x32_bf16 a[172:175], a[24:27], a[60:63], a[172:175]
ds_read_b64_tr_b16 a[24:25], v18 offset:1536
v_mfma_f32_16x16x32_bf16 a[176:179], a[28:31], a[48:51], a[176:179]
ds_read_b64_tr_b16 a[26:27], v19 offset:1536
v_mfma_f32_16x16x32_bf16 a[180:183], a[28:31], a[52:55], a[180:183]
buffer_load_dwordx4 v11, s[36:39], s55 offen offset:3072 lds
v_mfma_f32_16x16x32_bf16 a[184:187], a[28:31], a[56:59], a[184:187]
s_add_u32 s54, s54, 0x80
s_add_u32 s55, s55, 0x80
s_add_u32 s56, s56, 0x80000
s_add_u32 s68, s68, 0x80000
v_mfma_f32_16x16x32_bf16 a[188:191], a[28:31], a[60:63], a[188:191]
ds_read_b64_tr_b16 a[28:29], v20 offset:1536
ds_read_b64_tr_b16 a[30:31], v21 offset:1536
s_waitcnt lgkmcnt(6)
v_mfma_f32_16x16x32_bf16 a[64:67], a[0:3], a[32:35], a[64:67]
v_mbcnt_lo_u32_b32 v3, -1, 0
v_mbcnt_hi_u32_b32 v3, -1, v3
v_lshrrev_b32 v36, 5, v3
v_lshlrev_b32 v36, 10, v36
v_and_b32 v3, 31, v3
v_lshl_add_u32 v3, v3, 4, v36
s_lshr_b32 s27, s22, 1
s_lshl_b32 s27, s27, 15
s_lshl_b32 s28, s23, 13
s_add_u32 s27, s27, s28
s_and_b32 s28, s22, 1
s_lshl_b32 s28, s28, 9
s_add_u32 s27, s27, s28
v_add_u32 v3, s27, v3
s_lshl_b32 s28, s23, 12
s_add_u32 s28, s28, s24
s_add_u32 s28, s28, 0x24000
v_mfma_f32_16x16x32_bf16 a[68:71], a[0:3], a[36:39], a[68:71]
ds_read_b128 a[48:51], v17 offset:0
v_mfma_f32_16x16x32_bf16 a[72:75], a[0:3], a[40:43], a[72:75]
ds_read_b128 a[52:55], v17 offset:2048
v_mfma_f32_16x16x32_bf16 a[76:79], a[0:3], a[44:47], a[76:79]
ds_read_b64_tr_b16 a[0:1], v18 offset:8192
v_mfma_f32_16x16x32_bf16 a[80:83], a[4:7], a[32:35], a[80:83]
ds_read_b64_tr_b16 a[2:3], v19 offset:8192
v_mfma_f32_16x16x32_bf16 a[84:87], a[4:7], a[36:39], a[84:87]
ds_read_b128 a[56:59], v17 offset:4096
v_mfma_f32_16x16x32_bf16 a[88:91], a[4:7], a[40:43], a[88:91]
ds_read_b128 a[60:63], v17 offset:6144
v_mfma_f32_16x16x32_bf16 a[92:95], a[4:7], a[44:47], a[92:95]
ds_read_b64_tr_b16 a[4:5], v20 offset:8192
s_add_u32 m0, s28, 0
s_nop 0
global_load_lds_dwordx4 v3, s[96:97] offset:0
v_add_u32 v3, 0x800, v3
v_mfma_f32_16x16x32_bf16 a[96:99], a[8:11], a[32:35], a[96:99]
ds_read_b64_tr_b16 a[6:7], v21 offset:8192
v_mfma_f32_16x16x32_bf16 v[28:31], v[84:87], v[84:87], 0
v_mfma_f32_16x16x32_bf16 a[100:103], a[8:11], a[36:39], a[100:103]
s_add_u32 m0, s28, 0x400
s_nop 0
global_load_lds_dwordx4 v3, s[96:97] offset:0
v_add_u32 v3, 0x800, v3
v_mfma_f32_16x16x32_bf16 a[104:107], a[8:11], a[40:43], a[104:107]
s_add_u32 m0, s28, 0x800
s_nop 0
global_load_lds_dwordx4 v3, s[96:97] offset:0
v_add_u32 v3, 0x800, v3
v_mfma_f32_16x16x32_bf16 a[108:111], a[8:11], a[44:47], a[108:111]
ds_read_b64_tr_b16 a[8:9], v18 offset:8704
v_mfma_f32_16x16x32_bf16 a[112:115], a[12:15], a[32:35], a[112:115]
ds_read_b64_tr_b16 a[10:11], v19 offset:8704
v_max3_f32 v22, v22, v24, v25
v_max3_f32 v22, v22, v26, v27
v_mfma_f32_16x16x32_bf16 a[116:119], a[12:15], a[36:39], a[116:119]
s_add_u32 m0, s28, 0xc00
s_nop 0
global_load_lds_dwordx4 v3, s[96:97] offset:0
v_add_u32 v3, 0x800, v3
v_mfma_f32_16x16x32_bf16 a[120:123], a[12:15], a[40:43], a[120:123]
v_mfma_f32_16x16x32_bf16 a[124:127], a[12:15], a[44:47], a[124:127]
ds_read_b64_tr_b16 a[12:13], v20 offset:8704
v_mfma_f32_16x16x32_bf16 a[128:131], a[16:19], a[32:35], a[128:131]
ds_read_b64_tr_b16 a[14:15], v21 offset:8704
v_mfma_f32_16x16x32_bf16 a[132:135], a[16:19], a[36:39], a[132:135]
s_add_u32 s27, s58, 0x9000
buffer_store_dwordx4 v[84:87], v4, s[48:51], s27 offen offset:0 sc1
v_mfma_f32_16x16x32_bf16 a[136:139], a[16:19], a[40:43], a[136:139]
v_mfma_f32_16x16x32_bf16 a[140:143], a[16:19], a[44:47], a[140:143]
ds_read_b64_tr_b16 a[16:17], v18 offset:9216
s_waitcnt lgkmcnt(5)
v_mfma_f32_16x16x32_bf16 a[144:147], a[20:23], a[32:35], a[144:147]
ds_read_b64_tr_b16 a[18:19], v19 offset:9216
v_mfma_f32_16x16x32_bf16 v[28:31], v[148:151], v[148:151], v[28:31]
v_mfma_f32_16x16x32_bf16 a[148:151], a[20:23], a[36:39], a[148:151]
s_add_u32 s27, s58, 0x9000
buffer_store_dwordx4 v[148:151], v4, s[48:51], s27 offen offset:1024 sc1
v_mfma_f32_16x16x32_bf16 a[152:155], a[20:23], a[40:43], a[152:155]
v_mfma_f32_16x16x32_bf16 a[156:159], a[20:23], a[44:47], a[156:159]
ds_read_b64_tr_b16 a[20:21], v20 offset:9216
v_mfma_f32_16x16x32_bf16 a[160:163], a[24:27], a[32:35], a[160:163]
ds_read_b64_tr_b16 a[22:23], v21 offset:9216
v_mfma_f32_16x16x32_bf16 a[164:167], a[24:27], a[36:39], a[164:167]
s_add_u32 s27, s58, 0x9000
buffer_store_dwordx4 v[212:215], v4, s[48:51], s27 offen offset:2048 sc1
v_mfma_f32_16x16x32_bf16 a[168:171], a[24:27], a[40:43], a[168:171]
v_mfma_f32_16x16x32_bf16 a[172:175], a[24:27], a[44:47], a[172:175]
ds_read_b64_tr_b16 a[24:25], v18 offset:9728
v_mfma_f32_16x16x32_bf16 a[176:179], a[28:31], a[32:35], a[176:179]
ds_read_b64_tr_b16 a[26:27], v19 offset:9728
v_mfma_f32_16x16x32_bf16 a[180:183], a[28:31], a[36:39], a[180:183]
s_add_u32 s27, s58, 0x9000
buffer_store_dwordx4 v[116:119], v4, s[48:51], s27 offen offset:3072 sc1
v_mfma_f32_16x16x32_bf16 a[184:187], a[28:31], a[40:43], a[184:187]
v_mfma_f32_16x16x32_bf16 a[188:191], a[28:31], a[44:47], a[188:191]
ds_read_b64_tr_b16 a[28:29], v20 offset:9728
ds_read_b64_tr_b16 a[30:31], v21 offset:9728
v_mfma_f32_16x16x32_bf16 a[64:67], a[0:3], a[48:51], a[64:67]
v_mfma_f32_16x16x32_bf16 a[68:71], a[0:3], a[52:55], a[68:71]
v_mfma_f32_16x16x32_bf16 a[72:75], a[0:3], a[56:59], a[72:75]
v_mfma_f32_16x16x32_bf16 a[76:79], a[0:3], a[60:63], a[76:79]
v_mfma_f32_16x16x32_bf16 a[80:83], a[4:7], a[48:51], a[80:83]
s_waitcnt vmcnt(24) lgkmcnt(0)
s_barrier
v_add_u32 v16, 0xfffe8000, v16
v_add_u32 v17, 0xfffe8000, v17
v_add_u32 v18, 0xfffe8000, v18
v_add_u32 v19, 0xfffe8000, v19
v_add_u32 v20, 0xfffe8000, v20
v_add_u32 v21, 0xfffe8000, v21
v_mfma_f32_16x16x32_bf16 a[84:87], a[4:7], a[52:55], a[84:87]
ds_read_b64_tr_b16 a[0:1], v18 offset:0
v_mfma_f32_16x16x32_bf16 a[88:91], a[4:7], a[56:59], a[88:91]
ds_read_b64_tr_b16 a[2:3], v19 offset:0
v_mfma_f32_16x16x32_bf16 a[92:95], a[4:7], a[60:63], a[92:95]
ds_read_b64_tr_b16 a[4:5], v20 offset:0
v_mfma_f32_16x16x32_bf16 a[96:99], a[8:11], a[48:51], a[96:99]
ds_read_b64_tr_b16 a[6:7], v21 offset:0
v_mfma_f32_16x16x32_bf16 v[28:31], v[212:215], v[212:215], v[28:31]
v_mfma_f32_16x16x32_bf16 a[100:103], a[8:11], a[52:55], a[100:103]
ds_read_b128 a[32:35], v16 offset:0
v_mfma_f32_16x16x32_bf16 a[104:107], a[8:11], a[56:59], a[104:107]
ds_read_b128 a[36:39], v16 offset:2048
v_mbcnt_lo_u32_b32 v0, -1, 0
v_mbcnt_hi_u32_b32 v0, -1, v0
s_lshl_b32 s27, s23, 6
v_add_u32 v0, s27, v0
v_mfma_f32_16x16x32_bf16 a[108:111], a[8:11], a[60:63], a[108:111]
ds_read_b64_tr_b16 a[8:9], v18 offset:512
v_mfma_f32_16x16x32_bf16 a[112:115], a[12:15], a[48:51], a[112:115]
ds_read_b64_tr_b16 a[10:11], v19 offset:512
v_mfma_f32_16x16x32_bf16 a[116:119], a[12:15], a[52:55], a[116:119]
ds_read_b128 a[40:43], v16 offset:4096
v_mfma_f32_16x16x32_bf16 a[120:123], a[12:15], a[56:59], a[120:123]
ds_read_b128 a[44:47], v16 offset:6144
v_lshrrev_b32 v1, 3, v0
v_lshlrev_b32 v1, 11, v1
v_and_b32 v2, 7, v0
v_lshl_add_u32 v1, v2, 4, v1
s_lshr_b32 s27, s22, 3
s_lshl_b32 s27, s27, 16
s_and_b32 s28, s22, 7
s_lshl_b32 s28, s28, 7
s_add_u32 s27, s27, s28
v_add_u32 v1, s27, v1
v_mfma_f32_16x16x32_bf16 a[124:127], a[12:15], a[60:63], a[124:127]
ds_read_b64_tr_b16 a[12:13], v20 offset:512
v_mfma_f32_16x16x32_bf16 a[128:131], a[16:19], a[48:51], a[128:131]
ds_read_b64_tr_b16 a[14:15], v21 offset:512
v_mfma_f32_16x16x32_bf16 a[132:135], a[16:19], a[52:55], a[132:135]
buffer_load_dwordx4 a[212:215], v1, s[80:83], 0 offen offset:0
v_mfma_f32_16x16x32_bf16 a[136:139], a[16:19], a[56:59], a[136:139]
buffer_load_dwordx4 a[216:219], v1, s[80:83], 0 offen offset:1024
v_mfma_f32_16x16x32_bf16 a[140:143], a[16:19], a[60:63], a[140:143]
ds_read_b64_tr_b16 a[16:17], v18 offset:1024
v_mfma_f32_16x16x32_bf16 a[144:147], a[20:23], a[48:51], a[144:147]
ds_read_b64_tr_b16 a[18:19], v19 offset:1024
v_mfma_f32_16x16x32_bf16 v[28:31], v[116:119], v[116:119], v[28:31]
v_mfma_f32_16x16x32_bf16 a[148:151], a[20:23], a[52:55], a[148:151]
v_mfma_f32_16x16x32_bf16 a[152:155], a[20:23], a[56:59], a[152:155]
v_subrev_u32 v2, 0xc00, v1
buffer_load_dwordx4 a[192:195], v2, s[76:79], 0 offen offset:0
v_mfma_f32_16x16x32_bf16 a[156:159], a[20:23], a[60:63], a[156:159]
ds_read_b64_tr_b16 a[20:21], v20 offset:1024
v_mfma_f32_16x16x32_bf16 a[160:163], a[24:27], a[48:51], a[160:163]
ds_read_b64_tr_b16 a[22:23], v21 offset:1024
v_mfma_f32_16x16x32_bf16 a[164:167], a[24:27], a[52:55], a[164:167]
v_mfma_f32_16x16x32_bf16 a[168:171], a[24:27], a[56:59], a[168:171]
v_subrev_u32 v2, 0x800, v1
buffer_load_dwordx4 a[196:199], v2, s[76:79], 0 offen offset:0
v_mfma_f32_16x16x32_bf16 a[172:175], a[24:27], a[60:63], a[172:175]
ds_read_b64_tr_b16 a[24:25], v18 offset:1536
v_mfma_f32_16x16x32_bf16 a[176:179], a[28:31], a[48:51], a[176:179]
ds_read_b64_tr_b16 a[26:27], v19 offset:1536
v_mfma_f32_16x16x32_bf16 a[180:183], a[28:31], a[52:55], a[180:183]
v_mfma_f32_16x16x32_bf16 a[184:187], a[28:31], a[56:59], a[184:187]
v_mfma_f32_16x16x32_bf16 a[188:191], a[28:31], a[60:63], a[188:191]
ds_read_b64_tr_b16 a[28:29], v20 offset:1536
ds_read_b64_tr_b16 a[30:31], v21 offset:1536
s_waitcnt lgkmcnt(6)
v_mfma_f32_16x16x32_bf16 a[64:67], a[0:3], a[32:35], a[64:67]
v_subrev_u32 v2, 0x400, v1
buffer_load_dwordx4 a[200:203], v2, s[76:79], 0 offen offset:0
v_mfma_f32_16x16x32_bf16 a[68:71], a[0:3], a[36:39], a[68:71]
ds_read_b128 a[48:51], v17 offset:0
v_mfma_f32_16x16x32_bf16 a[72:75], a[0:3], a[40:43], a[72:75]
ds_read_b128 a[52:55], v17 offset:2048
v_mfma_f32_16x16x32_bf16 a[76:79], a[0:3], a[44:47], a[76:79]
ds_read_b64_tr_b16 a[0:1], v18 offset:8192
v_mfma_f32_16x16x32_bf16 a[80:83], a[4:7], a[32:35], a[80:83]
ds_read_b64_tr_b16 a[2:3], v19 offset:8192
v_mfma_f32_16x16x32_bf16 a[84:87], a[4:7], a[36:39], a[84:87]
ds_read_b128 a[56:59], v17 offset:4096
v_mfma_f32_16x16x32_bf16 a[88:91], a[4:7], a[40:43], a[88:91]
ds_read_b128 a[60:63], v17 offset:6144
v_mfma_f32_16x16x32_bf16 a[92:95], a[4:7], a[44:47], a[92:95]
ds_read_b64_tr_b16 a[4:5], v20 offset:8192
v_mfma_f32_16x16x32_bf16 a[96:99], a[8:11], a[32:35], a[96:99]
ds_read_b64_tr_b16 a[6:7], v21 offset:8192
v_mfma_f32_16x16x32_bf16 v[24:27], v[88:91], v[88:91], 0
v_mfma_f32_16x16x32_bf16 a[100:103], a[8:11], a[36:39], a[100:103]
buffer_load_dwordx4 a[204:207], v1, s[76:79], 0 offen offset:0
v_mfma_f32_16x16x32_bf16 a[104:107], a[8:11], a[40:43], a[104:107]
buffer_load_dwordx4 a[208:211], v1, s[76:79], 0 offen offset:1024
v_mfma_f32_16x16x32_bf16 a[108:111], a[8:11], a[44:47], a[108:111]
ds_read_b64_tr_b16 a[8:9], v18 offset:8704
v_mfma_f32_16x16x32_bf16 a[112:115], a[12:15], a[32:35], a[112:115]
ds_read_b64_tr_b16 a[10:11], v19 offset:8704
v_max3_f32 v22, v22, v28, v29
v_max3_f32 v22, v22, v30, v31
v_mfma_f32_16x16x32_bf16 a[116:119], a[12:15], a[36:39], a[116:119]
s_add_u32 s27, s58, 0xa000
buffer_store_dwordx4 v[88:91], v4, s[48:51], s27 offen offset:0 sc1
v_mfma_f32_16x16x32_bf16 a[120:123], a[12:15], a[40:43], a[120:123]
v_mfma_f32_16x16x32_bf16 a[124:127], a[12:15], a[44:47], a[124:127]
ds_read_b64_tr_b16 a[12:13], v20 offset:8704
v_mfma_f32_16x16x32_bf16 a[128:131], a[16:19], a[32:35], a[128:131]
ds_read_b64_tr_b16 a[14:15], v21 offset:8704
v_mfma_f32_16x16x32_bf16 a[132:135], a[16:19], a[36:39], a[132:135]
s_add_u32 s27, s58, 0xa000
buffer_store_dwordx4 v[152:155], v4, s[48:51], s27 offen offset:1024 sc1
v_mfma_f32_16x16x32_bf16 a[136:139], a[16:19], a[40:43], a[136:139]
v_mfma_f32_16x16x32_bf16 a[140:143], a[16:19], a[44:47], a[140:143]
ds_read_b64_tr_b16 a[16:17], v18 offset:9216
s_waitcnt lgkmcnt(5)
v_mfma_f32_16x16x32_bf16 a[144:147], a[20:23], a[32:35], a[144:147]
ds_read_b64_tr_b16 a[18:19], v19 offset:9216
v_mfma_f32_16x16x32_bf16 v[24:27], v[152:155], v[152:155], v[24:27]
v_mfma_f32_16x16x32_bf16 a[148:151], a[20:23], a[36:39], a[148:151]
v_mfma_f32_16x16x32_bf16 a[152:155], a[20:23], a[40:43], a[152:155]
s_add_u32 s27, s58, 0xa000
buffer_store_dwordx4 v[216:219], v4, s[48:51], s27 offen offset:2048 sc1
v_mfma_f32_16x16x32_bf16 a[156:159], a[20:23], a[44:47], a[156:159]
ds_read_b64_tr_b16 a[20:21], v20 offset:9216
v_mfma_f32_16x16x32_bf16 a[160:163], a[24:27], a[32:35], a[160:163]
ds_read_b64_tr_b16 a[22:23], v21 offset:9216
v_mfma_f32_16x16x32_bf16 a[164:167], a[24:27], a[36:39], a[164:167]
v_mfma_f32_16x16x32_bf16 a[168:171], a[24:27], a[40:43], a[168:171]
s_add_u32 s27, s58, 0xa000
buffer_store_dwordx4 v[120:123], v4, s[48:51], s27 offen offset:3072 sc1
v_mfma_f32_16x16x32_bf16 a[172:175], a[24:27], a[44:47], a[172:175]
ds_read_b64_tr_b16 a[24:25], v18 offset:9728
v_mfma_f32_16x16x32_bf16 a[176:179], a[28:31], a[32:35], a[176:179]
ds_read_b64_tr_b16 a[26:27], v19 offset:9728
v_mfma_f32_16x16x32_bf16 a[180:183], a[28:31], a[36:39], a[180:183]
v_mfma_f32_16x16x32_bf16 a[184:187], a[28:31], a[40:43], a[184:187]
v_mfma_f32_16x16x32_bf16 a[188:191], a[28:31], a[44:47], a[188:191]
ds_read_b64_tr_b16 a[28:29], v20 offset:9728
ds_read_b64_tr_b16 a[30:31], v21 offset:9728
v_mfma_f32_16x16x32_bf16 a[64:67], a[0:3], a[48:51], a[64:67]
v_mfma_f32_16x16x32_bf16 a[68:71], a[0:3], a[52:55], a[68:71]
v_mfma_f32_16x16x32_bf16 a[72:75], a[0:3], a[56:59], a[72:75]
v_mfma_f32_16x16x32_bf16 a[76:79], a[0:3], a[60:63], a[76:79]
v_mfma_f32_16x16x32_bf16 a[80:83], a[4:7], a[48:51], a[80:83]
s_waitcnt vmcnt(19) lgkmcnt(0)
s_barrier
v_add_u32 v16, 0xc000, v16
v_add_u32 v17, 0xc000, v17
v_add_u32 v18, 0xc000, v18
v_add_u32 v19, 0xc000, v19
v_add_u32 v20, 0xc000, v20
v_add_u32 v21, 0xc000, v21
v_mfma_f32_16x16x32_bf16 a[84:87], a[4:7], a[52:55], a[84:87]
ds_read_b64_tr_b16 a[0:1], v18 offset:0
v_mfma_f32_16x16x32_bf16 a[88:91], a[4:7], a[56:59], a[88:91]
ds_read_b64_tr_b16 a[2:3], v19 offset:0
v_mfma_f32_16x16x32_bf16 a[92:95], a[4:7], a[60:63], a[92:95]
ds_read_b64_tr_b16 a[4:5], v20 offset:0
v_mfma_f32_16x16x32_bf16 a[96:99], a[8:11], a[48:51], a[96:99]
ds_read_b64_tr_b16 a[6:7], v21 offset:0
v_mfma_f32_16x16x32_bf16 v[24:27], v[216:219], v[216:219], v[24:27]
v_mfma_f32_16x16x32_bf16 a[100:103], a[8:11], a[52:55], a[100:103]
ds_read_b128 a[32:35], v16 offset:0
v_mfma_f32_16x16x32_bf16 a[104:107], a[8:11], a[56:59], a[104:107]
ds_read_b128 a[36:39], v16 offset:2048
v_mfma_f32_16x16x32_bf16 a[108:111], a[8:11], a[60:63], a[108:111]
ds_read_b64_tr_b16 a[8:9], v18 offset:512
v_and_b32 v2, 7, v0
v_lshlrev_b32 v2, 6, v2
s_and_b32 s28, s22, 7
s_lshl_b32 s28, s28, 9
v_add_u32 v2, s28, v2
v_mfma_f32_16x16x32_bf16 a[112:115], a[12:15], a[48:51], a[112:115]
ds_read_b64_tr_b16 a[10:11], v19 offset:512
v_mfma_f32_16x16x32_bf16 a[116:119], a[12:15], a[52:55], a[116:119]
ds_read_b128 a[40:43], v16 offset:4096
v_mfma_f32_16x16x32_bf16 a[120:123], a[12:15], a[56:59], a[120:123]
ds_read_b128 a[44:47], v16 offset:6144
v_mfma_f32_16x16x32_bf16 a[124:127], a[12:15], a[60:63], a[124:127]
ds_read_b64_tr_b16 a[12:13], v20 offset:512
v_mfma_f32_16x16x32_bf16 a[128:131], a[16:19], a[48:51], a[128:131]
ds_read_b64_tr_b16 a[14:15], v21 offset:512
buffer_load_dwordx4 a[220:223], v2, s[84:87], 0 offen offset:0
v_mfma_f32_16x16x32_bf16 a[132:135], a[16:19], a[52:55], a[132:135]
buffer_load_dwordx4 a[224:227], v2, s[84:87], 0 offen offset:16
v_mfma_f32_16x16x32_bf16 a[136:139], a[16:19], a[56:59], a[136:139]
v_mfma_f32_16x16x32_bf16 a[140:143], a[16:19], a[60:63], a[140:143]
ds_read_b64_tr_b16 a[16:17], v18 offset:1024
v_mfma_f32_16x16x32_bf16 a[144:147], a[20:23], a[48:51], a[144:147]
ds_read_b64_tr_b16 a[18:19], v19 offset:1024
v_mfma_f32_16x16x32_bf16 v[24:27], v[120:123], v[120:123], v[24:27]
v_mfma_f32_16x16x32_bf16 a[148:151], a[20:23], a[52:55], a[148:151]
buffer_load_dwordx4 a[228:231], v2, s[84:87], 0 offen offset:32
v_mfma_f32_16x16x32_bf16 a[152:155], a[20:23], a[56:59], a[152:155]
v_mfma_f32_16x16x32_bf16 a[156:159], a[20:23], a[60:63], a[156:159]
ds_read_b64_tr_b16 a[20:21], v20 offset:1024
v_mfma_f32_16x16x32_bf16 a[160:163], a[24:27], a[48:51], a[160:163]
ds_read_b64_tr_b16 a[22:23], v21 offset:1024
v_mfma_f32_16x16x32_bf16 a[164:167], a[24:27], a[52:55], a[164:167]
buffer_load_dwordx4 a[232:235], v2, s[84:87], 0 offen offset:48
v_mfma_f32_16x16x32_bf16 a[168:171], a[24:27], a[56:59], a[168:171]
v_mfma_f32_16x16x32_bf16 a[172:175], a[24:27], a[60:63], a[172:175]
ds_read_b64_tr_b16 a[24:25], v18 offset:1536
v_mfma_f32_16x16x32_bf16 a[176:179], a[28:31], a[48:51], a[176:179]
ds_read_b64_tr_b16 a[26:27], v19 offset:1536
v_mfma_f32_16x16x32_bf16 a[180:183], a[28:31], a[52:55], a[180:183]
v_mfma_f32_16x16x32_bf16 a[184:187], a[28:31], a[56:59], a[184:187]
v_mfma_f32_16x16x32_bf16 a[188:191], a[28:31], a[60:63], a[188:191]
ds_read_b64_tr_b16 a[28:29], v20 offset:1536
ds_read_b64_tr_b16 a[30:31], v21 offset:1536
s_waitcnt lgkmcnt(6)
v_mfma_f32_16x16x32_bf16 a[64:67], a[0:3], a[32:35], a[64:67]
v_lshrrev_b32 v1, 5, v0
v_lshlrev_b32 v1, 10, v1
v_and_b32 v2, 31, v0
v_lshlrev_b32 v2, 5, v2
v_add_u32 v1, v1, v2
s_lshl_b32 s27, s22, 13
v_add_u32 v1, s27, v1
v_mfma_f32_16x16x32_bf16 a[68:71], a[0:3], a[36:39], a[68:71]
ds_read_b128 a[48:51], v17 offset:0
v_mfma_f32_16x16x32_bf16 a[72:75], a[0:3], a[40:43], a[72:75]
ds_read_b128 a[52:55], v17 offset:2048
v_mfma_f32_16x16x32_bf16 a[76:79], a[0:3], a[44:47], a[76:79]
ds_read_b64_tr_b16 a[0:1], v18 offset:8192
v_mfma_f32_16x16x32_bf16 a[80:83], a[4:7], a[32:35], a[80:83]
ds_read_b64_tr_b16 a[2:3], v19 offset:8192
v_mfma_f32_16x16x32_bf16 a[84:87], a[4:7], a[36:39], a[84:87]
ds_read_b128 a[56:59], v17 offset:4096
v_mfma_f32_16x16x32_bf16 a[88:91], a[4:7], a[40:43], a[88:91]
ds_read_b128 a[60:63], v17 offset:6144
v_mfma_f32_16x16x32_bf16 a[92:95], a[4:7], a[44:47], a[92:95]
ds_read_b64_tr_b16 a[4:5], v20 offset:8192
v_mfma_f32_16x16x32_bf16 a[96:99], a[8:11], a[32:35], a[96:99]
ds_read_b64_tr_b16 a[6:7], v21 offset:8192
v_mfma_f32_16x16x32_bf16 v[28:31], v[92:95], v[92:95], 0
v_mfma_f32_16x16x32_bf16 a[100:103], a[8:11], a[36:39], a[100:103]
global_load_dwordx4 a[236:239], v1, s[92:93] offset:0
v_mfma_f32_16x16x32_bf16 a[104:107], a[8:11], a[40:43], a[104:107]
global_load_dwordx4 a[240:243], v1, s[92:93] offset:16
v_mfma_f32_16x16x32_bf16 a[108:111], a[8:11], a[44:47], a[108:111]
ds_read_b64_tr_b16 a[8:9], v18 offset:8704
v_mfma_f32_16x16x32_bf16 a[112:115], a[12:15], a[32:35], a[112:115]
ds_read_b64_tr_b16 a[10:11], v19 offset:8704
v_max3_f32 v22, v22, v24, v25
v_max3_f32 v22, v22, v26, v27
v_mfma_f32_16x16x32_bf16 a[116:119], a[12:15], a[36:39], a[116:119]
s_add_u32 s27, s58, 0xb000
buffer_store_dwordx4 v[92:95], v4, s[48:51], s27 offen offset:0 sc1
v_mfma_f32_16x16x32_bf16 a[120:123], a[12:15], a[40:43], a[120:123]
v_mfma_f32_16x16x32_bf16 a[124:127], a[12:15], a[44:47], a[124:127]
ds_read_b64_tr_b16 a[12:13], v20 offset:8704
v_mfma_f32_16x16x32_bf16 a[128:131], a[16:19], a[32:35], a[128:131]
ds_read_b64_tr_b16 a[14:15], v21 offset:8704
v_mfma_f32_16x16x32_bf16 a[132:135], a[16:19], a[36:39], a[132:135]
s_add_u32 s27, s58, 0xb000
buffer_store_dwordx4 v[156:159], v4, s[48:51], s27 offen offset:1024 sc1
v_mfma_f32_16x16x32_bf16 a[136:139], a[16:19], a[40:43], a[136:139]
v_mfma_f32_16x16x32_bf16 a[140:143], a[16:19], a[44:47], a[140:143]
ds_read_b64_tr_b16 a[16:17], v18 offset:9216
s_waitcnt lgkmcnt(5)
v_mfma_f32_16x16x32_bf16 a[144:147], a[20:23], a[32:35], a[144:147]
ds_read_b64_tr_b16 a[18:19], v19 offset:9216
v_mfma_f32_16x16x32_bf16 v[28:31], v[156:159], v[156:159], v[28:31]
v_mfma_f32_16x16x32_bf16 a[148:151], a[20:23], a[36:39], a[148:151]
v_mfma_f32_16x16x32_bf16 a[152:155], a[20:23], a[40:43], a[152:155]
s_add_u32 s27, s58, 0xb000
buffer_store_dwordx4 v[220:223], v4, s[48:51], s27 offen offset:2048 sc1
v_mfma_f32_16x16x32_bf16 a[156:159], a[20:23], a[44:47], a[156:159]
ds_read_b64_tr_b16 a[20:21], v20 offset:9216
v_mfma_f32_16x16x32_bf16 a[160:163], a[24:27], a[32:35], a[160:163]
ds_read_b64_tr_b16 a[22:23], v21 offset:9216
v_mfma_f32_16x16x32_bf16 a[164:167], a[24:27], a[36:39], a[164:167]
v_mfma_f32_16x16x32_bf16 a[168:171], a[24:27], a[40:43], a[168:171]
s_add_u32 s27, s58, 0xb000
buffer_store_dwordx4 v[124:127], v4, s[48:51], s27 offen offset:3072 sc1
v_mfma_f32_16x16x32_bf16 a[172:175], a[24:27], a[44:47], a[172:175]
ds_read_b64_tr_b16 a[24:25], v18 offset:9728
v_mfma_f32_16x16x32_bf16 a[176:179], a[28:31], a[32:35], a[176:179]
ds_read_b64_tr_b16 a[26:27], v19 offset:9728
v_mfma_f32_16x16x32_bf16 a[180:183], a[28:31], a[36:39], a[180:183]
v_mfma_f32_16x16x32_bf16 a[184:187], a[28:31], a[40:43], a[184:187]
v_mfma_f32_16x16x32_bf16 a[188:191], a[28:31], a[44:47], a[188:191]
ds_read_b64_tr_b16 a[28:29], v20 offset:9728
ds_read_b64_tr_b16 a[30:31], v21 offset:9728
v_mfma_f32_16x16x32_bf16 a[64:67], a[0:3], a[48:51], a[64:67]
buffer_load_dwordx4 a[244:247], v2, s[88:91], 0 offen offset:0
v_mfma_f32_16x16x32_bf16 a[68:71], a[0:3], a[52:55], a[68:71]
v_mfma_f32_16x16x32_bf16 a[72:75], a[0:3], a[56:59], a[72:75]
v_mfma_f32_16x16x32_bf16 a[76:79], a[0:3], a[60:63], a[76:79]
v_mfma_f32_16x16x32_bf16 a[80:83], a[4:7], a[48:51], a[80:83]
v_mfma_f32_16x16x32_bf16 a[84:87], a[4:7], a[52:55], a[84:87]
v_mfma_f32_16x16x32_bf16 a[88:91], a[4:7], a[56:59], a[88:91]
v_mfma_f32_16x16x32_bf16 a[92:95], a[4:7], a[60:63], a[92:95]
s_waitcnt lgkmcnt(2)
v_mfma_f32_16x16x32_bf16 a[96:99], a[8:11], a[48:51], a[96:99]
v_mfma_f32_16x16x32_bf16 v[28:31], v[220:223], v[220:223], v[28:31]
v_mfma_f32_16x16x32_bf16 a[100:103], a[8:11], a[52:55], a[100:103]
v_mfma_f32_16x16x32_bf16 a[104:107], a[8:11], a[56:59], a[104:107]
v_mfma_f32_16x16x32_bf16 a[108:111], a[8:11], a[60:63], a[108:111]
v_mfma_f32_16x16x32_bf16 a[112:115], a[12:15], a[48:51], a[112:115]
v_mfma_f32_16x16x32_bf16 a[116:119], a[12:15], a[52:55], a[116:119]
v_mfma_f32_16x16x32_bf16 a[120:123], a[12:15], a[56:59], a[120:123]
v_mfma_f32_16x16x32_bf16 a[124:127], a[12:15], a[60:63], a[124:127]
buffer_load_dwordx4 a[248:251], v2, s[88:91], 0 offen offset:16
v_mfma_f32_16x16x32_bf16 a[128:131], a[16:19], a[48:51], a[128:131]
v_mfma_f32_16x16x32_bf16 a[132:135], a[16:19], a[52:55], a[132:135]
v_mfma_f32_16x16x32_bf16 a[136:139], a[16:19], a[56:59], a[136:139]
v_mfma_f32_16x16x32_bf16 a[140:143], a[16:19], a[60:63], a[140:143]
v_mfma_f32_16x16x32_bf16 a[144:147], a[20:23], a[48:51], a[144:147]
v_mfma_f32_16x16x32_bf16 v[28:31], v[124:127], v[124:127], v[28:31]
v_mfma_f32_16x16x32_bf16 a[148:151], a[20:23], a[52:55], a[148:151]
v_mfma_f32_16x16x32_bf16 a[152:155], a[20:23], a[56:59], a[152:155]
v_mfma_f32_16x16x32_bf16 a[156:159], a[20:23], a[60:63], a[156:159]
v_mfma_f32_16x16x32_bf16 a[160:163], a[24:27], a[48:51], a[160:163]
v_mfma_f32_16x16x32_bf16 a[164:167], a[24:27], a[52:55], a[164:167]
v_mfma_f32_16x16x32_bf16 a[168:171], a[24:27], a[56:59], a[168:171]
v_mfma_f32_16x16x32_bf16 a[172:175], a[24:27], a[60:63], a[172:175]
s_waitcnt lgkmcnt(0)
v_mfma_f32_16x16x32_bf16 a[176:179], a[28:31], a[48:51], a[176:179]
v_mfma_f32_16x16x32_bf16 a[180:183], a[28:31], a[52:55], a[180:183]
v_mfma_f32_16x16x32_bf16 a[184:187], a[28:31], a[56:59], a[184:187]
v_mfma_f32_16x16x32_bf16 a[188:191], a[28:31], a[60:63], a[188:191]
s_nop 7
v_max3_f32 v22, v22, v28, v29
v_max3_f32 v22, v22, v30, v31
s_nop 1
s_mov_b32 s30, 0x3e0293ee
v_accvgpr_read_b32 v32, a64
v_accvgpr_read_b32 v33, a65
v_accvgpr_read_b32 v34, a66
v_accvgpr_read_b32 v35, a67
v_accvgpr_read_b32 v36, a80
v_accvgpr_read_b32 v37, a81
v_accvgpr_read_b32 v38, a82
v_accvgpr_read_b32 v39, a83
v_pk_mul_f32 v[32:33], v[32:33], s[30:31] op_sel_hi:[1,0]
v_pk_mul_f32 v[34:35], v[34:35], s[30:31] op_sel_hi:[1,0]
v_pk_mul_f32 v[36:37], v[36:37], s[30:31] op_sel_hi:[1,0]
v_pk_mul_f32 v[38:39], v[38:39], s[30:31] op_sel_hi:[1,0]
v_cvt_pk_bf16_f32 v192, v32, v33
v_cvt_pk_bf16_f32 v193, v34, v35
v_cvt_pk_bf16_f32 v194, v36, v37
v_cvt_pk_bf16_f32 v195, v38, v39
v_accvgpr_read_b32 v40, a96
v_accvgpr_read_b32 v41, a97
v_accvgpr_read_b32 v42, a98
v_accvgpr_read_b32 v43, a99
v_accvgpr_read_b32 v44, a112
v_accvgpr_read_b32 v45, a113
v_accvgpr_read_b32 v46, a114
v_accvgpr_read_b32 v47, a115
v_pk_mul_f32 v[40:41], v[40:41], s[30:31] op_sel_hi:[1,0]
v_pk_mul_f32 v[42:43], v[42:43], s[30:31] op_sel_hi:[1,0]
v_pk_mul_f32 v[44:45], v[44:45], s[30:31] op_sel_hi:[1,0]
v_pk_mul_f32 v[46:47], v[46:47], s[30:31] op_sel_hi:[1,0]
v_cvt_pk_bf16_f32 v196, v40, v41
v_cvt_pk_bf16_f32 v197, v42, v43
v_cvt_pk_bf16_f32 v198, v44, v45
v_cvt_pk_bf16_f32 v199, v46, v47
v_accvgpr_read_b32 v32, a128
v_accvgpr_read_b32 v33, a129
v_accvgpr_read_b32 v34, a130
v_accvgpr_read_b32 v35, a131
v_accvgpr_read_b32 v36, a144
v_accvgpr_read_b32 v37, a145
v_accvgpr_read_b32 v38, a146
v_accvgpr_read_b32 v39, a147
v_pk_mul_f32 v[32:33], v[32:33], s[30:31] op_sel_hi:[1,0]
v_pk_mul_f32 v[34:35], v[34:35], s[30:31] op_sel_hi:[1,0]
v_pk_mul_f32 v[36:37], v[36:37], s[30:31] op_sel_hi:[1,0]
v_pk_mul_f32 v[38:39], v[38:39], s[30:31] op_sel_hi:[1,0]
v_cvt_pk_bf16_f32 v200, v32, v33
v_cvt_pk_bf16_f32 v201, v34, v35
v_cvt_pk_bf16_f32 v202, v36, v37
v_cvt_pk_bf16_f32 v203, v38, v39
v_accvgpr_read_b32 v40, a160
v_accvgpr_read_b32 v41, a161
v_accvgpr_read_b32 v42, a162
v_accvgpr_read_b32 v43, a163
v_accvgpr_read_b32 v44, a176
v_accvgpr_read_b32 v45, a177
v_accvgpr_read_b32 v46, a178
v_accvgpr_read_b32 v47, a179
v_pk_mul_f32 v[40:41], v[40:41], s[30:31] op_sel_hi:[1,0]
v_pk_mul_f32 v[42:43], v[42:43], s[30:31] op_sel_hi:[1,0]
v_pk_mul_f32 v[44:45], v[44:45], s[30:31] op_sel_hi:[1,0]
v_pk_mul_f32 v[46:47], v[46:47], s[30:31] op_sel_hi:[1,0]
v_cvt_pk_bf16_f32 v204, v40, v41
v_cvt_pk_bf16_f32 v205, v42, v43
v_cvt_pk_bf16_f32 v206, v44, v45
v_cvt_pk_bf16_f32 v207, v46, v47
v_accvgpr_read_b32 v32, a68
v_accvgpr_read_b32 v33, a69
v_accvgpr_read_b32 v34, a70
v_accvgpr_read_b32 v35, a71
v_accvgpr_read_b32 v36, a84
v_accvgpr_read_b32 v37, a85
v_accvgpr_read_b32 v38, a86
v_accvgpr_read_b32 v39, a87
v_pk_mul_f32 v[32:33], v[32:33], s[30:31] op_sel_hi:[1,0]
v_pk_mul_f32 v[34:35], v[34:35], s[30:31] op_sel_hi:[1,0]
v_pk_mul_f32 v[36:37], v[36:37], s[30:31] op_sel_hi:[1,0]
v_pk_mul_f32 v[38:39], v[38:39], s[30:31] op_sel_hi:[1,0]
v_cvt_pk_bf16_f32 v208, v32, v33
v_cvt_pk_bf16_f32 v209, v34, v35
v_cvt_pk_bf16_f32 v210, v36, v37
v_cvt_pk_bf16_f32 v211, v38, v39
v_accvgpr_read_b32 v40, a100
v_accvgpr_read_b32 v41, a101
v_accvgpr_read_b32 v42, a102
v_accvgpr_read_b32 v43, a103
v_accvgpr_read_b32 v44, a116
v_accvgpr_read_b32 v45, a117
v_accvgpr_read_b32 v46, a118
v_accvgpr_read_b32 v47, a119
v_pk_mul_f32 v[40:41], v[40:41], s[30:31] op_sel_hi:[1,0]
v_pk_mul_f32 v[42:43], v[42:43], s[30:31] op_sel_hi:[1,0]
v_pk_mul_f32 v[44:45], v[44:45], s[30:31] op_sel_hi:[1,0]
v_pk_mul_f32 v[46:47], v[46:47], s[30:31] op_sel_hi:[1,0]
v_cvt_pk_bf16_f32 v212, v40, v41
v_cvt_pk_bf16_f32 v213, v42, v43
v_cvt_pk_bf16_f32 v214, v44, v45
v_cvt_pk_bf16_f32 v215, v46, v47
v_accvgpr_read_b32 v32, a132
v_accvgpr_read_b32 v33, a133
v_accvgpr_read_b32 v34, a134
v_accvgpr_read_b32 v35, a135
v_accvgpr_read_b32 v36, a148
v_accvgpr_read_b32 v37, a149
v_accvgpr_read_b32 v38, a150
v_accvgpr_read_b32 v39, a151
v_pk_mul_f32 v[32:33], v[32:33], s[30:31] op_sel_hi:[1,0]
v_pk_mul_f32 v[34:35], v[34:35], s[30:31] op_sel_hi:[1,0]
v_pk_mul_f32 v[36:37], v[36:37], s[30:31] op_sel_hi:[1,0]
v_pk_mul_f32 v[38:39], v[38:39], s[30:31] op_sel_hi:[1,0]
v_cvt_pk_bf16_f32 v216, v32, v33
v_cvt_pk_bf16_f32 v217, v34, v35
v_cvt_pk_bf16_f32 v218, v36, v37
v_cvt_pk_bf16_f32 v219, v38, v39
v_accvgpr_read_b32 v40, a164
v_accvgpr_read_b32 v41, a165
v_accvgpr_read_b32 v42, a166
v_accvgpr_read_b32 v43, a167
v_accvgpr_read_b32 v44, a180
v_accvgpr_read_b32 v45, a181
v_accvgpr_read_b32 v46, a182
v_accvgpr_read_b32 v47, a183
v_pk_mul_f32 v[40:41], v[40:41], s[30:31] op_sel_hi:[1,0]
v_pk_mul_f32 v[42:43], v[42:43], s[30:31] op_sel_hi:[1,0]
v_pk_mul_f32 v[44:45], v[44:45], s[30:31] op_sel_hi:[1,0]
v_pk_mul_f32 v[46:47], v[46:47], s[30:31] op_sel_hi:[1,0]
v_cvt_pk_bf16_f32 v220, v40, v41
v_cvt_pk_bf16_f32 v221, v42, v43
v_cvt_pk_bf16_f32 v222, v44, v45
v_cvt_pk_bf16_f32 v223, v46, v47
v_accvgpr_read_b32 v32, a72
v_accvgpr_read_b32 v33, a73
v_accvgpr_read_b32 v34, a74
v_accvgpr_read_b32 v35, a75
v_accvgpr_read_b32 v36, a88
v_accvgpr_read_b32 v37, a89
v_accvgpr_read_b32 v38, a90
v_accvgpr_read_b32 v39, a91
v_pk_mul_f32 v[32:33], v[32:33], s[30:31] op_sel_hi:[1,0]
v_pk_mul_f32 v[34:35], v[34:35], s[30:31] op_sel_hi:[1,0]
v_pk_mul_f32 v[36:37], v[36:37], s[30:31] op_sel_hi:[1,0]
v_pk_mul_f32 v[38:39], v[38:39], s[30:31] op_sel_hi:[1,0]
v_cvt_pk_bf16_f32 v224, v32, v33
v_cvt_pk_bf16_f32 v225, v34, v35
v_cvt_pk_bf16_f32 v226, v36, v37
v_cvt_pk_bf16_f32 v227, v38, v39
v_accvgpr_read_b32 v40, a104
v_accvgpr_read_b32 v41, a105
v_accvgpr_read_b32 v42, a106
v_accvgpr_read_b32 v43, a107
v_accvgpr_read_b32 v44, a120
v_accvgpr_read_b32 v45, a121
v_accvgpr_read_b32 v46, a122
v_accvgpr_read_b32 v47, a123
v_pk_mul_f32 v[40:41], v[40:41], s[30:31] op_sel_hi:[1,0]
v_pk_mul_f32 v[42:43], v[42:43], s[30:31] op_sel_hi:[1,0]
v_pk_mul_f32 v[44:45], v[44:45], s[30:31] op_sel_hi:[1,0]
v_pk_mul_f32 v[46:47], v[46:47], s[30:31] op_sel_hi:[1,0]
v_cvt_pk_bf16_f32 v228, v40, v41
v_cvt_pk_bf16_f32 v229, v42, v43
v_cvt_pk_bf16_f32 v230, v44, v45
v_cvt_pk_bf16_f32 v231, v46, v47
v_accvgpr_read_b32 v32, a136
v_accvgpr_read_b32 v33, a137
v_accvgpr_read_b32 v34, a138
v_accvgpr_read_b32 v35, a139
v_accvgpr_read_b32 v36, a152
v_accvgpr_read_b32 v37, a153
v_accvgpr_read_b32 v38, a154
v_accvgpr_read_b32 v39, a155
v_pk_mul_f32 v[32:33], v[32:33], s[30:31] op_sel_hi:[1,0]
v_pk_mul_f32 v[34:35], v[34:35], s[30:31] op_sel_hi:[1,0]
v_pk_mul_f32 v[36:37], v[36:37], s[30:31] op_sel_hi:[1,0]
v_pk_mul_f32 v[38:39], v[38:39], s[30:31] op_sel_hi:[1,0]
v_cvt_pk_bf16_f32 v232, v32, v33
v_cvt_pk_bf16_f32 v233, v34, v35
v_cvt_pk_bf16_f32 v234, v36, v37
v_cvt_pk_bf16_f32 v235, v38, v39
v_accvgpr_read_b32 v40, a168
v_accvgpr_read_b32 v41, a169
v_accvgpr_read_b32 v42, a170
v_accvgpr_read_b32 v43, a171
v_accvgpr_read_b32 v44, a184
v_accvgpr_read_b32 v45, a185
v_accvgpr_read_b32 v46, a186
v_accvgpr_read_b32 v47, a187
v_pk_mul_f32 v[40:41], v[40:41], s[30:31] op_sel_hi:[1,0]
v_pk_mul_f32 v[42:43], v[42:43], s[30:31] op_sel_hi:[1,0]
v_pk_mul_f32 v[44:45], v[44:45], s[30:31] op_sel_hi:[1,0]
v_pk_mul_f32 v[46:47], v[46:47], s[30:31] op_sel_hi:[1,0]
v_cvt_pk_bf16_f32 v236, v40, v41
v_cvt_pk_bf16_f32 v237, v42, v43
v_cvt_pk_bf16_f32 v238, v44, v45
v_cvt_pk_bf16_f32 v239, v46, v47
v_accvgpr_read_b32 v32, a76
v_accvgpr_read_b32 v33, a77
v_accvgpr_read_b32 v34, a78
v_accvgpr_read_b32 v35, a79
v_accvgpr_read_b32 v36, a92
v_accvgpr_read_b32 v37, a93
v_accvgpr_read_b32 v38, a94
v_accvgpr_read_b32 v39, a95
v_pk_mul_f32 v[32:33], v[32:33], s[30:31] op_sel_hi:[1,0]
v_pk_mul_f32 v[34:35], v[34:35], s[30:31] op_sel_hi:[1,0]
v_pk_mul_f32 v[36:37], v[36:37], s[30:31] op_sel_hi:[1,0]
v_pk_mul_f32 v[38:39], v[38:39], s[30:31] op_sel_hi:[1,0]
v_cvt_pk_bf16_f32 v240, v32, v33
v_cvt_pk_bf16_f32 v241, v34, v35
v_cvt_pk_bf16_f32 v242, v36, v37
v_cvt_pk_bf16_f32 v243, v38, v39
v_accvgpr_read_b32 v40, a108
v_accvgpr_read_b32 v41, a109
v_accvgpr_read_b32 v42, a110
v_accvgpr_read_b32 v43, a111
v_accvgpr_read_b32 v44, a124
v_accvgpr_read_b32 v45, a125
v_accvgpr_read_b32 v46, a126
v_accvgpr_read_b32 v47, a127
v_pk_mul_f32 v[40:41], v[40:41], s[30:31] op_sel_hi:[1,0]
v_pk_mul_f32 v[42:43], v[42:43], s[30:31] op_sel_hi:[1,0]
v_pk_mul_f32 v[44:45], v[44:45], s[30:31] op_sel_hi:[1,0]
v_pk_mul_f32 v[46:47], v[46:47], s[30:31] op_sel_hi:[1,0]
v_cvt_pk_bf16_f32 v244, v40, v41
v_cvt_pk_bf16_f32 v245, v42, v43
v_cvt_pk_bf16_f32 v246, v44, v45
v_cvt_pk_bf16_f32 v247, v46, v47
v_accvgpr_read_b32 v32, a140
v_accvgpr_read_b32 v33, a141
v_accvgpr_read_b32 v34, a142
v_accvgpr_read_b32 v35, a143
v_accvgpr_read_b32 v36, a156
v_accvgpr_read_b32 v37, a157
v_accvgpr_read_b32 v38, a158
v_accvgpr_read_b32 v39, a159
v_pk_mul_f32 v[32:33], v[32:33], s[30:31] op_sel_hi:[1,0]
v_pk_mul_f32 v[34:35], v[34:35], s[30:31] op_sel_hi:[1,0]
v_pk_mul_f32 v[36:37], v[36:37], s[30:31] op_sel_hi:[1,0]
v_pk_mul_f32 v[38:39], v[38:39], s[30:31] op_sel_hi:[1,0]
v_cvt_pk_bf16_f32 v248, v32, v33
v_cvt_pk_bf16_f32 v249, v34, v35
v_cvt_pk_bf16_f32 v250, v36, v37
v_cvt_pk_bf16_f32 v251, v38, v39
v_accvgpr_read_b32 v40, a172
v_accvgpr_read_b32 v41, a173
v_accvgpr_read_b32 v42, a174
v_accvgpr_read_b32 v43, a175
v_accvgpr_read_b32 v44, a188
v_accvgpr_read_b32 v45, a189
v_accvgpr_read_b32 v46, a190
v_accvgpr_read_b32 v47, a191
v_pk_mul_f32 v[40:41], v[40:41], s[30:31] op_sel_hi:[1,0]
v_pk_mul_f32 v[42:43], v[42:43], s[30:31] op_sel_hi:[1,0]
v_pk_mul_f32 v[44:45], v[44:45], s[30:31] op_sel_hi:[1,0]
v_pk_mul_f32 v[46:47], v[46:47], s[30:31] op_sel_hi:[1,0]
v_cvt_pk_bf16_f32 v252, v40, v41
v_cvt_pk_bf16_f32 v253, v42, v43
v_cvt_pk_bf16_f32 v254, v44, v45
v_cvt_pk_bf16_f32 v255, v46, v47
v_mov_b32 v6, 0
v_mfma_f32_16x16x32_bf16 v[24:27], v[192:195], v[192:195], 0
v_mfma_f32_16x16x32_bf16 v[24:27], v[196:199], v[196:199], v[24:27]
v_mfma_f32_16x16x32_bf16 v[24:27], v[200:203], v[200:203], v[24:27]
v_mfma_f32_16x16x32_bf16 v[24:27], v[204:207], v[204:207], v[24:27]
v_mfma_f32_16x16x32_bf16 v[28:31], v[208:211], v[208:211], 0
v_mfma_f32_16x16x32_bf16 v[28:31], v[212:215], v[212:215], v[28:31]
v_mfma_f32_16x16x32_bf16 v[28:31], v[216:219], v[216:219], v[28:31]
v_mfma_f32_16x16x32_bf16 v[28:31], v[220:223], v[220:223], v[28:31]
s_nop 3
v_max3_f32 v6, v6, v24, v25
v_max3_f32 v6, v6, v26, v27
v_mfma_f32_16x16x32_bf16 v[24:27], v[224:227], v[224:227], 0
v_mfma_f32_16x16x32_bf16 v[24:27], v[228:231], v[228:231], v[24:27]
v_mfma_f32_16x16x32_bf16 v[24:27], v[232:235], v[232:235], v[24:27]
v_mfma_f32_16x16x32_bf16 v[24:27], v[236:239], v[236:239], v[24:27]
s_nop 3
v_max3_f32 v6, v6, v28, v29
v_max3_f32 v6, v6, v30, v31
v_mfma_f32_16x16x32_bf16 v[28:31], v[240:243], v[240:243], 0
v_mfma_f32_16x16x32_bf16 v[28:31], v[244:247], v[244:247], v[28:31]
v_mfma_f32_16x16x32_bf16 v[28:31], v[248:251], v[248:251], v[28:31]
v_mfma_f32_16x16x32_bf16 v[28:31], v[252:255], v[252:255], v[28:31]
s_nop 3
v_max3_f32 v6, v6, v24, v25
v_max3_f32 v6, v6, v26, v27
s_nop 7
v_max3_f32 v6, v6, v28, v29
v_max3_f32 v6, v6, v30, v31
s_waitcnt vmcnt(0)
s_branch A_fin_%=
A_V_%=:
ds_read_b128 a[0:3], v16 offset:0
ds_read_b64_tr_b16 a[32:33], v18 offset:0
ds_read_b64_tr_b16 a[34:35], v19 offset:0
ds_read_b128 a[4:7], v16 offset:2048
ds_read_b64_tr_b16 a[36:37], v20 offset:0
ds_read_b64_tr_b16 a[38:39], v21 offset:0
ds_read_b128 a[8:11], v16 offset:4096
ds_read_b64_tr_b16 a[40:41], v18 offset:512
ds_read_b64_tr_b16 a[42:43], v19 offset:512
ds_read_b128 a[12:15], v16 offset:6144
ds_read_b64_tr_b16 a[44:45], v20 offset:512
ds_read_b64_tr_b16 a[46:47], v21 offset:512
ds_read_b128 a[16:19], v16 offset:8192
ds_read_b64_tr_b16 a[48:49], v18 offset:1024
ds_read_b64_tr_b16 a[50:51], v19 offset:1024
ds_read_b128 a[20:23], v16 offset:10240
ds_read_b64_tr_b16 a[52:53], v20 offset:1024
ds_read_b64_tr_b16 a[54:55], v21 offset:1024
ds_read_b128 a[24:27], v16 offset:12288
ds_read_b64_tr_b16 a[56:57], v18 offset:1536
ds_read_b64_tr_b16 a[58:59], v19 offset:1536
ds_read_b128 a[28:31], v16 offset:14336
ds_read_b64_tr_b16 a[60:61], v20 offset:1536
ds_read_b64_tr_b16 a[62:63], v21 offset:1536
s_waitcnt vmcnt(9) lgkmcnt(0)
s_barrier
v_mfma_f32_16x16x32_bf16 v[64:67], a[0:3], a[32:35], 0
ds_read_b128 a[64:67], v17 offset:0
v_mfma_f32_16x16x32_bf16 v[68:71], a[0:3], a[36:39], 0
s_add_u32 m0, s53, 0
v_mfma_f32_16x16x32_bf16 v[72:75], a[0:3], a[40:43], 0
ds_read_b64_tr_b16 a[96:97], v18 offset:8192
v_mfma_f32_16x16x32_bf16 v[76:79], a[0:3], a[44:47], 0
v_mfma_f32_16x16x32_bf16 v[80:83], a[0:3], a[48:51], 0
ds_read_b64_tr_b16 a[98:99], v19 offset:8192
v_mfma_f32_16x16x32_bf16 v[84:87], a[0:3], a[52:55], 0
buffer_load_dwordx4 v12, s[40:43], s56 offen offset:0 lds
v_mfma_f32_16x16x32_bf16 v[88:91], a[0:3], a[56:59], 0
ds_read_b128 a[68:71], v17 offset:2048
v_mfma_f32_16x16x32_bf16 v[92:95], a[0:3], a[60:63], 0
v_mfma_f32_16x16x32_bf16 v[96:99], a[4:7], a[32:35], 0
ds_read_b64_tr_b16 a[100:101], v20 offset:8192
v_mfma_f32_16x16x32_bf16 v[100:103], a[4:7], a[36:39], 0
buffer_load_dwordx4 v13, s[40:43], s56 offen offset:1024 lds
v_mfma_f32_16x16x32_bf16 v[104:107], a[4:7], a[40:43], 0
ds_read_b64_tr_b16 a[102:103], v21 offset:8192
v_mfma_f32_16x16x32_bf16 v[108:111], a[4:7], a[44:47], 0
v_mfma_f32_16x16x32_bf16 v[112:115], a[4:7], a[48:51], 0
ds_read_b128 a[72:75], v17 offset:4096
v_mfma_f32_16x16x32_bf16 v[116:119], a[4:7], a[52:55], 0
v_mfma_f32_16x16x32_bf16 v[120:123], a[4:7], a[56:59], 0
ds_read_b64_tr_b16 a[104:105], v18 offset:8704
v_mfma_f32_16x16x32_bf16 v[124:127], a[4:7], a[60:63], 0
s_add_u32 m0, s53, 0x4000
v_mfma_f32_16x16x32_bf16 v[128:131], a[8:11], a[32:35], 0
ds_read_b64_tr_b16 a[106:107], v19 offset:8704
v_mfma_f32_16x16x32_bf16 v[132:135], a[8:11], a[36:39], 0
v_mfma_f32_16x16x32_bf16 v[136:139], a[8:11], a[40:43], 0
ds_read_b128 a[76:79], v17 offset:6144
v_mfma_f32_16x16x32_bf16 v[140:143], a[8:11], a[44:47], 0
buffer_load_dwordx4 v12, s[44:47], s56 offen offset:0 lds
v_mfma_f32_16x16x32_bf16 v[144:147], a[8:11], a[48:51], 0
ds_read_b64_tr_b16 a[108:109], v20 offset:8704
v_mfma_f32_16x16x32_bf16 v[148:151], a[8:11], a[52:55], 0
v_mfma_f32_16x16x32_bf16 v[152:155], a[8:11], a[56:59], 0
ds_read_b64_tr_b16 a[110:111], v21 offset:8704
v_mfma_f32_16x16x32_bf16 v[156:159], a[8:11], a[60:63], 0
buffer_load_dwordx4 v13, s[44:47], s56 offen offset:1024 lds
v_mfma_f32_16x16x32_bf16 v[160:163], a[12:15], a[32:35], 0
ds_read_b128 a[80:83], v17 offset:8192
v_mfma_f32_16x16x32_bf16 v[164:167], a[12:15], a[36:39], 0
v_mfma_f32_16x16x32_bf16 v[168:171], a[12:15], a[40:43], 0
ds_read_b64_tr_b16 a[112:113], v18 offset:9216
v_mfma_f32_16x16x32_bf16 v[172:175], a[12:15], a[44:47], 0
s_xor_b32 s53, s53, 0x10000
s_add_u32 s56, s56, 0x80000
v_mfma_f32_16x16x32_bf16 v[176:179], a[12:15], a[48:51], 0
ds_read_b64_tr_b16 a[114:115], v19 offset:9216
v_mfma_f32_16x16x32_bf16 v[180:183], a[12:15], a[52:55], 0
v_mfma_f32_16x16x32_bf16 v[184:187], a[12:15], a[56:59], 0
ds_read_b128 a[84:87], v17 offset:10240
v_mfma_f32_16x16x32_bf16 v[188:191], a[12:15], a[60:63], 0
s_add_u32 m0, s69, 0
v_mfma_f32_16x16x32_bf16 v[192:195], a[16:19], a[32:35], 0
ds_read_b64_tr_b16 a[116:117], v20 offset:9216
v_mfma_f32_16x16x32_bf16 v[196:199], a[16:19], a[36:39], 0
v_mfma_f32_16x16x32_bf16 v[200:203], a[16:19], a[40:43], 0
ds_read_b64_tr_b16 a[118:119], v21 offset:9216
v_mfma_f32_16x16x32_bf16 v[204:207], a[16:19], a[44:47], 0
buffer_load_dwordx4 v12, s[40:43], s68 offen offset:0 lds
v_mfma_f32_16x16x32_bf16 v[208:211], a[16:19], a[48:51], 0
ds_read_b128 a[88:91], v17 offset:12288
v_mfma_f32_16x16x32_bf16 v[212:215], a[16:19], a[52:55], 0
v_mfma_f32_16x16x32_bf16 v[216:219], a[16:19], a[56:59], 0
ds_read_b64_tr_b16 a[120:121], v18 offset:9728
v_mfma_f32_16x16x32_bf16 v[220:223], a[16:19], a[60:63], 0
v_mfma_f32_16x16x32_bf16 v[224:227], a[20:23], a[32:35], 0
ds_read_b64_tr_b16 a[122:123], v19 offset:9728
v_mfma_f32_16x16x32_bf16 v[228:231], a[20:23], a[36:39], 0
buffer_load_dwordx4 v13, s[40:43], s68 offen offset:1024 lds
v_mfma_f32_16x16x32_bf16 v[232:235], a[20:23], a[40:43], 0
ds_read_b128 a[92:95], v17 offset:14336
v_mfma_f32_16x16x32_bf16 v[236:239], a[20:23], a[44:47], 0
v_mfma_f32_16x16x32_bf16 v[240:243], a[20:23], a[48:51], 0
ds_read_b64_tr_b16 a[124:125], v20 offset:9728
v_mfma_f32_16x16x32_bf16 v[244:247], a[20:23], a[52:55], 0
s_add_u32 m0, s69, 0x4000
v_mfma_f32_16x16x32_bf16 v[248:251], a[20:23], a[56:59], 0
ds_read_b64_tr_b16 a[126:127], v21 offset:9728
v_mfma_f32_16x16x32_bf16 v[252:255], a[20:23], a[60:63], 0
v_mfma_f32_16x16x32_bf16 a[128:131], a[24:27], a[32:35], 0
buffer_load_dwordx4 v12, s[44:47], s68 offen offset:0 lds
v_mfma_f32_16x16x32_bf16 a[132:135], a[24:27], a[36:39], 0
v_mfma_f32_16x16x32_bf16 a[136:139], a[24:27], a[40:43], 0
v_mfma_f32_16x16x32_bf16 a[140:143], a[24:27], a[44:47], 0
v_mfma_f32_16x16x32_bf16 a[144:147], a[24:27], a[48:51], 0
v_mfma_f32_16x16x32_bf16 a[148:151], a[24:27], a[52:55], 0
buffer_load_dwordx4 v13, s[44:47], s68 offen offset:1024 lds
v_mfma_f32_16x16x32_bf16 a[152:155], a[24:27], a[56:59], 0
v_mfma_f32_16x16x32_bf16 a[156:159], a[24:27], a[60:63], 0
v_mfma_f32_16x16x32_bf16 a[160:163], a[28:31], a[32:35], 0
v_mfma_f32_16x16x32_bf16 a[164:167], a[28:31], a[36:39], 0
s_xor_b32 s69, s69, 0x10000
s_add_u32 s68, s68, 0x80000
v_mfma_f32_16x16x32_bf16 a[168:171], a[28:31], a[40:43], 0
v_mfma_f32_16x16x32_bf16 a[172:175], a[28:31], a[44:47], 0
v_mfma_f32_16x16x32_bf16 a[176:179], a[28:31], a[48:51], 0
v_mfma_f32_16x16x32_bf16 a[180:183], a[28:31], a[52:55], 0
v_mfma_f32_16x16x32_bf16 a[184:187], a[28:31], a[56:59], 0
v_mfma_f32_16x16x32_bf16 a[188:191], a[28:31], a[60:63], 0
s_waitcnt vmcnt(9) lgkmcnt(0)
s_barrier
v_xor_b32 v16, 0x10000, v16
v_xor_b32 v17, 0x10000, v17
v_xor_b32 v18, 0x10000, v18
v_xor_b32 v19, 0x10000, v19
v_xor_b32 v20, 0x10000, v20
v_xor_b32 v21, 0x10000, v21
v_mfma_f32_16x16x32_bf16 v[64:67], a[64:67], a[96:99], v[64:67]
ds_read_b128 a[0:3], v16 offset:0
v_mfma_f32_16x16x32_bf16 v[68:71], a[64:67], a[100:103], v[68:71]
s_add_u32 m0, s52, 0
v_mfma_f32_16x16x32_bf16 v[72:75], a[64:67], a[104:107], v[72:75]
ds_read_b64_tr_b16 a[32:33], v18 offset:0
v_mfma_f32_16x16x32_bf16 v[76:79], a[64:67], a[108:111], v[76:79]
v_mfma_f32_16x16x32_bf16 v[80:83], a[64:67], a[112:115], v[80:83]
ds_read_b64_tr_b16 a[34:35], v19 offset:0
v_mfma_f32_16x16x32_bf16 v[84:87], a[64:67], a[116:119], v[84:87]
v_mfma_f32_16x16x32_bf16 v[88:91], a[64:67], a[120:123], v[88:91]
ds_read_b128 a[4:7], v16 offset:2048
v_mfma_f32_16x16x32_bf16 v[92:95], a[64:67], a[124:127], v[92:95]
buffer_load_dwordx4 v8, s[36:39], s54 offen offset:0 lds
v_mfma_f32_16x16x32_bf16 v[96:99], a[68:71], a[96:99], v[96:99]
ds_read_b64_tr_b16 a[36:37], v20 offset:0
v_mfma_f32_16x16x32_bf16 v[100:103], a[68:71], a[100:103], v[100:103]
v_mfma_f32_16x16x32_bf16 v[104:107], a[68:71], a[104:107], v[104:107]
ds_read_b64_tr_b16 a[38:39], v21 offset:0
v_mfma_f32_16x16x32_bf16 v[108:111], a[68:71], a[108:111], v[108:111]
v_mfma_f32_16x16x32_bf16 v[112:115], a[68:71], a[112:115], v[112:115]
ds_read_b128 a[8:11], v16 offset:4096
v_mfma_f32_16x16x32_bf16 v[116:119], a[68:71], a[116:119], v[116:119]
buffer_load_dwordx4 v9, s[36:39], s54 offen offset:1024 lds
v_mfma_f32_16x16x32_bf16 v[120:123], a[68:71], a[120:123], v[120:123]
ds_read_b64_tr_b16 a[40:41], v18 offset:512
v_mfma_f32_16x16x32_bf16 v[124:127], a[68:71], a[124:127], v[124:127]
v_mfma_f32_16x16x32_bf16 v[128:131], a[72:75], a[96:99], v[128:131]
ds_read_b64_tr_b16 a[42:43], v19 offset:512
v_mfma_f32_16x16x32_bf16 v[132:135], a[72:75], a[100:103], v[132:135]
buffer_load_dwordx4 v10, s[36:39], s54 offen offset:2048 lds
v_mfma_f32_16x16x32_bf16 v[136:139], a[72:75], a[104:107], v[136:139]
ds_read_b128 a[12:15], v16 offset:6144
v_mfma_f32_16x16x32_bf16 v[140:143], a[72:75], a[108:111], v[140:143]
v_mfma_f32_16x16x32_bf16 v[144:147], a[72:75], a[112:115], v[144:147]
ds_read_b64_tr_b16 a[44:45], v20 offset:512
v_mfma_f32_16x16x32_bf16 v[148:151], a[72:75], a[116:119], v[148:151]
v_mfma_f32_16x16x32_bf16 v[152:155], a[72:75], a[120:123], v[152:155]
ds_read_b64_tr_b16 a[46:47], v21 offset:512
v_mfma_f32_16x16x32_bf16 v[156:159], a[72:75], a[124:127], v[156:159]
buffer_load_dwordx4 v11, s[36:39], s54 offen offset:3072 lds
v_mfma_f32_16x16x32_bf16 v[160:163], a[76:79], a[96:99], v[160:163]
ds_read_b128 a[16:19], v16 offset:8192
v_mfma_f32_16x16x32_bf16 v[164:167], a[76:79], a[100:103], v[164:167]
v_mfma_f32_16x16x32_bf16 v[168:171], a[76:79], a[104:107], v[168:171]
ds_read_b64_tr_b16 a[48:49], v18 offset:1024
v_mfma_f32_16x16x32_bf16 v[172:175], a[76:79], a[108:111], v[172:175]
v_mfma_f32_16x16x32_bf16 v[176:179], a[76:79], a[112:115], v[176:179]
ds_read_b64_tr_b16 a[50:51], v19 offset:1024
v_mfma_f32_16x16x32_bf16 v[180:183], a[76:79], a[116:119], v[180:183]
s_add_u32 m0, s52, 0x1000
v_mfma_f32_16x16x32_bf16 v[184:187], a[76:79], a[120:123], v[184:187]
ds_read_b128 a[20:23], v16 offset:10240
v_mfma_f32_16x16x32_bf16 v[188:191], a[76:79], a[124:127], v[188:191]
v_mfma_f32_16x16x32_bf16 v[192:195], a[80:83], a[96:99], v[192:195]
ds_read_b64_tr_b16 a[52:53], v20 offset:1024
v_mfma_f32_16x16x32_bf16 v[196:199], a[80:83], a[100:103], v[196:199]
v_mfma_f32_16x16x32_bf16 v[200:203], a[80:83], a[104:107], v[200:203]
ds_read_b64_tr_b16 a[54:55], v21 offset:1024
v_mfma_f32_16x16x32_bf16 v[204:207], a[80:83], a[108:111], v[204:207]
buffer_load_dwordx4 v8, s[36:39], s55 offen offset:0 lds
v_mfma_f32_16x16x32_bf16 v[208:211], a[80:83], a[112:115], v[208:211]
ds_read_b128 a[24:27], v16 offset:12288
v_mfma_f32_16x16x32_bf16 v[212:215], a[80:83], a[116:119], v[212:215]
v_mfma_f32_16x16x32_bf16 v[216:219], a[80:83], a[120:123], v[216:219]
ds_read_b64_tr_b16 a[56:57], v18 offset:1536
v_mfma_f32_16x16x32_bf16 v[220:223], a[80:83], a[124:127], v[220:223]
buffer_load_dwordx4 v9, s[36:39], s55 offen offset:1024 lds
v_mfma_f32_16x16x32_bf16 v[224:227], a[84:87], a[96:99], v[224:227]
ds_read_b64_tr_b16 a[58:59], v19 offset:1536
v_mfma_f32_16x16x32_bf16 v[228:231], a[84:87], a[100:103], v[228:231]
v_mfma_f32_16x16x32_bf16 v[232:235], a[84:87], a[104:107], v[232:235]
ds_read_b128 a[28:31], v16 offset:14336
v_mfma_f32_16x16x32_bf16 v[236:239], a[84:87], a[108:111], v[236:239]
v_mfma_f32_16x16x32_bf16 v[240:243], a[84:87], a[112:115], v[240:243]
ds_read_b64_tr_b16 a[60:61], v20 offset:1536
v_mfma_f32_16x16x32_bf16 v[244:247], a[84:87], a[116:119], v[244:247]
buffer_load_dwordx4 v10, s[36:39], s55 offen offset:2048 lds
v_mfma_f32_16x16x32_bf16 v[248:251], a[84:87], a[120:123], v[248:251]
ds_read_b64_tr_b16 a[62:63], v21 offset:1536
v_mfma_f32_16x16x32_bf16 v[252:255], a[84:87], a[124:127], v[252:255]
v_mfma_f32_16x16x32_bf16 a[128:131], a[88:91], a[96:99], a[128:131]
v_mfma_f32_16x16x32_bf16 a[132:135], a[88:91], a[100:103], a[132:135]
v_mfma_f32_16x16x32_bf16 a[136:139], a[88:91], a[104:107], a[136:139]
buffer_load_dwordx4 v11, s[36:39], s55 offen offset:3072 lds
v_mfma_f32_16x16x32_bf16 a[140:143], a[88:91], a[108:111], a[140:143]
v_mfma_f32_16x16x32_bf16 a[144:147], a[88:91], a[112:115], a[144:147]
v_mfma_f32_16x16x32_bf16 a[148:151], a[88:91], a[116:119], a[148:151]
v_mfma_f32_16x16x32_bf16 a[152:155], a[88:91], a[120:123], a[152:155]
v_mfma_f32_16x16x32_bf16 a[156:159], a[88:91], a[124:127], a[156:159]
v_mfma_f32_16x16x32_bf16 a[160:163], a[92:95], a[96:99], a[160:163]
s_xor_b32 s52, s52, 0x10000
s_add_u32 s54, s54, 0x80
s_add_u32 s55, s55, 0x80
v_mfma_f32_16x16x32_bf16 a[164:167], a[92:95], a[100:103], a[164:167]
v_mfma_f32_16x16x32_bf16 a[168:171], a[92:95], a[104:107], a[168:171]
v_mfma_f32_16x16x32_bf16 a[172:175], a[92:95], a[108:111], a[172:175]
v_mfma_f32_16x16x32_bf16 a[176:179], a[92:95], a[112:115], a[176:179]
v_mfma_f32_16x16x32_bf16 a[180:183], a[92:95], a[116:119], a[180:183]
v_mfma_f32_16x16x32_bf16 a[184:187], a[92:95], a[120:123], a[184:187]
v_mfma_f32_16x16x32_bf16 a[188:191], a[92:95], a[124:127], a[188:191]
s_waitcnt vmcnt(8) lgkmcnt(0)
s_barrier
v_mfma_f32_16x16x32_bf16 v[64:67], a[0:3], a[32:35], v[64:67]
ds_read_b128 a[64:67], v17 offset:0
v_mfma_f32_16x16x32_bf16 v[68:71], a[0:3], a[36:39], v[68:71]
s_add_u32 m0, s53, 0
v_mfma_f32_16x16x32_bf16 v[72:75], a[0:3], a[40:43], v[72:75]
ds_read_b64_tr_b16 a[96:97], v18 offset:8192
v_mfma_f32_16x16x32_bf16 v[76:79], a[0:3], a[44:47], v[76:79]
v_mfma_f32_16x16x32_bf16 v[80:83], a[0:3], a[48:51], v[80:83]
ds_read_b64_tr_b16 a[98:99], v19 offset:8192
v_mfma_f32_16x16x32_bf16 v[84:87], a[0:3], a[52:55], v[84:87]
buffer_load_dwordx4 v12, s[40:43], s56 offen offset:0 lds
v_mfma_f32_16x16x32_bf16 v[88:91], a[0:3], a[56:59], v[88:91]
ds_read_b128 a[68:71], v17 offset:2048
v_mfma_f32_16x16x32_bf16 v[92:95], a[0:3], a[60:63], v[92:95]
v_mfma_f32_16x16x32_bf16 v[96:99], a[4:7], a[32:35], v[96:99]
ds_read_b64_tr_b16 a[100:101], v20 offset:8192
v_mfma_f32_16x16x32_bf16 v[100:103], a[4:7], a[36:39], v[100:103]
buffer_load_dwordx4 v13, s[40:43], s56 offen offset:1024 lds
v_mfma_f32_16x16x32_bf16 v[104:107], a[4:7], a[40:43], v[104:107]
ds_read_b64_tr_b16 a[102:103], v21 offset:8192
v_mfma_f32_16x16x32_bf16 v[108:111], a[4:7], a[44:47], v[108:111]
v_mfma_f32_16x16x32_bf16 v[112:115], a[4:7], a[48:51], v[112:115]
ds_read_b128 a[72:75], v17 offset:4096
v_mfma_f32_16x16x32_bf16 v[116:119], a[4:7], a[52:55], v[116:119]
v_mfma_f32_16x16x32_bf16 v[120:123], a[4:7], a[56:59], v[120:123]
ds_read_b64_tr_b16 a[104:105], v18 offset:8704
v_mfma_f32_16x16x32_bf16 v[124:127], a[4:7], a[60:63], v[124:127]
s_add_u32 m0, s53, 0x4000
v_mfma_f32_16x16x32_bf16 v[128:131], a[8:11], a[32:35], v[128:131]
ds_read_b64_tr_b16 a[106:107], v19 offset:8704
v_mfma_f32_16x16x32_bf16 v[132:135], a[8:11], a[36:39], v[132:135]
v_mfma_f32_16x16x32_bf16 v[136:139], a[8:11], a[40:43], v[136:139]
ds_read_b128 a[76:79], v17 offset:6144
v_mfma_f32_16x16x32_bf16 v[140:143], a[8:11], a[44:47], v[140:143]
buffer_load_dwordx4 v12, s[44:47], s56 offen offset:0 lds
v_mfma_f32_16x16x32_bf16 v[144:147], a[8:11], a[48:51], v[144:147]
ds_read_b64_tr_b16 a[108:109], v20 offset:8704
v_mfma_f32_16x16x32_bf16 v[148:151], a[8:11], a[52:55], v[148:151]
v_mfma_f32_16x16x32_bf16 v[152:155], a[8:11], a[56:59], v[152:155]
ds_read_b64_tr_b16 a[110:111], v21 offset:8704
v_mfma_f32_16x16x32_bf16 v[156:159], a[8:11], a[60:63], v[156:159]
buffer_load_dwordx4 v13, s[44:47], s56 offen offset:1024 lds
v_mfma_f32_16x16x32_bf16 v[160:163], a[12:15], a[32:35], v[160:163]
ds_read_b128 a[80:83], v17 offset:8192
v_mfma_f32_16x16x32_bf16 v[164:167], a[12:15], a[36:39], v[164:167]
v_mfma_f32_16x16x32_bf16 v[168:171], a[12:15], a[40:43], v[168:171]
ds_read_b64_tr_b16 a[112:113], v18 offset:9216
v_mfma_f32_16x16x32_bf16 v[172:175], a[12:15], a[44:47], v[172:175]
s_xor_b32 s53, s53, 0x10000
s_add_u32 s56, s56, 0x80000
v_mfma_f32_16x16x32_bf16 v[176:179], a[12:15], a[48:51], v[176:179]
ds_read_b64_tr_b16 a[114:115], v19 offset:9216
v_mfma_f32_16x16x32_bf16 v[180:183], a[12:15], a[52:55], v[180:183]
v_mfma_f32_16x16x32_bf16 v[184:187], a[12:15], a[56:59], v[184:187]
ds_read_b128 a[84:87], v17 offset:10240
v_mfma_f32_16x16x32_bf16 v[188:191], a[12:15], a[60:63], v[188:191]
s_add_u32 m0, s69, 0
v_mfma_f32_16x16x32_bf16 v[192:195], a[16:19], a[32:35], v[192:195]
ds_read_b64_tr_b16 a[116:117], v20 offset:9216
v_mfma_f32_16x16x32_bf16 v[196:199], a[16:19], a[36:39], v[196:199]
v_mfma_f32_16x16x32_bf16 v[200:203], a[16:19], a[40:43], v[200:203]
ds_read_b64_tr_b16 a[118:119], v21 offset:9216
v_mfma_f32_16x16x32_bf16 v[204:207], a[16:19], a[44:47], v[204:207]
buffer_load_dwordx4 v12, s[40:43], s68 offen offset:0 lds
v_mfma_f32_16x16x32_bf16 v[208:211], a[16:19], a[48:51], v[208:211]
ds_read_b128 a[88:91], v17 offset:12288
v_mfma_f32_16x16x32_bf16 v[212:215], a[16:19], a[52:55], v[212:215]
v_mfma_f32_16x16x32_bf16 v[216:219], a[16:19], a[56:59], v[216:219]
ds_read_b64_tr_b16 a[120:121], v18 offset:9728
v_mfma_f32_16x16x32_bf16 v[220:223], a[16:19], a[60:63], v[220:223]
v_mfma_f32_16x16x32_bf16 v[224:227], a[20:23], a[32:35], v[224:227]
ds_read_b64_tr_b16 a[122:123], v19 offset:9728
v_mfma_f32_16x16x32_bf16 v[228:231], a[20:23], a[36:39], v[228:231]
buffer_load_dwordx4 v13, s[40:43], s68 offen offset:1024 lds
v_mfma_f32_16x16x32_bf16 v[232:235], a[20:23], a[40:43], v[232:235]
ds_read_b128 a[92:95], v17 offset:14336
v_mfma_f32_16x16x32_bf16 v[236:239], a[20:23], a[44:47], v[236:239]
v_mfma_f32_16x16x32_bf16 v[240:243], a[20:23], a[48:51], v[240:243]
ds_read_b64_tr_b16 a[124:125], v20 offset:9728
v_mfma_f32_16x16x32_bf16 v[244:247], a[20:23], a[52:55], v[244:247]
s_add_u32 m0, s69, 0x4000
v_mfma_f32_16x16x32_bf16 v[248:251], a[20:23], a[56:59], v[248:251]
ds_read_b64_tr_b16 a[126:127], v21 offset:9728
v_mfma_f32_16x16x32_bf16 v[252:255], a[20:23], a[60:63], v[252:255]
v_mfma_f32_16x16x32_bf16 a[128:131], a[24:27], a[32:35], a[128:131]
buffer_load_dwordx4 v12, s[44:47], s68 offen offset:0 lds
v_mfma_f32_16x16x32_bf16 a[132:135], a[24:27], a[36:39], a[132:135]
v_mfma_f32_16x16x32_bf16 a[136:139], a[24:27], a[40:43], a[136:139]
v_mfma_f32_16x16x32_bf16 a[140:143], a[24:27], a[44:47], a[140:143]
v_mfma_f32_16x16x32_bf16 a[144:147], a[24:27], a[48:51], a[144:147]
v_mfma_f32_16x16x32_bf16 a[148:151], a[24:27], a[52:55], a[148:151]
buffer_load_dwordx4 v13, s[44:47], s68 offen offset:1024 lds
v_mfma_f32_16x16x32_bf16 a[152:155], a[24:27], a[56:59], a[152:155]
v_mfma_f32_16x16x32_bf16 a[156:159], a[24:27], a[60:63], a[156:159]
v_mfma_f32_16x16x32_bf16 a[160:163], a[28:31], a[32:35], a[160:163]
v_mfma_f32_16x16x32_bf16 a[164:167], a[28:31], a[36:39], a[164:167]
s_xor_b32 s69, s69, 0x10000
s_add_u32 s68, s68, 0x80000
v_mfma_f32_16x16x32_bf16 a[168:171], a[28:31], a[40:43], a[168:171]
v_mfma_f32_16x16x32_bf16 a[172:175], a[28:31], a[44:47], a[172:175]
v_mfma_f32_16x16x32_bf16 a[176:179], a[28:31], a[48:51], a[176:179]
v_mfma_f32_16x16x32_bf16 a[180:183], a[28:31], a[52:55], a[180:183]
v_mfma_f32_16x16x32_bf16 a[184:187], a[28:31], a[56:59], a[184:187]
v_mfma_f32_16x16x32_bf16 a[188:191], a[28:31], a[60:63], a[188:191]
s_waitcnt vmcnt(8) lgkmcnt(0)
s_barrier
v_xor_b32 v16, 0x10000, v16
v_xor_b32 v17, 0x10000, v17
v_xor_b32 v18, 0x10000, v18
v_xor_b32 v19, 0x10000, v19
v_xor_b32 v20, 0x10000, v20
v_xor_b32 v21, 0x10000, v21
v_mfma_f32_16x16x32_bf16 v[64:67], a[64:67], a[96:99], v[64:67]
ds_read_b128 a[0:3], v16 offset:0
v_mfma_f32_16x16x32_bf16 v[68:71], a[64:67], a[100:103], v[68:71]
s_add_u32 m0, s52, 0
v_mfma_f32_16x16x32_bf16 v[72:75], a[64:67], a[104:107], v[72:75]
ds_read_b64_tr_b16 a[32:33], v18 offset:0
v_mfma_f32_16x16x32_bf16 v[76:79], a[64:67], a[108:111], v[76:79]
v_mfma_f32_16x16x32_bf16 v[80:83], a[64:67], a[112:115], v[80:83]
ds_read_b64_tr_b16 a[34:35], v19 offset:0
v_mfma_f32_16x16x32_bf16 v[84:87], a[64:67], a[116:119], v[84:87]
v_mfma_f32_16x16x32_bf16 v[88:91], a[64:67], a[120:123], v[88:91]
ds_read_b128 a[4:7], v16 offset:2048
v_mfma_f32_16x16x32_bf16 v[92:95], a[64:67], a[124:127], v[92:95]
buffer_load_dwordx4 v8, s[36:39], s54 offen offset:0 lds
v_mfma_f32_16x16x32_bf16 v[96:99], a[68:71], a[96:99], v[96:99]
ds_read_b64_tr_b16 a[36:37], v20 offset:0
v_mfma_f32_16x16x32_bf16 v[100:103], a[68:71], a[100:103], v[100:103]
v_mfma_f32_16x16x32_bf16 v[104:107], a[68:71], a[104:107], v[104:107]
ds_read_b64_tr_b16 a[38:39], v21 offset:0
v_mfma_f32_16x16x32_bf16 v[108:111], a[68:71], a[108:111], v[108:111]
v_mfma_f32_16x16x32_bf16 v[112:115], a[68:71], a[112:115], v[112:115]
ds_read_b128 a[8:11], v16 offset:4096
v_mfma_f32_16x16x32_bf16 v[116:119], a[68:71], a[116:119], v[116:119]
buffer_load_dwordx4 v9, s[36:39], s54 offen offset:1024 lds
v_mfma_f32_16x16x32_bf16 v[120:123], a[68:71], a[120:123], v[120:123]
ds_read_b64_tr_b16 a[40:41], v18 offset:512
v_mfma_f32_16x16x32_bf16 v[124:127], a[68:71], a[124:127], v[124:127]
v_mfma_f32_16x16x32_bf16 v[128:131], a[72:75], a[96:99], v[128:131]
ds_read_b64_tr_b16 a[42:43], v19 offset:512
v_mfma_f32_16x16x32_bf16 v[132:135], a[72:75], a[100:103], v[132:135]
buffer_load_dwordx4 v10, s[36:39], s54 offen offset:2048 lds
v_mfma_f32_16x16x32_bf16 v[136:139], a[72:75], a[104:107], v[136:139]
ds_read_b128 a[12:15], v16 offset:6144
v_mfma_f32_16x16x32_bf16 v[140:143], a[72:75], a[108:111], v[140:143]
v_mfma_f32_16x16x32_bf16 v[144:147], a[72:75], a[112:115], v[144:147]
ds_read_b64_tr_b16 a[44:45], v20 offset:512
v_mfma_f32_16x16x32_bf16 v[148:151], a[72:75], a[116:119], v[148:151]
v_mfma_f32_16x16x32_bf16 v[152:155], a[72:75], a[120:123], v[152:155]
ds_read_b64_tr_b16 a[46:47], v21 offset:512
v_mfma_f32_16x16x32_bf16 v[156:159], a[72:75], a[124:127], v[156:159]
buffer_load_dwordx4 v11, s[36:39], s54 offen offset:3072 lds
v_mfma_f32_16x16x32_bf16 v[160:163], a[76:79], a[96:99], v[160:163]
ds_read_b128 a[16:19], v16 offset:8192
v_mfma_f32_16x16x32_bf16 v[164:167], a[76:79], a[100:103], v[164:167]
v_mfma_f32_16x16x32_bf16 v[168:171], a[76:79], a[104:107], v[168:171]
ds_read_b64_tr_b16 a[48:49], v18 offset:1024
v_mfma_f32_16x16x32_bf16 v[172:175], a[76:79], a[108:111], v[172:175]
v_mfma_f32_16x16x32_bf16 v[176:179], a[76:79], a[112:115], v[176:179]
ds_read_b64_tr_b16 a[50:51], v19 offset:1024
v_mfma_f32_16x16x32_bf16 v[180:183], a[76:79], a[116:119], v[180:183]
s_add_u32 m0, s52, 0x1000
v_mfma_f32_16x16x32_bf16 v[184:187], a[76:79], a[120:123], v[184:187]
ds_read_b128 a[20:23], v16 offset:10240
v_mfma_f32_16x16x32_bf16 v[188:191], a[76:79], a[124:127], v[188:191]
v_mfma_f32_16x16x32_bf16 v[192:195], a[80:83], a[96:99], v[192:195]
ds_read_b64_tr_b16 a[52:53], v20 offset:1024
v_mfma_f32_16x16x32_bf16 v[196:199], a[80:83], a[100:103], v[196:199]
v_mfma_f32_16x16x32_bf16 v[200:203], a[80:83], a[104:107], v[200:203]
ds_read_b64_tr_b16 a[54:55], v21 offset:1024
v_mfma_f32_16x16x32_bf16 v[204:207], a[80:83], a[108:111], v[204:207]
buffer_load_dwordx4 v8, s[36:39], s55 offen offset:0 lds
v_mfma_f32_16x16x32_bf16 v[208:211], a[80:83], a[112:115], v[208:211]
ds_read_b128 a[24:27], v16 offset:12288
v_mfma_f32_16x16x32_bf16 v[212:215], a[80:83], a[116:119], v[212:215]
v_mfma_f32_16x16x32_bf16 v[216:219], a[80:83], a[120:123], v[216:219]
ds_read_b64_tr_b16 a[56:57], v18 offset:1536
v_mfma_f32_16x16x32_bf16 v[220:223], a[80:83], a[124:127], v[220:223]
buffer_load_dwordx4 v9, s[36:39], s55 offen offset:1024 lds
v_mfma_f32_16x16x32_bf16 v[224:227], a[84:87], a[96:99], v[224:227]
ds_read_b64_tr_b16 a[58:59], v19 offset:1536
v_mfma_f32_16x16x32_bf16 v[228:231], a[84:87], a[100:103], v[228:231]
v_mfma_f32_16x16x32_bf16 v[232:235], a[84:87], a[104:107], v[232:235]
ds_read_b128 a[28:31], v16 offset:14336
v_mfma_f32_16x16x32_bf16 v[236:239], a[84:87], a[108:111], v[236:239]
v_mfma_f32_16x16x32_bf16 v[240:243], a[84:87], a[112:115], v[240:243]
ds_read_b64_tr_b16 a[60:61], v20 offset:1536
v_mfma_f32_16x16x32_bf16 v[244:247], a[84:87], a[116:119], v[244:247]
buffer_load_dwordx4 v10, s[36:39], s55 offen offset:2048 lds
v_mfma_f32_16x16x32_bf16 v[248:251], a[84:87], a[120:123], v[248:251]
ds_read_b64_tr_b16 a[62:63], v21 offset:1536
v_mfma_f32_16x16x32_bf16 v[252:255], a[84:87], a[124:127], v[252:255]
v_mfma_f32_16x16x32_bf16 a[128:131], a[88:91], a[96:99], a[128:131]
v_mfma_f32_16x16x32_bf16 a[132:135], a[88:91], a[100:103], a[132:135]
v_mfma_f32_16x16x32_bf16 a[136:139], a[88:91], a[104:107], a[136:139]
buffer_load_dwordx4 v11, s[36:39], s55 offen offset:3072 lds
v_mfma_f32_16x16x32_bf16 a[140:143], a[88:91], a[108:111], a[140:143]
v_mfma_f32_16x16x32_bf16 a[144:147], a[88:91], a[112:115], a[144:147]
v_mfma_f32_16x16x32_bf16 a[148:151], a[88:91], a[116:119], a[148:151]
v_mfma_f32_16x16x32_bf16 a[152:155], a[88:91], a[120:123], a[152:155]
v_mfma_f32_16x16x32_bf16 a[156:159], a[88:91], a[124:127], a[156:159]
v_mfma_f32_16x16x32_bf16 a[160:163], a[92:95], a[96:99], a[160:163]
s_xor_b32 s52, s52, 0x10000
s_add_u32 s54, s54, 0x80
s_add_u32 s55, s55, 0x80
v_mfma_f32_16x16x32_bf16 a[164:167], a[92:95], a[100:103], a[164:167]
v_mfma_f32_16x16x32_bf16 a[168:171], a[92:95], a[104:107], a[168:171]
v_mfma_f32_16x16x32_bf16 a[172:175], a[92:95], a[108:111], a[172:175]
v_mfma_f32_16x16x32_bf16 a[176:179], a[92:95], a[112:115], a[176:179]
v_mfma_f32_16x16x32_bf16 a[180:183], a[92:95], a[116:119], a[180:183]
v_mfma_f32_16x16x32_bf16 a[184:187], a[92:95], a[120:123], a[184:187]
v_mfma_f32_16x16x32_bf16 a[188:191], a[92:95], a[124:127], a[188:191]
s_waitcnt vmcnt(8) lgkmcnt(0)
s_barrier
v_mfma_f32_16x16x32_bf16 v[64:67], a[0:3], a[32:35], v[64:67]
ds_read_b128 a[64:67], v17 offset:0
v_mfma_f32_16x16x32_bf16 v[68:71], a[0:3], a[36:39], v[68:71]
s_add_u32 m0, s53, 0
v_mfma_f32_16x16x32_bf16 v[72:75], a[0:3], a[40:43], v[72:75]
ds_read_b64_tr_b16 a[96:97], v18 offset:8192
v_mfma_f32_16x16x32_bf16 v[76:79], a[0:3], a[44:47], v[76:79]
v_mfma_f32_16x16x32_bf16 v[80:83], a[0:3], a[48:51], v[80:83]
ds_read_b64_tr_b16 a[98:99], v19 offset:8192
v_mfma_f32_16x16x32_bf16 v[84:87], a[0:3], a[52:55], v[84:87]
buffer_load_dwordx4 v12, s[40:43], s56 offen offset:0 lds
v_mfma_f32_16x16x32_bf16 v[88:91], a[0:3], a[56:59], v[88:91]
ds_read_b128 a[68:71], v17 offset:2048
v_mfma_f32_16x16x32_bf16 v[92:95], a[0:3], a[60:63], v[92:95]
v_mfma_f32_16x16x32_bf16 v[96:99], a[4:7], a[32:35], v[96:99]
ds_read_b64_tr_b16 a[100:101], v20 offset:8192
v_mfma_f32_16x16x32_bf16 v[100:103], a[4:7], a[36:39], v[100:103]
buffer_load_dwordx4 v13, s[40:43], s56 offen offset:1024 lds
v_mfma_f32_16x16x32_bf16 v[104:107], a[4:7], a[40:43], v[104:107]
ds_read_b64_tr_b16 a[102:103], v21 offset:8192
v_mfma_f32_16x16x32_bf16 v[108:111], a[4:7], a[44:47], v[108:111]
v_mfma_f32_16x16x32_bf16 v[112:115], a[4:7], a[48:51], v[112:115]
ds_read_b128 a[72:75], v17 offset:4096
v_mfma_f32_16x16x32_bf16 v[116:119], a[4:7], a[52:55], v[116:119]
s_add_u32 m0, s53, 0x4000
v_mfma_f32_16x16x32_bf16 v[120:123], a[4:7], a[56:59], v[120:123]
ds_read_b64_tr_b16 a[104:105], v18 offset:8704
v_mfma_f32_16x16x32_bf16 v[124:127], a[4:7], a[60:63], v[124:127]
v_mfma_f32_16x16x32_bf16 v[128:131], a[8:11], a[32:35], v[128:131]
ds_read_b64_tr_b16 a[106:107], v19 offset:8704
v_mfma_f32_16x16x32_bf16 v[132:135], a[8:11], a[36:39], v[132:135]
buffer_load_dwordx4 v12, s[44:47], s56 offen offset:0 lds
v_mfma_f32_16x16x32_bf16 v[136:139], a[8:11], a[40:43], v[136:139]
ds_read_b128 a[76:79], v17 offset:6144
v_mfma_f32_16x16x32_bf16 v[140:143], a[8:11], a[44:47], v[140:143]
v_mfma_f32_16x16x32_bf16 v[144:147], a[8:11], a[48:51], v[144:147]
ds_read_b64_tr_b16 a[108:109], v20 offset:8704
v_mfma_f32_16x16x32_bf16 v[148:151], a[8:11], a[52:55], v[148:151]
buffer_load_dwordx4 v13, s[44:47], s56 offen offset:1024 lds
v_mfma_f32_16x16x32_bf16 v[152:155], a[8:11], a[56:59], v[152:155]
ds_read_b64_tr_b16 a[110:111], v21 offset:8704
v_mfma_f32_16x16x32_bf16 v[156:159], a[8:11], a[60:63], v[156:159]
v_mfma_f32_16x16x32_bf16 v[160:163], a[12:15], a[32:35], v[160:163]
ds_read_b128 a[80:83], v17 offset:8192
v_mfma_f32_16x16x32_bf16 v[164:167], a[12:15], a[36:39], v[164:167]
s_xor_b32 s53, s53, 0x10000
s_add_u32 s56, s56, 0x80000
v_mfma_f32_16x16x32_bf16 v[168:171], a[12:15], a[40:43], v[168:171]
ds_read_b64_tr_b16 a[112:113], v18 offset:9216
v_mfma_f32_16x16x32_bf16 v[172:175], a[12:15], a[44:47], v[172:175]
v_mfma_f32_16x16x32_bf16 v[176:179], a[12:15], a[48:51], v[176:179]
ds_read_b64_tr_b16 a[114:115], v19 offset:9216
v_mfma_f32_16x16x32_bf16 v[180:183], a[12:15], a[52:55], v[180:183]
s_add_u32 m0, s69, 0
v_mfma_f32_16x16x32_bf16 v[184:187], a[12:15], a[56:59], v[184:187]
ds_read_b128 a[84:87], v17 offset:10240
v_mfma_f32_16x16x32_bf16 v[188:191], a[12:15], a[60:63], v[188:191]
v_mfma_f32_16x16x32_bf16 v[192:195], a[16:19], a[32:35], v[192:195]
ds_read_b64_tr_b16 a[116:117], v20 offset:9216
v_mfma_f32_16x16x32_bf16 v[196:199], a[16:19], a[36:39], v[196:199]
buffer_load_dwordx4 v12, s[40:43], s68 offen offset:0 lds
v_mfma_f32_16x16x32_bf16 v[200:203], a[16:19], a[40:43], v[200:203]
ds_read_b64_tr_b16 a[118:119], v21 offset:9216
v_mfma_f32_16x16x32_bf16 v[204:207], a[16:19], a[44:47], v[204:207]
v_mfma_f32_16x16x32_bf16 v[208:211], a[16:19], a[48:51], v[208:211]
ds_read_b128 a[88:91], v17 offset:12288
v_mfma_f32_16x16x32_bf16 v[212:215], a[16:19], a[52:55], v[212:215]
buffer_load_dwordx4 v13, s[40:43], s68 offen offset:1024 lds
v_mfma_f32_16x16x32_bf16 v[216:219], a[16:19], a[56:59], v[216:219]
ds_read_b64_tr_b16 a[120:121], v18 offset:9728
v_mfma_f32_16x16x32_bf16 v[220:223], a[16:19], a[60:63], v[220:223]
v_mfma_f32_16x16x32_bf16 v[224:227], a[20:23], a[32:35], v[224:227]
ds_read_b64_tr_b16 a[122:123], v19 offset:9728
v_mfma_f32_16x16x32_bf16 v[228:231], a[20:23], a[36:39], v[228:231]
s_add_u32 m0, s69, 0x4000
v_mfma_f32_16x16x32_bf16 v[232:235], a[20:23], a[40:43], v[232:235]
ds_read_b128 a[92:95], v17 offset:14336
v_mfma_f32_16x16x32_bf16 v[236:239], a[20:23], a[44:47], v[236:239]
v_mfma_f32_16x16x32_bf16 v[240:243], a[20:23], a[48:51], v[240:243]
ds_read_b64_tr_b16 a[124:125], v20 offset:9728
v_mfma_f32_16x16x32_bf16 v[244:247], a[20:23], a[52:55], v[244:247]
buffer_load_dwordx4 v12, s[44:47], s68 offen offset:0 lds
v_mfma_f32_16x16x32_bf16 v[248:251], a[20:23], a[56:59], v[248:251]
ds_read_b64_tr_b16 a[126:127], v21 offset:9728
v_mfma_f32_16x16x32_bf16 v[252:255], a[20:23], a[60:63], v[252:255]
v_mfma_f32_16x16x32_bf16 a[128:131], a[24:27], a[32:35], a[128:131]
v_mfma_f32_16x16x32_bf16 a[132:135], a[24:27], a[36:39], a[132:135]
buffer_load_dwordx4 v13, s[44:47], s68 offen offset:1024 lds
v_mfma_f32_16x16x32_bf16 a[136:139], a[24:27], a[40:43], a[136:139]
v_mfma_f32_16x16x32_bf16 a[140:143], a[24:27], a[44:47], a[140:143]
v_mfma_f32_16x16x32_bf16 a[144:147], a[24:27], a[48:51], a[144:147]
v_mfma_f32_16x16x32_bf16 a[148:151], a[24:27], a[52:55], a[148:151]
s_xor_b32 s69, s69, 0x10000
s_add_u32 s68, s68, 0x80000
v_mfma_f32_16x16x32_bf16 a[152:155], a[24:27], a[56:59], a[152:155]
v_mfma_f32_16x16x32_bf16 a[156:159], a[24:27], a[60:63], a[156:159]
v_mfma_f32_16x16x32_bf16 a[160:163], a[28:31], a[32:35], a[160:163]
v_mfma_f32_16x16x32_bf16 a[164:167], a[28:31], a[36:39], a[164:167]
v_lshrrev_b32 v0, 4, v4
v_bfe_u32 v1, v0, 3, 1
v_lshlrev_b32 v1, 13, v1
v_and_b32 v2, 7, v0
v_lshl_add_u32 v1, v2, 7, v1
v_add_u32 v1, s75, v1
s_add_u32 s75, s75, 0x4000
s_mov_b32 exec_lo, 0xffff
s_mov_b32 exec_hi, 0
global_load_dword v5, v1, s[94:95] offset:0
s_mov_b64 exec, -1
v_mfma_f32_16x16x32_bf16 a[168:171], a[28:31], a[40:43], a[168:171]
v_mfma_f32_16x16x32_bf16 a[172:175], a[28:31], a[44:47], a[172:175]
v_mfma_f32_16x16x32_bf16 a[176:179], a[28:31], a[48:51], a[176:179]
v_mfma_f32_16x16x32_bf16 a[180:183], a[28:31], a[52:55], a[180:183]
v_mfma_f32_16x16x32_bf16 a[184:187], a[28:31], a[56:59], a[184:187]
v_mfma_f32_16x16x32_bf16 a[188:191], a[28:31], a[60:63], a[188:191]
s_waitcnt vmcnt(9) lgkmcnt(0)
s_barrier
v_xor_b32 v16, 0x10000, v16
v_xor_b32 v17, 0x10000, v17
v_xor_b32 v18, 0x10000, v18
v_xor_b32 v19, 0x10000, v19
v_xor_b32 v20, 0x10000, v20
v_xor_b32 v21, 0x10000, v21
v_mfma_f32_16x16x32_bf16 v[64:67], a[64:67], a[96:99], v[64:67]
ds_read_b128 a[0:3], v16 offset:0
v_mfma_f32_16x16x32_bf16 v[68:71], a[64:67], a[100:103], v[68:71]
s_add_u32 m0, s52, 0
v_mfma_f32_16x16x32_bf16 v[72:75], a[64:67], a[104:107], v[72:75]
ds_read_b64_tr_b16 a[32:33], v18 offset:0
v_mfma_f32_16x16x32_bf16 v[76:79], a[64:67], a[108:111], v[76:79]
v_mfma_f32_16x16x32_bf16 v[80:83], a[64:67], a[112:115], v[80:83]
ds_read_b64_tr_b16 a[34:35], v19 offset:0
v_mfma_f32_16x16x32_bf16 v[84:87], a[64:67], a[116:119], v[84:87]
v_mfma_f32_16x16x32_bf16 v[88:91], a[64:67], a[120:123], v[88:91]
ds_read_b128 a[4:7], v16 offset:2048
v_mfma_f32_16x16x32_bf16 v[92:95], a[64:67], a[124:127], v[92:95]
buffer_load_dwordx4 v8, s[36:39], s54 offen offset:0 lds
v_mfma_f32_16x16x32_bf16 v[96:99], a[68:71], a[96:99], v[96:99]
ds_read_b64_tr_b16 a[36:37], v20 offset:0
v_mfma_f32_16x16x32_bf16 v[100:103], a[68:71], a[100:103], v[100:103]
v_mfma_f32_16x16x32_bf16 v[104:107], a[68:71], a[104:107], v[104:107]
ds_read_b64_tr_b16 a[38:39], v21 offset:0
v_mfma_f32_16x16x32_bf16 v[108:111], a[68:71], a[108:111], v[108:111]
buffer_load_dwordx4 v9, s[36:39], s54 offen offset:1024 lds
v_mfma_f32_16x16x32_bf16 v[112:115], a[68:71], a[112:115], v[112:115]
ds_read_b128 a[8:11], v16 offset:4096
v_mfma_f32_16x16x32_bf16 v[116:119], a[68:71], a[116:119], v[116:119]
v_mfma_f32_16x16x32_bf16 v[120:123], a[68:71], a[120:123], v[120:123]
ds_read_b64_tr_b16 a[40:41], v18 offset:512
v_mfma_f32_16x16x32_bf16 v[124:127], a[68:71], a[124:127], v[124:127]
v_mfma_f32_16x16x32_bf16 v[128:131], a[72:75], a[96:99], v[128:131]
ds_read_b64_tr_b16 a[42:43], v19 offset:512
v_mfma_f32_16x16x32_bf16 v[132:135], a[72:75], a[100:103], v[132:135]
buffer_load_dwordx4 v10, s[36:39], s54 offen offset:2048 lds
v_mfma_f32_16x16x32_bf16 v[136:139], a[72:75], a[104:107], v[136:139]
ds_read_b128 a[12:15], v16 offset:6144
v_mfma_f32_16x16x32_bf16 v[140:143], a[72:75], a[108:111], v[140:143]
v_mfma_f32_16x16x32_bf16 v[144:147], a[72:75], a[112:115], v[144:147]
ds_read_b64_tr_b16 a[44:45], v20 offset:512
v_mfma_f32_16x16x32_bf16 v[148:151], a[72:75], a[116:119], v[148:151]
buffer_load_dwordx4 v11, s[36:39], s54 offen offset:3072 lds
v_mfma_f32_16x16x32_bf16 v[152:155], a[72:75], a[120:123], v[152:155]
ds_read_b64_tr_b16 a[46:47], v21 offset:512
v_mfma_f32_16x16x32_bf16 v[156:159], a[72:75], a[124:127], v[156:159]
v_mfma_f32_16x16x32_bf16 v[160:163], a[76:79], a[96:99], v[160:163]
ds_read_b128 a[16:19], v16 offset:8192
v_mfma_f32_16x16x32_bf16 v[164:167], a[76:79], a[100:103], v[164:167]
v_mfma_f32_16x16x32_bf16 v[168:171], a[76:79], a[104:107], v[168:171]
ds_read_b64_tr_b16 a[48:49], v18 offset:1024
v_mfma_f32_16x16x32_bf16 v[172:175], a[76:79], a[108:111], v[172:175]
s_add_u32 m0, s52, 0x1000
v_mfma_f32_16x16x32_bf16 v[176:179], a[76:79], a[112:115], v[176:179]
ds_read_b64_tr_b16 a[50:51], v19 offset:1024
v_mfma_f32_16x16x32_bf16 v[180:183], a[76:79], a[116:119], v[180:183]
v_mfma_f32_16x16x32_bf16 v[184:187], a[76:79], a[120:123], v[184:187]
ds_read_b128 a[20:23], v16 offset:10240
v_mfma_f32_16x16x32_bf16 v[188:191], a[76:79], a[124:127], v[188:191]
buffer_load_dwordx4 v8, s[36:39], s55 offen offset:0 lds
v_mfma_f32_16x16x32_bf16 v[192:195], a[80:83], a[96:99], v[192:195]
ds_read_b64_tr_b16 a[52:53], v20 offset:1024
v_mfma_f32_16x16x32_bf16 v[196:199], a[80:83], a[100:103], v[196:199]
v_mfma_f32_16x16x32_bf16 v[200:203], a[80:83], a[104:107], v[200:203]
ds_read_b64_tr_b16 a[54:55], v21 offset:1024
v_mfma_f32_16x16x32_bf16 v[204:207], a[80:83], a[108:111], v[204:207]
v_mfma_f32_16x16x32_bf16 v[208:211], a[80:83], a[112:115], v[208:211]
ds_read_b128 a[24:27], v16 offset:12288
v_mfma_f32_16x16x32_bf16 v[212:215], a[80:83], a[116:119], v[212:215]
buffer_load_dwordx4 v9, s[36:39], s55 offen offset:1024 lds
v_mfma_f32_16x16x32_bf16 v[216:219], a[80:83], a[120:123], v[216:219]
ds_read_b64_tr_b16 a[56:57], v18 offset:1536
v_mfma_f32_16x16x32_bf16 v[220:223], a[80:83], a[124:127], v[220:223]
v_mfma_f32_16x16x32_bf16 v[224:227], a[84:87], a[96:99], v[224:227]
ds_read_b64_tr_b16 a[58:59], v19 offset:1536
v_mfma_f32_16x16x32_bf16 v[228:231], a[84:87], a[100:103], v[228:231]
buffer_load_dwordx4 v10, s[36:39], s55 offen offset:2048 lds
v_mfma_f32_16x16x32_bf16 v[232:235], a[84:87], a[104:107], v[232:235]
ds_read_b128 a[28:31], v16 offset:14336
v_mfma_f32_16x16x32_bf16 v[236:239], a[84:87], a[108:111], v[236:239]
v_mfma_f32_16x16x32_bf16 v[240:243], a[84:87], a[112:115], v[240:243]
ds_read_b64_tr_b16 a[60:61], v20 offset:1536
v_mfma_f32_16x16x32_bf16 v[244:247], a[84:87], a[116:119], v[244:247]
v_mfma_f32_16x16x32_bf16 v[248:251], a[84:87], a[120:123], v[248:251]
ds_read_b64_tr_b16 a[62:63], v21 offset:1536
v_mfma_f32_16x16x32_bf16 v[252:255], a[84:87], a[124:127], v[252:255]
buffer_load_dwordx4 v11, s[36:39], s55 offen offset:3072 lds
v_mfma_f32_16x16x32_bf16 a[128:131], a[88:91], a[96:99], a[128:131]
v_mfma_f32_16x16x32_bf16 a[132:135], a[88:91], a[100:103], a[132:135]
v_mfma_f32_16x16x32_bf16 a[136:139], a[88:91], a[104:107], a[136:139]
v_mfma_f32_16x16x32_bf16 a[140:143], a[88:91], a[108:111], a[140:143]
s_xor_b32 s52, s52, 0x10000
s_add_u32 s54, s54, 0x80
s_add_u32 s55, s55, 0x80
v_mfma_f32_16x16x32_bf16 a[144:147], a[88:91], a[112:115], a[144:147]
v_mfma_f32_16x16x32_bf16 a[148:151], a[88:91], a[116:119], a[148:151]
v_mfma_f32_16x16x32_bf16 a[152:155], a[88:91], a[120:123], a[152:155]
v_mfma_f32_16x16x32_bf16 a[156:159], a[88:91], a[124:127], a[156:159]
v_mfma_f32_16x16x32_bf16 a[160:163], a[92:95], a[96:99], a[160:163]
v_lshrrev_b32 v0, 4, v4
v_bfe_u32 v1, v0, 3, 1
v_lshlrev_b32 v1, 13, v1
v_and_b32 v2, 7, v0
v_lshl_add_u32 v1, v2, 7, v1
v_add_u32 v1, s75, v1
s_add_u32 s75, s75, 0x4000
s_mov_b32 exec_lo, 0xffff
s_mov_b32 exec_hi, 0
global_load_dword v5, v1, s[94:95] offset:0
s_mov_b64 exec, -1
v_mfma_f32_16x16x32_bf16 a[164:167], a[92:95], a[100:103], a[164:167]
v_mfma_f32_16x16x32_bf16 a[168:171], a[92:95], a[104:107], a[168:171]
v_mfma_f32_16x16x32_bf16 a[172:175], a[92:95], a[108:111], a[172:175]
v_mfma_f32_16x16x32_bf16 a[176:179], a[92:95], a[112:115], a[176:179]
v_mfma_f32_16x16x32_bf16 a[180:183], a[92:95], a[116:119], a[180:183]
v_mfma_f32_16x16x32_bf16 a[184:187], a[92:95], a[120:123], a[184:187]
v_mfma_f32_16x16x32_bf16 a[188:191], a[92:95], a[124:127], a[188:191]
s_waitcnt vmcnt(10) lgkmcnt(0)
s_barrier
v_mfma_f32_16x16x32_bf16 v[64:67], a[0:3], a[32:35], v[64:67]
ds_read_b128 a[64:67], v17 offset:0
v_mfma_f32_16x16x32_bf16 v[68:71], a[0:3], a[36:39], v[68:71]
s_add_u32 m0, s53, 0
v_mfma_f32_16x16x32_bf16 v[72:75], a[0:3], a[40:43], v[72:75]
ds_read_b64_tr_b16 a[96:97], v18 offset:8192
v_mfma_f32_16x16x32_bf16 v[76:79], a[0:3], a[44:47], v[76:79]
v_mfma_f32_16x16x32_bf16 v[80:83], a[0:3], a[48:51], v[80:83]
ds_read_b64_tr_b16 a[98:99], v19 offset:8192
v_mfma_f32_16x16x32_bf16 v[84:87], a[0:3], a[52:55], v[84:87]
buffer_load_dwordx4 v12, s[40:43], s56 offen offset:0 lds
v_mfma_f32_16x16x32_bf16 v[88:91], a[0:3], a[56:59], v[88:91]
ds_read_b128 a[68:71], v17 offset:2048
v_mfma_f32_16x16x32_bf16 v[92:95], a[0:3], a[60:63], v[92:95]
v_mfma_f32_16x16x32_bf16 v[96:99], a[4:7], a[32:35], v[96:99]
ds_read_b64_tr_b16 a[100:101], v20 offset:8192
v_mfma_f32_16x16x32_bf16 v[100:103], a[4:7], a[36:39], v[100:103]
buffer_load_dwordx4 v13, s[40:43], s56 offen offset:1024 lds
v_mfma_f32_16x16x32_bf16 v[104:107], a[4:7], a[40:43], v[104:107]
ds_read_b64_tr_b16 a[102:103], v21 offset:8192
v_mfma_f32_16x16x32_bf16 v[108:111], a[4:7], a[44:47], v[108:111]
v_mfma_f32_16x16x32_bf16 v[112:115], a[4:7], a[48:51], v[112:115]
ds_read_b128 a[72:75], v17 offset:4096
v_mfma_f32_16x16x32_bf16 v[116:119], a[4:7], a[52:55], v[116:119]
s_add_u32 m0, s53, 0x4000
v_mfma_f32_16x16x32_bf16 v[120:123], a[4:7], a[56:59], v[120:123]
ds_read_b64_tr_b16 a[104:105], v18 offset:8704
v_mfma_f32_16x16x32_bf16 v[124:127], a[4:7], a[60:63], v[124:127]
v_mfma_f32_16x16x32_bf16 v[128:131], a[8:11], a[32:35], v[128:131]
ds_read_b64_tr_b16 a[106:107], v19 offset:8704
v_mfma_f32_16x16x32_bf16 v[132:135], a[8:11], a[36:39], v[132:135]
buffer_load_dwordx4 v12, s[44:47], s56 offen offset:0 lds
v_mfma_f32_16x16x32_bf16 v[136:139], a[8:11], a[40:43], v[136:139]
ds_read_b128 a[76:79], v17 offset:6144
v_mfma_f32_16x16x32_bf16 v[140:143], a[8:11], a[44:47], v[140:143]
v_mfma_f32_16x16x32_bf16 v[144:147], a[8:11], a[48:51], v[144:147]
ds_read_b64_tr_b16 a[108:109], v20 offset:8704
v_mfma_f32_16x16x32_bf16 v[148:151], a[8:11], a[52:55], v[148:151]
buffer_load_dwordx4 v13, s[44:47], s56 offen offset:1024 lds
v_mfma_f32_16x16x32_bf16 v[152:155], a[8:11], a[56:59], v[152:155]
ds_read_b64_tr_b16 a[110:111], v21 offset:8704
v_mfma_f32_16x16x32_bf16 v[156:159], a[8:11], a[60:63], v[156:159]
v_mfma_f32_16x16x32_bf16 v[160:163], a[12:15], a[32:35], v[160:163]
ds_read_b128 a[80:83], v17 offset:8192
v_mfma_f32_16x16x32_bf16 v[164:167], a[12:15], a[36:39], v[164:167]
s_xor_b32 s53, s53, 0x10000
s_add_u32 s56, s56, 0x80000
v_mfma_f32_16x16x32_bf16 v[168:171], a[12:15], a[40:43], v[168:171]
ds_read_b64_tr_b16 a[112:113], v18 offset:9216
v_mfma_f32_16x16x32_bf16 v[172:175], a[12:15], a[44:47], v[172:175]
v_mfma_f32_16x16x32_bf16 v[176:179], a[12:15], a[48:51], v[176:179]
ds_read_b64_tr_b16 a[114:115], v19 offset:9216
v_mfma_f32_16x16x32_bf16 v[180:183], a[12:15], a[52:55], v[180:183]
s_add_u32 m0, s69, 0
v_mfma_f32_16x16x32_bf16 v[184:187], a[12:15], a[56:59], v[184:187]
ds_read_b128 a[84:87], v17 offset:10240
v_mfma_f32_16x16x32_bf16 v[188:191], a[12:15], a[60:63], v[188:191]
v_mfma_f32_16x16x32_bf16 v[192:195], a[16:19], a[32:35], v[192:195]
ds_read_b64_tr_b16 a[116:117], v20 offset:9216
v_mfma_f32_16x16x32_bf16 v[196:199], a[16:19], a[36:39], v[196:199]
buffer_load_dwordx4 v12, s[40:43], s68 offen offset:0 lds
v_mfma_f32_16x16x32_bf16 v[200:203], a[16:19], a[40:43], v[200:203]
ds_read_b64_tr_b16 a[118:119], v21 offset:9216
v_mfma_f32_16x16x32_bf16 v[204:207], a[16:19], a[44:47], v[204:207]
v_mfma_f32_16x16x32_bf16 v[208:211], a[16:19], a[48:51], v[208:211]
ds_read_b128 a[88:91], v17 offset:12288
v_mfma_f32_16x16x32_bf16 v[212:215], a[16:19], a[52:55], v[212:215]
buffer_load_dwordx4 v13, s[40:43], s68 offen offset:1024 lds
v_mfma_f32_16x16x32_bf16 v[216:219], a[16:19], a[56:59], v[216:219]
ds_read_b64_tr_b16 a[120:121], v18 offset:9728
v_mfma_f32_16x16x32_bf16 v[220:223], a[16:19], a[60:63], v[220:223]
v_mfma_f32_16x16x32_bf16 v[224:227], a[20:23], a[32:35], v[224:227]
ds_read_b64_tr_b16 a[122:123], v19 offset:9728
v_mfma_f32_16x16x32_bf16 v[228:231], a[20:23], a[36:39], v[228:231]
s_add_u32 m0, s69, 0x4000
v_mfma_f32_16x16x32_bf16 v[232:235], a[20:23], a[40:43], v[232:235]
ds_read_b128 a[92:95], v17 offset:14336
v_mfma_f32_16x16x32_bf16 v[236:239], a[20:23], a[44:47], v[236:239]
v_mfma_f32_16x16x32_bf16 v[240:243], a[20:23], a[48:51], v[240:243]
ds_read_b64_tr_b16 a[124:125], v20 offset:9728
v_mfma_f32_16x16x32_bf16 v[244:247], a[20:23], a[52:55], v[244:247]
buffer_load_dwordx4 v12, s[44:47], s68 offen offset:0 lds
v_mfma_f32_16x16x32_bf16 v[248:251], a[20:23], a[56:59], v[248:251]
ds_read_b64_tr_b16 a[126:127], v21 offset:9728
v_mfma_f32_16x16x32_bf16 v[252:255], a[20:23], a[60:63], v[252:255]
v_mfma_f32_16x16x32_bf16 a[128:131], a[24:27], a[32:35], a[128:131]
v_mfma_f32_16x16x32_bf16 a[132:135], a[24:27], a[36:39], a[132:135]
buffer_load_dwordx4 v13, s[44:47], s68 offen offset:1024 lds
v_mfma_f32_16x16x32_bf16 a[136:139], a[24:27], a[40:43], a[136:139]
v_mfma_f32_16x16x32_bf16 a[140:143], a[24:27], a[44:47], a[140:143]
v_mfma_f32_16x16x32_bf16 a[144:147], a[24:27], a[48:51], a[144:147]
v_mfma_f32_16x16x32_bf16 a[148:151], a[24:27], a[52:55], a[148:151]
s_xor_b32 s69, s69, 0x10000
s_add_u32 s68, s68, 0x80000
v_mfma_f32_16x16x32_bf16 a[152:155], a[24:27], a[56:59], a[152:155]
v_mfma_f32_16x16x32_bf16 a[156:159], a[24:27], a[60:63], a[156:159]
v_mfma_f32_16x16x32_bf16 a[160:163], a[28:31], a[32:35], a[160:163]
v_mfma_f32_16x16x32_bf16 a[164:167], a[28:31], a[36:39], a[164:167]
v_lshrrev_b32 v0, 4, v4
v_bfe_u32 v1, v0, 3, 1
v_lshlrev_b32 v1, 10, v1
v_and_b32 v2, 7, v0
v_lshl_add_u32 v1, v2, 7, v1
v_add_u32 v1, s59, v1
s_add_u32 s59, s59, 0x800
s_mov_b32 exec_lo, 0xffff
s_mov_b32 exec_hi, 0
global_load_dword v5, v1, s[92:93] offset:0
s_mov_b64 exec, -1
v_mfma_f32_16x16x32_bf16 a[168:171], a[28:31], a[40:43], a[168:171]
v_mfma_f32_16x16x32_bf16 a[172:175], a[28:31], a[44:47], a[172:175]
v_mfma_f32_16x16x32_bf16 a[176:179], a[28:31], a[48:51], a[176:179]
v_mfma_f32_16x16x32_bf16 a[180:183], a[28:31], a[52:55], a[180:183]
v_mfma_f32_16x16x32_bf16 a[184:187], a[28:31], a[56:59], a[184:187]
v_mfma_f32_16x16x32_bf16 a[188:191], a[28:31], a[60:63], a[188:191]
s_waitcnt vmcnt(10) lgkmcnt(0)
s_barrier
v_xor_b32 v16, 0x10000, v16
v_xor_b32 v17, 0x10000, v17
v_xor_b32 v18, 0x10000, v18
v_xor_b32 v19, 0x10000, v19
v_xor_b32 v20, 0x10000, v20
v_xor_b32 v21, 0x10000, v21
v_mfma_f32_16x16x32_bf16 v[64:67], a[64:67], a[96:99], v[64:67]
ds_read_b128 a[0:3], v16 offset:0
v_mfma_f32_16x16x32_bf16 v[68:71], a[64:67], a[100:103], v[68:71]
s_add_u32 m0, s52, 0
v_mfma_f32_16x16x32_bf16 v[72:75], a[64:67], a[104:107], v[72:75]
ds_read_b64_tr_b16 a[32:33], v18 offset:0
v_mfma_f32_16x16x32_bf16 v[76:79], a[64:67], a[108:111], v[76:79]
v_mfma_f32_16x16x32_bf16 v[80:83], a[64:67], a[112:115], v[80:83]
ds_read_b64_tr_b16 a[34:35], v19 offset:0
v_mfma_f32_16x16x32_bf16 v[84:87], a[64:67], a[116:119], v[84:87]
v_mfma_f32_16x16x32_bf16 v[88:91], a[64:67], a[120:123], v[88:91]
ds_read_b128 a[4:7], v16 offset:2048
v_mfma_f32_16x16x32_bf16 v[92:95], a[64:67], a[124:127], v[92:95]
buffer_load_dwordx4 v8, s[36:39], s54 offen offset:0 lds
v_mfma_f32_16x16x32_bf16 v[96:99], a[68:71], a[96:99], v[96:99]
ds_read_b64_tr_b16 a[36:37], v20 offset:0
v_mfma_f32_16x16x32_bf16 v[100:103], a[68:71], a[100:103], v[100:103]
v_mfma_f32_16x16x32_bf16 v[104:107], a[68:71], a[104:107], v[104:107]
ds_read_b64_tr_b16 a[38:39], v21 offset:0
v_mfma_f32_16x16x32_bf16 v[108:111], a[68:71], a[108:111], v[108:111]
buffer_load_dwordx4 v9, s[36:39], s54 offen offset:1024 lds
v_mfma_f32_16x16x32_bf16 v[112:115], a[68:71], a[112:115], v[112:115]
ds_read_b128 a[8:11], v16 offset:4096
v_mfma_f32_16x16x32_bf16 v[116:119], a[68:71], a[116:119], v[116:119]
v_mfma_f32_16x16x32_bf16 v[120:123], a[68:71], a[120:123], v[120:123]
ds_read_b64_tr_b16 a[40:41], v18 offset:512
v_mfma_f32_16x16x32_bf16 v[124:127], a[68:71], a[124:127], v[124:127]
v_mfma_f32_16x16x32_bf16 v[128:131], a[72:75], a[96:99], v[128:131]
ds_read_b64_tr_b16 a[42:43], v19 offset:512
v_mfma_f32_16x16x32_bf16 v[132:135], a[72:75], a[100:103], v[132:135]
buffer_load_dwordx4 v10, s[36:39], s54 offen offset:2048 lds
v_mfma_f32_16x16x32_bf16 v[136:139], a[72:75], a[104:107], v[136:139]
ds_read_b128 a[12:15], v16 offset:6144
v_mfma_f32_16x16x32_bf16 v[140:143], a[72:75], a[108:111], v[140:143]
v_mfma_f32_16x16x32_bf16 v[144:147], a[72:75], a[112:115], v[144:147]
ds_read_b64_tr_b16 a[44:45], v20 offset:512
v_mfma_f32_16x16x32_bf16 v[148:151], a[72:75], a[116:119], v[148:151]
buffer_load_dwordx4 v11, s[36:39], s54 offen offset:3072 lds
v_mfma_f32_16x16x32_bf16 v[152:155], a[72:75], a[120:123], v[152:155]
ds_read_b64_tr_b16 a[46:47], v21 offset:512
v_mfma_f32_16x16x32_bf16 v[156:159], a[72:75], a[124:127], v[156:159]
v_mfma_f32_16x16x32_bf16 v[160:163], a[76:79], a[96:99], v[160:163]
ds_read_b128 a[16:19], v16 offset:8192
v_mfma_f32_16x16x32_bf16 v[164:167], a[76:79], a[100:103], v[164:167]
v_mfma_f32_16x16x32_bf16 v[168:171], a[76:79], a[104:107], v[168:171]
ds_read_b64_tr_b16 a[48:49], v18 offset:1024
v_mfma_f32_16x16x32_bf16 v[172:175], a[76:79], a[108:111], v[172:175]
s_add_u32 m0, s52, 0x1000
v_mfma_f32_16x16x32_bf16 v[176:179], a[76:79], a[112:115], v[176:179]
ds_read_b64_tr_b16 a[50:51], v19 offset:1024
v_mfma_f32_16x16x32_bf16 v[180:183], a[76:79], a[116:119], v[180:183]
v_mfma_f32_16x16x32_bf16 v[184:187], a[76:79], a[120:123], v[184:187]
ds_read_b128 a[20:23], v16 offset:10240
v_mfma_f32_16x16x32_bf16 v[188:191], a[76:79], a[124:127], v[188:191]
buffer_load_dwordx4 v8, s[36:39], s55 offen offset:0 lds
v_mfma_f32_16x16x32_bf16 v[192:195], a[80:83], a[96:99], v[192:195]
ds_read_b64_tr_b16 a[52:53], v20 offset:1024
v_mfma_f32_16x16x32_bf16 v[196:199], a[80:83], a[100:103], v[196:199]
v_mfma_f32_16x16x32_bf16 v[200:203], a[80:83], a[104:107], v[200:203]
ds_read_b64_tr_b16 a[54:55], v21 offset:1024
v_mfma_f32_16x16x32_bf16 v[204:207], a[80:83], a[108:111], v[204:207]
v_mfma_f32_16x16x32_bf16 v[208:211], a[80:83], a[112:115], v[208:211]
ds_read_b128 a[24:27], v16 offset:12288
v_mfma_f32_16x16x32_bf16 v[212:215], a[80:83], a[116:119], v[212:215]
buffer_load_dwordx4 v9, s[36:39], s55 offen offset:1024 lds
v_mfma_f32_16x16x32_bf16 v[216:219], a[80:83], a[120:123], v[216:219]
ds_read_b64_tr_b16 a[56:57], v18 offset:1536
v_mfma_f32_16x16x32_bf16 v[220:223], a[80:83], a[124:127], v[220:223]
v_mfma_f32_16x16x32_bf16 v[224:227], a[84:87], a[96:99], v[224:227]
ds_read_b64_tr_b16 a[58:59], v19 offset:1536
v_mfma_f32_16x16x32_bf16 v[228:231], a[84:87], a[100:103], v[228:231]
buffer_load_dwordx4 v10, s[36:39], s55 offen offset:2048 lds
v_mfma_f32_16x16x32_bf16 v[232:235], a[84:87], a[104:107], v[232:235]
ds_read_b128 a[28:31], v16 offset:14336
v_mfma_f32_16x16x32_bf16 v[236:239], a[84:87], a[108:111], v[236:239]
v_mfma_f32_16x16x32_bf16 v[240:243], a[84:87], a[112:115], v[240:243]
ds_read_b64_tr_b16 a[60:61], v20 offset:1536
v_mfma_f32_16x16x32_bf16 v[244:247], a[84:87], a[116:119], v[244:247]
v_mfma_f32_16x16x32_bf16 v[248:251], a[84:87], a[120:123], v[248:251]
ds_read_b64_tr_b16 a[62:63], v21 offset:1536
v_mfma_f32_16x16x32_bf16 v[252:255], a[84:87], a[124:127], v[252:255]
buffer_load_dwordx4 v11, s[36:39], s55 offen offset:3072 lds
v_mfma_f32_16x16x32_bf16 a[128:131], a[88:91], a[96:99], a[128:131]
v_mfma_f32_16x16x32_bf16 a[132:135], a[88:91], a[100:103], a[132:135]
v_mfma_f32_16x16x32_bf16 a[136:139], a[88:91], a[104:107], a[136:139]
v_mfma_f32_16x16x32_bf16 a[140:143], a[88:91], a[108:111], a[140:143]
s_xor_b32 s52, s52, 0x10000
s_add_u32 s54, s54, 0x80
s_add_u32 s55, s55, 0x80
v_mfma_f32_16x16x32_bf16 a[144:147], a[88:91], a[112:115], a[144:147]
v_mfma_f32_16x16x32_bf16 a[148:151], a[88:91], a[116:119], a[148:151]
v_mfma_f32_16x16x32_bf16 a[152:155], a[88:91], a[120:123], a[152:155]
v_mfma_f32_16x16x32_bf16 a[156:159], a[88:91], a[124:127], a[156:159]
v_mfma_f32_16x16x32_bf16 a[160:163], a[92:95], a[96:99], a[160:163]
v_lshrrev_b32 v0, 4, v4
v_bfe_u32 v1, v0, 3, 1
v_lshlrev_b32 v1, 10, v1
v_and_b32 v2, 7, v0
v_lshl_add_u32 v1, v2, 7, v1
v_add_u32 v1, s59, v1
s_add_u32 s59, s59, 0x800
s_mov_b32 exec_lo, 0xffff
s_mov_b32 exec_hi, 0
global_load_dword v5, v1, s[92:93] offset:0
s_mov_b64 exec, -1
v_mfma_f32_16x16x32_bf16 a[164:167], a[92:95], a[100:103], a[164:167]
v_mfma_f32_16x16x32_bf16 a[168:171], a[92:95], a[104:107], a[168:171]
v_mfma_f32_16x16x32_bf16 a[172:175], a[92:95], a[108:111], a[172:175]
v_mfma_f32_16x16x32_bf16 a[176:179], a[92:95], a[112:115], a[176:179]
v_mfma_f32_16x16x32_bf16 a[180:183], a[92:95], a[116:119], a[180:183]
v_mfma_f32_16x16x32_bf16 a[184:187], a[92:95], a[120:123], a[184:187]
v_mfma_f32_16x16x32_bf16 a[188:191], a[92:95], a[124:127], a[188:191]
s_waitcnt vmcnt(10) lgkmcnt(0)
s_barrier
v_mfma_f32_16x16x32_bf16 v[64:67], a[0:3], a[32:35], v[64:67]
ds_read_b128 a[64:67], v17 offset:0
v_mfma_f32_16x16x32_bf16 v[68:71], a[0:3], a[36:39], v[68:71]
s_add_u32 m0, s53, 0
v_mfma_f32_16x16x32_bf16 v[72:75], a[0:3], a[40:43], v[72:75]
ds_read_b64_tr_b16 a[96:97], v18 offset:8192
v_mfma_f32_16x16x32_bf16 v[76:79], a[0:3], a[44:47], v[76:79]
v_mfma_f32_16x16x32_bf16 v[80:83], a[0:3], a[48:51], v[80:83]
ds_read_b64_tr_b16 a[98:99], v19 offset:8192
v_mfma_f32_16x16x32_bf16 v[84:87], a[0:3], a[52:55], v[84:87]
buffer_load_dwordx4 v12, s[40:43], s56 offen offset:0 lds
v_mfma_f32_16x16x32_bf16 v[88:91], a[0:3], a[56:59], v[88:91]
ds_read_b128 a[68:71], v17 offset:2048
v_mfma_f32_16x16x32_bf16 v[92:95], a[0:3], a[60:63], v[92:95]
v_mfma_f32_16x16x32_bf16 v[96:99], a[4:7], a[32:35], v[96:99]
ds_read_b64_tr_b16 a[100:101], v20 offset:8192
v_mfma_f32_16x16x32_bf16 v[100:103], a[4:7], a[36:39], v[100:103]
buffer_load_dwordx4 v13, s[40:43], s56 offen offset:1024 lds
v_mfma_f32_16x16x32_bf16 v[104:107], a[4:7], a[40:43], v[104:107]
ds_read_b64_tr_b16 a[102:103], v21 offset:8192
v_mfma_f32_16x16x32_bf16 v[108:111], a[4:7], a[44:47], v[108:111]
v_mfma_f32_16x16x32_bf16 v[112:115], a[4:7], a[48:51], v[112:115]
ds_read_b128 a[72:75], v17 offset:4096
v_mfma_f32_16x16x32_bf16 v[116:119], a[4:7], a[52:55], v[116:119]
s_add_u32 m0, s53, 0x4000
v_mfma_f32_16x16x32_bf16 v[120:123], a[4:7], a[56:59], v[120:123]
ds_read_b64_tr_b16 a[104:105], v18 offset:8704
v_mfma_f32_16x16x32_bf16 v[124:127], a[4:7], a[60:63], v[124:127]
v_mfma_f32_16x16x32_bf16 v[128:131], a[8:11], a[32:35], v[128:131]
ds_read_b64_tr_b16 a[106:107], v19 offset:8704
v_mfma_f32_16x16x32_bf16 v[132:135], a[8:11], a[36:39], v[132:135]
buffer_load_dwordx4 v12, s[44:47], s56 offen offset:0 lds
v_mfma_f32_16x16x32_bf16 v[136:139], a[8:11], a[40:43], v[136:139]
ds_read_b128 a[76:79], v17 offset:6144
v_mfma_f32_16x16x32_bf16 v[140:143], a[8:11], a[44:47], v[140:143]
v_mfma_f32_16x16x32_bf16 v[144:147], a[8:11], a[48:51], v[144:147]
ds_read_b64_tr_b16 a[108:109], v20 offset:8704
v_mfma_f32_16x16x32_bf16 v[148:151], a[8:11], a[52:55], v[148:151]
buffer_load_dwordx4 v13, s[44:47], s56 offen offset:1024 lds
v_mfma_f32_16x16x32_bf16 v[152:155], a[8:11], a[56:59], v[152:155]
ds_read_b64_tr_b16 a[110:111], v21 offset:8704
v_mfma_f32_16x16x32_bf16 v[156:159], a[8:11], a[60:63], v[156:159]
v_mfma_f32_16x16x32_bf16 v[160:163], a[12:15], a[32:35], v[160:163]
ds_read_b128 a[80:83], v17 offset:8192
v_mfma_f32_16x16x32_bf16 v[164:167], a[12:15], a[36:39], v[164:167]
s_xor_b32 s53, s53, 0x10000
s_add_u32 s56, s56, 0x80000
v_mfma_f32_16x16x32_bf16 v[168:171], a[12:15], a[40:43], v[168:171]
ds_read_b64_tr_b16 a[112:113], v18 offset:9216
v_mfma_f32_16x16x32_bf16 v[172:175], a[12:15], a[44:47], v[172:175]
v_mfma_f32_16x16x32_bf16 v[176:179], a[12:15], a[48:51], v[176:179]
ds_read_b64_tr_b16 a[114:115], v19 offset:9216
v_mfma_f32_16x16x32_bf16 v[180:183], a[12:15], a[52:55], v[180:183]
s_add_u32 m0, s69, 0
v_mfma_f32_16x16x32_bf16 v[184:187], a[12:15], a[56:59], v[184:187]
ds_read_b128 a[84:87], v17 offset:10240
v_mfma_f32_16x16x32_bf16 v[188:191], a[12:15], a[60:63], v[188:191]
v_mfma_f32_16x16x32_bf16 v[192:195], a[16:19], a[32:35], v[192:195]
ds_read_b64_tr_b16 a[116:117], v20 offset:9216
v_mfma_f32_16x16x32_bf16 v[196:199], a[16:19], a[36:39], v[196:199]
buffer_load_dwordx4 v12, s[40:43], s68 offen offset:0 lds
v_mfma_f32_16x16x32_bf16 v[200:203], a[16:19], a[40:43], v[200:203]
ds_read_b64_tr_b16 a[118:119], v21 offset:9216
v_mfma_f32_16x16x32_bf16 v[204:207], a[16:19], a[44:47], v[204:207]
v_mfma_f32_16x16x32_bf16 v[208:211], a[16:19], a[48:51], v[208:211]
ds_read_b128 a[88:91], v17 offset:12288
v_mfma_f32_16x16x32_bf16 v[212:215], a[16:19], a[52:55], v[212:215]
buffer_load_dwordx4 v13, s[40:43], s68 offen offset:1024 lds
v_mfma_f32_16x16x32_bf16 v[216:219], a[16:19], a[56:59], v[216:219]
ds_read_b64_tr_b16 a[120:121], v18 offset:9728
v_mfma_f32_16x16x32_bf16 v[220:223], a[16:19], a[60:63], v[220:223]
v_mfma_f32_16x16x32_bf16 v[224:227], a[20:23], a[32:35], v[224:227]
ds_read_b64_tr_b16 a[122:123], v19 offset:9728
v_mfma_f32_16x16x32_bf16 v[228:231], a[20:23], a[36:39], v[228:231]
s_add_u32 m0, s69, 0x4000
v_mfma_f32_16x16x32_bf16 v[232:235], a[20:23], a[40:43], v[232:235]
ds_read_b128 a[92:95], v17 offset:14336
v_mfma_f32_16x16x32_bf16 v[236:239], a[20:23], a[44:47], v[236:239]
v_mfma_f32_16x16x32_bf16 v[240:243], a[20:23], a[48:51], v[240:243]
ds_read_b64_tr_b16 a[124:125], v20 offset:9728
v_mfma_f32_16x16x32_bf16 v[244:247], a[20:23], a[52:55], v[244:247]
buffer_load_dwordx4 v12, s[44:47], s68 offen offset:0 lds
v_mfma_f32_16x16x32_bf16 v[248:251], a[20:23], a[56:59], v[248:251]
ds_read_b64_tr_b16 a[126:127], v21 offset:9728
v_mfma_f32_16x16x32_bf16 v[252:255], a[20:23], a[60:63], v[252:255]
v_mfma_f32_16x16x32_bf16 a[128:131], a[24:27], a[32:35], a[128:131]
v_mfma_f32_16x16x32_bf16 a[132:135], a[24:27], a[36:39], a[132:135]
buffer_load_dwordx4 v13, s[44:47], s68 offen offset:1024 lds
v_mfma_f32_16x16x32_bf16 a[136:139], a[24:27], a[40:43], a[136:139]
v_mfma_f32_16x16x32_bf16 a[140:143], a[24:27], a[44:47], a[140:143]
v_mfma_f32_16x16x32_bf16 a[144:147], a[24:27], a[48:51], a[144:147]
v_mfma_f32_16x16x32_bf16 a[148:151], a[24:27], a[52:55], a[148:151]
s_xor_b32 s69, s69, 0x10000
s_add_u32 s68, s68, 0x80000
v_mfma_f32_16x16x32_bf16 a[152:155], a[24:27], a[56:59], a[152:155]
v_mfma_f32_16x16x32_bf16 a[156:159], a[24:27], a[60:63], a[156:159]
v_mfma_f32_16x16x32_bf16 a[160:163], a[28:31], a[32:35], a[160:163]
v_mfma_f32_16x16x32_bf16 a[164:167], a[28:31], a[36:39], a[164:167]
v_lshrrev_b32 v0, 4, v4
v_bfe_u32 v1, v0, 3, 1
v_lshlrev_b32 v1, 10, v1
v_and_b32 v2, 7, v0
v_lshl_add_u32 v1, v2, 7, v1
v_add_u32 v1, s59, v1
s_add_u32 s59, s59, 0x800
s_mov_b32 exec_lo, 0xffff
s_mov_b32 exec_hi, 0
global_load_dword v5, v1, s[92:93] offset:0
s_mov_b64 exec, -1
v_mfma_f32_16x16x32_bf16 a[168:171], a[28:31], a[40:43], a[168:171]
v_mfma_f32_16x16x32_bf16 a[172:175], a[28:31], a[44:47], a[172:175]
v_mfma_f32_16x16x32_bf16 a[176:179], a[28:31], a[48:51], a[176:179]
v_mfma_f32_16x16x32_bf16 a[180:183], a[28:31], a[52:55], a[180:183]
v_mfma_f32_16x16x32_bf16 a[184:187], a[28:31], a[56:59], a[184:187]
v_mfma_f32_16x16x32_bf16 a[188:191], a[28:31], a[60:63], a[188:191]
s_waitcnt vmcnt(10) lgkmcnt(0)
s_barrier
v_xor_b32 v16, 0x10000, v16
v_xor_b32 v17, 0x10000, v17
v_xor_b32 v18, 0x10000, v18
v_xor_b32 v19, 0x10000, v19
v_xor_b32 v20, 0x10000, v20
v_xor_b32 v21, 0x10000, v21
v_mfma_f32_16x16x32_bf16 v[64:67], a[64:67], a[96:99], v[64:67]
ds_read_b128 a[0:3], v16 offset:0
v_mfma_f32_16x16x32_bf16 v[68:71], a[64:67], a[100:103], v[68:71]
s_add_u32 m0, s52, 0
v_mfma_f32_16x16x32_bf16 v[72:75], a[64:67], a[104:107], v[72:75]
ds_read_b64_tr_b16 a[32:33], v18 offset:0
v_mfma_f32_16x16x32_bf16 v[76:79], a[64:67], a[108:111], v[76:79]
v_mfma_f32_16x16x32_bf16 v[80:83], a[64:67], a[112:115], v[80:83]
ds_read_b64_tr_b16 a[34:35], v19 offset:0
v_mfma_f32_16x16x32_bf16 v[84:87], a[64:67], a[116:119], v[84:87]
v_mfma_f32_16x16x32_bf16 v[88:91], a[64:67], a[120:123], v[88:91]
ds_read_b128 a[4:7], v16 offset:2048
v_mfma_f32_16x16x32_bf16 v[92:95], a[64:67], a[124:127], v[92:95]
buffer_load_dwordx4 v8, s[36:39], s54 offen offset:0 lds
v_mfma_f32_16x16x32_bf16 v[96:99], a[68:71], a[96:99], v[96:99]
ds_read_b64_tr_b16 a[36:37], v20 offset:0
v_mfma_f32_16x16x32_bf16 v[100:103], a[68:71], a[100:103], v[100:103]
v_mfma_f32_16x16x32_bf16 v[104:107], a[68:71], a[104:107], v[104:107]
ds_read_b64_tr_b16 a[38:39], v21 offset:0
v_mfma_f32_16x16x32_bf16 v[108:111], a[68:71], a[108:111], v[108:111]
buffer_load_dwordx4 v9, s[36:39], s54 offen offset:1024 lds
v_mfma_f32_16x16x32_bf16 v[112:115], a[68:71], a[112:115], v[112:115]
ds_read_b128 a[8:11], v16 offset:4096
v_mfma_f32_16x16x32_bf16 v[116:119], a[68:71], a[116:119], v[116:119]
v_mfma_f32_16x16x32_bf16 v[120:123], a[68:71], a[120:123], v[120:123]
ds_read_b64_tr_b16 a[40:41], v18 offset:512
v_mfma_f32_16x16x32_bf16 v[124:127], a[68:71], a[124:127], v[124:127]
v_mfma_f32_16x16x32_bf16 v[128:131], a[72:75], a[96:99], v[128:131]
ds_read_b64_tr_b16 a[42:43], v19 offset:512
v_mfma_f32_16x16x32_bf16 v[132:135], a[72:75], a[100:103], v[132:135]
buffer_load_dwordx4 v10, s[36:39], s54 offen offset:2048 lds
v_mfma_f32_16x16x32_bf16 v[136:139], a[72:75], a[104:107], v[136:139]
ds_read_b128 a[12:15], v16 offset:6144
v_mfma_f32_16x16x32_bf16 v[140:143], a[72:75], a[108:111], v[140:143]
v_mfma_f32_16x16x32_bf16 v[144:147], a[72:75], a[112:115], v[144:147]
ds_read_b64_tr_b16 a[44:45], v20 offset:512
v_mfma_f32_16x16x32_bf16 v[148:151], a[72:75], a[116:119], v[148:151]
buffer_load_dwordx4 v11, s[36:39], s54 offen offset:3072 lds
v_mfma_f32_16x16x32_bf16 v[152:155], a[72:75], a[120:123], v[152:155]
ds_read_b64_tr_b16 a[46:47], v21 offset:512
v_mfma_f32_16x16x32_bf16 v[156:159], a[72:75], a[124:127], v[156:159]
v_mfma_f32_16x16x32_bf16 v[160:163], a[76:79], a[96:99], v[160:163]
ds_read_b128 a[16:19], v16 offset:8192
v_mfma_f32_16x16x32_bf16 v[164:167], a[76:79], a[100:103], v[164:167]
v_mfma_f32_16x16x32_bf16 v[168:171], a[76:79], a[104:107], v[168:171]
ds_read_b64_tr_b16 a[48:49], v18 offset:1024
v_mfma_f32_16x16x32_bf16 v[172:175], a[76:79], a[108:111], v[172:175]
s_add_u32 m0, s52, 0x1000
v_mfma_f32_16x16x32_bf16 v[176:179], a[76:79], a[112:115], v[176:179]
ds_read_b64_tr_b16 a[50:51], v19 offset:1024
v_mfma_f32_16x16x32_bf16 v[180:183], a[76:79], a[116:119], v[180:183]
v_mfma_f32_16x16x32_bf16 v[184:187], a[76:79], a[120:123], v[184:187]
ds_read_b128 a[20:23], v16 offset:10240
v_mfma_f32_16x16x32_bf16 v[188:191], a[76:79], a[124:127], v[188:191]
buffer_load_dwordx4 v8, s[36:39], s55 offen offset:0 lds
v_mfma_f32_16x16x32_bf16 v[192:195], a[80:83], a[96:99], v[192:195]
ds_read_b64_tr_b16 a[52:53], v20 offset:1024
v_mfma_f32_16x16x32_bf16 v[196:199], a[80:83], a[100:103], v[196:199]
v_mfma_f32_16x16x32_bf16 v[200:203], a[80:83], a[104:107], v[200:203]
ds_read_b64_tr_b16 a[54:55], v21 offset:1024
v_mfma_f32_16x16x32_bf16 v[204:207], a[80:83], a[108:111], v[204:207]
v_mfma_f32_16x16x32_bf16 v[208:211], a[80:83], a[112:115], v[208:211]
ds_read_b128 a[24:27], v16 offset:12288
v_mfma_f32_16x16x32_bf16 v[212:215], a[80:83], a[116:119], v[212:215]
buffer_load_dwordx4 v9, s[36:39], s55 offen offset:1024 lds
v_mfma_f32_16x16x32_bf16 v[216:219], a[80:83], a[120:123], v[216:219]
ds_read_b64_tr_b16 a[56:57], v18 offset:1536
v_mfma_f32_16x16x32_bf16 v[220:223], a[80:83], a[124:127], v[220:223]
v_mfma_f32_16x16x32_bf16 v[224:227], a[84:87], a[96:99], v[224:227]
ds_read_b64_tr_b16 a[58:59], v19 offset:1536
v_mfma_f32_16x16x32_bf16 v[228:231], a[84:87], a[100:103], v[228:231]
buffer_load_dwordx4 v10, s[36:39], s55 offen offset:2048 lds
v_mfma_f32_16x16x32_bf16 v[232:235], a[84:87], a[104:107], v[232:235]
ds_read_b128 a[28:31], v16 offset:14336
v_mfma_f32_16x16x32_bf16 v[236:239], a[84:87], a[108:111], v[236:239]
v_mfma_f32_16x16x32_bf16 v[240:243], a[84:87], a[112:115], v[240:243]
ds_read_b64_tr_b16 a[60:61], v20 offset:1536
v_mfma_f32_16x16x32_bf16 v[244:247], a[84:87], a[116:119], v[244:247]
v_mfma_f32_16x16x32_bf16 v[248:251], a[84:87], a[120:123], v[248:251]
ds_read_b64_tr_b16 a[62:63], v21 offset:1536
v_mfma_f32_16x16x32_bf16 v[252:255], a[84:87], a[124:127], v[252:255]
buffer_load_dwordx4 v11, s[36:39], s55 offen offset:3072 lds
v_mfma_f32_16x16x32_bf16 a[128:131], a[88:91], a[96:99], a[128:131]
v_mfma_f32_16x16x32_bf16 a[132:135], a[88:91], a[100:103], a[132:135]
v_mfma_f32_16x16x32_bf16 a[136:139], a[88:91], a[104:107], a[136:139]
v_mfma_f32_16x16x32_bf16 a[140:143], a[88:91], a[108:111], a[140:143]
s_xor_b32 s52, s52, 0x10000
s_add_u32 s54, s54, 0x80
s_add_u32 s55, s55, 0x80
v_mfma_f32_16x16x32_bf16 a[144:147], a[88:91], a[112:115], a[144:147]
v_mfma_f32_16x16x32_bf16 a[148:151], a[88:91], a[116:119], a[148:151]
v_mfma_f32_16x16x32_bf16 a[152:155], a[88:91], a[120:123], a[152:155]
v_mfma_f32_16x16x32_bf16 a[156:159], a[88:91], a[124:127], a[156:159]
v_mfma_f32_16x16x32_bf16 a[160:163], a[92:95], a[96:99], a[160:163]
v_lshrrev_b32 v0, 4, v4
v_bfe_u32 v1, v0, 3, 1
v_lshlrev_b32 v1, 10, v1
v_and_b32 v2, 7, v0
v_lshl_add_u32 v1, v2, 7, v1
v_add_u32 v1, s59, v1
s_add_u32 s59, s59, 0x800
s_mov_b32 exec_lo, 0xffff
s_mov_b32 exec_hi, 0
global_load_dword v5, v1, s[92:93] offset:0
s_mov_b64 exec, -1
v_mfma_f32_16x16x32_bf16 a[164:167], a[92:95], a[100:103], a[164:167]
v_mfma_f32_16x16x32_bf16 a[168:171], a[92:95], a[104:107], a[168:171]
v_mfma_f32_16x16x32_bf16 a[172:175], a[92:95], a[108:111], a[172:175]
v_mfma_f32_16x16x32_bf16 a[176:179], a[92:95], a[112:115], a[176:179]
v_mfma_f32_16x16x32_bf16 a[180:183], a[92:95], a[116:119], a[180:183]
v_mfma_f32_16x16x32_bf16 a[184:187], a[92:95], a[120:123], a[184:187]
v_mfma_f32_16x16x32_bf16 a[188:191], a[92:95], a[124:127], a[188:191]
s_waitcnt vmcnt(10) lgkmcnt(0)
s_barrier
v_mfma_f32_16x16x32_bf16 v[64:67], a[0:3], a[32:35], v[64:67]
ds_read_b128 a[64:67], v17 offset:0
v_mfma_f32_16x16x32_bf16 v[68:71], a[0:3], a[36:39], v[68:71]
s_add_u32 m0, s53, 0
v_mfma_f32_16x16x32_bf16 v[72:75], a[0:3], a[40:43], v[72:75]
ds_read_b64_tr_b16 a[96:97], v18 offset:8192
v_mfma_f32_16x16x32_bf16 v[76:79], a[0:3], a[44:47], v[76:79]
v_mfma_f32_16x16x32_bf16 v[80:83], a[0:3], a[48:51], v[80:83]
ds_read_b64_tr_b16 a[98:99], v19 offset:8192
v_mfma_f32_16x16x32_bf16 v[84:87], a[0:3], a[52:55], v[84:87]
buffer_load_dwordx4 v12, s[40:43], s56 offen offset:0 lds
v_mfma_f32_16x16x32_bf16 v[88:91], a[0:3], a[56:59], v[88:91]
ds_read_b128 a[68:71], v17 offset:2048
v_mfma_f32_16x16x32_bf16 v[92:95], a[0:3], a[60:63], v[92:95]
v_mfma_f32_16x16x32_bf16 v[96:99], a[4:7], a[32:35], v[96:99]
ds_read_b64_tr_b16 a[100:101], v20 offset:8192
v_mfma_f32_16x16x32_bf16 v[100:103], a[4:7], a[36:39], v[100:103]
buffer_load_dwordx4 v13, s[40:43], s56 offen offset:1024 lds
v_mfma_f32_16x16x32_bf16 v[104:107], a[4:7], a[40:43], v[104:107]
ds_read_b64_tr_b16 a[102:103], v21 offset:8192
v_mfma_f32_16x16x32_bf16 v[108:111], a[4:7], a[44:47], v[108:111]
v_mfma_f32_16x16x32_bf16 v[112:115], a[4:7], a[48:51], v[112:115]
ds_read_b128 a[72:75], v17 offset:4096
v_mfma_f32_16x16x32_bf16 v[116:119], a[4:7], a[52:55], v[116:119]
s_add_u32 m0, s53, 0x4000
v_mfma_f32_16x16x32_bf16 v[120:123], a[4:7], a[56:59], v[120:123]
ds_read_b64_tr_b16 a[104:105], v18 offset:8704
v_mfma_f32_16x16x32_bf16 v[124:127], a[4:7], a[60:63], v[124:127]
v_mfma_f32_16x16x32_bf16 v[128:131], a[8:11], a[32:35], v[128:131]
ds_read_b64_tr_b16 a[106:107], v19 offset:8704
v_mfma_f32_16x16x32_bf16 v[132:135], a[8:11], a[36:39], v[132:135]
buffer_load_dwordx4 v12, s[44:47], s56 offen offset:0 lds
v_mfma_f32_16x16x32_bf16 v[136:139], a[8:11], a[40:43], v[136:139]
ds_read_b128 a[76:79], v17 offset:6144
v_mfma_f32_16x16x32_bf16 v[140:143], a[8:11], a[44:47], v[140:143]
v_mfma_f32_16x16x32_bf16 v[144:147], a[8:11], a[48:51], v[144:147]
ds_read_b64_tr_b16 a[108:109], v20 offset:8704
v_mfma_f32_16x16x32_bf16 v[148:151], a[8:11], a[52:55], v[148:151]
buffer_load_dwordx4 v13, s[44:47], s56 offen offset:1024 lds
v_mfma_f32_16x16x32_bf16 v[152:155], a[8:11], a[56:59], v[152:155]
ds_read_b64_tr_b16 a[110:111], v21 offset:8704
v_mfma_f32_16x16x32_bf16 v[156:159], a[8:11], a[60:63], v[156:159]
v_mfma_f32_16x16x32_bf16 v[160:163], a[12:15], a[32:35], v[160:163]
ds_read_b128 a[80:83], v17 offset:8192
v_mfma_f32_16x16x32_bf16 v[164:167], a[12:15], a[36:39], v[164:167]
s_xor_b32 s53, s53, 0x10000
s_add_u32 s56, s56, 0x80000
v_mfma_f32_16x16x32_bf16 v[168:171], a[12:15], a[40:43], v[168:171]
ds_read_b64_tr_b16 a[112:113], v18 offset:9216
v_mfma_f32_16x16x32_bf16 v[172:175], a[12:15], a[44:47], v[172:175]
v_mfma_f32_16x16x32_bf16 v[176:179], a[12:15], a[48:51], v[176:179]
ds_read_b64_tr_b16 a[114:115], v19 offset:9216
v_mfma_f32_16x16x32_bf16 v[180:183], a[12:15], a[52:55], v[180:183]
s_add_u32 m0, s69, 0
v_mfma_f32_16x16x32_bf16 v[184:187], a[12:15], a[56:59], v[184:187]
ds_read_b128 a[84:87], v17 offset:10240
v_mfma_f32_16x16x32_bf16 v[188:191], a[12:15], a[60:63], v[188:191]
v_mfma_f32_16x16x32_bf16 v[192:195], a[16:19], a[32:35], v[192:195]
ds_read_b64_tr_b16 a[116:117], v20 offset:9216
v_mfma_f32_16x16x32_bf16 v[196:199], a[16:19], a[36:39], v[196:199]
buffer_load_dwordx4 v12, s[40:43], s68 offen offset:0 lds
v_mfma_f32_16x16x32_bf16 v[200:203], a[16:19], a[40:43], v[200:203]
ds_read_b64_tr_b16 a[118:119], v21 offset:9216
v_mfma_f32_16x16x32_bf16 v[204:207], a[16:19], a[44:47], v[204:207]
v_mfma_f32_16x16x32_bf16 v[208:211], a[16:19], a[48:51], v[208:211]
ds_read_b128 a[88:91], v17 offset:12288
v_mfma_f32_16x16x32_bf16 v[212:215], a[16:19], a[52:55], v[212:215]
buffer_load_dwordx4 v13, s[40:43], s68 offen offset:1024 lds
v_mfma_f32_16x16x32_bf16 v[216:219], a[16:19], a[56:59], v[216:219]
ds_read_b64_tr_b16 a[120:121], v18 offset:9728
v_mfma_f32_16x16x32_bf16 v[220:223], a[16:19], a[60:63], v[220:223]
v_mfma_f32_16x16x32_bf16 v[224:227], a[20:23], a[32:35], v[224:227]
ds_read_b64_tr_b16 a[122:123], v19 offset:9728
v_mfma_f32_16x16x32_bf16 v[228:231], a[20:23], a[36:39], v[228:231]
s_add_u32 m0, s69, 0x4000
v_mfma_f32_16x16x32_bf16 v[232:235], a[20:23], a[40:43], v[232:235]
ds_read_b128 a[92:95], v17 offset:14336
v_mfma_f32_16x16x32_bf16 v[236:239], a[20:23], a[44:47], v[236:239]
v_mfma_f32_16x16x32_bf16 v[240:243], a[20:23], a[48:51], v[240:243]
ds_read_b64_tr_b16 a[124:125], v20 offset:9728
v_mfma_f32_16x16x32_bf16 v[244:247], a[20:23], a[52:55], v[244:247]
buffer_load_dwordx4 v12, s[44:47], s68 offen offset:0 lds
v_mfma_f32_16x16x32_bf16 v[248:251], a[20:23], a[56:59], v[248:251]
ds_read_b64_tr_b16 a[126:127], v21 offset:9728
v_mfma_f32_16x16x32_bf16 v[252:255], a[20:23], a[60:63], v[252:255]
v_mfma_f32_16x16x32_bf16 a[128:131], a[24:27], a[32:35], a[128:131]
v_mfma_f32_16x16x32_bf16 a[132:135], a[24:27], a[36:39], a[132:135]
buffer_load_dwordx4 v13, s[44:47], s68 offen offset:1024 lds
v_mfma_f32_16x16x32_bf16 a[136:139], a[24:27], a[40:43], a[136:139]
v_mfma_f32_16x16x32_bf16 a[140:143], a[24:27], a[44:47], a[140:143]
v_mfma_f32_16x16x32_bf16 a[144:147], a[24:27], a[48:51], a[144:147]
v_mfma_f32_16x16x32_bf16 a[148:151], a[24:27], a[52:55], a[148:151]
s_xor_b32 s69, s69, 0x10000
s_add_u32 s68, s68, 0x80000
v_mfma_f32_16x16x32_bf16 a[152:155], a[24:27], a[56:59], a[152:155]
v_mfma_f32_16x16x32_bf16 a[156:159], a[24:27], a[60:63], a[156:159]
v_mfma_f32_16x16x32_bf16 a[160:163], a[28:31], a[32:35], a[160:163]
v_mfma_f32_16x16x32_bf16 a[164:167], a[28:31], a[36:39], a[164:167]
v_lshrrev_b32 v0, 4, v4
v_bfe_u32 v1, v0, 3, 1
v_lshlrev_b32 v1, 10, v1
v_and_b32 v2, 7, v0
v_lshl_add_u32 v1, v2, 7, v1
v_add_u32 v1, s59, v1
s_add_u32 s59, s59, 0x800
s_mov_b32 exec_lo, 0xffff
s_mov_b32 exec_hi, 0
global_load_dword v5, v1, s[92:93] offset:0
s_mov_b64 exec, -1
v_mfma_f32_16x16x32_bf16 a[168:171], a[28:31], a[40:43], a[168:171]
v_mfma_f32_16x16x32_bf16 a[172:175], a[28:31], a[44:47], a[172:175]
v_mfma_f32_16x16x32_bf16 a[176:179], a[28:31], a[48:51], a[176:179]
v_mfma_f32_16x16x32_bf16 a[180:183], a[28:31], a[52:55], a[180:183]
v_mfma_f32_16x16x32_bf16 a[184:187], a[28:31], a[56:59], a[184:187]
v_mfma_f32_16x16x32_bf16 a[188:191], a[28:31], a[60:63], a[188:191]
s_waitcnt vmcnt(10) lgkmcnt(0)
s_barrier
v_xor_b32 v16, 0x10000, v16
v_xor_b32 v17, 0x10000, v17
v_xor_b32 v18, 0x10000, v18
v_xor_b32 v19, 0x10000, v19
v_xor_b32 v20, 0x10000, v20
v_xor_b32 v21, 0x10000, v21
v_mfma_f32_16x16x32_bf16 v[64:67], a[64:67], a[96:99], v[64:67]
ds_read_b128 a[0:3], v16 offset:0
v_mfma_f32_16x16x32_bf16 v[68:71], a[64:67], a[100:103], v[68:71]
s_add_u32 m0, s52, 0
v_mfma_f32_16x16x32_bf16 v[72:75], a[64:67], a[104:107], v[72:75]
ds_read_b64_tr_b16 a[32:33], v18 offset:0
v_mfma_f32_16x16x32_bf16 v[76:79], a[64:67], a[108:111], v[76:79]
v_mfma_f32_16x16x32_bf16 v[80:83], a[64:67], a[112:115], v[80:83]
ds_read_b64_tr_b16 a[34:35], v19 offset:0
v_mfma_f32_16x16x32_bf16 v[84:87], a[64:67], a[116:119], v[84:87]
v_mfma_f32_16x16x32_bf16 v[88:91], a[64:67], a[120:123], v[88:91]
ds_read_b128 a[4:7], v16 offset:2048
v_mfma_f32_16x16x32_bf16 v[92:95], a[64:67], a[124:127], v[92:95]
buffer_load_dwordx4 v8, s[36:39], s54 offen offset:0 lds
v_mfma_f32_16x16x32_bf16 v[96:99], a[68:71], a[96:99], v[96:99]
ds_read_b64_tr_b16 a[36:37], v20 offset:0
v_mfma_f32_16x16x32_bf16 v[100:103], a[68:71], a[100:103], v[100:103]
v_mfma_f32_16x16x32_bf16 v[104:107], a[68:71], a[104:107], v[104:107]
ds_read_b64_tr_b16 a[38:39], v21 offset:0
v_mfma_f32_16x16x32_bf16 v[108:111], a[68:71], a[108:111], v[108:111]
buffer_load_dwordx4 v9, s[36:39], s54 offen offset:1024 lds
v_mfma_f32_16x16x32_bf16 v[112:115], a[68:71], a[112:115], v[112:115]
ds_read_b128 a[8:11], v16 offset:4096
v_mfma_f32_16x16x32_bf16 v[116:119], a[68:71], a[116:119], v[116:119]
v_mfma_f32_16x16x32_bf16 v[120:123], a[68:71], a[120:123], v[120:123]
ds_read_b64_tr_b16 a[40:41], v18 offset:512
v_mfma_f32_16x16x32_bf16 v[124:127], a[68:71], a[124:127], v[124:127]
v_mfma_f32_16x16x32_bf16 v[128:131], a[72:75], a[96:99], v[128:131]
ds_read_b64_tr_b16 a[42:43], v19 offset:512
v_mfma_f32_16x16x32_bf16 v[132:135], a[72:75], a[100:103], v[132:135]
buffer_load_dwordx4 v10, s[36:39], s54 offen offset:2048 lds
v_mfma_f32_16x16x32_bf16 v[136:139], a[72:75], a[104:107], v[136:139]
ds_read_b128 a[12:15], v16 offset:6144
v_mfma_f32_16x16x32_bf16 v[140:143], a[72:75], a[108:111], v[140:143]
v_mfma_f32_16x16x32_bf16 v[144:147], a[72:75], a[112:115], v[144:147]
ds_read_b64_tr_b16 a[44:45], v20 offset:512
v_mfma_f32_16x16x32_bf16 v[148:151], a[72:75], a[116:119], v[148:151]
buffer_load_dwordx4 v11, s[36:39], s54 offen offset:3072 lds
v_mfma_f32_16x16x32_bf16 v[152:155], a[72:75], a[120:123], v[152:155]
ds_read_b64_tr_b16 a[46:47], v21 offset:512
v_mfma_f32_16x16x32_bf16 v[156:159], a[72:75], a[124:127], v[156:159]
v_mfma_f32_16x16x32_bf16 v[160:163], a[76:79], a[96:99], v[160:163]
ds_read_b128 a[16:19], v16 offset:8192
v_mfma_f32_16x16x32_bf16 v[164:167], a[76:79], a[100:103], v[164:167]
v_mfma_f32_16x16x32_bf16 v[168:171], a[76:79], a[104:107], v[168:171]
ds_read_b64_tr_b16 a[48:49], v18 offset:1024
v_mfma_f32_16x16x32_bf16 v[172:175], a[76:79], a[108:111], v[172:175]
s_add_u32 m0, s52, 0x1000
v_mfma_f32_16x16x32_bf16 v[176:179], a[76:79], a[112:115], v[176:179]
ds_read_b64_tr_b16 a[50:51], v19 offset:1024
v_mfma_f32_16x16x32_bf16 v[180:183], a[76:79], a[116:119], v[180:183]
v_mfma_f32_16x16x32_bf16 v[184:187], a[76:79], a[120:123], v[184:187]
ds_read_b128 a[20:23], v16 offset:10240
v_mfma_f32_16x16x32_bf16 v[188:191], a[76:79], a[124:127], v[188:191]
buffer_load_dwordx4 v8, s[36:39], s55 offen offset:0 lds
v_mfma_f32_16x16x32_bf16 v[192:195], a[80:83], a[96:99], v[192:195]
ds_read_b64_tr_b16 a[52:53], v20 offset:1024
v_mfma_f32_16x16x32_bf16 v[196:199], a[80:83], a[100:103], v[196:199]
v_mfma_f32_16x16x32_bf16 v[200:203], a[80:83], a[104:107], v[200:203]
ds_read_b64_tr_b16 a[54:55], v21 offset:1024
v_mfma_f32_16x16x32_bf16 v[204:207], a[80:83], a[108:111], v[204:207]
v_mfma_f32_16x16x32_bf16 v[208:211], a[80:83], a[112:115], v[208:211]
ds_read_b128 a[24:27], v16 offset:12288
v_mfma_f32_16x16x32_bf16 v[212:215], a[80:83], a[116:119], v[212:215]
buffer_load_dwordx4 v9, s[36:39], s55 offen offset:1024 lds
v_mfma_f32_16x16x32_bf16 v[216:219], a[80:83], a[120:123], v[216:219]
ds_read_b64_tr_b16 a[56:57], v18 offset:1536
v_mfma_f32_16x16x32_bf16 v[220:223], a[80:83], a[124:127], v[220:223]
v_mfma_f32_16x16x32_bf16 v[224:227], a[84:87], a[96:99], v[224:227]
ds_read_b64_tr_b16 a[58:59], v19 offset:1536
v_mfma_f32_16x16x32_bf16 v[228:231], a[84:87], a[100:103], v[228:231]
buffer_load_dwordx4 v10, s[36:39], s55 offen offset:2048 lds
v_mfma_f32_16x16x32_bf16 v[232:235], a[84:87], a[104:107], v[232:235]
ds_read_b128 a[28:31], v16 offset:14336
v_mfma_f32_16x16x32_bf16 v[236:239], a[84:87], a[108:111], v[236:239]
v_mfma_f32_16x16x32_bf16 v[240:243], a[84:87], a[112:115], v[240:243]
ds_read_b64_tr_b16 a[60:61], v20 offset:1536
v_mfma_f32_16x16x32_bf16 v[244:247], a[84:87], a[116:119], v[244:247]
v_mfma_f32_16x16x32_bf16 v[248:251], a[84:87], a[120:123], v[248:251]
ds_read_b64_tr_b16 a[62:63], v21 offset:1536
v_mfma_f32_16x16x32_bf16 v[252:255], a[84:87], a[124:127], v[252:255]
buffer_load_dwordx4 v11, s[36:39], s55 offen offset:3072 lds
v_mfma_f32_16x16x32_bf16 a[128:131], a[88:91], a[96:99], a[128:131]
v_mfma_f32_16x16x32_bf16 a[132:135], a[88:91], a[100:103], a[132:135]
v_mfma_f32_16x16x32_bf16 a[136:139], a[88:91], a[104:107], a[136:139]
v_mfma_f32_16x16x32_bf16 a[140:143], a[88:91], a[108:111], a[140:143]
s_xor_b32 s52, s52, 0x10000
s_add_u32 s54, s54, 0x80
s_add_u32 s55, s55, 0x80
v_mfma_f32_16x16x32_bf16 a[144:147], a[88:91], a[112:115], a[144:147]
v_mfma_f32_16x16x32_bf16 a[148:151], a[88:91], a[116:119], a[148:151]
v_mfma_f32_16x16x32_bf16 a[152:155], a[88:91], a[120:123], a[152:155]
v_mfma_f32_16x16x32_bf16 a[156:159], a[88:91], a[124:127], a[156:159]
v_mfma_f32_16x16x32_bf16 a[160:163], a[92:95], a[96:99], a[160:163]
v_lshrrev_b32 v0, 4, v4
v_bfe_u32 v1, v0, 3, 1
v_lshlrev_b32 v1, 10, v1
v_and_b32 v2, 7, v0
v_lshl_add_u32 v1, v2, 7, v1
v_add_u32 v1, s59, v1
s_add_u32 s59, s59, 0x800
s_mov_b32 exec_lo, 0xffff
s_mov_b32 exec_hi, 0
global_load_dword v5, v1, s[92:93] offset:0
s_mov_b64 exec, -1
v_mfma_f32_16x16x32_bf16 a[164:167], a[92:95], a[100:103], a[164:167]
v_mfma_f32_16x16x32_bf16 a[168:171], a[92:95], a[104:107], a[168:171]
v_mfma_f32_16x16x32_bf16 a[172:175], a[92:95], a[108:111], a[172:175]
v_mfma_f32_16x16x32_bf16 a[176:179], a[92:95], a[112:115], a[176:179]
v_mfma_f32_16x16x32_bf16 a[180:183], a[92:95], a[116:119], a[180:183]
v_mfma_f32_16x16x32_bf16 a[184:187], a[92:95], a[120:123], a[184:187]
v_mfma_f32_16x16x32_bf16 a[188:191], a[92:95], a[124:127], a[188:191]
s_waitcnt vmcnt(10) lgkmcnt(0)
s_barrier
v_mfma_f32_16x16x32_bf16 v[64:67], a[0:3], a[32:35], v[64:67]
ds_read_b128 a[64:67], v17 offset:0
v_mfma_f32_16x16x32_bf16 v[68:71], a[0:3], a[36:39], v[68:71]
s_add_u32 m0, s69, 0
v_mfma_f32_16x16x32_bf16 v[72:75], a[0:3], a[40:43], v[72:75]
ds_read_b64_tr_b16 a[96:97], v18 offset:8192
v_mfma_f32_16x16x32_bf16 v[76:79], a[0:3], a[44:47], v[76:79]
buffer_load_dwordx4 v12, s[40:43], s68 offen offset:0 lds
v_mfma_f32_16x16x32_bf16 v[80:83], a[0:3], a[48:51], v[80:83]
ds_read_b64_tr_b16 a[98:99], v19 offset:8192
v_mfma_f32_16x16x32_bf16 v[84:87], a[0:3], a[52:55], v[84:87]
buffer_load_dwordx4 v13, s[40:43], s68 offen offset:1024 lds
v_mfma_f32_16x16x32_bf16 v[88:91], a[0:3], a[56:59], v[88:91]
ds_read_b128 a[68:71], v17 offset:2048
v_mfma_f32_16x16x32_bf16 v[92:95], a[0:3], a[60:63], v[92:95]
s_add_u32 m0, s69, 0x4000
v_mfma_f32_16x16x32_bf16 v[96:99], a[4:7], a[32:35], v[96:99]
ds_read_b64_tr_b16 a[100:101], v20 offset:8192
v_mfma_f32_16x16x32_bf16 v[100:103], a[4:7], a[36:39], v[100:103]
buffer_load_dwordx4 v12, s[44:47], s68 offen offset:0 lds
v_mfma_f32_16x16x32_bf16 v[104:107], a[4:7], a[40:43], v[104:107]
ds_read_b64_tr_b16 a[102:103], v21 offset:8192
v_mfma_f32_16x16x32_bf16 v[108:111], a[4:7], a[44:47], v[108:111]
buffer_load_dwordx4 v13, s[44:47], s68 offen offset:1024 lds
v_mfma_f32_16x16x32_bf16 v[112:115], a[4:7], a[48:51], v[112:115]
ds_read_b128 a[72:75], v17 offset:4096
v_mfma_f32_16x16x32_bf16 v[116:119], a[4:7], a[52:55], v[116:119]
s_xor_b32 s69, s69, 0x10000
s_add_u32 s68, s68, 0x80000
v_mfma_f32_16x16x32_bf16 v[120:123], a[4:7], a[56:59], v[120:123]
ds_read_b64_tr_b16 a[104:105], v18 offset:8704
v_mfma_f32_16x16x32_bf16 v[124:127], a[4:7], a[60:63], v[124:127]
s_mov_b32 s36, s92
v_mfma_f32_16x16x32_bf16 v[128:131], a[8:11], a[32:35], v[128:131]
ds_read_b64_tr_b16 a[106:107], v19 offset:8704
v_mfma_f32_16x16x32_bf16 v[132:135], a[8:11], a[36:39], v[132:135]
s_and_b32 s37, s93, 0xffff
v_mfma_f32_16x16x32_bf16 v[136:139], a[8:11], a[40:43], v[136:139]
ds_read_b128 a[76:79], v17 offset:6144
v_mfma_f32_16x16x32_bf16 v[140:143], a[8:11], a[44:47], v[140:143]
s_mov_b32 s38, 0x200000
v_mfma_f32_16x16x32_bf16 v[144:147], a[8:11], a[48:51], v[144:147]
ds_read_b64_tr_b16 a[108:109], v20 offset:8704
v_mfma_f32_16x16x32_bf16 v[148:151], a[8:11], a[52:55], v[148:151]
s_mov_b32 s39, 0x20000
v_mfma_f32_16x16x32_bf16 v[152:155], a[8:11], a[56:59], v[152:155]
ds_read_b64_tr_b16 a[110:111], v21 offset:8704
v_mfma_f32_16x16x32_bf16 v[156:159], a[8:11], a[60:63], v[156:159]
s_sub_u32 s40, s94, 0x400
v_mfma_f32_16x16x32_bf16 v[160:163], a[12:15], a[32:35], v[160:163]
ds_read_b128 a[80:83], v17 offset:8192
v_mfma_f32_16x16x32_bf16 v[164:167], a[12:15], a[36:39], v[164:167]
s_subb_u32 s41, s95, 0
v_mfma_f32_16x16x32_bf16 v[168:171], a[12:15], a[40:43], v[168:171]
ds_read_b64_tr_b16 a[112:113], v18 offset:9216
v_mfma_f32_16x16x32_bf16 v[172:175], a[12:15], a[44:47], v[172:175]
s_and_b32 s41, s41, 0xffff
v_mfma_f32_16x16x32_bf16 v[176:179], a[12:15], a[48:51], v[176:179]
ds_read_b64_tr_b16 a[114:115], v19 offset:9216
v_mfma_f32_16x16x32_bf16 v[180:183], a[12:15], a[52:55], v[180:183]
s_mov_b32 s42, 0x400400
v_mfma_f32_16x16x32_bf16 v[184:187], a[12:15], a[56:59], v[184:187]
ds_read_b128 a[84:87], v17 offset:10240
v_mfma_f32_16x16x32_bf16 v[188:191], a[12:15], a[60:63], v[188:191]
s_mov_b32 s43, 0x20000
v_mfma_f32_16x16x32_bf16 v[192:195], a[16:19], a[32:35], v[192:195]
ds_read_b64_tr_b16 a[116:117], v20 offset:9216
v_mfma_f32_16x16x32_bf16 v[196:199], a[16:19], a[36:39], v[196:199]
s_lshl_b32 s27, s26, 17
v_mfma_f32_16x16x32_bf16 v[200:203], a[16:19], a[40:43], v[200:203]
ds_read_b64_tr_b16 a[118:119], v21 offset:9216
v_mfma_f32_16x16x32_bf16 v[204:207], a[16:19], a[44:47], v[204:207]
s_lshl_b32 s28, s23, 15
v_mfma_f32_16x16x32_bf16 v[208:211], a[16:19], a[48:51], v[208:211]
ds_read_b128 a[88:91], v17 offset:12288
v_mfma_f32_16x16x32_bf16 v[212:215], a[16:19], a[52:55], v[212:215]
s_add_u32 s54, s27, s28
v_mfma_f32_16x16x32_bf16 v[216:219], a[16:19], a[56:59], v[216:219]
ds_read_b64_tr_b16 a[120:121], v18 offset:9728
v_mfma_f32_16x16x32_bf16 v[220:223], a[16:19], a[60:63], v[220:223]
s_sub_u32 s27, 15, s26
v_mfma_f32_16x16x32_bf16 v[224:227], a[20:23], a[32:35], v[224:227]
ds_read_b64_tr_b16 a[122:123], v19 offset:9728
v_mfma_f32_16x16x32_bf16 v[228:231], a[20:23], a[36:39], v[228:231]
s_lshl_b32 s27, s27, 17
v_mfma_f32_16x16x32_bf16 v[232:235], a[20:23], a[40:43], v[232:235]
ds_read_b128 a[92:95], v17 offset:14336
v_mfma_f32_16x16x32_bf16 v[236:239], a[20:23], a[44:47], v[236:239]
s_add_u32 s55, s27, s28
v_mfma_f32_16x16x32_bf16 v[240:243], a[20:23], a[48:51], v[240:243]
ds_read_b64_tr_b16 a[124:125], v20 offset:9728
v_mfma_f32_16x16x32_bf16 v[244:247], a[20:23], a[52:55], v[244:247]
s_lshl_b32 s27, s23, 16
v_mfma_f32_16x16x32_bf16 v[248:251], a[20:23], a[56:59], v[248:251]
ds_read_b64_tr_b16 a[126:127], v21 offset:9728
v_mfma_f32_16x16x32_bf16 v[252:255], a[20:23], a[60:63], v[252:255]
s_lshl_b32 s28, s25, 8
v_mfma_f32_16x16x32_bf16 a[128:131], a[24:27], a[32:35], a[128:131]
s_add_u32 s56, s27, s28
v_mfma_f32_16x16x32_bf16 a[132:135], a[24:27], a[36:39], a[132:135]
s_add_u32 s68, s56, 0x40000
v_mfma_f32_16x16x32_bf16 a[136:139], a[24:27], a[40:43], a[136:139]
s_lshl_b32 s27, s23, 13
v_mfma_f32_16x16x32_bf16 a[140:143], a[24:27], a[44:47], a[140:143]
v_mfma_f32_16x16x32_bf16 a[144:147], a[24:27], a[48:51], a[144:147]
s_add_u32 s52, s24, s27
v_mfma_f32_16x16x32_bf16 a[148:151], a[24:27], a[52:55], a[148:151]
v_mfma_f32_16x16x32_bf16 a[152:155], a[24:27], a[56:59], a[152:155]
s_lshl_b32 s27, s23, 11
v_mfma_f32_16x16x32_bf16 a[156:159], a[24:27], a[60:63], a[156:159]
v_mfma_f32_16x16x32_bf16 a[160:163], a[28:31], a[32:35], a[160:163]
s_add_u32 s53, s24, s27
v_mfma_f32_16x16x32_bf16 a[164:167], a[28:31], a[36:39], a[164:167]
v_mfma_f32_16x16x32_bf16 a[168:171], a[28:31], a[40:43], a[168:171]
s_add_u32 s53, s53, 0x8000
v_mfma_f32_16x16x32_bf16 a[172:175], a[28:31], a[44:47], a[172:175]
v_mfma_f32_16x16x32_bf16 a[176:179], a[28:31], a[48:51], a[176:179]
v_lshrrev_b32 v0, 4, v4
v_bfe_u32 v1, v0, 3, 1
v_lshlrev_b32 v1, 10, v1
v_and_b32 v2, 7, v0
v_lshl_add_u32 v1, v2, 7, v1
v_add_u32 v1, s59, v1
s_add_u32 s59, s59, 0x800
s_mov_b32 exec_lo, 0xffff
s_mov_b32 exec_hi, 0
global_load_dword v5, v1, s[92:93] offset:0
s_mov_b64 exec, -1
v_mfma_f32_16x16x32_bf16 a[180:183], a[28:31], a[52:55], a[180:183]
v_mfma_f32_16x16x32_bf16 a[184:187], a[28:31], a[56:59], a[184:187]
v_mfma_f32_16x16x32_bf16 a[188:191], a[28:31], a[60:63], a[188:191]
s_waitcnt vmcnt(6) lgkmcnt(0)
s_barrier
v_xor_b32 v16, 0x10000, v16
v_xor_b32 v17, 0x10000, v17
v_xor_b32 v18, 0x10000, v18
v_xor_b32 v19, 0x10000, v19
v_xor_b32 v20, 0x10000, v20
v_xor_b32 v21, 0x10000, v21
v_mfma_f32_16x16x32_bf16 v[64:67], a[64:67], a[96:99], v[64:67]
ds_read_b128 a[0:3], v16 offset:0
v_mfma_f32_16x16x32_bf16 v[68:71], a[64:67], a[100:103], v[68:71]
s_add_u32 m0, s53, 0
v_mfma_f32_16x16x32_bf16 v[72:75], a[64:67], a[104:107], v[72:75]
ds_read_b64_tr_b16 a[32:33], v18 offset:0
v_mfma_f32_16x16x32_bf16 v[76:79], a[64:67], a[108:111], v[76:79]
v_mfma_f32_16x16x32_bf16 v[80:83], a[64:67], a[112:115], v[80:83]
ds_read_b64_tr_b16 a[34:35], v19 offset:0
v_mfma_f32_16x16x32_bf16 v[84:87], a[64:67], a[116:119], v[84:87]
v_mfma_f32_16x16x32_bf16 v[88:91], a[64:67], a[120:123], v[88:91]
ds_read_b128 a[4:7], v16 offset:2048
v_mfma_f32_16x16x32_bf16 v[92:95], a[64:67], a[124:127], v[92:95]
buffer_load_dwordx4 v12, s[40:43], s56 offen offset:0 lds
v_mfma_f32_16x16x32_bf16 v[96:99], a[68:71], a[96:99], v[96:99]
ds_read_b64_tr_b16 a[36:37], v20 offset:0
v_mfma_f32_16x16x32_bf16 v[100:103], a[68:71], a[100:103], v[100:103]
v_mfma_f32_16x16x32_bf16 v[104:107], a[68:71], a[104:107], v[104:107]
ds_read_b64_tr_b16 a[38:39], v21 offset:0
v_mfma_f32_16x16x32_bf16 v[108:111], a[68:71], a[108:111], v[108:111]
v_mfma_f32_16x16x32_bf16 v[112:115], a[68:71], a[112:115], v[112:115]
ds_read_b128 a[8:11], v16 offset:4096
v_mfma_f32_16x16x32_bf16 v[116:119], a[68:71], a[116:119], v[116:119]
buffer_load_dwordx4 v13, s[40:43], s56 offen offset:1024 lds
v_mfma_f32_16x16x32_bf16 v[120:123], a[68:71], a[120:123], v[120:123]
ds_read_b64_tr_b16 a[40:41], v18 offset:512
v_mfma_f32_16x16x32_bf16 v[124:127], a[68:71], a[124:127], v[124:127]
v_mfma_f32_16x16x32_bf16 v[128:131], a[72:75], a[96:99], v[128:131]
ds_read_b64_tr_b16 a[42:43], v19 offset:512
v_mfma_f32_16x16x32_bf16 v[132:135], a[72:75], a[100:103], v[132:135]
v_mfma_f32_16x16x32_bf16 v[136:139], a[72:75], a[104:107], v[136:139]
ds_read_b128 a[12:15], v16 offset:6144
v_mfma_f32_16x16x32_bf16 v[140:143], a[72:75], a[108:111], v[140:143]
s_add_u32 m0, s53, 0x2000
v_mfma_f32_16x16x32_bf16 v[144:147], a[72:75], a[112:115], v[144:147]
ds_read_b64_tr_b16 a[44:45], v20 offset:512
v_mfma_f32_16x16x32_bf16 v[148:151], a[72:75], a[116:119], v[148:151]
v_mfma_f32_16x16x32_bf16 v[152:155], a[72:75], a[120:123], v[152:155]
ds_read_b64_tr_b16 a[46:47], v21 offset:512
v_mfma_f32_16x16x32_bf16 v[156:159], a[72:75], a[124:127], v[156:159]
v_mfma_f32_16x16x32_bf16 v[160:163], a[76:79], a[96:99], v[160:163]
ds_read_b128 a[16:19], v16 offset:8192
v_mfma_f32_16x16x32_bf16 v[164:167], a[76:79], a[100:103], v[164:167]
buffer_load_dwordx4 v12, s[40:43], s68 offen offset:0 lds
v_mfma_f32_16x16x32_bf16 v[168:171], a[76:79], a[104:107], v[168:171]
ds_read_b64_tr_b16 a[48:49], v18 offset:1024
v_mfma_f32_16x16x32_bf16 v[172:175], a[76:79], a[108:111], v[172:175]
v_mfma_f32_16x16x32_bf16 v[176:179], a[76:79], a[112:115], v[176:179]
ds_read_b64_tr_b16 a[50:51], v19 offset:1024
v_mfma_f32_16x16x32_bf16 v[180:183], a[76:79], a[116:119], v[180:183]
v_mfma_f32_16x16x32_bf16 v[184:187], a[76:79], a[120:123], v[184:187]
ds_read_b128 a[20:23], v16 offset:10240
v_mfma_f32_16x16x32_bf16 v[188:191], a[76:79], a[124:127], v[188:191]
buffer_load_dwordx4 v13, s[40:43], s68 offen offset:1024 lds
v_mfma_f32_16x16x32_bf16 v[192:195], a[80:83], a[96:99], v[192:195]
ds_read_b64_tr_b16 a[52:53], v20 offset:1024
v_mfma_f32_16x16x32_bf16 v[196:199], a[80:83], a[100:103], v[196:199]
v_mfma_f32_16x16x32_bf16 v[200:203], a[80:83], a[104:107], v[200:203]
ds_read_b64_tr_b16 a[54:55], v21 offset:1024
v_mfma_f32_16x16x32_bf16 v[204:207], a[80:83], a[108:111], v[204:207]
v_mfma_f32_16x16x32_bf16 v[208:211], a[80:83], a[112:115], v[208:211]
ds_read_b128 a[24:27], v16 offset:12288
v_mfma_f32_16x16x32_bf16 v[212:215], a[80:83], a[116:119], v[212:215]
s_add_u32 m0, s52, 0
v_mfma_f32_16x16x32_bf16 v[216:219], a[80:83], a[120:123], v[216:219]
ds_read_b64_tr_b16 a[56:57], v18 offset:1536
v_mfma_f32_16x16x32_bf16 v[220:223], a[80:83], a[124:127], v[220:223]
v_mfma_f32_16x16x32_bf16 v[224:227], a[84:87], a[96:99], v[224:227]
ds_read_b64_tr_b16 a[58:59], v19 offset:1536
v_mfma_f32_16x16x32_bf16 v[228:231], a[84:87], a[100:103], v[228:231]
v_mfma_f32_16x16x32_bf16 v[232:235], a[84:87], a[104:107], v[232:235]
ds_read_b128 a[28:31], v16 offset:14336
v_mfma_f32_16x16x32_bf16 v[236:239], a[84:87], a[108:111], v[236:239]
buffer_load_dwordx4 v8, s[36:39], s54 offen offset:0 lds
v_mfma_f32_16x16x32_bf16 v[240:243], a[84:87], a[112:115], v[240:243]
ds_read_b64_tr_b16 a[60:61], v20 offset:1536
v_mfma_f32_16x16x32_bf16 v[244:247], a[84:87], a[116:119], v[244:247]
v_mfma_f32_16x16x32_bf16 v[248:251], a[84:87], a[120:123], v[248:251]
ds_read_b64_tr_b16 a[62:63], v21 offset:1536
v_mfma_f32_16x16x32_bf16 v[252:255], a[84:87], a[124:127], v[252:255]
v_mfma_f32_16x16x32_bf16 a[128:131], a[88:91], a[96:99], a[128:131]
v_mfma_f32_16x16x32_bf16 a[132:135], a[88:91], a[100:103], a[132:135]
buffer_load_dwordx4 v9, s[36:39], s54 offen offset:1024 lds
v_mfma_f32_16x16x32_bf16 a[136:139], a[88:91], a[104:107], a[136:139]
v_mfma_f32_16x16x32_bf16 a[140:143], a[88:91], a[108:111], a[140:143]
v_mfma_f32_16x16x32_bf16 a[144:147], a[88:91], a[112:115], a[144:147]
v_mfma_f32_16x16x32_bf16 a[148:151], a[88:91], a[116:119], a[148:151]
v_mfma_f32_16x16x32_bf16 a[152:155], a[88:91], a[120:123], a[152:155]
v_mfma_f32_16x16x32_bf16 a[156:159], a[88:91], a[124:127], a[156:159]
v_lshrrev_b32 v0, 4, v4
v_bfe_u32 v1, v0, 3, 1
v_lshlrev_b32 v1, 10, v1
v_and_b32 v2, 7, v0
v_lshl_add_u32 v1, v2, 7, v1
v_add_u32 v1, s59, v1
s_add_u32 s59, s59, 0x800
s_mov_b32 exec_lo, 0xffff
s_mov_b32 exec_hi, 0
global_load_dword v5, v1, s[92:93] offset:0
s_mov_b64 exec, -1
v_mfma_f32_16x16x32_bf16 a[160:163], a[92:95], a[96:99], a[160:163]
v_mfma_f32_16x16x32_bf16 a[164:167], a[92:95], a[100:103], a[164:167]
v_mfma_f32_16x16x32_bf16 a[168:171], a[92:95], a[104:107], a[168:171]
v_mfma_f32_16x16x32_bf16 a[172:175], a[92:95], a[108:111], a[172:175]
v_mfma_f32_16x16x32_bf16 a[176:179], a[92:95], a[112:115], a[176:179]
v_mfma_f32_16x16x32_bf16 a[180:183], a[92:95], a[116:119], a[180:183]
v_mfma_f32_16x16x32_bf16 a[184:187], a[92:95], a[120:123], a[184:187]
v_mfma_f32_16x16x32_bf16 a[188:191], a[92:95], a[124:127], a[188:191]
s_waitcnt vmcnt(8) lgkmcnt(0)
s_barrier
v_mfma_f32_16x16x32_bf16 v[64:67], a[0:3], a[32:35], v[64:67]
ds_read_b128 a[64:67], v17 offset:0
v_mfma_f32_16x16x32_bf16 v[68:71], a[0:3], a[36:39], v[68:71]
buffer_load_dwordx4 v10, s[36:39], s54 offen offset:2048 lds
v_mfma_f32_16x16x32_bf16 v[72:75], a[0:3], a[40:43], v[72:75]
ds_read_b64_tr_b16 a[96:97], v18 offset:8192
v_mfma_f32_16x16x32_bf16 v[76:79], a[0:3], a[44:47], v[76:79]
v_mfma_f32_16x16x32_bf16 v[80:83], a[0:3], a[48:51], v[80:83]
ds_read_b64_tr_b16 a[98:99], v19 offset:8192
v_mfma_f32_16x16x32_bf16 v[84:87], a[0:3], a[52:55], v[84:87]
v_mfma_f32_16x16x32_bf16 v[88:91], a[0:3], a[56:59], v[88:91]
ds_read_b128 a[68:71], v17 offset:2048
v_mfma_f32_16x16x32_bf16 v[92:95], a[0:3], a[60:63], v[92:95]
buffer_load_dwordx4 v11, s[36:39], s54 offen offset:3072 lds
v_mfma_f32_16x16x32_bf16 v[96:99], a[4:7], a[32:35], v[96:99]
ds_read_b64_tr_b16 a[100:101], v20 offset:8192
v_mfma_f32_16x16x32_bf16 v[100:103], a[4:7], a[36:39], v[100:103]
v_mfma_f32_16x16x32_bf16 v[104:107], a[4:7], a[40:43], v[104:107]
ds_read_b64_tr_b16 a[102:103], v21 offset:8192
v_mfma_f32_16x16x32_bf16 v[108:111], a[4:7], a[44:47], v[108:111]
v_mfma_f32_16x16x32_bf16 v[112:115], a[4:7], a[48:51], v[112:115]
ds_read_b128 a[72:75], v17 offset:4096
v_mfma_f32_16x16x32_bf16 v[116:119], a[4:7], a[52:55], v[116:119]
v_mfma_f32_16x16x32_bf16 v[120:123], a[4:7], a[56:59], v[120:123]
ds_read_b64_tr_b16 a[104:105], v18 offset:8704
v_mfma_f32_16x16x32_bf16 v[124:127], a[4:7], a[60:63], v[124:127]
s_add_u32 m0, s52, 0x1000
v_mfma_f32_16x16x32_bf16 v[128:131], a[8:11], a[32:35], v[128:131]
ds_read_b64_tr_b16 a[106:107], v19 offset:8704
v_mfma_f32_16x16x32_bf16 v[132:135], a[8:11], a[36:39], v[132:135]
v_mfma_f32_16x16x32_bf16 v[136:139], a[8:11], a[40:43], v[136:139]
ds_read_b128 a[76:79], v17 offset:6144
v_mfma_f32_16x16x32_bf16 v[140:143], a[8:11], a[44:47], v[140:143]
v_mfma_f32_16x16x32_bf16 v[144:147], a[8:11], a[48:51], v[144:147]
ds_read_b64_tr_b16 a[108:109], v20 offset:8704
v_mfma_f32_16x16x32_bf16 v[148:151], a[8:11], a[52:55], v[148:151]
buffer_load_dwordx4 v8, s[36:39], s55 offen offset:0 lds
v_mfma_f32_16x16x32_bf16 v[152:155], a[8:11], a[56:59], v[152:155]
ds_read_b64_tr_b16 a[110:111], v21 offset:8704
v_mfma_f32_16x16x32_bf16 v[156:159], a[8:11], a[60:63], v[156:159]
v_mfma_f32_16x16x32_bf16 v[160:163], a[12:15], a[32:35], v[160:163]
ds_read_b128 a[80:83], v17 offset:8192
v_mfma_f32_16x16x32_bf16 v[164:167], a[12:15], a[36:39], v[164:167]
v_mfma_f32_16x16x32_bf16 v[168:171], a[12:15], a[40:43], v[168:171]
ds_read_b64_tr_b16 a[112:113], v18 offset:9216
v_mfma_f32_16x16x32_bf16 v[172:175], a[12:15], a[44:47], v[172:175]
v_mfma_f32_16x16x32_bf16 v[176:179], a[12:15], a[48:51], v[176:179]
ds_read_b64_tr_b16 a[114:115], v19 offset:9216
v_mfma_f32_16x16x32_bf16 v[180:183], a[12:15], a[52:55], v[180:183]
buffer_load_dwordx4 v9, s[36:39], s55 offen offset:1024 lds
v_mfma_f32_16x16x32_bf16 v[184:187], a[12:15], a[56:59], v[184:187]
ds_read_b128 a[84:87], v17 offset:10240
v_mfma_f32_16x16x32_bf16 v[188:191], a[12:15], a[60:63], v[188:191]
v_mfma_f32_16x16x32_bf16 v[192:195], a[16:19], a[32:35], v[192:195]
ds_read_b64_tr_b16 a[116:117], v20 offset:9216
v_mfma_f32_16x16x32_bf16 v[196:199], a[16:19], a[36:39], v[196:199]
v_mfma_f32_16x16x32_bf16 v[200:203], a[16:19], a[40:43], v[200:203]
ds_read_b64_tr_b16 a[118:119], v21 offset:9216
v_mfma_f32_16x16x32_bf16 v[204:207], a[16:19], a[44:47], v[204:207]
buffer_load_dwordx4 v10, s[36:39], s55 offen offset:2048 lds
v_mfma_f32_16x16x32_bf16 v[208:211], a[16:19], a[48:51], v[208:211]
ds_read_b128 a[88:91], v17 offset:12288
v_mfma_f32_16x16x32_bf16 v[212:215], a[16:19], a[52:55], v[212:215]
v_mfma_f32_16x16x32_bf16 v[216:219], a[16:19], a[56:59], v[216:219]
ds_read_b64_tr_b16 a[120:121], v18 offset:9728
v_mfma_f32_16x16x32_bf16 v[220:223], a[16:19], a[60:63], v[220:223]
v_mfma_f32_16x16x32_bf16 v[224:227], a[20:23], a[32:35], v[224:227]
ds_read_b64_tr_b16 a[122:123], v19 offset:9728
v_mfma_f32_16x16x32_bf16 v[228:231], a[20:23], a[36:39], v[228:231]
buffer_load_dwordx4 v11, s[36:39], s55 offen offset:3072 lds
v_mfma_f32_16x16x32_bf16 v[232:235], a[20:23], a[40:43], v[232:235]
ds_read_b128 a[92:95], v17 offset:14336
v_mfma_f32_16x16x32_bf16 v[236:239], a[20:23], a[44:47], v[236:239]
v_mfma_f32_16x16x32_bf16 v[240:243], a[20:23], a[48:51], v[240:243]
ds_read_b64_tr_b16 a[124:125], v20 offset:9728
v_mfma_f32_16x16x32_bf16 v[244:247], a[20:23], a[52:55], v[244:247]
v_mfma_f32_16x16x32_bf16 v[248:251], a[20:23], a[56:59], v[248:251]
ds_read_b64_tr_b16 a[126:127], v21 offset:9728
v_mfma_f32_16x16x32_bf16 v[252:255], a[20:23], a[60:63], v[252:255]
v_mfma_f32_16x16x32_bf16 a[128:131], a[24:27], a[32:35], a[128:131]
s_add_u32 s54, s54, 0x80
s_add_u32 s55, s55, 0x80
v_mfma_f32_16x16x32_bf16 a[132:135], a[24:27], a[36:39], a[132:135]
v_mfma_f32_16x16x32_bf16 a[136:139], a[24:27], a[40:43], a[136:139]
v_mfma_f32_16x16x32_bf16 a[140:143], a[24:27], a[44:47], a[140:143]
v_mfma_f32_16x16x32_bf16 a[144:147], a[24:27], a[48:51], a[144:147]
v_mfma_f32_16x16x32_bf16 a[148:151], a[24:27], a[52:55], a[148:151]
v_mfma_f32_16x16x32_bf16 a[152:155], a[24:27], a[56:59], a[152:155]
v_mfma_f32_16x16x32_bf16 a[156:159], a[24:27], a[60:63], a[156:159]
s_add_u32 s56, s56, 0x80000
s_add_u32 s68, s68, 0x80000
v_mfma_f32_16x16x32_bf16 a[160:163], a[28:31], a[32:35], a[160:163]
v_mfma_f32_16x16x32_bf16 a[164:167], a[28:31], a[36:39], a[164:167]
v_mfma_f32_16x16x32_bf16 a[168:171], a[28:31], a[40:43], a[168:171]
v_mfma_f32_16x16x32_bf16 a[172:175], a[28:31], a[44:47], a[172:175]
v_mfma_f32_16x16x32_bf16 a[176:179], a[28:31], a[48:51], a[176:179]
v_mfma_f32_16x16x32_bf16 a[180:183], a[28:31], a[52:55], a[180:183]
v_mfma_f32_16x16x32_bf16 a[184:187], a[28:31], a[56:59], a[184:187]
v_mfma_f32_16x16x32_bf16 a[188:191], a[28:31], a[60:63], a[188:191]
s_waitcnt lgkmcnt(0)
s_barrier
v_mfma_f32_16x16x32_bf16 v[64:67], a[64:67], a[96:99], v[64:67]
s_add_u32 m0, s53, 0xc000
v_mfma_f32_16x16x32_bf16 v[68:71], a[64:67], a[100:103], v[68:71]
v_mfma_f32_16x16x32_bf16 v[72:75], a[64:67], a[104:107], v[72:75]
v_mfma_f32_16x16x32_bf16 v[76:79], a[64:67], a[108:111], v[76:79]
buffer_load_dwordx4 v12, s[40:43], s56 offen offset:0 lds
v_mfma_f32_16x16x32_bf16 v[80:83], a[64:67], a[112:115], v[80:83]
v_mfma_f32_16x16x32_bf16 v[84:87], a[64:67], a[116:119], v[84:87]
v_mfma_f32_16x16x32_bf16 v[88:91], a[64:67], a[120:123], v[88:91]
buffer_load_dwordx4 v13, s[40:43], s56 offen offset:1024 lds
v_mfma_f32_16x16x32_bf16 v[92:95], a[64:67], a[124:127], v[92:95]
v_mfma_f32_16x16x32_bf16 v[96:99], a[68:71], a[96:99], v[96:99]
v_mfma_f32_16x16x32_bf16 v[100:103], a[68:71], a[100:103], v[100:103]
v_mfma_f32_16x16x32_bf16 v[104:107], a[68:71], a[104:107], v[104:107]
s_add_u32 m0, s53, 0xe000
v_mfma_f32_16x16x32_bf16 v[108:111], a[68:71], a[108:111], v[108:111]
v_mfma_f32_16x16x32_bf16 v[112:115], a[68:71], a[112:115], v[112:115]
v_mfma_f32_16x16x32_bf16 v[116:119], a[68:71], a[116:119], v[116:119]
buffer_load_dwordx4 v12, s[40:43], s68 offen offset:0 lds
v_mfma_f32_16x16x32_bf16 v[120:123], a[68:71], a[120:123], v[120:123]
v_mfma_f32_16x16x32_bf16 v[124:127], a[68:71], a[124:127], v[124:127]
v_mfma_f32_16x16x32_bf16 v[128:131], a[72:75], a[96:99], v[128:131]
v_mfma_f32_16x16x32_bf16 v[132:135], a[72:75], a[100:103], v[132:135]
buffer_load_dwordx4 v13, s[40:43], s68 offen offset:1024 lds
v_mfma_f32_16x16x32_bf16 v[136:139], a[72:75], a[104:107], v[136:139]
v_mfma_f32_16x16x32_bf16 v[140:143], a[72:75], a[108:111], v[140:143]
v_cvt_pk_bf16_f32 v64, v64, v65
v_cvt_pk_bf16_f32 v65, v66, v67
v_mfma_f32_16x16x32_bf16 v[144:147], a[72:75], a[112:115], v[144:147]
s_add_u32 m0, s52, 0xc000
v_cvt_pk_bf16_f32 v66, v96, v97
v_cvt_pk_bf16_f32 v67, v98, v99
v_mfma_f32_16x16x32_bf16 v[148:151], a[72:75], a[116:119], v[148:151]
v_cvt_pk_bf16_f32 v68, v68, v69
v_cvt_pk_bf16_f32 v69, v70, v71
v_mfma_f32_16x16x32_bf16 v[152:155], a[72:75], a[120:123], v[152:155]
v_cvt_pk_bf16_f32 v70, v100, v101
v_cvt_pk_bf16_f32 v71, v102, v103
v_mfma_f32_16x16x32_bf16 v[156:159], a[72:75], a[124:127], v[156:159]
v_cvt_pk_bf16_f32 v72, v72, v73
v_cvt_pk_bf16_f32 v73, v74, v75
v_mfma_f32_16x16x32_bf16 v[160:163], a[76:79], a[96:99], v[160:163]
buffer_load_dwordx4 v8, s[36:39], s54 offen offset:0 lds
v_cvt_pk_bf16_f32 v74, v104, v105
v_cvt_pk_bf16_f32 v75, v106, v107
v_mfma_f32_16x16x32_bf16 v[164:167], a[76:79], a[100:103], v[164:167]
v_cvt_pk_bf16_f32 v76, v76, v77
v_cvt_pk_bf16_f32 v77, v78, v79
v_mfma_f32_16x16x32_bf16 v[168:171], a[76:79], a[104:107], v[168:171]
v_cvt_pk_bf16_f32 v78, v108, v109
v_cvt_pk_bf16_f32 v79, v110, v111
v_mfma_f32_16x16x32_bf16 v[172:175], a[76:79], a[108:111], v[172:175]
buffer_load_dwordx4 v9, s[36:39], s54 offen offset:1024 lds
v_cvt_pk_bf16_f32 v80, v80, v81
v_cvt_pk_bf16_f32 v81, v82, v83
v_mfma_f32_16x16x32_bf16 v[176:179], a[76:79], a[112:115], v[176:179]
v_cvt_pk_bf16_f32 v82, v112, v113
v_cvt_pk_bf16_f32 v83, v114, v115
v_mfma_f32_16x16x32_bf16 v[180:183], a[76:79], a[116:119], v[180:183]
v_cvt_pk_bf16_f32 v84, v84, v85
v_cvt_pk_bf16_f32 v85, v86, v87
v_mfma_f32_16x16x32_bf16 v[184:187], a[76:79], a[120:123], v[184:187]
v_cvt_pk_bf16_f32 v86, v116, v117
v_cvt_pk_bf16_f32 v87, v118, v119
v_mfma_f32_16x16x32_bf16 v[188:191], a[76:79], a[124:127], v[188:191]
buffer_load_dwordx4 v10, s[36:39], s54 offen offset:2048 lds
v_cvt_pk_bf16_f32 v88, v88, v89
v_cvt_pk_bf16_f32 v89, v90, v91
v_mfma_f32_16x16x32_bf16 v[192:195], a[80:83], a[96:99], v[192:195]
v_cvt_pk_bf16_f32 v90, v120, v121
v_cvt_pk_bf16_f32 v91, v122, v123
v_mfma_f32_16x16x32_bf16 v[196:199], a[80:83], a[100:103], v[196:199]
v_cvt_pk_bf16_f32 v92, v92, v93
v_cvt_pk_bf16_f32 v93, v94, v95
v_mfma_f32_16x16x32_bf16 v[200:203], a[80:83], a[104:107], v[200:203]
buffer_load_dwordx4 v11, s[36:39], s54 offen offset:3072 lds
v_cvt_pk_bf16_f32 v94, v124, v125
v_cvt_pk_bf16_f32 v95, v126, v127
v_mfma_f32_16x16x32_bf16 v[204:207], a[80:83], a[108:111], v[204:207]
v_cvt_pk_bf16_f32 v128, v128, v129
v_cvt_pk_bf16_f32 v129, v130, v131
v_mfma_f32_16x16x32_bf16 v[208:211], a[80:83], a[112:115], v[208:211]
v_cvt_pk_bf16_f32 v130, v160, v161
v_cvt_pk_bf16_f32 v131, v162, v163
v_mfma_f32_16x16x32_bf16 v[212:215], a[80:83], a[116:119], v[212:215]
s_add_u32 m0, s52, 0xd000
v_cvt_pk_bf16_f32 v132, v132, v133
v_cvt_pk_bf16_f32 v133, v134, v135
v_mfma_f32_16x16x32_bf16 v[216:219], a[80:83], a[120:123], v[216:219]
v_cvt_pk_bf16_f32 v134, v164, v165
v_cvt_pk_bf16_f32 v135, v166, v167
v_mfma_f32_16x16x32_bf16 v[220:223], a[80:83], a[124:127], v[220:223]
v_cvt_pk_bf16_f32 v136, v136, v137
v_cvt_pk_bf16_f32 v137, v138, v139
v_mfma_f32_16x16x32_bf16 v[224:227], a[84:87], a[96:99], v[224:227]
v_cvt_pk_bf16_f32 v138, v168, v169
v_cvt_pk_bf16_f32 v139, v170, v171
v_mfma_f32_16x16x32_bf16 v[228:231], a[84:87], a[100:103], v[228:231]
buffer_load_dwordx4 v8, s[36:39], s55 offen offset:0 lds
v_cvt_pk_bf16_f32 v140, v140, v141
v_cvt_pk_bf16_f32 v141, v142, v143
v_mfma_f32_16x16x32_bf16 v[232:235], a[84:87], a[104:107], v[232:235]
v_cvt_pk_bf16_f32 v142, v172, v173
v_cvt_pk_bf16_f32 v143, v174, v175
v_mfma_f32_16x16x32_bf16 v[236:239], a[84:87], a[108:111], v[236:239]
v_cvt_pk_bf16_f32 v144, v144, v145
v_cvt_pk_bf16_f32 v145, v146, v147
v_mfma_f32_16x16x32_bf16 v[240:243], a[84:87], a[112:115], v[240:243]
buffer_load_dwordx4 v9, s[36:39], s55 offen offset:1024 lds
v_cvt_pk_bf16_f32 v146, v176, v177
v_cvt_pk_bf16_f32 v147, v178, v179
v_mfma_f32_16x16x32_bf16 v[244:247], a[84:87], a[116:119], v[244:247]
v_cvt_pk_bf16_f32 v148, v148, v149
v_cvt_pk_bf16_f32 v149, v150, v151
v_mfma_f32_16x16x32_bf16 v[248:251], a[84:87], a[120:123], v[248:251]
v_cvt_pk_bf16_f32 v150, v180, v181
v_cvt_pk_bf16_f32 v151, v182, v183
v_mfma_f32_16x16x32_bf16 v[252:255], a[84:87], a[124:127], v[252:255]
v_cvt_pk_bf16_f32 v152, v152, v153
v_cvt_pk_bf16_f32 v153, v154, v155
v_mfma_f32_16x16x32_bf16 a[128:131], a[88:91], a[96:99], a[128:131]
buffer_load_dwordx4 v10, s[36:39], s55 offen offset:2048 lds
v_cvt_pk_bf16_f32 v154, v184, v185
v_cvt_pk_bf16_f32 v155, v186, v187
v_mfma_f32_16x16x32_bf16 a[132:135], a[88:91], a[100:103], a[132:135]
v_cvt_pk_bf16_f32 v156, v156, v157
v_cvt_pk_bf16_f32 v157, v158, v159
v_mfma_f32_16x16x32_bf16 a[136:139], a[88:91], a[104:107], a[136:139]
v_cvt_pk_bf16_f32 v158, v188, v189
v_cvt_pk_bf16_f32 v159, v190, v191
v_mfma_f32_16x16x32_bf16 a[140:143], a[88:91], a[108:111], a[140:143]
buffer_load_dwordx4 v11, s[36:39], s55 offen offset:3072 lds
v_cvt_pk_bf16_f32 v192, v192, v193
v_cvt_pk_bf16_f32 v193, v194, v195
v_mfma_f32_16x16x32_bf16 a[144:147], a[88:91], a[112:115], a[144:147]
v_cvt_pk_bf16_f32 v194, v224, v225
v_cvt_pk_bf16_f32 v195, v226, v227
v_mfma_f32_16x16x32_bf16 a[148:151], a[88:91], a[116:119], a[148:151]
v_cvt_pk_bf16_f32 v196, v196, v197
v_cvt_pk_bf16_f32 v197, v198, v199
v_mfma_f32_16x16x32_bf16 a[152:155], a[88:91], a[120:123], a[152:155]
v_cvt_pk_bf16_f32 v198, v228, v229
v_cvt_pk_bf16_f32 v199, v230, v231
v_mfma_f32_16x16x32_bf16 a[156:159], a[88:91], a[124:127], a[156:159]
s_add_u32 s54, s54, 0x80
s_add_u32 s55, s55, 0x80
v_cvt_pk_bf16_f32 v200, v200, v201
v_cvt_pk_bf16_f32 v201, v202, v203
v_mfma_f32_16x16x32_bf16 a[160:163], a[92:95], a[96:99], a[160:163]
v_cvt_pk_bf16_f32 v202, v232, v233
v_cvt_pk_bf16_f32 v203, v234, v235
v_mfma_f32_16x16x32_bf16 a[164:167], a[92:95], a[100:103], a[164:167]
v_cvt_pk_bf16_f32 v204, v204, v205
v_cvt_pk_bf16_f32 v205, v206, v207
v_mfma_f32_16x16x32_bf16 a[168:171], a[92:95], a[104:107], a[168:171]
s_add_u32 s56, s56, 0x80000
s_add_u32 s68, s68, 0x80000
v_cvt_pk_bf16_f32 v206, v236, v237
v_cvt_pk_bf16_f32 v207, v238, v239
v_mfma_f32_16x16x32_bf16 a[172:175], a[92:95], a[108:111], a[172:175]
v_cvt_pk_bf16_f32 v208, v208, v209
v_cvt_pk_bf16_f32 v209, v210, v211
v_mfma_f32_16x16x32_bf16 a[176:179], a[92:95], a[112:115], a[176:179]
v_cvt_pk_bf16_f32 v210, v240, v241
v_cvt_pk_bf16_f32 v211, v242, v243
v_mfma_f32_16x16x32_bf16 a[180:183], a[92:95], a[116:119], a[180:183]
v_cvt_pk_bf16_f32 v212, v212, v213
v_cvt_pk_bf16_f32 v213, v214, v215
v_mfma_f32_16x16x32_bf16 a[184:187], a[92:95], a[120:123], a[184:187]
v_cvt_pk_bf16_f32 v214, v244, v245
v_cvt_pk_bf16_f32 v215, v246, v247
v_mfma_f32_16x16x32_bf16 a[188:191], a[92:95], a[124:127], a[188:191]
v_cvt_pk_bf16_f32 v216, v216, v217
v_cvt_pk_bf16_f32 v217, v218, v219
s_nop 7
v_cvt_pk_bf16_f32 v218, v248, v249
v_cvt_pk_bf16_f32 v219, v250, v251
v_cvt_pk_bf16_f32 v220, v220, v221
v_cvt_pk_bf16_f32 v221, v222, v223
v_cvt_pk_bf16_f32 v222, v252, v253
v_cvt_pk_bf16_f32 v223, v254, v255
v_accvgpr_read_b32 v0, a128
v_accvgpr_read_b32 v1, a129
v_accvgpr_read_b32 v2, a130
v_accvgpr_read_b32 v3, a131
v_accvgpr_read_b32 v32, a160
v_accvgpr_read_b32 v33, a161
v_accvgpr_read_b32 v34, a162
v_accvgpr_read_b32 v35, a163
v_cvt_pk_bf16_f32 v96, v0, v1
v_cvt_pk_bf16_f32 v97, v2, v3
v_cvt_pk_bf16_f32 v98, v32, v33
v_cvt_pk_bf16_f32 v99, v34, v35
s_add_u32 m0, s53, 0x18000
s_nop 0
buffer_load_dwordx4 v12, s[40:43], s56 offen offset:0 lds
v_accvgpr_read_b32 v0, a132
v_accvgpr_read_b32 v1, a133
v_accvgpr_read_b32 v2, a134
v_accvgpr_read_b32 v3, a135
v_accvgpr_read_b32 v32, a164
v_accvgpr_read_b32 v33, a165
v_accvgpr_read_b32 v34, a166
v_accvgpr_read_b32 v35, a167
v_cvt_pk_bf16_f32 v100, v0, v1
v_cvt_pk_bf16_f32 v101, v2, v3
v_cvt_pk_bf16_f32 v102, v32, v33
v_cvt_pk_bf16_f32 v103, v34, v35
buffer_load_dwordx4 v13, s[40:43], s56 offen offset:1024 lds
s_add_u32 m0, s53, 0x1a000
s_nop 0
v_accvgpr_read_b32 v0, a136
v_accvgpr_read_b32 v1, a137
v_accvgpr_read_b32 v2, a138
v_accvgpr_read_b32 v3, a139
v_accvgpr_read_b32 v32, a168
v_accvgpr_read_b32 v33, a169
v_accvgpr_read_b32 v34, a170
v_accvgpr_read_b32 v35, a171
v_cvt_pk_bf16_f32 v104, v0, v1
v_cvt_pk_bf16_f32 v105, v2, v3
v_cvt_pk_bf16_f32 v106, v32, v33
v_cvt_pk_bf16_f32 v107, v34, v35
buffer_load_dwordx4 v12, s[40:43], s68 offen offset:0 lds
buffer_load_dwordx4 v13, s[40:43], s68 offen offset:1024 lds
v_accvgpr_read_b32 v0, a140
v_accvgpr_read_b32 v1, a141
v_accvgpr_read_b32 v2, a142
v_accvgpr_read_b32 v3, a143
v_accvgpr_read_b32 v32, a172
v_accvgpr_read_b32 v33, a173
v_accvgpr_read_b32 v34, a174
v_accvgpr_read_b32 v35, a175
v_cvt_pk_bf16_f32 v108, v0, v1
v_cvt_pk_bf16_f32 v109, v2, v3
v_cvt_pk_bf16_f32 v110, v32, v33
v_cvt_pk_bf16_f32 v111, v34, v35
s_add_u32 m0, s52, 0x18000
s_nop 0
buffer_load_dwordx4 v8, s[36:39], s54 offen offset:0 lds
v_accvgpr_read_b32 v0, a144
v_accvgpr_read_b32 v1, a145
v_accvgpr_read_b32 v2, a146
v_accvgpr_read_b32 v3, a147
v_accvgpr_read_b32 v32, a176
v_accvgpr_read_b32 v33, a177
v_accvgpr_read_b32 v34, a178
v_accvgpr_read_b32 v35, a179
v_cvt_pk_bf16_f32 v112, v0, v1
v_cvt_pk_bf16_f32 v113, v2, v3
v_cvt_pk_bf16_f32 v114, v32, v33
v_cvt_pk_bf16_f32 v115, v34, v35
buffer_load_dwordx4 v9, s[36:39], s54 offen offset:1024 lds
buffer_load_dwordx4 v10, s[36:39], s54 offen offset:2048 lds
v_accvgpr_read_b32 v0, a148
v_accvgpr_read_b32 v1, a149
v_accvgpr_read_b32 v2, a150
v_accvgpr_read_b32 v3, a151
v_accvgpr_read_b32 v32, a180
v_accvgpr_read_b32 v33, a181
v_accvgpr_read_b32 v34, a182
v_accvgpr_read_b32 v35, a183
v_cvt_pk_bf16_f32 v116, v0, v1
v_cvt_pk_bf16_f32 v117, v2, v3
v_cvt_pk_bf16_f32 v118, v32, v33
v_cvt_pk_bf16_f32 v119, v34, v35
buffer_load_dwordx4 v11, s[36:39], s54 offen offset:3072 lds
s_add_u32 m0, s52, 0x19000
s_nop 0
v_accvgpr_read_b32 v0, a152
v_accvgpr_read_b32 v1, a153
v_accvgpr_read_b32 v2, a154
v_accvgpr_read_b32 v3, a155
v_accvgpr_read_b32 v32, a184
v_accvgpr_read_b32 v33, a185
v_accvgpr_read_b32 v34, a186
v_accvgpr_read_b32 v35, a187
v_cvt_pk_bf16_f32 v120, v0, v1
v_cvt_pk_bf16_f32 v121, v2, v3
v_cvt_pk_bf16_f32 v122, v32, v33
v_cvt_pk_bf16_f32 v123, v34, v35
buffer_load_dwordx4 v8, s[36:39], s55 offen offset:0 lds
buffer_load_dwordx4 v9, s[36:39], s55 offen offset:1024 lds
v_accvgpr_read_b32 v0, a156
v_accvgpr_read_b32 v1, a157
v_accvgpr_read_b32 v2, a158
v_accvgpr_read_b32 v3, a159
v_accvgpr_read_b32 v32, a188
v_accvgpr_read_b32 v33, a189
v_accvgpr_read_b32 v34, a190
v_accvgpr_read_b32 v35, a191
v_cvt_pk_bf16_f32 v124, v0, v1
v_cvt_pk_bf16_f32 v125, v2, v3
v_cvt_pk_bf16_f32 v126, v32, v33
v_cvt_pk_bf16_f32 v127, v34, v35
buffer_load_dwordx4 v10, s[36:39], s55 offen offset:2048 lds
buffer_load_dwordx4 v11, s[36:39], s55 offen offset:3072 lds
s_add_u32 s54, s54, 0x80
s_add_u32 s55, s55, 0x80
s_add_u32 s56, s56, 0x80000
s_add_u32 s68, s68, 0x80000
v_mbcnt_lo_u32_b32 v0, -1, 0
v_mbcnt_hi_u32_b32 v0, -1, v0
s_lshl_b32 s27, s23, 13
s_add_u32 s27, s27, s24
v_lshrrev_b32 v1, 4, v0
v_bfe_u32 v2, v0, 1, 3
v_xor_b32 v1, v1, v2
v_lshlrev_b32 v1, 4, v1
v_and_b32 v2, 15, v0
v_lshl_add_u32 v1, v2, 7, v1
v_add_u32 v16, s27, v1
v_xor_b32 v17, 64, v16
v_lshrrev_b32 v1, 4, v0
v_lshlrev_b32 v2, 11, v1
v_bfe_u32 v3, v0, 2, 2
v_lshl_add_u32 v2, v3, 6, v2
v_and_b32 v3, 1, v0
v_lshl_add_u32 v2, v3, 3, v2
v_and_b32 v1, 1, v1
v_bfe_u32 v3, v0, 1, 1
v_lshl_or_b32 v1, v1, 1, v3
v_lshl_add_u32 v2, v1, 4, v2
v_add_u32 v2, s24, v2
v_mov_b32 v18, v2
v_add_u32 v18, 0x8000, v18
v_xor_b32 v19, 16, v2
v_add_u32 v19, 0x8100, v19
v_xor_b32 v20, 32, v2
v_add_u32 v20, 0x8000, v20
v_xor_b32 v21, 48, v2
v_add_u32 v21, 0x8100, v21
s_waitcnt vmcnt(24)
s_barrier
ds_read_b128 a[32:35], v16 offset:0
ds_read_b128 a[36:39], v16 offset:2048
ds_read_b128 a[40:43], v16 offset:4096
ds_read_b128 a[44:47], v16 offset:6144
ds_read_b64_tr_b16 a[0:1], v18 offset:0
ds_read_b64_tr_b16 a[2:3], v19 offset:0
ds_read_b64_tr_b16 a[4:5], v20 offset:0
ds_read_b64_tr_b16 a[6:7], v21 offset:0
ds_read_b64_tr_b16 a[8:9], v18 offset:512
ds_read_b64_tr_b16 a[10:11], v19 offset:512
ds_read_b64_tr_b16 a[12:13], v20 offset:512
ds_read_b64_tr_b16 a[14:15], v21 offset:512
ds_read_b64_tr_b16 a[16:17], v18 offset:1024
ds_read_b64_tr_b16 a[18:19], v19 offset:1024
ds_read_b64_tr_b16 a[20:21], v20 offset:1024
ds_read_b64_tr_b16 a[22:23], v21 offset:1024
ds_read_b64_tr_b16 a[24:25], v18 offset:1536
ds_read_b64_tr_b16 a[26:27], v19 offset:1536
ds_read_b64_tr_b16 a[28:29], v20 offset:1536
ds_read_b64_tr_b16 a[30:31], v21 offset:1536
v_mov_b32 v22, 0
s_waitcnt lgkmcnt(14)
v_mfma_f32_16x16x32_bf16 a[64:67], a[0:3], a[32:35], 0
s_add_u32 s27, s58, 0x4000
buffer_store_dwordx4 v[64:67], v4, s[48:51], s27 offen offset:0 sc1
v_mfma_f32_16x16x32_bf16 a[68:71], a[0:3], a[36:39], 0
ds_read_b128 a[48:51], v17 offset:0
v_mfma_f32_16x16x32_bf16 a[72:75], a[0:3], a[40:43], 0
ds_read_b128 a[52:55], v17 offset:2048
v_mfma_f32_16x16x32_bf16 a[76:79], a[0:3], a[44:47], 0
ds_read_b64_tr_b16 a[0:1], v18 offset:8192
s_waitcnt lgkmcnt(14)
v_mfma_f32_16x16x32_bf16 a[80:83], a[4:7], a[32:35], 0
ds_read_b64_tr_b16 a[2:3], v19 offset:8192
v_mfma_f32_16x16x32_bf16 a[84:87], a[4:7], a[36:39], 0
ds_read_b128 a[56:59], v17 offset:4096
v_mfma_f32_16x16x32_bf16 a[88:91], a[4:7], a[40:43], 0
ds_read_b128 a[60:63], v17 offset:6144
v_mfma_f32_16x16x32_bf16 a[92:95], a[4:7], a[44:47], 0
ds_read_b64_tr_b16 a[4:5], v20 offset:8192
s_waitcnt lgkmcnt(14)
v_mfma_f32_16x16x32_bf16 a[96:99], a[8:11], a[32:35], 0
ds_read_b64_tr_b16 a[6:7], v21 offset:8192
v_mfma_f32_16x16x32_bf16 v[24:27], v[64:67], v[64:67], 0
v_mfma_f32_16x16x32_bf16 a[100:103], a[8:11], a[36:39], 0
s_add_u32 s27, s58, 0x6000
buffer_store_dwordx4 v[128:131], v4, s[48:51], s27 offen offset:0 sc1
v_mfma_f32_16x16x32_bf16 a[104:107], a[8:11], a[40:43], 0
v_mfma_f32_16x16x32_bf16 a[108:111], a[8:11], a[44:47], 0
ds_read_b64_tr_b16 a[8:9], v18 offset:8704
v_mfma_f32_16x16x32_bf16 a[112:115], a[12:15], a[32:35], 0
ds_read_b64_tr_b16 a[10:11], v19 offset:8704
v_mfma_f32_16x16x32_bf16 a[116:119], a[12:15], a[36:39], 0
v_mfma_f32_16x16x32_bf16 a[120:123], a[12:15], a[40:43], 0
v_mfma_f32_16x16x32_bf16 a[124:127], a[12:15], a[44:47], 0
ds_read_b64_tr_b16 a[12:13], v20 offset:8704
s_waitcnt lgkmcnt(4)
v_mfma_f32_16x16x32_bf16 a[128:131], a[16:19], a[32:35], 0
ds_read_b64_tr_b16 a[14:15], v21 offset:8704
v_mfma_f32_16x16x32_bf16 a[132:135], a[16:19], a[36:39], 0
s_add_u32 s27, s58, 0xc000
buffer_store_dwordx4 v[192:195], v4, s[48:51], s27 offen offset:0 sc1
v_mfma_f32_16x16x32_bf16 a[136:139], a[16:19], a[40:43], 0
v_mfma_f32_16x16x32_bf16 a[140:143], a[16:19], a[44:47], 0
ds_read_b64_tr_b16 a[16:17], v18 offset:9216
v_mfma_f32_16x16x32_bf16 a[144:147], a[20:23], a[32:35], 0
ds_read_b64_tr_b16 a[18:19], v19 offset:9216
v_mfma_f32_16x16x32_bf16 v[24:27], v[128:131], v[128:131], v[24:27]
v_mfma_f32_16x16x32_bf16 a[148:151], a[20:23], a[36:39], 0
v_mfma_f32_16x16x32_bf16 a[152:155], a[20:23], a[40:43], 0
v_mfma_f32_16x16x32_bf16 a[156:159], a[20:23], a[44:47], 0
ds_read_b64_tr_b16 a[20:21], v20 offset:9216
v_mfma_f32_16x16x32_bf16 a[160:163], a[24:27], a[32:35], 0
ds_read_b64_tr_b16 a[22:23], v21 offset:9216
v_mfma_f32_16x16x32_bf16 a[164:167], a[24:27], a[36:39], 0
s_add_u32 s27, s58, 0xe000
buffer_store_dwordx4 v[96:99], v4, s[48:51], s27 offen offset:0 sc1
v_mfma_f32_16x16x32_bf16 a[168:171], a[24:27], a[40:43], 0
v_mfma_f32_16x16x32_bf16 a[172:175], a[24:27], a[44:47], 0
ds_read_b64_tr_b16 a[24:25], v18 offset:9728
v_mfma_f32_16x16x32_bf16 a[176:179], a[28:31], a[32:35], 0
ds_read_b64_tr_b16 a[26:27], v19 offset:9728
v_mfma_f32_16x16x32_bf16 a[180:183], a[28:31], a[36:39], 0
v_mfma_f32_16x16x32_bf16 a[184:187], a[28:31], a[40:43], 0
v_mfma_f32_16x16x32_bf16 a[188:191], a[28:31], a[44:47], 0
ds_read_b64_tr_b16 a[28:29], v20 offset:9728
ds_read_b64_tr_b16 a[30:31], v21 offset:9728
v_mfma_f32_16x16x32_bf16 a[64:67], a[0:3], a[48:51], a[64:67]
v_mfma_f32_16x16x32_bf16 a[68:71], a[0:3], a[52:55], a[68:71]
v_mfma_f32_16x16x32_bf16 a[72:75], a[0:3], a[56:59], a[72:75]
v_mfma_f32_16x16x32_bf16 a[76:79], a[0:3], a[60:63], a[76:79]
s_waitcnt lgkmcnt(4)
v_mfma_f32_16x16x32_bf16 a[80:83], a[4:7], a[48:51], a[80:83]
s_waitcnt vmcnt(16) lgkmcnt(0)
s_barrier
v_add_u32 v16, 0xc000, v16
v_add_u32 v17, 0xc000, v17
v_add_u32 v18, 0xc000, v18
v_add_u32 v19, 0xc000, v19
v_add_u32 v20, 0xc000, v20
v_add_u32 v21, 0xc000, v21
v_mfma_f32_16x16x32_bf16 a[84:87], a[4:7], a[52:55], a[84:87]
ds_read_b64_tr_b16 a[0:1], v18 offset:0
v_mfma_f32_16x16x32_bf16 a[88:91], a[4:7], a[56:59], a[88:91]
ds_read_b64_tr_b16 a[2:3], v19 offset:0
v_mfma_f32_16x16x32_bf16 a[92:95], a[4:7], a[60:63], a[92:95]
ds_read_b64_tr_b16 a[4:5], v20 offset:0
s_add_u32 m0, s53, 0
v_mfma_f32_16x16x32_bf16 a[96:99], a[8:11], a[48:51], a[96:99]
ds_read_b64_tr_b16 a[6:7], v21 offset:0
buffer_load_dwordx4 v12, s[40:43], s56 offen offset:0 lds
v_mfma_f32_16x16x32_bf16 v[24:27], v[192:195], v[192:195], v[24:27]
v_mfma_f32_16x16x32_bf16 a[100:103], a[8:11], a[52:55], a[100:103]
ds_read_b128 a[32:35], v16 offset:0
v_mfma_f32_16x16x32_bf16 a[104:107], a[8:11], a[56:59], a[104:107]
ds_read_b128 a[36:39], v16 offset:2048
buffer_load_dwordx4 v13, s[40:43], s56 offen offset:1024 lds
v_mfma_f32_16x16x32_bf16 a[108:111], a[8:11], a[60:63], a[108:111]
ds_read_b64_tr_b16 a[8:9], v18 offset:512
s_add_u32 m0, s53, 0x2000
v_mfma_f32_16x16x32_bf16 a[112:115], a[12:15], a[48:51], a[112:115]
ds_read_b64_tr_b16 a[10:11], v19 offset:512
v_mfma_f32_16x16x32_bf16 a[116:119], a[12:15], a[52:55], a[116:119]
ds_read_b128 a[40:43], v16 offset:4096
buffer_load_dwordx4 v12, s[40:43], s68 offen offset:0 lds
v_mfma_f32_16x16x32_bf16 a[120:123], a[12:15], a[56:59], a[120:123]
ds_read_b128 a[44:47], v16 offset:6144
buffer_load_dwordx4 v13, s[40:43], s68 offen offset:1024 lds
v_mfma_f32_16x16x32_bf16 a[124:127], a[12:15], a[60:63], a[124:127]
ds_read_b64_tr_b16 a[12:13], v20 offset:512
v_mfma_f32_16x16x32_bf16 a[128:131], a[16:19], a[48:51], a[128:131]
ds_read_b64_tr_b16 a[14:15], v21 offset:512
s_add_u32 m0, s52, 0
v_mfma_f32_16x16x32_bf16 a[132:135], a[16:19], a[52:55], a[132:135]
buffer_load_dwordx4 v8, s[36:39], s54 offen offset:0 lds
v_mfma_f32_16x16x32_bf16 a[136:139], a[16:19], a[56:59], a[136:139]
buffer_load_dwordx4 v9, s[36:39], s54 offen offset:1024 lds
v_mfma_f32_16x16x32_bf16 a[140:143], a[16:19], a[60:63], a[140:143]
ds_read_b64_tr_b16 a[16:17], v18 offset:1024
v_mfma_f32_16x16x32_bf16 a[144:147], a[20:23], a[48:51], a[144:147]
ds_read_b64_tr_b16 a[18:19], v19 offset:1024
buffer_load_dwordx4 v10, s[36:39], s54 offen offset:2048 lds
v_mfma_f32_16x16x32_bf16 v[24:27], v[96:99], v[96:99], v[24:27]
v_mfma_f32_16x16x32_bf16 a[148:151], a[20:23], a[52:55], a[148:151]
buffer_load_dwordx4 v11, s[36:39], s54 offen offset:3072 lds
v_mfma_f32_16x16x32_bf16 a[152:155], a[20:23], a[56:59], a[152:155]
s_add_u32 m0, s52, 0x1000
v_mfma_f32_16x16x32_bf16 a[156:159], a[20:23], a[60:63], a[156:159]
ds_read_b64_tr_b16 a[20:21], v20 offset:1024
v_mfma_f32_16x16x32_bf16 a[160:163], a[24:27], a[48:51], a[160:163]
ds_read_b64_tr_b16 a[22:23], v21 offset:1024
buffer_load_dwordx4 v8, s[36:39], s55 offen offset:0 lds
v_mfma_f32_16x16x32_bf16 a[164:167], a[24:27], a[52:55], a[164:167]
buffer_load_dwordx4 v9, s[36:39], s55 offen offset:1024 lds
v_mfma_f32_16x16x32_bf16 a[168:171], a[24:27], a[56:59], a[168:171]
buffer_load_dwordx4 v10, s[36:39], s55 offen offset:2048 lds
v_mfma_f32_16x16x32_bf16 a[172:175], a[24:27], a[60:63], a[172:175]
ds_read_b64_tr_b16 a[24:25], v18 offset:1536
v_mfma_f32_16x16x32_bf16 a[176:179], a[28:31], a[48:51], a[176:179]
ds_read_b64_tr_b16 a[26:27], v19 offset:1536
v_mfma_f32_16x16x32_bf16 a[180:183], a[28:31], a[52:55], a[180:183]
buffer_load_dwordx4 v11, s[36:39], s55 offen offset:3072 lds
v_mfma_f32_16x16x32_bf16 a[184:187], a[28:31], a[56:59], a[184:187]
s_add_u32 s54, s54, 0x80
s_add_u32 s55, s55, 0x80
s_add_u32 s56, s56, 0x80000
s_add_u32 s68, s68, 0x80000
v_mfma_f32_16x16x32_bf16 a[188:191], a[28:31], a[60:63], a[188:191]
ds_read_b64_tr_b16 a[28:29], v20 offset:1536
ds_read_b64_tr_b16 a[30:31], v21 offset:1536
s_waitcnt lgkmcnt(6)
v_mfma_f32_16x16x32_bf16 a[64:67], a[0:3], a[32:35], a[64:67]
s_add_u32 s27, s58, 0x4000
buffer_store_dwordx4 v[68:71], v4, s[48:51], s27 offen offset:1024 sc1
v_mfma_f32_16x16x32_bf16 a[68:71], a[0:3], a[36:39], a[68:71]
ds_read_b128 a[48:51], v17 offset:0
v_mfma_f32_16x16x32_bf16 a[72:75], a[0:3], a[40:43], a[72:75]
ds_read_b128 a[52:55], v17 offset:2048
v_mfma_f32_16x16x32_bf16 a[76:79], a[0:3], a[44:47], a[76:79]
ds_read_b64_tr_b16 a[0:1], v18 offset:8192
v_mfma_f32_16x16x32_bf16 a[80:83], a[4:7], a[32:35], a[80:83]
ds_read_b64_tr_b16 a[2:3], v19 offset:8192
v_mfma_f32_16x16x32_bf16 a[84:87], a[4:7], a[36:39], a[84:87]
ds_read_b128 a[56:59], v17 offset:4096
v_mfma_f32_16x16x32_bf16 a[88:91], a[4:7], a[40:43], a[88:91]
ds_read_b128 a[60:63], v17 offset:6144
v_mfma_f32_16x16x32_bf16 a[92:95], a[4:7], a[44:47], a[92:95]
ds_read_b64_tr_b16 a[4:5], v20 offset:8192
v_mfma_f32_16x16x32_bf16 a[96:99], a[8:11], a[32:35], a[96:99]
ds_read_b64_tr_b16 a[6:7], v21 offset:8192
v_mfma_f32_16x16x32_bf16 v[28:31], v[68:71], v[68:71], 0
v_mfma_f32_16x16x32_bf16 a[100:103], a[8:11], a[36:39], a[100:103]
s_add_u32 s27, s58, 0x6000
buffer_store_dwordx4 v[132:135], v4, s[48:51], s27 offen offset:1024 sc1
v_mfma_f32_16x16x32_bf16 a[104:107], a[8:11], a[40:43], a[104:107]
v_mfma_f32_16x16x32_bf16 a[108:111], a[8:11], a[44:47], a[108:111]
ds_read_b64_tr_b16 a[8:9], v18 offset:8704
v_mfma_f32_16x16x32_bf16 a[112:115], a[12:15], a[32:35], a[112:115]
ds_read_b64_tr_b16 a[10:11], v19 offset:8704
v_max3_f32 v22, v22, v24, v25
v_max3_f32 v22, v22, v26, v27
v_mfma_f32_16x16x32_bf16 a[116:119], a[12:15], a[36:39], a[116:119]
v_mfma_f32_16x16x32_bf16 a[120:123], a[12:15], a[40:43], a[120:123]
v_mfma_f32_16x16x32_bf16 a[124:127], a[12:15], a[44:47], a[124:127]
ds_read_b64_tr_b16 a[12:13], v20 offset:8704
v_mfma_f32_16x16x32_bf16 a[128:131], a[16:19], a[32:35], a[128:131]
ds_read_b64_tr_b16 a[14:15], v21 offset:8704
v_mfma_f32_16x16x32_bf16 a[132:135], a[16:19], a[36:39], a[132:135]
s_add_u32 s27, s58, 0xc000
buffer_store_dwordx4 v[196:199], v4, s[48:51], s27 offen offset:1024 sc1
v_mfma_f32_16x16x32_bf16 a[136:139], a[16:19], a[40:43], a[136:139]
v_mfma_f32_16x16x32_bf16 a[140:143], a[16:19], a[44:47], a[140:143]
ds_read_b64_tr_b16 a[16:17], v18 offset:9216
s_waitcnt lgkmcnt(5)
v_mfma_f32_16x16x32_bf16 a[144:147], a[20:23], a[32:35], a[144:147]
ds_read_b64_tr_b16 a[18:19], v19 offset:9216
v_mfma_f32_16x16x32_bf16 v[28:31], v[132:135], v[132:135], v[28:31]
v_mfma_f32_16x16x32_bf16 a[148:151], a[20:23], a[36:39], a[148:151]
v_mfma_f32_16x16x32_bf16 a[152:155], a[20:23], a[40:43], a[152:155]
v_mfma_f32_16x16x32_bf16 a[156:159], a[20:23], a[44:47], a[156:159]
ds_read_b64_tr_b16 a[20:21], v20 offset:9216
v_mfma_f32_16x16x32_bf16 a[160:163], a[24:27], a[32:35], a[160:163]
ds_read_b64_tr_b16 a[22:23], v21 offset:9216
v_mfma_f32_16x16x32_bf16 a[164:167], a[24:27], a[36:39], a[164:167]
s_add_u32 s27, s58, 0xe000
buffer_store_dwordx4 v[100:103], v4, s[48:51], s27 offen offset:1024 sc1
v_mfma_f32_16x16x32_bf16 a[168:171], a[24:27], a[40:43], a[168:171]
v_mfma_f32_16x16x32_bf16 a[172:175], a[24:27], a[44:47], a[172:175]
ds_read_b64_tr_b16 a[24:25], v18 offset:9728
v_mfma_f32_16x16x32_bf16 a[176:179], a[28:31], a[32:35], a[176:179]
ds_read_b64_tr_b16 a[26:27], v19 offset:9728
v_mfma_f32_16x16x32_bf16 a[180:183], a[28:31], a[36:39], a[180:183]
v_mfma_f32_16x16x32_bf16 a[184:187], a[28:31], a[40:43], a[184:187]
v_mfma_f32_16x16x32_bf16 a[188:191], a[28:31], a[44:47], a[188:191]
ds_read_b64_tr_b16 a[28:29], v20 offset:9728
ds_read_b64_tr_b16 a[30:31], v21 offset:9728
v_mfma_f32_16x16x32_bf16 a[64:67], a[0:3], a[48:51], a[64:67]
v_mfma_f32_16x16x32_bf16 a[68:71], a[0:3], a[52:55], a[68:71]
v_mfma_f32_16x16x32_bf16 a[72:75], a[0:3], a[56:59], a[72:75]
v_mfma_f32_16x16x32_bf16 a[76:79], a[0:3], a[60:63], a[76:79]
v_mfma_f32_16x16x32_bf16 a[80:83], a[4:7], a[48:51], a[80:83]
s_waitcnt vmcnt(20) lgkmcnt(0)
s_barrier
v_add_u32 v16, 0xc000, v16
v_add_u32 v17, 0xc000, v17
v_add_u32 v18, 0xc000, v18
v_add_u32 v19, 0xc000, v19
v_add_u32 v20, 0xc000, v20
v_add_u32 v21, 0xc000, v21
v_mfma_f32_16x16x32_bf16 a[84:87], a[4:7], a[52:55], a[84:87]
ds_read_b64_tr_b16 a[0:1], v18 offset:0
v_mfma_f32_16x16x32_bf16 a[88:91], a[4:7], a[56:59], a[88:91]
ds_read_b64_tr_b16 a[2:3], v19 offset:0
v_mfma_f32_16x16x32_bf16 a[92:95], a[4:7], a[60:63], a[92:95]
ds_read_b64_tr_b16 a[4:5], v20 offset:0
s_add_u32 m0, s53, 0xc000
v_mfma_f32_16x16x32_bf16 a[96:99], a[8:11], a[48:51], a[96:99]
ds_read_b64_tr_b16 a[6:7], v21 offset:0
buffer_load_dwordx4 v12, s[40:43], s56 offen offset:0 lds
v_mfma_f32_16x16x32_bf16 v[28:31], v[196:199], v[196:199], v[28:31]
v_mfma_f32_16x16x32_bf16 a[100:103], a[8:11], a[52:55], a[100:103]
ds_read_b128 a[32:35], v16 offset:0
v_mfma_f32_16x16x32_bf16 a[104:107], a[8:11], a[56:59], a[104:107]
ds_read_b128 a[36:39], v16 offset:2048
buffer_load_dwordx4 v13, s[40:43], s56 offen offset:1024 lds
v_mfma_f32_16x16x32_bf16 a[108:111], a[8:11], a[60:63], a[108:111]
ds_read_b64_tr_b16 a[8:9], v18 offset:512
s_add_u32 m0, s53, 0xe000
v_mfma_f32_16x16x32_bf16 a[112:115], a[12:15], a[48:51], a[112:115]
ds_read_b64_tr_b16 a[10:11], v19 offset:512
v_mfma_f32_16x16x32_bf16 a[116:119], a[12:15], a[52:55], a[116:119]
ds_read_b128 a[40:43], v16 offset:4096
buffer_load_dwordx4 v12, s[40:43], s68 offen offset:0 lds
v_mfma_f32_16x16x32_bf16 a[120:123], a[12:15], a[56:59], a[120:123]
ds_read_b128 a[44:47], v16 offset:6144
buffer_load_dwordx4 v13, s[40:43], s68 offen offset:1024 lds
v_mfma_f32_16x16x32_bf16 a[124:127], a[12:15], a[60:63], a[124:127]
ds_read_b64_tr_b16 a[12:13], v20 offset:512
v_mfma_f32_16x16x32_bf16 a[128:131], a[16:19], a[48:51], a[128:131]
ds_read_b64_tr_b16 a[14:15], v21 offset:512
s_add_u32 m0, s52, 0xc000
v_mfma_f32_16x16x32_bf16 a[132:135], a[16:19], a[52:55], a[132:135]
buffer_load_dwordx4 v8, s[36:39], s54 offen offset:0 lds
v_mfma_f32_16x16x32_bf16 a[136:139], a[16:19], a[56:59], a[136:139]
buffer_load_dwordx4 v9, s[36:39], s54 offen offset:1024 lds
v_mfma_f32_16x16x32_bf16 a[140:143], a[16:19], a[60:63], a[140:143]
ds_read_b64_tr_b16 a[16:17], v18 offset:1024
v_mfma_f32_16x16x32_bf16 a[144:147], a[20:23], a[48:51], a[144:147]
ds_read_b64_tr_b16 a[18:19], v19 offset:1024
buffer_load_dwordx4 v10, s[36:39], s54 offen offset:2048 lds
v_mfma_f32_16x16x32_bf16 v[28:31], v[100:103], v[100:103], v[28:31]
v_mfma_f32_16x16x32_bf16 a[148:151], a[20:23], a[52:55], a[148:151]
buffer_load_dwordx4 v11, s[36:39], s54 offen offset:3072 lds
v_mfma_f32_16x16x32_bf16 a[152:155], a[20:23], a[56:59], a[152:155]
s_add_u32 m0, s52, 0xd000
v_mfma_f32_16x16x32_bf16 a[156:159], a[20:23], a[60:63], a[156:159]
ds_read_b64_tr_b16 a[20:21], v20 offset:1024
v_mfma_f32_16x16x32_bf16 a[160:163], a[24:27], a[48:51], a[160:163]
ds_read_b64_tr_b16 a[22:23], v21 offset:1024
buffer_load_dwordx4 v8, s[36:39], s55 offen offset:0 lds
v_mfma_f32_16x16x32_bf16 a[164:167], a[24:27], a[52:55], a[164:167]
buffer_load_dwordx4 v9, s[36:39], s55 offen offset:1024 lds
v_mfma_f32_16x16x32_bf16 a[168:171], a[24:27], a[56:59], a[168:171]
buffer_load_dwordx4 v10, s[36:39], s55 offen offset:2048 lds
v_mfma_f32_16x16x32_bf16 a[172:175], a[24:27], a[60:63], a[172:175]
ds_read_b64_tr_b16 a[24:25], v18 offset:1536
v_mfma_f32_16x16x32_bf16 a[176:179], a[28:31], a[48:51], a[176:179]
ds_read_b64_tr_b16 a[26:27], v19 offset:1536
v_mfma_f32_16x16x32_bf16 a[180:183], a[28:31], a[52:55], a[180:183]
buffer_load_dwordx4 v11, s[36:39], s55 offen offset:3072 lds
v_mfma_f32_16x16x32_bf16 a[184:187], a[28:31], a[56:59], a[184:187]
s_add_u32 s54, s54, 0x80
s_add_u32 s55, s55, 0x80
s_add_u32 s56, s56, 0x80000
s_add_u32 s68, s68, 0x80000
v_mfma_f32_16x16x32_bf16 a[188:191], a[28:31], a[60:63], a[188:191]
ds_read_b64_tr_b16 a[28:29], v20 offset:1536
ds_read_b64_tr_b16 a[30:31], v21 offset:1536
s_waitcnt lgkmcnt(6)
v_mfma_f32_16x16x32_bf16 a[64:67], a[0:3], a[32:35], a[64:67]
s_add_u32 s27, s58, 0x4000
buffer_store_dwordx4 v[72:75], v4, s[48:51], s27 offen offset:2048 sc1
v_mfma_f32_16x16x32_bf16 a[68:71], a[0:3], a[36:39], a[68:71]
ds_read_b128 a[48:51], v17 offset:0
v_mfma_f32_16x16x32_bf16 a[72:75], a[0:3], a[40:43], a[72:75]
ds_read_b128 a[52:55], v17 offset:2048
v_mfma_f32_16x16x32_bf16 a[76:79], a[0:3], a[44:47], a[76:79]
ds_read_b64_tr_b16 a[0:1], v18 offset:8192
v_mfma_f32_16x16x32_bf16 a[80:83], a[4:7], a[32:35], a[80:83]
ds_read_b64_tr_b16 a[2:3], v19 offset:8192
v_mfma_f32_16x16x32_bf16 a[84:87], a[4:7], a[36:39], a[84:87]
ds_read_b128 a[56:59], v17 offset:4096
v_mfma_f32_16x16x32_bf16 a[88:91], a[4:7], a[40:43], a[88:91]
ds_read_b128 a[60:63], v17 offset:6144
v_mfma_f32_16x16x32_bf16 a[92:95], a[4:7], a[44:47], a[92:95]
ds_read_b64_tr_b16 a[4:5], v20 offset:8192
v_mfma_f32_16x16x32_bf16 a[96:99], a[8:11], a[32:35], a[96:99]
ds_read_b64_tr_b16 a[6:7], v21 offset:8192
v_mfma_f32_16x16x32_bf16 v[24:27], v[72:75], v[72:75], 0
v_mfma_f32_16x16x32_bf16 a[100:103], a[8:11], a[36:39], a[100:103]
s_add_u32 s27, s58, 0x6000
buffer_store_dwordx4 v[136:139], v4, s[48:51], s27 offen offset:2048 sc1
v_mfma_f32_16x16x32_bf16 a[104:107], a[8:11], a[40:43], a[104:107]
v_mfma_f32_16x16x32_bf16 a[108:111], a[8:11], a[44:47], a[108:111]
ds_read_b64_tr_b16 a[8:9], v18 offset:8704
v_mfma_f32_16x16x32_bf16 a[112:115], a[12:15], a[32:35], a[112:115]
ds_read_b64_tr_b16 a[10:11], v19 offset:8704
v_max3_f32 v22, v22, v28, v29
v_max3_f32 v22, v22, v30, v31
v_mfma_f32_16x16x32_bf16 a[116:119], a[12:15], a[36:39], a[116:119]
v_mfma_f32_16x16x32_bf16 a[120:123], a[12:15], a[40:43], a[120:123]
v_mfma_f32_16x16x32_bf16 a[124:127], a[12:15], a[44:47], a[124:127]
ds_read_b64_tr_b16 a[12:13], v20 offset:8704
v_mfma_f32_16x16x32_bf16 a[128:131], a[16:19], a[32:35], a[128:131]
ds_read_b64_tr_b16 a[14:15], v21 offset:8704
v_mfma_f32_16x16x32_bf16 a[132:135], a[16:19], a[36:39], a[132:135]
s_add_u32 s27, s58, 0xc000
buffer_store_dwordx4 v[200:203], v4, s[48:51], s27 offen offset:2048 sc1
v_mfma_f32_16x16x32_bf16 a[136:139], a[16:19], a[40:43], a[136:139]
v_mfma_f32_16x16x32_bf16 a[140:143], a[16:19], a[44:47], a[140:143]
ds_read_b64_tr_b16 a[16:17], v18 offset:9216
s_waitcnt lgkmcnt(5)
v_mfma_f32_16x16x32_bf16 a[144:147], a[20:23], a[32:35], a[144:147]
ds_read_b64_tr_b16 a[18:19], v19 offset:9216
v_mfma_f32_16x16x32_bf16 v[24:27], v[136:139], v[136:139], v[24:27]
v_mfma_f32_16x16x32_bf16 a[148:151], a[20:23], a[36:39], a[148:151]
v_mfma_f32_16x16x32_bf16 a[152:155], a[20:23], a[40:43], a[152:155]
v_mfma_f32_16x16x32_bf16 a[156:159], a[20:23], a[44:47], a[156:159]
ds_read_b64_tr_b16 a[20:21], v20 offset:9216
v_mfma_f32_16x16x32_bf16 a[160:163], a[24:27], a[32:35], a[160:163]
ds_read_b64_tr_b16 a[22:23], v21 offset:9216
v_mfma_f32_16x16x32_bf16 a[164:167], a[24:27], a[36:39], a[164:167]
s_add_u32 s27, s58, 0xe000
buffer_store_dwordx4 v[104:107], v4, s[48:51], s27 offen offset:2048 sc1
v_mfma_f32_16x16x32_bf16 a[168:171], a[24:27], a[40:43], a[168:171]
v_mfma_f32_16x16x32_bf16 a[172:175], a[24:27], a[44:47], a[172:175]
ds_read_b64_tr_b16 a[24:25], v18 offset:9728
v_mfma_f32_16x16x32_bf16 a[176:179], a[28:31], a[32:35], a[176:179]
ds_read_b64_tr_b16 a[26:27], v19 offset:9728
v_mfma_f32_16x16x32_bf16 a[180:183], a[28:31], a[36:39], a[180:183]
v_mfma_f32_16x16x32_bf16 a[184:187], a[28:31], a[40:43], a[184:187]
v_mfma_f32_16x16x32_bf16 a[188:191], a[28:31], a[44:47], a[188:191]
ds_read_b64_tr_b16 a[28:29], v20 offset:9728
ds_read_b64_tr_b16 a[30:31], v21 offset:9728
v_mfma_f32_16x16x32_bf16 a[64:67], a[0:3], a[48:51], a[64:67]
v_mfma_f32_16x16x32_bf16 a[68:71], a[0:3], a[52:55], a[68:71]
v_mfma_f32_16x16x32_bf16 a[72:75], a[0:3], a[56:59], a[72:75]
v_mfma_f32_16x16x32_bf16 a[76:79], a[0:3], a[60:63], a[76:79]
v_mfma_f32_16x16x32_bf16 a[80:83], a[4:7], a[48:51], a[80:83]
s_waitcnt vmcnt(20) lgkmcnt(0)
s_barrier
v_add_u32 v16, 0xfffe8000, v16
v_add_u32 v17, 0xfffe8000, v17
v_add_u32 v18, 0xfffe8000, v18
v_add_u32 v19, 0xfffe8000, v19
v_add_u32 v20, 0xfffe8000, v20
v_add_u32 v21, 0xfffe8000, v21
v_mfma_f32_16x16x32_bf16 a[84:87], a[4:7], a[52:55], a[84:87]
ds_read_b64_tr_b16 a[0:1], v18 offset:0
v_mfma_f32_16x16x32_bf16 a[88:91], a[4:7], a[56:59], a[88:91]
ds_read_b64_tr_b16 a[2:3], v19 offset:0
v_mfma_f32_16x16x32_bf16 a[92:95], a[4:7], a[60:63], a[92:95]
ds_read_b64_tr_b16 a[4:5], v20 offset:0
s_add_u32 m0, s53, 0x18000
v_mfma_f32_16x16x32_bf16 a[96:99], a[8:11], a[48:51], a[96:99]
ds_read_b64_tr_b16 a[6:7], v21 offset:0
buffer_load_dwordx4 v12, s[40:43], s56 offen offset:0 lds
v_mfma_f32_16x16x32_bf16 v[24:27], v[200:203], v[200:203], v[24:27]
v_mfma_f32_16x16x32_bf16 a[100:103], a[8:11], a[52:55], a[100:103]
ds_read_b128 a[32:35], v16 offset:0
v_mfma_f32_16x16x32_bf16 a[104:107], a[8:11], a[56:59], a[104:107]
ds_read_b128 a[36:39], v16 offset:2048
buffer_load_dwordx4 v13, s[40:43], s56 offen offset:1024 lds
v_mfma_f32_16x16x32_bf16 a[108:111], a[8:11], a[60:63], a[108:111]
ds_read_b64_tr_b16 a[8:9], v18 offset:512
s_add_u32 m0, s53, 0x1a000
v_mfma_f32_16x16x32_bf16 a[112:115], a[12:15], a[48:51], a[112:115]
ds_read_b64_tr_b16 a[10:11], v19 offset:512
v_mfma_f32_16x16x32_bf16 a[116:119], a[12:15], a[52:55], a[116:119]
ds_read_b128 a[40:43], v16 offset:4096
buffer_load_dwordx4 v12, s[40:43], s68 offen offset:0 lds
v_mfma_f32_16x16x32_bf16 a[120:123], a[12:15], a[56:59], a[120:123]
ds_read_b128 a[44:47], v16 offset:6144
buffer_load_dwordx4 v13, s[40:43], s68 offen offset:1024 lds
v_mfma_f32_16x16x32_bf16 a[124:127], a[12:15], a[60:63], a[124:127]
ds_read_b64_tr_b16 a[12:13], v20 offset:512
v_mfma_f32_16x16x32_bf16 a[128:131], a[16:19], a[48:51], a[128:131]
ds_read_b64_tr_b16 a[14:15], v21 offset:512
s_add_u32 m0, s52, 0x18000
v_mfma_f32_16x16x32_bf16 a[132:135], a[16:19], a[52:55], a[132:135]
buffer_load_dwordx4 v8, s[36:39], s54 offen offset:0 lds
v_mfma_f32_16x16x32_bf16 a[136:139], a[16:19], a[56:59], a[136:139]
buffer_load_dwordx4 v9, s[36:39], s54 offen offset:1024 lds
v_mfma_f32_16x16x32_bf16 a[140:143], a[16:19], a[60:63], a[140:143]
ds_read_b64_tr_b16 a[16:17], v18 offset:1024
v_mfma_f32_16x16x32_bf16 a[144:147], a[20:23], a[48:51], a[144:147]
ds_read_b64_tr_b16 a[18:19], v19 offset:1024
buffer_load_dwordx4 v10, s[36:39], s54 offen offset:2048 lds
v_mfma_f32_16x16x32_bf16 v[24:27], v[104:107], v[104:107], v[24:27]
v_mfma_f32_16x16x32_bf16 a[148:151], a[20:23], a[52:55], a[148:151]
buffer_load_dwordx4 v11, s[36:39], s54 offen offset:3072 lds
v_mfma_f32_16x16x32_bf16 a[152:155], a[20:23], a[56:59], a[152:155]
s_add_u32 m0, s52, 0x19000
v_mfma_f32_16x16x32_bf16 a[156:159], a[20:23], a[60:63], a[156:159]
ds_read_b64_tr_b16 a[20:21], v20 offset:1024
v_mfma_f32_16x16x32_bf16 a[160:163], a[24:27], a[48:51], a[160:163]
ds_read_b64_tr_b16 a[22:23], v21 offset:1024
buffer_load_dwordx4 v8, s[36:39], s55 offen offset:0 lds
v_mfma_f32_16x16x32_bf16 a[164:167], a[24:27], a[52:55], a[164:167]
buffer_load_dwordx4 v9, s[36:39], s55 offen offset:1024 lds
v_mfma_f32_16x16x32_bf16 a[168:171], a[24:27], a[56:59], a[168:171]
buffer_load_dwordx4 v10, s[36:39], s55 offen offset:2048 lds
v_mfma_f32_16x16x32_bf16 a[172:175], a[24:27], a[60:63], a[172:175]
ds_read_b64_tr_b16 a[24:25], v18 offset:1536
v_mfma_f32_16x16x32_bf16 a[176:179], a[28:31], a[48:51], a[176:179]
ds_read_b64_tr_b16 a[26:27], v19 offset:1536
v_mfma_f32_16x16x32_bf16 a[180:183], a[28:31], a[52:55], a[180:183]
buffer_load_dwordx4 v11, s[36:39], s55 offen offset:3072 lds
v_mfma_f32_16x16x32_bf16 a[184:187], a[28:31], a[56:59], a[184:187]
s_add_u32 s54, s54, 0x80
s_add_u32 s55, s55, 0x80
s_add_u32 s56, s56, 0x80000
s_add_u32 s68, s68, 0x80000
v_mfma_f32_16x16x32_bf16 a[188:191], a[28:31], a[60:63], a[188:191]
ds_read_b64_tr_b16 a[28:29], v20 offset:1536
ds_read_b64_tr_b16 a[30:31], v21 offset:1536
s_waitcnt lgkmcnt(6)
v_mfma_f32_16x16x32_bf16 a[64:67], a[0:3], a[32:35], a[64:67]
s_add_u32 s27, s58, 0x4000
buffer_store_dwordx4 v[76:79], v4, s[48:51], s27 offen offset:3072 sc1
v_mfma_f32_16x16x32_bf16 a[68:71], a[0:3], a[36:39], a[68:71]
ds_read_b128 a[48:51], v17 offset:0
v_mfma_f32_16x16x32_bf16 a[72:75], a[0:3], a[40:43], a[72:75]
ds_read_b128 a[52:55], v17 offset:2048
v_mfma_f32_16x16x32_bf16 a[76:79], a[0:3], a[44:47], a[76:79]
ds_read_b64_tr_b16 a[0:1], v18 offset:8192
v_mfma_f32_16x16x32_bf16 a[80:83], a[4:7], a[32:35], a[80:83]
ds_read_b64_tr_b16 a[2:3], v19 offset:8192
v_mfma_f32_16x16x32_bf16 a[84:87], a[4:7], a[36:39], a[84:87]
ds_read_b128 a[56:59], v17 offset:4096
v_mfma_f32_16x16x32_bf16 a[88:91], a[4:7], a[40:43], a[88:91]
ds_read_b128 a[60:63], v17 offset:6144
v_mfma_f32_16x16x32_bf16 a[92:95], a[4:7], a[44:47], a[92:95]
ds_read_b64_tr_b16 a[4:5], v20 offset:8192
v_mfma_f32_16x16x32_bf16 a[96:99], a[8:11], a[32:35], a[96:99]
ds_read_b64_tr_b16 a[6:7], v21 offset:8192
v_mfma_f32_16x16x32_bf16 v[28:31], v[76:79], v[76:79], 0
v_mfma_f32_16x16x32_bf16 a[100:103], a[8:11], a[36:39], a[100:103]
s_add_u32 s27, s58, 0x6000
buffer_store_dwordx4 v[140:143], v4, s[48:51], s27 offen offset:3072 sc1
v_mfma_f32_16x16x32_bf16 a[104:107], a[8:11], a[40:43], a[104:107]
v_mfma_f32_16x16x32_bf16 a[108:111], a[8:11], a[44:47], a[108:111]
ds_read_b64_tr_b16 a[8:9], v18 offset:8704
v_mfma_f32_16x16x32_bf16 a[112:115], a[12:15], a[32:35], a[112:115]
ds_read_b64_tr_b16 a[10:11], v19 offset:8704
v_max3_f32 v22, v22, v24, v25
v_max3_f32 v22, v22, v26, v27
v_mfma_f32_16x16x32_bf16 a[116:119], a[12:15], a[36:39], a[116:119]
v_mfma_f32_16x16x32_bf16 a[120:123], a[12:15], a[40:43], a[120:123]
v_mfma_f32_16x16x32_bf16 a[124:127], a[12:15], a[44:47], a[124:127]
ds_read_b64_tr_b16 a[12:13], v20 offset:8704
v_mfma_f32_16x16x32_bf16 a[128:131], a[16:19], a[32:35], a[128:131]
ds_read_b64_tr_b16 a[14:15], v21 offset:8704
v_mfma_f32_16x16x32_bf16 a[132:135], a[16:19], a[36:39], a[132:135]
s_add_u32 s27, s58, 0xc000
buffer_store_dwordx4 v[204:207], v4, s[48:51], s27 offen offset:3072 sc1
v_mfma_f32_16x16x32_bf16 a[136:139], a[16:19], a[40:43], a[136:139]
v_mfma_f32_16x16x32_bf16 a[140:143], a[16:19], a[44:47], a[140:143]
ds_read_b64_tr_b16 a[16:17], v18 offset:9216
s_waitcnt lgkmcnt(5)
v_mfma_f32_16x16x32_bf16 a[144:147], a[20:23], a[32:35], a[144:147]
ds_read_b64_tr_b16 a[18:19], v19 offset:9216
v_mfma_f32_16x16x32_bf16 v[28:31], v[140:143], v[140:143], v[28:31]
v_mfma_f32_16x16x32_bf16 a[148:151], a[20:23], a[36:39], a[148:151]
v_mfma_f32_16x16x32_bf16 a[152:155], a[20:23], a[40:43], a[152:155]
v_mfma_f32_16x16x32_bf16 a[156:159], a[20:23], a[44:47], a[156:159]
ds_read_b64_tr_b16 a[20:21], v20 offset:9216
v_mfma_f32_16x16x32_bf16 a[160:163], a[24:27], a[32:35], a[160:163]
ds_read_b64_tr_b16 a[22:23], v21 offset:9216
v_mfma_f32_16x16x32_bf16 a[164:167], a[24:27], a[36:39], a[164:167]
s_add_u32 s27, s58, 0xe000
buffer_store_dwordx4 v[108:111], v4, s[48:51], s27 offen offset:3072 sc1
v_mfma_f32_16x16x32_bf16 a[168:171], a[24:27], a[40:43], a[168:171]
v_mfma_f32_16x16x32_bf16 a[172:175], a[24:27], a[44:47], a[172:175]
ds_read_b64_tr_b16 a[24:25], v18 offset:9728
v_mfma_f32_16x16x32_bf16 a[176:179], a[28:31], a[32:35], a[176:179]
ds_read_b64_tr_b16 a[26:27], v19 offset:9728
v_mfma_f32_16x16x32_bf16 a[180:183], a[28:31], a[36:39], a[180:183]
v_mfma_f32_16x16x32_bf16 a[184:187], a[28:31], a[40:43], a[184:187]
v_mfma_f32_16x16x32_bf16 a[188:191], a[28:31], a[44:47], a[188:191]
ds_read_b64_tr_b16 a[28:29], v20 offset:9728
ds_read_b64_tr_b16 a[30:31], v21 offset:9728
v_mfma_f32_16x16x32_bf16 a[64:67], a[0:3], a[48:51], a[64:67]
v_mfma_f32_16x16x32_bf16 a[68:71], a[0:3], a[52:55], a[68:71]
v_mfma_f32_16x16x32_bf16 a[72:75], a[0:3], a[56:59], a[72:75]
v_mfma_f32_16x16x32_bf16 a[76:79], a[0:3], a[60:63], a[76:79]
v_mfma_f32_16x16x32_bf16 a[80:83], a[4:7], a[48:51], a[80:83]
s_waitcnt vmcnt(20) lgkmcnt(0)
s_barrier
v_add_u32 v16, 0xc000, v16
v_add_u32 v17, 0xc000, v17
v_add_u32 v18, 0xc000, v18
v_add_u32 v19, 0xc000, v19
v_add_u32 v20, 0xc000, v20
v_add_u32 v21, 0xc000, v21
v_mfma_f32_16x16x32_bf16 a[84:87], a[4:7], a[52:55], a[84:87]
ds_read_b64_tr_b16 a[0:1], v18 offset:0
v_mfma_f32_16x16x32_bf16 a[88:91], a[4:7], a[56:59], a[88:91]
ds_read_b64_tr_b16 a[2:3], v19 offset:0
v_mfma_f32_16x16x32_bf16 a[92:95], a[4:7], a[60:63], a[92:95]
ds_read_b64_tr_b16 a[4:5], v20 offset:0
s_add_u32 m0, s53, 0
v_mfma_f32_16x16x32_bf16 a[96:99], a[8:11], a[48:51], a[96:99]
ds_read_b64_tr_b16 a[6:7], v21 offset:0
buffer_load_dwordx4 v12, s[40:43], s56 offen offset:0 lds
v_mfma_f32_16x16x32_bf16 v[28:31], v[204:207], v[204:207], v[28:31]
v_mfma_f32_16x16x32_bf16 a[100:103], a[8:11], a[52:55], a[100:103]
ds_read_b128 a[32:35], v16 offset:0
v_mfma_f32_16x16x32_bf16 a[104:107], a[8:11], a[56:59], a[104:107]
ds_read_b128 a[36:39], v16 offset:2048
buffer_load_dwordx4 v13, s[40:43], s56 offen offset:1024 lds
v_mfma_f32_16x16x32_bf16 a[108:111], a[8:11], a[60:63], a[108:111]
ds_read_b64_tr_b16 a[8:9], v18 offset:512
s_add_u32 m0, s53, 0x2000
v_mfma_f32_16x16x32_bf16 a[112:115], a[12:15], a[48:51], a[112:115]
ds_read_b64_tr_b16 a[10:11], v19 offset:512
v_mfma_f32_16x16x32_bf16 a[116:119], a[12:15], a[52:55], a[116:119]
ds_read_b128 a[40:43], v16 offset:4096
buffer_load_dwordx4 v12, s[40:43], s68 offen offset:0 lds
v_mfma_f32_16x16x32_bf16 a[120:123], a[12:15], a[56:59], a[120:123]
ds_read_b128 a[44:47], v16 offset:6144
buffer_load_dwordx4 v13, s[40:43], s68 offen offset:1024 lds
v_mfma_f32_16x16x32_bf16 a[124:127], a[12:15], a[60:63], a[124:127]
ds_read_b64_tr_b16 a[12:13], v20 offset:512
v_mfma_f32_16x16x32_bf16 a[128:131], a[16:19], a[48:51], a[128:131]
ds_read_b64_tr_b16 a[14:15], v21 offset:512
s_add_u32 m0, s52, 0
v_mfma_f32_16x16x32_bf16 a[132:135], a[16:19], a[52:55], a[132:135]
buffer_load_dwordx4 v8, s[36:39], s54 offen offset:0 lds
v_mfma_f32_16x16x32_bf16 a[136:139], a[16:19], a[56:59], a[136:139]
buffer_load_dwordx4 v9, s[36:39], s54 offen offset:1024 lds
v_mfma_f32_16x16x32_bf16 a[140:143], a[16:19], a[60:63], a[140:143]
ds_read_b64_tr_b16 a[16:17], v18 offset:1024
v_mfma_f32_16x16x32_bf16 a[144:147], a[20:23], a[48:51], a[144:147]
ds_read_b64_tr_b16 a[18:19], v19 offset:1024
buffer_load_dwordx4 v10, s[36:39], s54 offen offset:2048 lds
v_mfma_f32_16x16x32_bf16 v[28:31], v[108:111], v[108:111], v[28:31]
v_mfma_f32_16x16x32_bf16 a[148:151], a[20:23], a[52:55], a[148:151]
buffer_load_dwordx4 v11, s[36:39], s54 offen offset:3072 lds
v_mfma_f32_16x16x32_bf16 a[152:155], a[20:23], a[56:59], a[152:155]
s_add_u32 m0, s52, 0x1000
v_mfma_f32_16x16x32_bf16 a[156:159], a[20:23], a[60:63], a[156:159]
ds_read_b64_tr_b16 a[20:21], v20 offset:1024
v_mfma_f32_16x16x32_bf16 a[160:163], a[24:27], a[48:51], a[160:163]
ds_read_b64_tr_b16 a[22:23], v21 offset:1024
buffer_load_dwordx4 v8, s[36:39], s55 offen offset:0 lds
v_mfma_f32_16x16x32_bf16 a[164:167], a[24:27], a[52:55], a[164:167]
buffer_load_dwordx4 v9, s[36:39], s55 offen offset:1024 lds
v_mfma_f32_16x16x32_bf16 a[168:171], a[24:27], a[56:59], a[168:171]
buffer_load_dwordx4 v10, s[36:39], s55 offen offset:2048 lds
v_mfma_f32_16x16x32_bf16 a[172:175], a[24:27], a[60:63], a[172:175]
ds_read_b64_tr_b16 a[24:25], v18 offset:1536
v_mfma_f32_16x16x32_bf16 a[176:179], a[28:31], a[48:51], a[176:179]
ds_read_b64_tr_b16 a[26:27], v19 offset:1536
v_mfma_f32_16x16x32_bf16 a[180:183], a[28:31], a[52:55], a[180:183]
buffer_load_dwordx4 v11, s[36:39], s55 offen offset:3072 lds
v_mfma_f32_16x16x32_bf16 a[184:187], a[28:31], a[56:59], a[184:187]
s_add_u32 s54, s54, 0x80
s_add_u32 s55, s55, 0x80
s_add_u32 s56, s56, 0x80000
s_add_u32 s68, s68, 0x80000
v_mfma_f32_16x16x32_bf16 a[188:191], a[28:31], a[60:63], a[188:191]
ds_read_b64_tr_b16 a[28:29], v20 offset:1536
ds_read_b64_tr_b16 a[30:31], v21 offset:1536
s_waitcnt lgkmcnt(6)
v_mfma_f32_16x16x32_bf16 a[64:67], a[0:3], a[32:35], a[64:67]
s_add_u32 s27, s58, 0x5000
buffer_store_dwordx4 v[80:83], v4, s[48:51], s27 offen offset:0 sc1
v_mfma_f32_16x16x32_bf16 a[68:71], a[0:3], a[36:39], a[68:71]
ds_read_b128 a[48:51], v17 offset:0
v_mfma_f32_16x16x32_bf16 a[72:75], a[0:3], a[40:43], a[72:75]
ds_read_b128 a[52:55], v17 offset:2048
v_mfma_f32_16x16x32_bf16 a[76:79], a[0:3], a[44:47], a[76:79]
ds_read_b64_tr_b16 a[0:1], v18 offset:8192
v_mfma_f32_16x16x32_bf16 a[80:83], a[4:7], a[32:35], a[80:83]
ds_read_b64_tr_b16 a[2:3], v19 offset:8192
v_mfma_f32_16x16x32_bf16 a[84:87], a[4:7], a[36:39], a[84:87]
ds_read_b128 a[56:59], v17 offset:4096
v_mfma_f32_16x16x32_bf16 a[88:91], a[4:7], a[40:43], a[88:91]
ds_read_b128 a[60:63], v17 offset:6144
v_mfma_f32_16x16x32_bf16 a[92:95], a[4:7], a[44:47], a[92:95]
ds_read_b64_tr_b16 a[4:5], v20 offset:8192
v_mfma_f32_16x16x32_bf16 a[96:99], a[8:11], a[32:35], a[96:99]
ds_read_b64_tr_b16 a[6:7], v21 offset:8192
v_mfma_f32_16x16x32_bf16 v[24:27], v[80:83], v[80:83], 0
v_mfma_f32_16x16x32_bf16 a[100:103], a[8:11], a[36:39], a[100:103]
s_add_u32 s27, s58, 0x7000
buffer_store_dwordx4 v[144:147], v4, s[48:51], s27 offen offset:0 sc1
v_mfma_f32_16x16x32_bf16 a[104:107], a[8:11], a[40:43], a[104:107]
v_mfma_f32_16x16x32_bf16 a[108:111], a[8:11], a[44:47], a[108:111]
ds_read_b64_tr_b16 a[8:9], v18 offset:8704
v_mfma_f32_16x16x32_bf16 a[112:115], a[12:15], a[32:35], a[112:115]
ds_read_b64_tr_b16 a[10:11], v19 offset:8704
v_max3_f32 v22, v22, v28, v29
v_max3_f32 v22, v22, v30, v31
v_mfma_f32_16x16x32_bf16 a[116:119], a[12:15], a[36:39], a[116:119]
v_mfma_f32_16x16x32_bf16 a[120:123], a[12:15], a[40:43], a[120:123]
v_mfma_f32_16x16x32_bf16 a[124:127], a[12:15], a[44:47], a[124:127]
ds_read_b64_tr_b16 a[12:13], v20 offset:8704
v_mfma_f32_16x16x32_bf16 a[128:131], a[16:19], a[32:35], a[128:131]
ds_read_b64_tr_b16 a[14:15], v21 offset:8704
v_mfma_f32_16x16x32_bf16 a[132:135], a[16:19], a[36:39], a[132:135]
s_add_u32 s27, s58, 0xd000
buffer_store_dwordx4 v[208:211], v4, s[48:51], s27 offen offset:0 sc1
v_mfma_f32_16x16x32_bf16 a[136:139], a[16:19], a[40:43], a[136:139]
v_mfma_f32_16x16x32_bf16 a[140:143], a[16:19], a[44:47], a[140:143]
ds_read_b64_tr_b16 a[16:17], v18 offset:9216
s_waitcnt lgkmcnt(5)
v_mfma_f32_16x16x32_bf16 a[144:147], a[20:23], a[32:35], a[144:147]
ds_read_b64_tr_b16 a[18:19], v19 offset:9216
v_mfma_f32_16x16x32_bf16 v[24:27], v[144:147], v[144:147], v[24:27]
v_mfma_f32_16x16x32_bf16 a[148:151], a[20:23], a[36:39], a[148:151]
v_mfma_f32_16x16x32_bf16 a[152:155], a[20:23], a[40:43], a[152:155]
v_mfma_f32_16x16x32_bf16 a[156:159], a[20:23], a[44:47], a[156:159]
ds_read_b64_tr_b16 a[20:21], v20 offset:9216
v_mfma_f32_16x16x32_bf16 a[160:163], a[24:27], a[32:35], a[160:163]
ds_read_b64_tr_b16 a[22:23], v21 offset:9216
v_mfma_f32_16x16x32_bf16 a[164:167], a[24:27], a[36:39], a[164:167]
s_add_u32 s27, s58, 0xf000
buffer_store_dwordx4 v[112:115], v4, s[48:51], s27 offen offset:0 sc1
v_mfma_f32_16x16x32_bf16 a[168:171], a[24:27], a[40:43], a[168:171]
v_mfma_f32_16x16x32_bf16 a[172:175], a[24:27], a[44:47], a[172:175]
ds_read_b64_tr_b16 a[24:25], v18 offset:9728
v_mfma_f32_16x16x32_bf16 a[176:179], a[28:31], a[32:35], a[176:179]
ds_read_b64_tr_b16 a[26:27], v19 offset:9728
v_mfma_f32_16x16x32_bf16 a[180:183], a[28:31], a[36:39], a[180:183]
v_mfma_f32_16x16x32_bf16 a[184:187], a[28:31], a[40:43], a[184:187]
v_mfma_f32_16x16x32_bf16 a[188:191], a[28:31], a[44:47], a[188:191]
ds_read_b64_tr_b16 a[28:29], v20 offset:9728
ds_read_b64_tr_b16 a[30:31], v21 offset:9728
v_mfma_f32_16x16x32_bf16 a[64:67], a[0:3], a[48:51], a[64:67]
v_mfma_f32_16x16x32_bf16 a[68:71], a[0:3], a[52:55], a[68:71]
v_mfma_f32_16x16x32_bf16 a[72:75], a[0:3], a[56:59], a[72:75]
v_mfma_f32_16x16x32_bf16 a[76:79], a[0:3], a[60:63], a[76:79]
v_mfma_f32_16x16x32_bf16 a[80:83], a[4:7], a[48:51], a[80:83]
s_waitcnt vmcnt(20) lgkmcnt(0)
s_barrier
v_add_u32 v16, 0xc000, v16
v_add_u32 v17, 0xc000, v17
v_add_u32 v18, 0xc000, v18
v_add_u32 v19, 0xc000, v19
v_add_u32 v20, 0xc000, v20
v_add_u32 v21, 0xc000, v21
v_mfma_f32_16x16x32_bf16 a[84:87], a[4:7], a[52:55], a[84:87]
ds_read_b64_tr_b16 a[0:1], v18 offset:0
v_mfma_f32_16x16x32_bf16 a[88:91], a[4:7], a[56:59], a[88:91]
ds_read_b64_tr_b16 a[2:3], v19 offset:0
v_mfma_f32_16x16x32_bf16 a[92:95], a[4:7], a[60:63], a[92:95]
ds_read_b64_tr_b16 a[4:5], v20 offset:0
s_add_u32 m0, s53, 0xc000
v_mfma_f32_16x16x32_bf16 a[96:99], a[8:11], a[48:51], a[96:99]
ds_read_b64_tr_b16 a[6:7], v21 offset:0
buffer_load_dwordx4 v12, s[40:43], s56 offen offset:0 lds
v_mfma_f32_16x16x32_bf16 v[24:27], v[208:211], v[208:211], v[24:27]
v_mfma_f32_16x16x32_bf16 a[100:103], a[8:11], a[52:55], a[100:103]
ds_read_b128 a[32:35], v16 offset:0
v_mfma_f32_16x16x32_bf16 a[104:107], a[8:11], a[56:59], a[104:107]
ds_read_b128 a[36:39], v16 offset:2048
buffer_load_dwordx4 v13, s[40:43], s56 offen offset:1024 lds
v_mfma_f32_16x16x32_bf16 a[108:111], a[8:11], a[60:63], a[108:111]
ds_read_b64_tr_b16 a[8:9], v18 offset:512
s_add_u32 m0, s53, 0xe000
v_mfma_f32_16x16x32_bf16 a[112:115], a[12:15], a[48:51], a[112:115]
ds_read_b64_tr_b16 a[10:11], v19 offset:512
v_mfma_f32_16x16x32_bf16 a[116:119], a[12:15], a[52:55], a[116:119]
ds_read_b128 a[40:43], v16 offset:4096
buffer_load_dwordx4 v12, s[40:43], s68 offen offset:0 lds
v_mfma_f32_16x16x32_bf16 a[120:123], a[12:15], a[56:59], a[120:123]
ds_read_b128 a[44:47], v16 offset:6144
buffer_load_dwordx4 v13, s[40:43], s68 offen offset:1024 lds
v_mfma_f32_16x16x32_bf16 a[124:127], a[12:15], a[60:63], a[124:127]
ds_read_b64_tr_b16 a[12:13], v20 offset:512
v_mfma_f32_16x16x32_bf16 a[128:131], a[16:19], a[48:51], a[128:131]
ds_read_b64_tr_b16 a[14:15], v21 offset:512
s_add_u32 m0, s52, 0xc000
v_mfma_f32_16x16x32_bf16 a[132:135], a[16:19], a[52:55], a[132:135]
buffer_load_dwordx4 v8, s[36:39], s54 offen offset:0 lds
v_mfma_f32_16x16x32_bf16 a[136:139], a[16:19], a[56:59], a[136:139]
buffer_load_dwordx4 v9, s[36:39], s54 offen offset:1024 lds
v_mfma_f32_16x16x32_bf16 a[140:143], a[16:19], a[60:63], a[140:143]
ds_read_b64_tr_b16 a[16:17], v18 offset:1024
v_mfma_f32_16x16x32_bf16 a[144:147], a[20:23], a[48:51], a[144:147]
ds_read_b64_tr_b16 a[18:19], v19 offset:1024
buffer_load_dwordx4 v10, s[36:39], s54 offen offset:2048 lds
v_mfma_f32_16x16x32_bf16 v[24:27], v[112:115], v[112:115], v[24:27]
v_mfma_f32_16x16x32_bf16 a[148:151], a[20:23], a[52:55], a[148:151]
buffer_load_dwordx4 v11, s[36:39], s54 offen offset:3072 lds
v_mfma_f32_16x16x32_bf16 a[152:155], a[20:23], a[56:59], a[152:155]
s_add_u32 m0, s52, 0xd000
v_mfma_f32_16x16x32_bf16 a[156:159], a[20:23], a[60:63], a[156:159]
ds_read_b64_tr_b16 a[20:21], v20 offset:1024
v_mfma_f32_16x16x32_bf16 a[160:163], a[24:27], a[48:51], a[160:163]
ds_read_b64_tr_b16 a[22:23], v21 offset:1024
buffer_load_dwordx4 v8, s[36:39], s55 offen offset:0 lds
v_mfma_f32_16x16x32_bf16 a[164:167], a[24:27], a[52:55], a[164:167]
buffer_load_dwordx4 v9, s[36:39], s55 offen offset:1024 lds
v_mfma_f32_16x16x32_bf16 a[168:171], a[24:27], a[56:59], a[168:171]
buffer_load_dwordx4 v10, s[36:39], s55 offen offset:2048 lds
v_mfma_f32_16x16x32_bf16 a[172:175], a[24:27], a[60:63], a[172:175]
ds_read_b64_tr_b16 a[24:25], v18 offset:1536
v_mfma_f32_16x16x32_bf16 a[176:179], a[28:31], a[48:51], a[176:179]
ds_read_b64_tr_b16 a[26:27], v19 offset:1536
v_mfma_f32_16x16x32_bf16 a[180:183], a[28:31], a[52:55], a[180:183]
buffer_load_dwordx4 v11, s[36:39], s55 offen offset:3072 lds
v_mfma_f32_16x16x32_bf16 a[184:187], a[28:31], a[56:59], a[184:187]
s_add_u32 s54, s54, 0x80
s_add_u32 s55, s55, 0x80
s_add_u32 s56, s56, 0x80000
s_add_u32 s68, s68, 0x80000
v_mfma_f32_16x16x32_bf16 a[188:191], a[28:31], a[60:63], a[188:191]
ds_read_b64_tr_b16 a[28:29], v20 offset:1536
ds_read_b64_tr_b16 a[30:31], v21 offset:1536
s_waitcnt lgkmcnt(6)
v_mfma_f32_16x16x32_bf16 a[64:67], a[0:3], a[32:35], a[64:67]
v_mbcnt_lo_u32_b32 v3, -1, 0
v_mbcnt_hi_u32_b32 v3, -1, v3
v_lshrrev_b32 v36, 5, v3
v_lshlrev_b32 v36, 10, v36
v_and_b32 v3, 31, v3
v_lshl_add_u32 v3, v3, 4, v36
s_lshr_b32 s27, s22, 1
s_lshl_b32 s27, s27, 15
s_lshl_b32 s28, s23, 13
s_add_u32 s27, s27, s28
s_and_b32 s28, s22, 1
s_lshl_b32 s28, s28, 9
s_add_u32 s27, s27, s28
v_add_u32 v3, s27, v3
s_lshl_b32 s28, s23, 12
s_add_u32 s28, s28, s24
s_add_u32 s28, s28, 0x24000
v_mfma_f32_16x16x32_bf16 a[68:71], a[0:3], a[36:39], a[68:71]
ds_read_b128 a[48:51], v17 offset:0
v_mfma_f32_16x16x32_bf16 a[72:75], a[0:3], a[40:43], a[72:75]
ds_read_b128 a[52:55], v17 offset:2048
v_mfma_f32_16x16x32_bf16 a[76:79], a[0:3], a[44:47], a[76:79]
ds_read_b64_tr_b16 a[0:1], v18 offset:8192
v_mfma_f32_16x16x32_bf16 a[80:83], a[4:7], a[32:35], a[80:83]
ds_read_b64_tr_b16 a[2:3], v19 offset:8192
v_mfma_f32_16x16x32_bf16 a[84:87], a[4:7], a[36:39], a[84:87]
ds_read_b128 a[56:59], v17 offset:4096
v_mfma_f32_16x16x32_bf16 a[88:91], a[4:7], a[40:43], a[88:91]
ds_read_b128 a[60:63], v17 offset:6144
v_mfma_f32_16x16x32_bf16 a[92:95], a[4:7], a[44:47], a[92:95]
ds_read_b64_tr_b16 a[4:5], v20 offset:8192
s_add_u32 m0, s28, 0
s_nop 0
global_load_lds_dwordx4 v3, s[96:97] offset:0
v_add_u32 v3, 0x800, v3
v_mfma_f32_16x16x32_bf16 a[96:99], a[8:11], a[32:35], a[96:99]
ds_read_b64_tr_b16 a[6:7], v21 offset:8192
v_mfma_f32_16x16x32_bf16 v[28:31], v[84:87], v[84:87], 0
v_mfma_f32_16x16x32_bf16 a[100:103], a[8:11], a[36:39], a[100:103]
s_add_u32 m0, s28, 0x400
s_nop 0
global_load_lds_dwordx4 v3, s[96:97] offset:0
v_add_u32 v3, 0x800, v3
v_mfma_f32_16x16x32_bf16 a[104:107], a[8:11], a[40:43], a[104:107]
s_add_u32 m0, s28, 0x800
s_nop 0
global_load_lds_dwordx4 v3, s[96:97] offset:0
v_add_u32 v3, 0x800, v3
v_mfma_f32_16x16x32_bf16 a[108:111], a[8:11], a[44:47], a[108:111]
ds_read_b64_tr_b16 a[8:9], v18 offset:8704
v_mfma_f32_16x16x32_bf16 a[112:115], a[12:15], a[32:35], a[112:115]
ds_read_b64_tr_b16 a[10:11], v19 offset:8704
v_max3_f32 v22, v22, v24, v25
v_max3_f32 v22, v22, v26, v27
v_mfma_f32_16x16x32_bf16 a[116:119], a[12:15], a[36:39], a[116:119]
s_add_u32 m0, s28, 0xc00
s_nop 0
global_load_lds_dwordx4 v3, s[96:97] offset:0
v_add_u32 v3, 0x800, v3
v_mfma_f32_16x16x32_bf16 a[120:123], a[12:15], a[40:43], a[120:123]
v_mfma_f32_16x16x32_bf16 a[124:127], a[12:15], a[44:47], a[124:127]
ds_read_b64_tr_b16 a[12:13], v20 offset:8704
v_mfma_f32_16x16x32_bf16 a[128:131], a[16:19], a[32:35], a[128:131]
ds_read_b64_tr_b16 a[14:15], v21 offset:8704
v_mfma_f32_16x16x32_bf16 a[132:135], a[16:19], a[36:39], a[132:135]
s_add_u32 s27, s58, 0x5000
buffer_store_dwordx4 v[84:87], v4, s[48:51], s27 offen offset:1024 sc1
v_mfma_f32_16x16x32_bf16 a[136:139], a[16:19], a[40:43], a[136:139]
v_mfma_f32_16x16x32_bf16 a[140:143], a[16:19], a[44:47], a[140:143]
ds_read_b64_tr_b16 a[16:17], v18 offset:9216
s_waitcnt lgkmcnt(5)
v_mfma_f32_16x16x32_bf16 a[144:147], a[20:23], a[32:35], a[144:147]
ds_read_b64_tr_b16 a[18:19], v19 offset:9216
v_mfma_f32_16x16x32_bf16 v[28:31], v[148:151], v[148:151], v[28:31]
v_mfma_f32_16x16x32_bf16 a[148:151], a[20:23], a[36:39], a[148:151]
s_add_u32 s27, s58, 0x7000
buffer_store_dwordx4 v[148:151], v4, s[48:51], s27 offen offset:1024 sc1
v_mfma_f32_16x16x32_bf16 a[152:155], a[20:23], a[40:43], a[152:155]
v_mfma_f32_16x16x32_bf16 a[156:159], a[20:23], a[44:47], a[156:159]
ds_read_b64_tr_b16 a[20:21], v20 offset:9216
v_mfma_f32_16x16x32_bf16 a[160:163], a[24:27], a[32:35], a[160:163]
ds_read_b64_tr_b16 a[22:23], v21 offset:9216
v_mfma_f32_16x16x32_bf16 a[164:167], a[24:27], a[36:39], a[164:167]
s_add_u32 s27, s58, 0xd000
buffer_store_dwordx4 v[212:215], v4, s[48:51], s27 offen offset:1024 sc1
v_mfma_f32_16x16x32_bf16 a[168:171], a[24:27], a[40:43], a[168:171]
v_mfma_f32_16x16x32_bf16 a[172:175], a[24:27], a[44:47], a[172:175]
ds_read_b64_tr_b16 a[24:25], v18 offset:9728
v_mfma_f32_16x16x32_bf16 a[176:179], a[28:31], a[32:35], a[176:179]
ds_read_b64_tr_b16 a[26:27], v19 offset:9728
v_mfma_f32_16x16x32_bf16 a[180:183], a[28:31], a[36:39], a[180:183]
s_add_u32 s27, s58, 0xf000
buffer_store_dwordx4 v[116:119], v4, s[48:51], s27 offen offset:1024 sc1
v_mfma_f32_16x16x32_bf16 a[184:187], a[28:31], a[40:43], a[184:187]
v_mfma_f32_16x16x32_bf16 a[188:191], a[28:31], a[44:47], a[188:191]
ds_read_b64_tr_b16 a[28:29], v20 offset:9728
ds_read_b64_tr_b16 a[30:31], v21 offset:9728
v_mfma_f32_16x16x32_bf16 a[64:67], a[0:3], a[48:51], a[64:67]
v_mfma_f32_16x16x32_bf16 a[68:71], a[0:3], a[52:55], a[68:71]
v_mfma_f32_16x16x32_bf16 a[72:75], a[0:3], a[56:59], a[72:75]
v_mfma_f32_16x16x32_bf16 a[76:79], a[0:3], a[60:63], a[76:79]
v_mfma_f32_16x16x32_bf16 a[80:83], a[4:7], a[48:51], a[80:83]
s_waitcnt vmcnt(24) lgkmcnt(0)
s_barrier
v_add_u32 v16, 0xfffe8000, v16
v_add_u32 v17, 0xfffe8000, v17
v_add_u32 v18, 0xfffe8000, v18
v_add_u32 v19, 0xfffe8000, v19
v_add_u32 v20, 0xfffe8000, v20
v_add_u32 v21, 0xfffe8000, v21
v_mfma_f32_16x16x32_bf16 a[84:87], a[4:7], a[52:55], a[84:87]
ds_read_b64_tr_b16 a[0:1], v18 offset:0
v_mfma_f32_16x16x32_bf16 a[88:91], a[4:7], a[56:59], a[88:91]
ds_read_b64_tr_b16 a[2:3], v19 offset:0
v_mfma_f32_16x16x32_bf16 a[92:95], a[4:7], a[60:63], a[92:95]
ds_read_b64_tr_b16 a[4:5], v20 offset:0
v_mfma_f32_16x16x32_bf16 a[96:99], a[8:11], a[48:51], a[96:99]
ds_read_b64_tr_b16 a[6:7], v21 offset:0
v_mfma_f32_16x16x32_bf16 v[28:31], v[212:215], v[212:215], v[28:31]
v_mfma_f32_16x16x32_bf16 a[100:103], a[8:11], a[52:55], a[100:103]
ds_read_b128 a[32:35], v16 offset:0
v_mfma_f32_16x16x32_bf16 a[104:107], a[8:11], a[56:59], a[104:107]
ds_read_b128 a[36:39], v16 offset:2048
v_mbcnt_lo_u32_b32 v0, -1, 0
v_mbcnt_hi_u32_b32 v0, -1, v0
s_lshl_b32 s27, s23, 6
v_add_u32 v0, s27, v0
v_mfma_f32_16x16x32_bf16 a[108:111], a[8:11], a[60:63], a[108:111]
ds_read_b64_tr_b16 a[8:9], v18 offset:512
v_mfma_f32_16x16x32_bf16 a[112:115], a[12:15], a[48:51], a[112:115]
ds_read_b64_tr_b16 a[10:11], v19 offset:512
v_mfma_f32_16x16x32_bf16 a[116:119], a[12:15], a[52:55], a[116:119]
ds_read_b128 a[40:43], v16 offset:4096
v_mfma_f32_16x16x32_bf16 a[120:123], a[12:15], a[56:59], a[120:123]
ds_read_b128 a[44:47], v16 offset:6144
v_lshrrev_b32 v1, 3, v0
v_lshlrev_b32 v1, 11, v1
v_and_b32 v2, 7, v0
v_lshl_add_u32 v1, v2, 4, v1
s_lshr_b32 s27, s22, 3
s_lshl_b32 s27, s27, 16
s_and_b32 s28, s22, 7
s_lshl_b32 s28, s28, 7
s_add_u32 s27, s27, s28
v_add_u32 v1, s27, v1
v_mfma_f32_16x16x32_bf16 a[124:127], a[12:15], a[60:63], a[124:127]
ds_read_b64_tr_b16 a[12:13], v20 offset:512
v_mfma_f32_16x16x32_bf16 a[128:131], a[16:19], a[48:51], a[128:131]
ds_read_b64_tr_b16 a[14:15], v21 offset:512
v_mfma_f32_16x16x32_bf16 a[132:135], a[16:19], a[52:55], a[132:135]
buffer_load_dwordx4 a[212:215], v1, s[80:83], 0 offen offset:0
v_mfma_f32_16x16x32_bf16 a[136:139], a[16:19], a[56:59], a[136:139]
buffer_load_dwordx4 a[216:219], v1, s[80:83], 0 offen offset:1024
v_mfma_f32_16x16x32_bf16 a[140:143], a[16:19], a[60:63], a[140:143]
ds_read_b64_tr_b16 a[16:17], v18 offset:1024
v_mfma_f32_16x16x32_bf16 a[144:147], a[20:23], a[48:51], a[144:147]
ds_read_b64_tr_b16 a[18:19], v19 offset:1024
v_mfma_f32_16x16x32_bf16 v[28:31], v[116:119], v[116:119], v[28:31]
v_mfma_f32_16x16x32_bf16 a[148:151], a[20:23], a[52:55], a[148:151]
v_mfma_f32_16x16x32_bf16 a[152:155], a[20:23], a[56:59], a[152:155]
v_subrev_u32 v2, 0xc00, v1
buffer_load_dwordx4 a[192:195], v2, s[76:79], 0 offen offset:0
v_mfma_f32_16x16x32_bf16 a[156:159], a[20:23], a[60:63], a[156:159]
ds_read_b64_tr_b16 a[20:21], v20 offset:1024
v_mfma_f32_16x16x32_bf16 a[160:163], a[24:27], a[48:51], a[160:163]
ds_read_b64_tr_b16 a[22:23], v21 offset:1024
v_mfma_f32_16x16x32_bf16 a[164:167], a[24:27], a[52:55], a[164:167]
v_mfma_f32_16x16x32_bf16 a[168:171], a[24:27], a[56:59], a[168:171]
v_subrev_u32 v2, 0x800, v1
buffer_load_dwordx4 a[196:199], v2, s[76:79], 0 offen offset:0
v_mfma_f32_16x16x32_bf16 a[172:175], a[24:27], a[60:63], a[172:175]
ds_read_b64_tr_b16 a[24:25], v18 offset:1536
v_mfma_f32_16x16x32_bf16 a[176:179], a[28:31], a[48:51], a[176:179]
ds_read_b64_tr_b16 a[26:27], v19 offset:1536
v_mfma_f32_16x16x32_bf16 a[180:183], a[28:31], a[52:55], a[180:183]
v_mfma_f32_16x16x32_bf16 a[184:187], a[28:31], a[56:59], a[184:187]
v_mfma_f32_16x16x32_bf16 a[188:191], a[28:31], a[60:63], a[188:191]
ds_read_b64_tr_b16 a[28:29], v20 offset:1536
ds_read_b64_tr_b16 a[30:31], v21 offset:1536
s_waitcnt lgkmcnt(6)
v_mfma_f32_16x16x32_bf16 a[64:67], a[0:3], a[32:35], a[64:67]
v_subrev_u32 v2, 0x400, v1
buffer_load_dwordx4 a[200:203], v2, s[76:79], 0 offen offset:0
v_mfma_f32_16x16x32_bf16 a[68:71], a[0:3], a[36:39], a[68:71]
ds_read_b128 a[48:51], v17 offset:0
v_mfma_f32_16x16x32_bf16 a[72:75], a[0:3], a[40:43], a[72:75]
ds_read_b128 a[52:55], v17 offset:2048
v_mfma_f32_16x16x32_bf16 a[76:79], a[0:3], a[44:47], a[76:79]
ds_read_b64_tr_b16 a[0:1], v18 offset:8192
v_mfma_f32_16x16x32_bf16 a[80:83], a[4:7], a[32:35], a[80:83]
ds_read_b64_tr_b16 a[2:3], v19 offset:8192
v_mfma_f32_16x16x32_bf16 a[84:87], a[4:7], a[36:39], a[84:87]
ds_read_b128 a[56:59], v17 offset:4096
v_mfma_f32_16x16x32_bf16 a[88:91], a[4:7], a[40:43], a[88:91]
ds_read_b128 a[60:63], v17 offset:6144
v_mfma_f32_16x16x32_bf16 a[92:95], a[4:7], a[44:47], a[92:95]
ds_read_b64_tr_b16 a[4:5], v20 offset:8192
v_mfma_f32_16x16x32_bf16 a[96:99], a[8:11], a[32:35], a[96:99]
ds_read_b64_tr_b16 a[6:7], v21 offset:8192
v_mfma_f32_16x16x32_bf16 v[24:27], v[88:91], v[88:91], 0
v_mfma_f32_16x16x32_bf16 a[100:103], a[8:11], a[36:39], a[100:103]
buffer_load_dwordx4 a[204:207], v1, s[76:79], 0 offen offset:0
v_mfma_f32_16x16x32_bf16 a[104:107], a[8:11], a[40:43], a[104:107]
buffer_load_dwordx4 a[208:211], v1, s[76:79], 0 offen offset:1024
v_mfma_f32_16x16x32_bf16 a[108:111], a[8:11], a[44:47], a[108:111]
ds_read_b64_tr_b16 a[8:9], v18 offset:8704
v_mfma_f32_16x16x32_bf16 a[112:115], a[12:15], a[32:35], a[112:115]
ds_read_b64_tr_b16 a[10:11], v19 offset:8704
v_max3_f32 v22, v22, v28, v29
v_max3_f32 v22, v22, v30, v31
v_mfma_f32_16x16x32_bf16 a[116:119], a[12:15], a[36:39], a[116:119]
s_add_u32 s27, s58, 0x5000
buffer_store_dwordx4 v[88:91], v4, s[48:51], s27 offen offset:2048 sc1
v_mfma_f32_16x16x32_bf16 a[120:123], a[12:15], a[40:43], a[120:123]
v_mfma_f32_16x16x32_bf16 a[124:127], a[12:15], a[44:47], a[124:127]
ds_read_b64_tr_b16 a[12:13], v20 offset:8704
v_mfma_f32_16x16x32_bf16 a[128:131], a[16:19], a[32:35], a[128:131]
ds_read_b64_tr_b16 a[14:15], v21 offset:8704
v_mfma_f32_16x16x32_bf16 a[132:135], a[16:19], a[36:39], a[132:135]
s_add_u32 s27, s58, 0x7000
buffer_store_dwordx4 v[152:155], v4, s[48:51], s27 offen offset:2048 sc1
v_mfma_f32_16x16x32_bf16 a[136:139], a[16:19], a[40:43], a[136:139]
v_mfma_f32_16x16x32_bf16 a[140:143], a[16:19], a[44:47], a[140:143]
ds_read_b64_tr_b16 a[16:17], v18 offset:9216
s_waitcnt lgkmcnt(5)
v_mfma_f32_16x16x32_bf16 a[144:147], a[20:23], a[32:35], a[144:147]
ds_read_b64_tr_b16 a[18:19], v19 offset:9216
v_mfma_f32_16x16x32_bf16 v[24:27], v[152:155], v[152:155], v[24:27]
v_mfma_f32_16x16x32_bf16 a[148:151], a[20:23], a[36:39], a[148:151]
v_mfma_f32_16x16x32_bf16 a[152:155], a[20:23], a[40:43], a[152:155]
s_add_u32 s27, s58, 0xd000
buffer_store_dwordx4 v[216:219], v4, s[48:51], s27 offen offset:2048 sc1
v_mfma_f32_16x16x32_bf16 a[156:159], a[20:23], a[44:47], a[156:159]
ds_read_b64_tr_b16 a[20:21], v20 offset:9216
v_mfma_f32_16x16x32_bf16 a[160:163], a[24:27], a[32:35], a[160:163]
ds_read_b64_tr_b16 a[22:23], v21 offset:9216
v_mfma_f32_16x16x32_bf16 a[164:167], a[24:27], a[36:39], a[164:167]
v_mfma_f32_16x16x32_bf16 a[168:171], a[24:27], a[40:43], a[168:171]
s_add_u32 s27, s58, 0xf000
buffer_store_dwordx4 v[120:123], v4, s[48:51], s27 offen offset:2048 sc1
v_mfma_f32_16x16x32_bf16 a[172:175], a[24:27], a[44:47], a[172:175]
ds_read_b64_tr_b16 a[24:25], v18 offset:9728
v_mfma_f32_16x16x32_bf16 a[176:179], a[28:31], a[32:35], a[176:179]
ds_read_b64_tr_b16 a[26:27], v19 offset:9728
v_mfma_f32_16x16x32_bf16 a[180:183], a[28:31], a[36:39], a[180:183]
v_mfma_f32_16x16x32_bf16 a[184:187], a[28:31], a[40:43], a[184:187]
v_mfma_f32_16x16x32_bf16 a[188:191], a[28:31], a[44:47], a[188:191]
ds_read_b64_tr_b16 a[28:29], v20 offset:9728
ds_read_b64_tr_b16 a[30:31], v21 offset:9728
v_mfma_f32_16x16x32_bf16 a[64:67], a[0:3], a[48:51], a[64:67]
v_mfma_f32_16x16x32_bf16 a[68:71], a[0:3], a[52:55], a[68:71]
v_mfma_f32_16x16x32_bf16 a[72:75], a[0:3], a[56:59], a[72:75]
v_mfma_f32_16x16x32_bf16 a[76:79], a[0:3], a[60:63], a[76:79]
v_mfma_f32_16x16x32_bf16 a[80:83], a[4:7], a[48:51], a[80:83]
s_waitcnt vmcnt(19) lgkmcnt(0)
s_barrier
v_add_u32 v16, 0xc000, v16
v_add_u32 v17, 0xc000, v17
v_add_u32 v18, 0xc000, v18
v_add_u32 v19, 0xc000, v19
v_add_u32 v20, 0xc000, v20
v_add_u32 v21, 0xc000, v21
v_mfma_f32_16x16x32_bf16 a[84:87], a[4:7], a[52:55], a[84:87]
ds_read_b64_tr_b16 a[0:1], v18 offset:0
v_mfma_f32_16x16x32_bf16 a[88:91], a[4:7], a[56:59], a[88:91]
ds_read_b64_tr_b16 a[2:3], v19 offset:0
v_mfma_f32_16x16x32_bf16 a[92:95], a[4:7], a[60:63], a[92:95]
ds_read_b64_tr_b16 a[4:5], v20 offset:0
v_mfma_f32_16x16x32_bf16 a[96:99], a[8:11], a[48:51], a[96:99]
ds_read_b64_tr_b16 a[6:7], v21 offset:0
v_mfma_f32_16x16x32_bf16 v[24:27], v[216:219], v[216:219], v[24:27]
v_mfma_f32_16x16x32_bf16 a[100:103], a[8:11], a[52:55], a[100:103]
ds_read_b128 a[32:35], v16 offset:0
v_mfma_f32_16x16x32_bf16 a[104:107], a[8:11], a[56:59], a[104:107]
ds_read_b128 a[36:39], v16 offset:2048
v_mfma_f32_16x16x32_bf16 a[108:111], a[8:11], a[60:63], a[108:111]
ds_read_b64_tr_b16 a[8:9], v18 offset:512
v_and_b32 v2, 7, v0
v_lshlrev_b32 v2, 6, v2
s_and_b32 s28, s22, 7
s_lshl_b32 s28, s28, 9
v_add_u32 v2, s28, v2
v_mfma_f32_16x16x32_bf16 a[112:115], a[12:15], a[48:51], a[112:115]
ds_read_b64_tr_b16 a[10:11], v19 offset:512
v_mfma_f32_16x16x32_bf16 a[116:119], a[12:15], a[52:55], a[116:119]
ds_read_b128 a[40:43], v16 offset:4096
v_mfma_f32_16x16x32_bf16 a[120:123], a[12:15], a[56:59], a[120:123]
ds_read_b128 a[44:47], v16 offset:6144
v_mfma_f32_16x16x32_bf16 a[124:127], a[12:15], a[60:63], a[124:127]
ds_read_b64_tr_b16 a[12:13], v20 offset:512
v_mfma_f32_16x16x32_bf16 a[128:131], a[16:19], a[48:51], a[128:131]
ds_read_b64_tr_b16 a[14:15], v21 offset:512
buffer_load_dwordx4 a[220:223], v2, s[84:87], 0 offen offset:0
v_mfma_f32_16x16x32_bf16 a[132:135], a[16:19], a[52:55], a[132:135]
buffer_load_dwordx4 a[224:227], v2, s[84:87], 0 offen offset:16
v_mfma_f32_16x16x32_bf16 a[136:139], a[16:19], a[56:59], a[136:139]
v_mfma_f32_16x16x32_bf16 a[140:143], a[16:19], a[60:63], a[140:143]
ds_read_b64_tr_b16 a[16:17], v18 offset:1024
v_mfma_f32_16x16x32_bf16 a[144:147], a[20:23], a[48:51], a[144:147]
ds_read_b64_tr_b16 a[18:19], v19 offset:1024
v_mfma_f32_16x16x32_bf16 v[24:27], v[120:123], v[120:123], v[24:27]
v_mfma_f32_16x16x32_bf16 a[148:151], a[20:23], a[52:55], a[148:151]
buffer_load_dwordx4 a[228:231], v2, s[84:87], 0 offen offset:32
v_mfma_f32_16x16x32_bf16 a[152:155], a[20:23], a[56:59], a[152:155]
v_mfma_f32_16x16x32_bf16 a[156:159], a[20:23], a[60:63], a[156:159]
ds_read_b64_tr_b16 a[20:21], v20 offset:1024
v_mfma_f32_16x16x32_bf16 a[160:163], a[24:27], a[48:51], a[160:163]
ds_read_b64_tr_b16 a[22:23], v21 offset:1024
v_mfma_f32_16x16x32_bf16 a[164:167], a[24:27], a[52:55], a[164:167]
buffer_load_dwordx4 a[232:235], v2, s[84:87], 0 offen offset:48
v_mfma_f32_16x16x32_bf16 a[168:171], a[24:27], a[56:59], a[168:171]
v_mfma_f32_16x16x32_bf16 a[172:175], a[24:27], a[60:63], a[172:175]
ds_read_b64_tr_b16 a[24:25], v18 offset:1536
v_mfma_f32_16x16x32_bf16 a[176:179], a[28:31], a[48:51], a[176:179]
ds_read_b64_tr_b16 a[26:27], v19 offset:1536
v_mfma_f32_16x16x32_bf16 a[180:183], a[28:31], a[52:55], a[180:183]
v_mfma_f32_16x16x32_bf16 a[184:187], a[28:31], a[56:59], a[184:187]
v_mfma_f32_16x16x32_bf16 a[188:191], a[28:31], a[60:63], a[188:191]
ds_read_b64_tr_b16 a[28:29], v20 offset:1536
ds_read_b64_tr_b16 a[30:31], v21 offset:1536
s_waitcnt lgkmcnt(6)
v_mfma_f32_16x16x32_bf16 a[64:67], a[0:3], a[32:35], a[64:67]
v_lshrrev_b32 v1, 5, v0
v_lshlrev_b32 v1, 10, v1
v_and_b32 v2, 31, v0
v_lshlrev_b32 v2, 5, v2
v_add_u32 v1, v1, v2
s_lshl_b32 s27, s22, 13
v_add_u32 v1, s27, v1
v_mfma_f32_16x16x32_bf16 a[68:71], a[0:3], a[36:39], a[68:71]
ds_read_b128 a[48:51], v17 offset:0
v_mfma_f32_16x16x32_bf16 a[72:75], a[0:3], a[40:43], a[72:75]
ds_read_b128 a[52:55], v17 offset:2048
v_mfma_f32_16x16x32_bf16 a[76:79], a[0:3], a[44:47], a[76:79]
ds_read_b64_tr_b16 a[0:1], v18 offset:8192
v_mfma_f32_16x16x32_bf16 a[80:83], a[4:7], a[32:35], a[80:83]
ds_read_b64_tr_b16 a[2:3], v19 offset:8192
v_mfma_f32_16x16x32_bf16 a[84:87], a[4:7], a[36:39], a[84:87]
ds_read_b128 a[56:59], v17 offset:4096
v_mfma_f32_16x16x32_bf16 a[88:91], a[4:7], a[40:43], a[88:91]
ds_read_b128 a[60:63], v17 offset:6144
v_mfma_f32_16x16x32_bf16 a[92:95], a[4:7], a[44:47], a[92:95]
ds_read_b64_tr_b16 a[4:5], v20 offset:8192
v_mfma_f32_16x16x32_bf16 a[96:99], a[8:11], a[32:35], a[96:99]
ds_read_b64_tr_b16 a[6:7], v21 offset:8192
v_mfma_f32_16x16x32_bf16 v[28:31], v[92:95], v[92:95], 0
v_mfma_f32_16x16x32_bf16 a[100:103], a[8:11], a[36:39], a[100:103]
global_load_dwordx4 a[236:239], v1, s[92:93] offset:0
v_mfma_f32_16x16x32_bf16 a[104:107], a[8:11], a[40:43], a[104:107]
global_load_dwordx4 a[240:243], v1, s[92:93] offset:16
v_mfma_f32_16x16x32_bf16 a[108:111], a[8:11], a[44:47], a[108:111]
ds_read_b64_tr_b16 a[8:9], v18 offset:8704
v_mfma_f32_16x16x32_bf16 a[112:115], a[12:15], a[32:35], a[112:115]
ds_read_b64_tr_b16 a[10:11], v19 offset:8704
v_max3_f32 v22, v22, v24, v25
v_max3_f32 v22, v22, v26, v27
v_mfma_f32_16x16x32_bf16 a[116:119], a[12:15], a[36:39], a[116:119]
s_add_u32 s27, s58, 0x5000
buffer_store_dwordx4 v[92:95], v4, s[48:51], s27 offen offset:3072 sc1
v_mfma_f32_16x16x32_bf16 a[120:123], a[12:15], a[40:43], a[120:123]
v_mfma_f32_16x16x32_bf16 a[124:127], a[12:15], a[44:47], a[124:127]
ds_read_b64_tr_b16 a[12:13], v20 offset:8704
v_mfma_f32_16x16x32_bf16 a[128:131], a[16:19], a[32:35], a[128:131]
ds_read_b64_tr_b16 a[14:15], v21 offset:8704
v_mfma_f32_16x16x32_bf16 a[132:135], a[16:19], a[36:39], a[132:135]
s_add_u32 s27, s58, 0x7000
buffer_store_dwordx4 v[156:159], v4, s[48:51], s27 offen offset:3072 sc1
v_mfma_f32_16x16x32_bf16 a[136:139], a[16:19], a[40:43], a[136:139]
v_mfma_f32_16x16x32_bf16 a[140:143], a[16:19], a[44:47], a[140:143]
ds_read_b64_tr_b16 a[16:17], v18 offset:9216
s_waitcnt lgkmcnt(5)
v_mfma_f32_16x16x32_bf16 a[144:147], a[20:23], a[32:35], a[144:147]
ds_read_b64_tr_b16 a[18:19], v19 offset:9216
v_mfma_f32_16x16x32_bf16 v[28:31], v[156:159], v[156:159], v[28:31]
v_mfma_f32_16x16x32_bf16 a[148:151], a[20:23], a[36:39], a[148:151]
v_mfma_f32_16x16x32_bf16 a[152:155], a[20:23], a[40:43], a[152:155]
s_add_u32 s27, s58, 0xd000
buffer_store_dwordx4 v[220:223], v4, s[48:51], s27 offen offset:3072 sc1
v_mfma_f32_16x16x32_bf16 a[156:159], a[20:23], a[44:47], a[156:159]
ds_read_b64_tr_b16 a[20:21], v20 offset:9216
v_mfma_f32_16x16x32_bf16 a[160:163], a[24:27], a[32:35], a[160:163]
ds_read_b64_tr_b16 a[22:23], v21 offset:9216
v_mfma_f32_16x16x32_bf16 a[164:167], a[24:27], a[36:39], a[164:167]
v_mfma_f32_16x16x32_bf16 a[168:171], a[24:27], a[40:43], a[168:171]
s_add_u32 s27, s58, 0xf000
buffer_store_dwordx4 v[124:127], v4, s[48:51], s27 offen offset:3072 sc1
v_mfma_f32_16x16x32_bf16 a[172:175], a[24:27], a[44:47], a[172:175]
ds_read_b64_tr_b16 a[24:25], v18 offset:9728
v_mfma_f32_16x16x32_bf16 a[176:179], a[28:31], a[32:35], a[176:179]
ds_read_b64_tr_b16 a[26:27], v19 offset:9728
v_mfma_f32_16x16x32_bf16 a[180:183], a[28:31], a[36:39], a[180:183]
v_mfma_f32_16x16x32_bf16 a[184:187], a[28:31], a[40:43], a[184:187]
v_mfma_f32_16x16x32_bf16 a[188:191], a[28:31], a[44:47], a[188:191]
ds_read_b64_tr_b16 a[28:29], v20 offset:9728
ds_read_b64_tr_b16 a[30:31], v21 offset:9728
v_mfma_f32_16x16x32_bf16 a[64:67], a[0:3], a[48:51], a[64:67]
buffer_load_dwordx4 a[244:247], v2, s[88:91], 0 offen offset:0
v_mfma_f32_16x16x32_bf16 a[68:71], a[0:3], a[52:55], a[68:71]
v_mfma_f32_16x16x32_bf16 a[72:75], a[0:3], a[56:59], a[72:75]
v_mfma_f32_16x16x32_bf16 a[76:79], a[0:3], a[60:63], a[76:79]
v_mfma_f32_16x16x32_bf16 a[80:83], a[4:7], a[48:51], a[80:83]
v_mfma_f32_16x16x32_bf16 a[84:87], a[4:7], a[52:55], a[84:87]
v_mfma_f32_16x16x32_bf16 a[88:91], a[4:7], a[56:59], a[88:91]
v_mfma_f32_16x16x32_bf16 a[92:95], a[4:7], a[60:63], a[92:95]
s_waitcnt lgkmcnt(2)
v_mfma_f32_16x16x32_bf16 a[96:99], a[8:11], a[48:51], a[96:99]
v_mfma_f32_16x16x32_bf16 v[28:31], v[220:223], v[220:223], v[28:31]
v_mfma_f32_16x16x32_bf16 a[100:103], a[8:11], a[52:55], a[100:103]
v_mfma_f32_16x16x32_bf16 a[104:107], a[8:11], a[56:59], a[104:107]
v_mfma_f32_16x16x32_bf16 a[108:111], a[8:11], a[60:63], a[108:111]
v_mfma_f32_16x16x32_bf16 a[112:115], a[12:15], a[48:51], a[112:115]
v_mfma_f32_16x16x32_bf16 a[116:119], a[12:15], a[52:55], a[116:119]
v_mfma_f32_16x16x32_bf16 a[120:123], a[12:15], a[56:59], a[120:123]
v_mfma_f32_16x16x32_bf16 a[124:127], a[12:15], a[60:63], a[124:127]
buffer_load_dwordx4 a[248:251], v2, s[88:91], 0 offen offset:16
v_mfma_f32_16x16x32_bf16 a[128:131], a[16:19], a[48:51], a[128:131]
v_mfma_f32_16x16x32_bf16 a[132:135], a[16:19], a[52:55], a[132:135]
v_mfma_f32_16x16x32_bf16 a[136:139], a[16:19], a[56:59], a[136:139]
v_mfma_f32_16x16x32_bf16 a[140:143], a[16:19], a[60:63], a[140:143]
v_mfma_f32_16x16x32_bf16 a[144:147], a[20:23], a[48:51], a[144:147]
v_mfma_f32_16x16x32_bf16 v[28:31], v[124:127], v[124:127], v[28:31]
v_mfma_f32_16x16x32_bf16 a[148:151], a[20:23], a[52:55], a[148:151]
v_mfma_f32_16x16x32_bf16 a[152:155], a[20:23], a[56:59], a[152:155]
v_mfma_f32_16x16x32_bf16 a[156:159], a[20:23], a[60:63], a[156:159]
v_mfma_f32_16x16x32_bf16 a[160:163], a[24:27], a[48:51], a[160:163]
v_mfma_f32_16x16x32_bf16 a[164:167], a[24:27], a[52:55], a[164:167]
v_mfma_f32_16x16x32_bf16 a[168:171], a[24:27], a[56:59], a[168:171]
v_mfma_f32_16x16x32_bf16 a[172:175], a[24:27], a[60:63], a[172:175]
s_waitcnt lgkmcnt(0)
v_mfma_f32_16x16x32_bf16 a[176:179], a[28:31], a[48:51], a[176:179]
v_mfma_f32_16x16x32_bf16 a[180:183], a[28:31], a[52:55], a[180:183]
v_mfma_f32_16x16x32_bf16 a[184:187], a[28:31], a[56:59], a[184:187]
v_mfma_f32_16x16x32_bf16 a[188:191], a[28:31], a[60:63], a[188:191]
s_nop 7
v_max3_f32 v22, v22, v28, v29
v_max3_f32 v22, v22, v30, v31
s_nop 1
s_mov_b32 s30, 0x3e0293ee
v_accvgpr_read_b32 v32, a64
v_accvgpr_read_b32 v33, a65
v_accvgpr_read_b32 v34, a66
v_accvgpr_read_b32 v35, a67
v_accvgpr_read_b32 v36, a80
v_accvgpr_read_b32 v37, a81
v_accvgpr_read_b32 v38, a82
v_accvgpr_read_b32 v39, a83
v_pk_mul_f32 v[32:33], v[32:33], s[30:31] op_sel_hi:[1,0]
v_pk_mul_f32 v[34:35], v[34:35], s[30:31] op_sel_hi:[1,0]
v_pk_mul_f32 v[36:37], v[36:37], s[30:31] op_sel_hi:[1,0]
v_pk_mul_f32 v[38:39], v[38:39], s[30:31] op_sel_hi:[1,0]
v_cvt_pk_bf16_f32 v192, v32, v33
v_cvt_pk_bf16_f32 v193, v34, v35
v_cvt_pk_bf16_f32 v194, v36, v37
v_cvt_pk_bf16_f32 v195, v38, v39
v_accvgpr_read_b32 v40, a96
v_accvgpr_read_b32 v41, a97
v_accvgpr_read_b32 v42, a98
v_accvgpr_read_b32 v43, a99
v_accvgpr_read_b32 v44, a112
v_accvgpr_read_b32 v45, a113
v_accvgpr_read_b32 v46, a114
v_accvgpr_read_b32 v47, a115
v_pk_mul_f32 v[40:41], v[40:41], s[30:31] op_sel_hi:[1,0]
v_pk_mul_f32 v[42:43], v[42:43], s[30:31] op_sel_hi:[1,0]
v_pk_mul_f32 v[44:45], v[44:45], s[30:31] op_sel_hi:[1,0]
v_pk_mul_f32 v[46:47], v[46:47], s[30:31] op_sel_hi:[1,0]
v_cvt_pk_bf16_f32 v196, v40, v41
v_cvt_pk_bf16_f32 v197, v42, v43
v_cvt_pk_bf16_f32 v198, v44, v45
v_cvt_pk_bf16_f32 v199, v46, v47
v_accvgpr_read_b32 v32, a128
v_accvgpr_read_b32 v33, a129
v_accvgpr_read_b32 v34, a130
v_accvgpr_read_b32 v35, a131
v_accvgpr_read_b32 v36, a144
v_accvgpr_read_b32 v37, a145
v_accvgpr_read_b32 v38, a146
v_accvgpr_read_b32 v39, a147
v_pk_mul_f32 v[32:33], v[32:33], s[30:31] op_sel_hi:[1,0]
v_pk_mul_f32 v[34:35], v[34:35], s[30:31] op_sel_hi:[1,0]
v_pk_mul_f32 v[36:37], v[36:37], s[30:31] op_sel_hi:[1,0]
v_pk_mul_f32 v[38:39], v[38:39], s[30:31] op_sel_hi:[1,0]
v_cvt_pk_bf16_f32 v200, v32, v33
v_cvt_pk_bf16_f32 v201, v34, v35
v_cvt_pk_bf16_f32 v202, v36, v37
v_cvt_pk_bf16_f32 v203, v38, v39
v_accvgpr_read_b32 v40, a160
v_accvgpr_read_b32 v41, a161
v_accvgpr_read_b32 v42, a162
v_accvgpr_read_b32 v43, a163
v_accvgpr_read_b32 v44, a176
v_accvgpr_read_b32 v45, a177
v_accvgpr_read_b32 v46, a178
v_accvgpr_read_b32 v47, a179
v_pk_mul_f32 v[40:41], v[40:41], s[30:31] op_sel_hi:[1,0]
v_pk_mul_f32 v[42:43], v[42:43], s[30:31] op_sel_hi:[1,0]
v_pk_mul_f32 v[44:45], v[44:45], s[30:31] op_sel_hi:[1,0]
v_pk_mul_f32 v[46:47], v[46:47], s[30:31] op_sel_hi:[1,0]
v_cvt_pk_bf16_f32 v204, v40, v41
v_cvt_pk_bf16_f32 v205, v42, v43
v_cvt_pk_bf16_f32 v206, v44, v45
v_cvt_pk_bf16_f32 v207, v46, v47
v_accvgpr_read_b32 v32, a68
v_accvgpr_read_b32 v33, a69
v_accvgpr_read_b32 v34, a70
v_accvgpr_read_b32 v35, a71
v_accvgpr_read_b32 v36, a84
v_accvgpr_read_b32 v37, a85
v_accvgpr_read_b32 v38, a86
v_accvgpr_read_b32 v39, a87
v_pk_mul_f32 v[32:33], v[32:33], s[30:31] op_sel_hi:[1,0]
v_pk_mul_f32 v[34:35], v[34:35], s[30:31] op_sel_hi:[1,0]
v_pk_mul_f32 v[36:37], v[36:37], s[30:31] op_sel_hi:[1,0]
v_pk_mul_f32 v[38:39], v[38:39], s[30:31] op_sel_hi:[1,0]
v_cvt_pk_bf16_f32 v208, v32, v33
v_cvt_pk_bf16_f32 v209, v34, v35
v_cvt_pk_bf16_f32 v210, v36, v37
v_cvt_pk_bf16_f32 v211, v38, v39
v_accvgpr_read_b32 v40, a100
v_accvgpr_read_b32 v41, a101
v_accvgpr_read_b32 v42, a102
v_accvgpr_read_b32 v43, a103
v_accvgpr_read_b32 v44, a116
v_accvgpr_read_b32 v45, a117
v_accvgpr_read_b32 v46, a118
v_accvgpr_read_b32 v47, a119
v_pk_mul_f32 v[40:41], v[40:41], s[30:31] op_sel_hi:[1,0]
v_pk_mul_f32 v[42:43], v[42:43], s[30:31] op_sel_hi:[1,0]
v_pk_mul_f32 v[44:45], v[44:45], s[30:31] op_sel_hi:[1,0]
v_pk_mul_f32 v[46:47], v[46:47], s[30:31] op_sel_hi:[1,0]
v_cvt_pk_bf16_f32 v212, v40, v41
v_cvt_pk_bf16_f32 v213, v42, v43
v_cvt_pk_bf16_f32 v214, v44, v45
v_cvt_pk_bf16_f32 v215, v46, v47
v_accvgpr_read_b32 v32, a132
v_accvgpr_read_b32 v33, a133
v_accvgpr_read_b32 v34, a134
v_accvgpr_read_b32 v35, a135
v_accvgpr_read_b32 v36, a148
v_accvgpr_read_b32 v37, a149
v_accvgpr_read_b32 v38, a150
v_accvgpr_read_b32 v39, a151
v_pk_mul_f32 v[32:33], v[32:33], s[30:31] op_sel_hi:[1,0]
v_pk_mul_f32 v[34:35], v[34:35], s[30:31] op_sel_hi:[1,0]
v_pk_mul_f32 v[36:37], v[36:37], s[30:31] op_sel_hi:[1,0]
v_pk_mul_f32 v[38:39], v[38:39], s[30:31] op_sel_hi:[1,0]
v_cvt_pk_bf16_f32 v216, v32, v33
v_cvt_pk_bf16_f32 v217, v34, v35
v_cvt_pk_bf16_f32 v218, v36, v37
v_cvt_pk_bf16_f32 v219, v38, v39
v_accvgpr_read_b32 v40, a164
v_accvgpr_read_b32 v41, a165
v_accvgpr_read_b32 v42, a166
v_accvgpr_read_b32 v43, a167
v_accvgpr_read_b32 v44, a180
v_accvgpr_read_b32 v45, a181
v_accvgpr_read_b32 v46, a182
v_accvgpr_read_b32 v47, a183
v_pk_mul_f32 v[40:41], v[40:41], s[30:31] op_sel_hi:[1,0]
v_pk_mul_f32 v[42:43], v[42:43], s[30:31] op_sel_hi:[1,0]
v_pk_mul_f32 v[44:45], v[44:45], s[30:31] op_sel_hi:[1,0]
v_pk_mul_f32 v[46:47], v[46:47], s[30:31] op_sel_hi:[1,0]
v_cvt_pk_bf16_f32 v220, v40, v41
v_cvt_pk_bf16_f32 v221, v42, v43
v_cvt_pk_bf16_f32 v222, v44, v45
v_cvt_pk_bf16_f32 v223, v46, v47
v_accvgpr_read_b32 v32, a72
v_accvgpr_read_b32 v33, a73
v_accvgpr_read_b32 v34, a74
v_accvgpr_read_b32 v35, a75
v_accvgpr_read_b32 v36, a88
v_accvgpr_read_b32 v37, a89
v_accvgpr_read_b32 v38, a90
v_accvgpr_read_b32 v39, a91
v_pk_mul_f32 v[32:33], v[32:33], s[30:31] op_sel_hi:[1,0]
v_pk_mul_f32 v[34:35], v[34:35], s[30:31] op_sel_hi:[1,0]
v_pk_mul_f32 v[36:37], v[36:37], s[30:31] op_sel_hi:[1,0]
v_pk_mul_f32 v[38:39], v[38:39], s[30:31] op_sel_hi:[1,0]
v_cvt_pk_bf16_f32 v224, v32, v33
v_cvt_pk_bf16_f32 v225, v34, v35
v_cvt_pk_bf16_f32 v226, v36, v37
v_cvt_pk_bf16_f32 v227, v38, v39
v_accvgpr_read_b32 v40, a104
v_accvgpr_read_b32 v41, a105
v_accvgpr_read_b32 v42, a106
v_accvgpr_read_b32 v43, a107
v_accvgpr_read_b32 v44, a120
v_accvgpr_read_b32 v45, a121
v_accvgpr_read_b32 v46, a122
v_accvgpr_read_b32 v47, a123
v_pk_mul_f32 v[40:41], v[40:41], s[30:31] op_sel_hi:[1,0]
v_pk_mul_f32 v[42:43], v[42:43], s[30:31] op_sel_hi:[1,0]
v_pk_mul_f32 v[44:45], v[44:45], s[30:31] op_sel_hi:[1,0]
v_pk_mul_f32 v[46:47], v[46:47], s[30:31] op_sel_hi:[1,0]
v_cvt_pk_bf16_f32 v228, v40, v41
v_cvt_pk_bf16_f32 v229, v42, v43
v_cvt_pk_bf16_f32 v230, v44, v45
v_cvt_pk_bf16_f32 v231, v46, v47
v_accvgpr_read_b32 v32, a136
v_accvgpr_read_b32 v33, a137
v_accvgpr_read_b32 v34, a138
v_accvgpr_read_b32 v35, a139
v_accvgpr_read_b32 v36, a152
v_accvgpr_read_b32 v37, a153
v_accvgpr_read_b32 v38, a154
v_accvgpr_read_b32 v39, a155
v_pk_mul_f32 v[32:33], v[32:33], s[30:31] op_sel_hi:[1,0]
v_pk_mul_f32 v[34:35], v[34:35], s[30:31] op_sel_hi:[1,0]
v_pk_mul_f32 v[36:37], v[36:37], s[30:31] op_sel_hi:[1,0]
v_pk_mul_f32 v[38:39], v[38:39], s[30:31] op_sel_hi:[1,0]
v_cvt_pk_bf16_f32 v232, v32, v33
v_cvt_pk_bf16_f32 v233, v34, v35
v_cvt_pk_bf16_f32 v234, v36, v37
v_cvt_pk_bf16_f32 v235, v38, v39
v_accvgpr_read_b32 v40, a168
v_accvgpr_read_b32 v41, a169
v_accvgpr_read_b32 v42, a170
v_accvgpr_read_b32 v43, a171
v_accvgpr_read_b32 v44, a184
v_accvgpr_read_b32 v45, a185
v_accvgpr_read_b32 v46, a186
v_accvgpr_read_b32 v47, a187
v_pk_mul_f32 v[40:41], v[40:41], s[30:31] op_sel_hi:[1,0]
v_pk_mul_f32 v[42:43], v[42:43], s[30:31] op_sel_hi:[1,0]
v_pk_mul_f32 v[44:45], v[44:45], s[30:31] op_sel_hi:[1,0]
v_pk_mul_f32 v[46:47], v[46:47], s[30:31] op_sel_hi:[1,0]
v_cvt_pk_bf16_f32 v236, v40, v41
v_cvt_pk_bf16_f32 v237, v42, v43
v_cvt_pk_bf16_f32 v238, v44, v45
v_cvt_pk_bf16_f32 v239, v46, v47
v_accvgpr_read_b32 v32, a76
v_accvgpr_read_b32 v33, a77
v_accvgpr_read_b32 v34, a78
v_accvgpr_read_b32 v35, a79
v_accvgpr_read_b32 v36, a92
v_accvgpr_read_b32 v37, a93
v_accvgpr_read_b32 v38, a94
v_accvgpr_read_b32 v39, a95
v_pk_mul_f32 v[32:33], v[32:33], s[30:31] op_sel_hi:[1,0]
v_pk_mul_f32 v[34:35], v[34:35], s[30:31] op_sel_hi:[1,0]
v_pk_mul_f32 v[36:37], v[36:37], s[30:31] op_sel_hi:[1,0]
v_pk_mul_f32 v[38:39], v[38:39], s[30:31] op_sel_hi:[1,0]
v_cvt_pk_bf16_f32 v240, v32, v33
v_cvt_pk_bf16_f32 v241, v34, v35
v_cvt_pk_bf16_f32 v242, v36, v37
v_cvt_pk_bf16_f32 v243, v38, v39
v_accvgpr_read_b32 v40, a108
v_accvgpr_read_b32 v41, a109
v_accvgpr_read_b32 v42, a110
v_accvgpr_read_b32 v43, a111
v_accvgpr_read_b32 v44, a124
v_accvgpr_read_b32 v45, a125
v_accvgpr_read_b32 v46, a126
v_accvgpr_read_b32 v47, a127
v_pk_mul_f32 v[40:41], v[40:41], s[30:31] op_sel_hi:[1,0]
v_pk_mul_f32 v[42:43], v[42:43], s[30:31] op_sel_hi:[1,0]
v_pk_mul_f32 v[44:45], v[44:45], s[30:31] op_sel_hi:[1,0]
v_pk_mul_f32 v[46:47], v[46:47], s[30:31] op_sel_hi:[1,0]
v_cvt_pk_bf16_f32 v244, v40, v41
v_cvt_pk_bf16_f32 v245, v42, v43
v_cvt_pk_bf16_f32 v246, v44, v45
v_cvt_pk_bf16_f32 v247, v46, v47
v_accvgpr_read_b32 v32, a140
v_accvgpr_read_b32 v33, a141
v_accvgpr_read_b32 v34, a142
v_accvgpr_read_b32 v35, a143
v_accvgpr_read_b32 v36, a156
v_accvgpr_read_b32 v37, a157
v_accvgpr_read_b32 v38, a158
v_accvgpr_read_b32 v39, a159
v_pk_mul_f32 v[32:33], v[32:33], s[30:31] op_sel_hi:[1,0]
v_pk_mul_f32 v[34:35], v[34:35], s[30:31] op_sel_hi:[1,0]
v_pk_mul_f32 v[36:37], v[36:37], s[30:31] op_sel_hi:[1,0]
v_pk_mul_f32 v[38:39], v[38:39], s[30:31] op_sel_hi:[1,0]
v_cvt_pk_bf16_f32 v248, v32, v33
v_cvt_pk_bf16_f32 v249, v34, v35
v_cvt_pk_bf16_f32 v250, v36, v37
v_cvt_pk_bf16_f32 v251, v38, v39
v_accvgpr_read_b32 v40, a172
v_accvgpr_read_b32 v41, a173
v_accvgpr_read_b32 v42, a174
v_accvgpr_read_b32 v43, a175
v_accvgpr_read_b32 v44, a188
v_accvgpr_read_b32 v45, a189
v_accvgpr_read_b32 v46, a190
v_accvgpr_read_b32 v47, a191
v_pk_mul_f32 v[40:41], v[40:41], s[30:31] op_sel_hi:[1,0]
v_pk_mul_f32 v[42:43], v[42:43], s[30:31] op_sel_hi:[1,0]
v_pk_mul_f32 v[44:45], v[44:45], s[30:31] op_sel_hi:[1,0]
v_pk_mul_f32 v[46:47], v[46:47], s[30:31] op_sel_hi:[1,0]
v_cvt_pk_bf16_f32 v252, v40, v41
v_cvt_pk_bf16_f32 v253, v42, v43
v_cvt_pk_bf16_f32 v254, v44, v45
v_cvt_pk_bf16_f32 v255, v46, v47
v_mov_b32 v6, 0
v_mfma_f32_16x16x32_bf16 v[24:27], v[192:195], v[192:195], 0
v_mfma_f32_16x16x32_bf16 v[24:27], v[196:199], v[196:199], v[24:27]
v_mfma_f32_16x16x32_bf16 v[24:27], v[200:203], v[200:203], v[24:27]
v_mfma_f32_16x16x32_bf16 v[24:27], v[204:207], v[204:207], v[24:27]
v_mfma_f32_16x16x32_bf16 v[28:31], v[208:211], v[208:211], 0
v_mfma_f32_16x16x32_bf16 v[28:31], v[212:215], v[212:215], v[28:31]
v_mfma_f32_16x16x32_bf16 v[28:31], v[216:219], v[216:219], v[28:31]
v_mfma_f32_16x16x32_bf16 v[28:31], v[220:223], v[220:223], v[28:31]
s_nop 3
v_max3_f32 v6, v6, v24, v25
v_max3_f32 v6, v6, v26, v27
v_mfma_f32_16x16x32_bf16 v[24:27], v[224:227], v[224:227], 0
v_mfma_f32_16x16x32_bf16 v[24:27], v[228:231], v[228:231], v[24:27]
v_mfma_f32_16x16x32_bf16 v[24:27], v[232:235], v[232:235], v[24:27]
v_mfma_f32_16x16x32_bf16 v[24:27], v[236:239], v[236:239], v[24:27]
s_nop 3
v_max3_f32 v6, v6, v28, v29
v_max3_f32 v6, v6, v30, v31
v_mfma_f32_16x16x32_bf16 v[28:31], v[240:243], v[240:243], 0
v_mfma_f32_16x16x32_bf16 v[28:31], v[244:247], v[244:247], v[28:31]
v_mfma_f32_16x16x32_bf16 v[28:31], v[248:251], v[248:251], v[28:31]
v_mfma_f32_16x16x32_bf16 v[28:31], v[252:255], v[252:255], v[28:31]
s_nop 3
v_max3_f32 v6, v6, v24, v25
v_max3_f32 v6, v6, v26, v27
s_nop 7
v_max3_f32 v6, v6, v28, v29
v_max3_f32 v6, v6, v30, v31
s_waitcnt vmcnt(0)
A_fin_%=:
v_readfirstlane_b32 s98, v61
s_nop 0
s_add_u32 s98, s98, 1
s_load_dwordx2 s[64:65], s[20:21], 0x20
s_load_dwordx2 s[66:67], s[20:21], 0x68
s_load_dwordx2 s[88:89], s[20:21], 0x0
s_load_dwordx2 s[90:91], s[20:21], 0x60
s_load_dwordx2 s[80:81], s[20:21], 0x28
s_nop 1
v_max_f32_dpp v22, v22, v22 row_ror:8
s_nop 1
v_max_f32_dpp v22, v22, v22 row_ror:4
s_nop 1
v_max_f32_dpp v22, v22, v22 row_ror:2
s_nop 1
v_max_f32_dpp v22, v22, v22 row_ror:1
s_nop 0
v_readlane_b32 s27, v22, 0
v_readlane_b32 s28, v22, 16
v_readlane_b32 s29, v22, 32
v_readlane_b32 s30, v22, 48
s_nop 0
s_max_u32 s27, s27, s28
s_max_u32 s29, s29, s30
s_max_u32 s27, s27, s29
v_mov_b32 v12, s27
s_nop 1
v_max_f32_dpp v6, v6, v6 row_ror:8
s_nop 1
v_max_f32_dpp v6, v6, v6 row_ror:4
s_nop 1
v_max_f32_dpp v6, v6, v6 row_ror:2
s_nop 1
v_max_f32_dpp v6, v6, v6 row_ror:1
s_nop 0
v_readlane_b32 s27, v6, 0
v_readlane_b32 s28, v6, 16
v_readlane_b32 s29, v6, 32
v_readlane_b32 s30, v6, 48
s_nop 0
s_max_u32 s27, s27, s28
s_max_u32 s29, s29, s30
s_max_u32 s27, s27, s29
v_mov_b32 v13, s27
s_lshl_b32 s28, s23, 2
s_add_u32 s28, s28, s24
s_add_u32 s28, s28, 0x20000
v_mov_b32 v14, s28
ds_write_b32 v14, v12 offset:0
ds_write_b32 v14, v13 offset:16
s_waitcnt lgkmcnt(0)
s_and_b32 s27, s22, 7
s_lshl_b32 s27, s27, 6
s_lshr_b32 s28, s22, 3
s_lshl_b32 s28, s28, 1
s_add_u32 s27, s27, s28
s_lshl_b32 s27, s27, 1
s_add_u32 s80, s80, s27
s_addc_u32 s81, s81, 0
s_load_dword s74, s[80:81], 0x0
s_mov_b32 s40, s66
s_and_b32 s41, s67, 0xffff
s_mov_b32 s42, 0x8000000
s_mov_b32 s43, 0x20000
s_mov_b32 s66, s66
s_mov_b32 s67, s67
s_waitcnt vmcnt(0) lgkmcnt(0)
s_barrier
s_cmp_lg_u32 s23, 0
s_cbranch_scc1 M_g_%=
s_add_u32 s28, s24, 0x20000
v_mov_b32 v14, s28
ds_read_b128 v[16:19], v14 offset:0
ds_read_b128 v[20:23], v14 offset:16
s_waitcnt lgkmcnt(0)
v_max_u32 v16, v16, v17
v_max_u32 v18, v18, v19
v_mov_b32 v14, 0x67800000
v_cmp_le_u32 vcc, v18, v14
v_mov_b32 v14, 0x7f800000
s_nop 0
v_cndmask_b32 v16, v14, v16, vcc
v_max_u32 v20, v20, v21
v_max_u32 v22, v22, v23
v_max_u32 v20, v20, v22
v_mov_b32 v17, s98
v_mov_b32 v14, 0
v_readfirstlane_b32 s75, v20
s_and_b32 s27, s22, 7
s_lshl_b32 s27, s27, 5
s_lshr_b32 s28, s22, 3
s_add_u32 s28, s28, s27
s_lshl_b32 s28, s28, 3
s_add_u32 s28, s28, 0x1000
s_mov_b64 s[92:93], exec
s_mov_b64 exec, 1
buffer_store_dwordx2 v[16:17], v14, s[40:43], s28 offen offset:0 sc1
s_mov_b64 exec, s[92:93]
M_g_%=:
v_mbcnt_lo_u32_b32 v5, -1, 0
v_mbcnt_hi_u32_b32 v5, -1, v5
s_lshl_b32 s27, s23, 6
v_add_u32 v5, s27, v5
v_accvgpr_read_b32 v64, a192
v_accvgpr_read_b32 v65, a193
v_accvgpr_read_b32 v66, a194
v_accvgpr_read_b32 v67, a195
v_accvgpr_read_b32 v68, a196
v_accvgpr_read_b32 v69, a197
v_accvgpr_read_b32 v70, a198
v_accvgpr_read_b32 v71, a199
v_accvgpr_read_b32 v72, a200
v_accvgpr_read_b32 v73, a201
v_accvgpr_read_b32 v74, a202
v_accvgpr_read_b32 v75, a203
v_accvgpr_read_b32 v76, a204
v_accvgpr_read_b32 v77, a205
v_accvgpr_read_b32 v78, a206
v_accvgpr_read_b32 v79, a207
v_accvgpr_read_b32 v80, a208
v_accvgpr_read_b32 v81, a209
v_accvgpr_read_b32 v82, a210
v_accvgpr_read_b32 v83, a211
v_accvgpr_read_b32 v84, a212
v_accvgpr_read_b32 v85, a213
v_accvgpr_read_b32 v86, a214
v_accvgpr_read_b32 v87, a215
v_accvgpr_read_b32 v88, a216
v_accvgpr_read_b32 v89, a217
v_accvgpr_read_b32 v90, a218
v_accvgpr_read_b32 v91, a219
v_accvgpr_read_b32 v92, a220
v_accvgpr_read_b32 v93, a221
v_accvgpr_read_b32 v94, a222
v_accvgpr_read_b32 v95, a223
v_accvgpr_read_b32 v96, a224
v_accvgpr_read_b32 v97, a225
v_accvgpr_read_b32 v98, a226
v_accvgpr_read_b32 v99, a227
v_accvgpr_read_b32 v100, a228
v_accvgpr_read_b32 v101, a229
v_accvgpr_read_b32 v102, a230
v_accvgpr_read_b32 v103, a231
v_accvgpr_read_b32 v104, a232
v_accvgpr_read_b32 v105, a233
v_accvgpr_read_b32 v106, a234
v_accvgpr_read_b32 v107, a235
v_accvgpr_read_b32 v108, a236
v_accvgpr_read_b32 v109, a237
v_accvgpr_read_b32 v110, a238
v_accvgpr_read_b32 v111, a239
v_accvgpr_read_b32 v112, a240
v_accvgpr_read_b32 v113, a241
v_accvgpr_read_b32 v114, a242
v_accvgpr_read_b32 v115, a243
v_accvgpr_read_b32 v116, a244
v_accvgpr_read_b32 v117, a245
v_accvgpr_read_b32 v118, a246
v_accvgpr_read_b32 v119, a247
v_accvgpr_read_b32 v120, a248
v_accvgpr_read_b32 v121, a249
v_accvgpr_read_b32 v122, a250
v_accvgpr_read_b32 v123, a251
s_mov_b32 s27, 0xffff0000
v_and_b32 v0, 7, v5
v_mul_u32_u24 v0, 0x480, v0
v_lshrrev_b32 v1, 3, v5
v_lshl_add_u32 v8, v1, 2, v0
v_add_u32 v8, s24, v8
v_lshlrev_b32 v128, 16, v92
v_and_b32 v129, s27, v92
v_lshlrev_b32 v130, 16, v93
v_and_b32 v131, s27, v93
v_lshlrev_b32 v132, 16, v64
v_lshlrev_b32 v133, 16, v68
v_lshlrev_b32 v134, 16, v72
v_lshlrev_b32 v135, 16, v76
v_lshlrev_b32 v136, 16, v80
v_lshlrev_b32 v137, 16, v84
v_lshlrev_b32 v138, 16, v88
v_mul_f32 v140, v132, v131
v_mul_f32 v141, v133, v131
v_fmac_f32 v140, v133, v130
v_fmac_f32 v141, v134, v130
v_fmac_f32 v140, v134, v129
v_fmac_f32 v141, v135, v129
v_fmac_f32 v140, v135, v128
v_fmac_f32 v141, v136, v128
v_mul_f32 v140, v140, v137
v_mul_f32 v141, v141, v138
v_cvt_pk_bf16_f32 v142, v140, v141
ds_write_b32 v8, v142 offset:0
v_lshlrev_b32 v128, 16, v94
v_and_b32 v129, s27, v94
v_lshlrev_b32 v130, 16, v95
v_and_b32 v131, s27, v95
v_and_b32 v132, s27, v64
v_and_b32 v133, s27, v68
v_and_b32 v134, s27, v72
v_and_b32 v135, s27, v76
v_and_b32 v136, s27, v80
v_and_b32 v137, s27, v84
v_and_b32 v138, s27, v88
v_mul_f32 v140, v132, v131
v_mul_f32 v141, v133, v131
v_fmac_f32 v140, v133, v130
v_fmac_f32 v141, v134, v130
v_fmac_f32 v140, v134, v129
v_fmac_f32 v141, v135, v129
v_fmac_f32 v140, v135, v128
v_fmac_f32 v141, v136, v128
v_mul_f32 v140, v140, v137
v_mul_f32 v141, v141, v138
v_cvt_pk_bf16_f32 v143, v140, v141
ds_write_b32 v8, v143 offset:144
v_lshlrev_b32 v128, 16, v96
v_and_b32 v129, s27, v96
v_lshlrev_b32 v130, 16, v97
v_and_b32 v131, s27, v97
v_lshlrev_b32 v132, 16, v65
v_lshlrev_b32 v133, 16, v69
v_lshlrev_b32 v134, 16, v73
v_lshlrev_b32 v135, 16, v77
v_lshlrev_b32 v136, 16, v81
v_lshlrev_b32 v137, 16, v85
v_lshlrev_b32 v138, 16, v89
v_mul_f32 v140, v132, v131
v_mul_f32 v141, v133, v131
v_fmac_f32 v140, v133, v130
v_fmac_f32 v141, v134, v130
v_fmac_f32 v140, v134, v129
v_fmac_f32 v141, v135, v129
v_fmac_f32 v140, v135, v128
v_fmac_f32 v141, v136, v128
v_mul_f32 v140, v140, v137
v_mul_f32 v141, v141, v138
v_cvt_pk_bf16_f32 v142, v140, v141
ds_write_b32 v8, v142 offset:288
v_lshlrev_b32 v128, 16, v98
v_and_b32 v129, s27, v98
v_lshlrev_b32 v130, 16, v99
v_and_b32 v131, s27, v99
v_and_b32 v132, s27, v65
v_and_b32 v133, s27, v69
v_and_b32 v134, s27, v73
v_and_b32 v135, s27, v77
v_and_b32 v136, s27, v81
v_and_b32 v137, s27, v85
v_and_b32 v138, s27, v89
v_mul_f32 v140, v132, v131
v_mul_f32 v141, v133, v131
v_fmac_f32 v140, v133, v130
v_fmac_f32 v141, v134, v130
v_fmac_f32 v140, v134, v129
v_fmac_f32 v141, v135, v129
v_fmac_f32 v140, v135, v128
v_fmac_f32 v141, v136, v128
v_mul_f32 v140, v140, v137
v_mul_f32 v141, v141, v138
v_cvt_pk_bf16_f32 v143, v140, v141
ds_write_b32 v8, v143 offset:432
v_lshlrev_b32 v128, 16, v100
v_and_b32 v129, s27, v100
v_lshlrev_b32 v130, 16, v101
v_and_b32 v131, s27, v101
v_lshlrev_b32 v132, 16, v66
v_lshlrev_b32 v133, 16, v70
v_lshlrev_b32 v134, 16, v74
v_lshlrev_b32 v135, 16, v78
v_lshlrev_b32 v136, 16, v82
v_lshlrev_b32 v137, 16, v86
v_lshlrev_b32 v138, 16, v90
v_mul_f32 v140, v132, v131
v_mul_f32 v141, v133, v131
v_fmac_f32 v140, v133, v130
v_fmac_f32 v141, v134, v130
v_fmac_f32 v140, v134, v129
v_fmac_f32 v141, v135, v129
v_fmac_f32 v140, v135, v128
v_fmac_f32 v141, v136, v128
v_mul_f32 v140, v140, v137
v_mul_f32 v141, v141, v138
v_cvt_pk_bf16_f32 v142, v140, v141
ds_write_b32 v8, v142 offset:576
v_lshlrev_b32 v128, 16, v102
v_and_b32 v129, s27, v102
v_lshlrev_b32 v130, 16, v103
v_and_b32 v131, s27, v103
v_and_b32 v132, s27, v66
v_and_b32 v133, s27, v70
v_and_b32 v134, s27, v74
v_and_b32 v135, s27, v78
v_and_b32 v136, s27, v82
v_and_b32 v137, s27, v86
v_and_b32 v138, s27, v90
v_mul_f32 v140, v132, v131
v_mul_f32 v141, v133, v131
v_fmac_f32 v140, v133, v130
v_fmac_f32 v141, v134, v130
v_fmac_f32 v140, v134, v129
v_fmac_f32 v141, v135, v129
v_fmac_f32 v140, v135, v128
v_fmac_f32 v141, v136, v128
v_mul_f32 v140, v140, v137
v_mul_f32 v141, v141, v138
v_cvt_pk_bf16_f32 v143, v140, v141
ds_write_b32 v8, v143 offset:720
v_lshlrev_b32 v128, 16, v104
v_and_b32 v129, s27, v104
v_lshlrev_b32 v130, 16, v105
v_and_b32 v131, s27, v105
v_lshlrev_b32 v132, 16, v67
v_lshlrev_b32 v133, 16, v71
v_lshlrev_b32 v134, 16, v75
v_lshlrev_b32 v135, 16, v79
v_lshlrev_b32 v136, 16, v83
v_lshlrev_b32 v137, 16, v87
v_lshlrev_b32 v138, 16, v91
v_mul_f32 v140, v132, v131
v_mul_f32 v141, v133, v131
v_fmac_f32 v140, v133, v130
v_fmac_f32 v141, v134, v130
v_fmac_f32 v140, v134, v129
v_fmac_f32 v141, v135, v129
v_fmac_f32 v140, v135, v128
v_fmac_f32 v141, v136, v128
v_mul_f32 v140, v140, v137
v_mul_f32 v141, v141, v138
v_cvt_pk_bf16_f32 v142, v140, v141
ds_write_b32 v8, v142 offset:864
v_lshlrev_b32 v128, 16, v106
v_and_b32 v129, s27, v106
v_lshlrev_b32 v130, 16, v107
v_and_b32 v131, s27, v107
v_and_b32 v132, s27, v67
v_and_b32 v133, s27, v71
v_and_b32 v134, s27, v75
v_and_b32 v135, s27, v79
v_and_b32 v136, s27, v83
v_and_b32 v137, s27, v87
v_and_b32 v138, s27, v91
v_mul_f32 v140, v132, v131
v_mul_f32 v141, v133, v131
v_fmac_f32 v140, v133, v130
v_fmac_f32 v141, v134, v130
v_fmac_f32 v140, v134, v129
v_fmac_f32 v141, v135, v129
v_fmac_f32 v140, v135, v128
v_fmac_f32 v141, v136, v128
v_mul_f32 v140, v140, v137
v_mul_f32 v141, v141, v138
v_cvt_pk_bf16_f32 v143, v140, v141
ds_write_b32 v8, v143 offset:1008
v_mov_b32 v144, 0
v_dot2c_f32_bf16 v144, v108, v116
v_dot2c_f32_bf16 v144, v109, v117
v_dot2c_f32_bf16 v144, v110, v118
v_dot2c_f32_bf16 v144, v111, v119
v_dot2c_f32_bf16 v144, v112, v120
v_dot2c_f32_bf16 v144, v113, v121
v_dot2c_f32_bf16 v144, v114, v122
v_dot2c_f32_bf16 v144, v115, v123
s_nop 2
s_nop 1
v_add_f32_dpp v144, v144, v144 row_ror:8
s_nop 1
v_add_f32_dpp v144, v144, v144 row_ror:4
s_nop 1
v_add_f32_dpp v144, v144, v144 row_ror:2
s_nop 1
v_add_f32_dpp v144, v144, v144 row_ror:1
v_mov_b32 v145, v144
s_nop 1
v_permlane16_swap_b32 v144, v145
v_add_f32 v144, v144, v145
v_cvt_pk_bf16_f32 v144, v144, v144
v_lshlrev_b32 v144, 16, v144
v_mul_f32 v144, 0xbfb8aa3b, v144
v_exp_f32 v144, v144
s_nop 0
v_add_f32 v144, 1.0, v144
v_rcp_f32 v144, v144
s_nop 0
v_cvt_pk_bf16_f32 v144, v144, v144
v_lshlrev_b32 v144, 16, v144
v_and_b32 v0, 63, v5
v_lshrrev_b32 v1, 4, v0
v_lshlrev_b32 v1, 2, v1
v_bfe_u32 v2, v0, 2, 2
v_add_u32 v1, v1, v2
v_lshlrev_b32 v1, 9, v1
v_and_b32 v2, 3, v0
v_lshl_add_u32 v7, v2, 4, v1
s_lshl_b32 s27, s23, 7
s_add_u32 s27, s27, s24
s_add_u32 s27, s27, 0x24000
v_add_u32 v7, s27, v7
ds_read_b64_tr_b16 v[148:149], v7 offset:0
ds_read_b64_tr_b16 v[150:151], v7 offset:8192
ds_read_b64_tr_b16 v[152:153], v7 offset:8
ds_read_b64_tr_b16 v[154:155], v7 offset:8200
ds_read_b64_tr_b16 v[156:157], v7 offset:64
ds_read_b64_tr_b16 v[158:159], v7 offset:8256
ds_read_b64_tr_b16 v[160:161], v7 offset:72
ds_read_b64_tr_b16 v[162:163], v7 offset:8264
s_waitcnt lgkmcnt(0)
s_barrier
v_lshrrev_b32 v0, 3, v5
v_mul_u32_u24 v1, 0x90, v0
v_and_b32 v2, 7, v5
v_lshl_add_u32 v1, v2, 4, v1
v_add_u32 v1, s24, v1
ds_read_b128 v[164:167], v1 offset:0
ds_read_b128 v[168:171], v1 offset:4608
v_lshlrev_b32 v0, 12, v0
v_lshl_add_u32 v0, v2, 4, v0
s_waitcnt lgkmcnt(0)
s_cmp_lg_u32 s23, 0
s_cbranch_scc1 M_w0skip_%=
s_and_b32 s27, s22, 7
s_lshl_b32 s27, s27, 8
s_add_u32 s27, s27, 0x1000
v_mbcnt_lo_u32_b32 v16, -1, 0
v_mbcnt_hi_u32_b32 v16, -1, v16
v_and_b32 v16, 31, v16
v_lshlrev_b32 v17, 3, v16
s_memrealtime s[94:95]
s_waitcnt lgkmcnt(0)
M_poll_%=:
buffer_load_dwordx2 v[18:19], v17, s[40:43], s27 offen offset:0 sc1
s_waitcnt vmcnt(0)
v_cmp_ne_u32 vcc, s98, v19
s_nop 1
s_cbranch_vccz M_poll_ok_%=
s_sleep 1
s_memrealtime s[96:97]
s_waitcnt lgkmcnt(0)
s_sub_u32 s29, s96, s94
s_cmp_lt_u32 s29, 0x1e8480
s_cbranch_scc1 M_poll_%=
M_poll_ok_%=:
s_lshr_b32 s28, s22, 3
s_and_b32 s28, s28, 3
v_and_b32 v16, 3, v16
v_cmp_eq_u32 vcc, s28, v16
s_nop 1
v_cndmask_b32 v18, 0, v18, vcc
s_nop 1
v_max_u32_dpp v18, v18, v18 row_ror:8
s_nop 1
v_max_u32_dpp v18, v18, v18 row_ror:4
s_nop 1
v_max_u32_dpp v18, v18, v18 row_ror:2
s_nop 1
v_max_u32_dpp v18, v18, v18 row_ror:1
s_nop 0
v_readlane_b32 s28, v18, 0
v_readlane_b32 s29, v18, 16
s_nop 0
s_max_u32 s28, s28, s29
v_mov_b32 v20, s75
v_mul_f32 v20, s28, v20
v_mov_b32 v19, 0x45610000
v_cmp_le_f32 vcc, v20, v19
s_nop 1
v_cndmask_b32 v16, 1, 0, vcc
s_add_u32 s28, s24, 0x20000
v_mov_b32 v17, s28
ds_write_b32 v17, v16 offset:32
M_w0skip_%=:
s_and_b32 s27, s22, 7
s_lshl_b32 s27, s27, 18
s_lshr_b32 s28, s22, 3
s_lshl_b32 s28, s28, 7
s_add_u32 s27, s27, s28
s_add_u32 s27, s27, 0x4100000
buffer_store_dwordx4 v[164:167], v0, s[40:43], s27 offen offset:0 sc1
s_add_u32 s27, s27, 0x20000
buffer_store_dwordx4 v[168:171], v0, s[40:43], s27 offen offset:0 sc1
s_lshr_b32 s27, s22, 1
s_lshl_b32 s27, s27, 5
s_and_b32 s28, s22, 1
s_lshl_b32 s28, s28, 4
s_add_u32 s27, s27, s28
s_lshl_b32 s28, s23, 2
s_add_u32 s27, s27, s28
s_lshl_b32 s27, s27, 10
s_add_u32 s27, s27, 0x4510000
buffer_store_dwordx4 v[148:151], v4, s[40:43], s27 offen offset:0 sc1
buffer_store_dwordx4 v[152:155], v4, s[40:43], s27 offen offset:1024 sc1
buffer_store_dwordx4 v[156:159], v4, s[40:43], s27 offen offset:2048 sc1
buffer_store_dwordx4 v[160:163], v4, s[40:43], s27 offen offset:3072 sc1
v_lshrrev_b32 v1, 5, v5
v_lshlrev_b32 v1, 2, v1
v_and_b32 v2, 31, v5
v_cmp_eq_u32 vcc, 0, v2
s_lshl_b32 s27, s22, 5
s_add_u32 s27, s27, 0x4500000
s_mov_b64 s[92:93], exec
s_nop 1
s_mov_b64 exec, vcc
buffer_store_dword v144, v1, s[40:43], s27 offen offset:0 sc1
s_mov_b64 exec, s[92:93]
s_and_b32 s27, s22, 7
s_lshl_b32 s27, s27, 6
s_lshr_b32 s28, s22, 3
s_lshl_b32 s28, s28, 1
s_add_u32 s27, s27, s28
s_lshr_b32 s28, s23, 1
s_add_u32 s27, s27, s28
s_lshl_b32 s27, s27, 12
s_add_u32 s60, s64, s27
s_addc_u32 s61, s65, 0
s_mov_b32 s76, s60
s_and_b32 s77, s61, 0xffff
s_mov_b32 s78, 0x1000
s_mov_b32 s79, 0x20000
s_add_u32 s27, s27, 0x4100000
s_add_u32 s62, s66, s27
s_addc_u32 s63, s67, 0
s_mov_b32 s84, s62
s_and_b32 s85, s63, 0xffff
s_mov_b32 s86, 0x1000
s_mov_b32 s87, 0x20000
s_add_u32 s60, s66, 0x3100000
s_addc_u32 s61, s67, 0
s_mov_b32 s36, s60
s_and_b32 s37, s61, 0xffff
s_mov_b32 s38, 0x1000000
s_mov_b32 s39, 0x20000
s_lshl_b32 s27, s26, 1
s_lshr_b32 s28, s23, 1
s_add_u32 s27, s27, s28
s_lshl_b32 s27, s27, 5
s_add_u32 s27, s27, s25
s_lshl_b32 s27, s27, 14
s_and_b32 s28, s23, 1
s_lshl_b32 s28, s28, 11
s_add_u32 s55, s27, s28
s_sub_u32 s27, 15, s26
s_lshl_b32 s27, s27, 1
s_lshr_b32 s28, s23, 1
s_add_u32 s27, s27, s28
s_lshl_b32 s27, s27, 5
s_add_u32 s27, s27, s25
s_lshl_b32 s27, s27, 14
s_and_b32 s28, s23, 1
s_lshl_b32 s28, s28, 11
s_add_u32 s56, s27, s28
s_mov_b32 s69, 0x400
v_mbcnt_lo_u32_b32 v0, -1, 0
v_mbcnt_hi_u32_b32 v0, -1, v0
v_lshlrev_b32 v6, 4, v0
v_lshrrev_b32 v1, 4, v0
v_lshlrev_b32 v1, 2, v1
v_and_b32 v2, 15, v0
v_sub_u32 v1, v1, v2
v_mov_b32 v2, 0xf149f2ca
v_cmp_lt_i32 vcc, 0, v1
s_nop 1
v_cndmask_b32 v8, 0, v2, vcc
v_cmp_lt_i32 vcc, -1, v1
s_nop 1
v_cndmask_b32 v9, 0, v2, vcc
v_cmp_lt_i32 vcc, -2, v1
s_nop 1
v_cndmask_b32 v10, 0, v2, vcc
v_cmp_lt_i32 vcc, -3, v1
s_nop 1
v_cndmask_b32 v11, 0, v2, vcc
v_mov_b32 v56, 0x3f803f80
v_mov_b32 v57, 0x3f803f80
v_mov_b32 v58, 0x3f803f80
v_mov_b32 v59, 0x3f803f80
v_mov_b32 v16, 0
v_mov_b32 v17, 0
v_mov_b32 v18, 0
v_mov_b32 v19, 0
v_mov_b32 v64, 0
v_mov_b32 v65, 0
v_mov_b32 v66, 0
v_mov_b32 v67, 0
v_mov_b32 v68, 0
v_mov_b32 v69, 0
v_mov_b32 v70, 0
v_mov_b32 v71, 0
v_mov_b32 v72, 0
v_mov_b32 v73, 0
v_mov_b32 v74, 0
v_mov_b32 v75, 0
v_mov_b32 v76, 0
v_mov_b32 v77, 0
v_mov_b32 v78, 0
v_mov_b32 v79, 0
v_mov_b32 v80, 0
v_mov_b32 v81, 0
v_mov_b32 v82, 0
v_mov_b32 v83, 0
v_mov_b32 v84, 0
v_mov_b32 v85, 0
v_mov_b32 v86, 0
v_mov_b32 v87, 0
v_mov_b32 v88, 0
v_mov_b32 v89, 0
v_mov_b32 v90, 0
v_mov_b32 v91, 0
v_mov_b32 v92, 0
v_mov_b32 v93, 0
v_mov_b32 v94, 0
v_mov_b32 v95, 0
v_mov_b32 v20, 0
v_mov_b32 v21, 0
v_mov_b32 v22, 0
v_mov_b32 v23, 0
v_mov_b32 v96, 0
v_mov_b32 v97, 0
v_mov_b32 v98, 0
v_mov_b32 v99, 0
v_mov_b32 v100, 0
v_mov_b32 v101, 0
v_mov_b32 v102, 0
v_mov_b32 v103, 0
v_mov_b32 v104, 0
v_mov_b32 v105, 0
v_mov_b32 v106, 0
v_mov_b32 v107, 0
v_mov_b32 v108, 0
v_mov_b32 v109, 0
v_mov_b32 v110, 0
v_mov_b32 v111, 0
v_mov_b32 v112, 0
v_mov_b32 v113, 0
v_mov_b32 v114, 0
v_mov_b32 v115, 0
v_mov_b32 v116, 0
v_mov_b32 v117, 0
v_mov_b32 v118, 0
v_mov_b32 v119, 0
v_mov_b32 v120, 0
v_mov_b32 v121, 0
v_mov_b32 v122, 0
v_mov_b32 v123, 0
v_mov_b32 v124, 0
v_mov_b32 v125, 0
v_mov_b32 v126, 0
v_mov_b32 v127, 0
v_mov_b32 v24, 0
v_mov_b32 v25, 0
v_mov_b32 v26, 0
v_mov_b32 v27, 0
v_mov_b32 v128, 0
v_mov_b32 v129, 0
v_mov_b32 v130, 0
v_mov_b32 v131, 0
v_mov_b32 v132, 0
v_mov_b32 v133, 0
v_mov_b32 v134, 0
v_mov_b32 v135, 0
v_mov_b32 v136, 0
v_mov_b32 v137, 0
v_mov_b32 v138, 0
v_mov_b32 v139, 0
v_mov_b32 v140, 0
v_mov_b32 v141, 0
v_mov_b32 v142, 0
v_mov_b32 v143, 0
v_mov_b32 v144, 0
v_mov_b32 v145, 0
v_mov_b32 v146, 0
v_mov_b32 v147, 0
v_mov_b32 v148, 0
v_mov_b32 v149, 0
v_mov_b32 v150, 0
v_mov_b32 v151, 0
v_mov_b32 v152, 0
v_mov_b32 v153, 0
v_mov_b32 v154, 0
v_mov_b32 v155, 0
v_mov_b32 v156, 0
v_mov_b32 v157, 0
v_mov_b32 v158, 0
v_mov_b32 v159, 0
v_mov_b32 v28, 0
v_mov_b32 v29, 0
v_mov_b32 v30, 0
v_mov_b32 v31, 0
v_mov_b32 v160, 0
v_mov_b32 v161, 0
v_mov_b32 v162, 0
v_mov_b32 v163, 0
v_mov_b32 v164, 0
v_mov_b32 v165, 0
v_mov_b32 v166, 0
v_mov_b32 v167, 0
v_mov_b32 v168, 0
v_mov_b32 v169, 0
v_mov_b32 v170, 0
v_mov_b32 v171, 0
v_mov_b32 v172, 0
v_mov_b32 v173, 0
v_mov_b32 v174, 0
v_mov_b32 v175, 0
v_mov_b32 v176, 0
v_mov_b32 v177, 0
v_mov_b32 v178, 0
v_mov_b32 v179, 0
v_mov_b32 v180, 0
v_mov_b32 v181, 0
v_mov_b32 v182, 0
v_mov_b32 v183, 0
v_mov_b32 v184, 0
v_mov_b32 v185, 0
v_mov_b32 v186, 0
v_mov_b32 v187, 0
v_mov_b32 v188, 0
v_mov_b32 v189, 0
v_mov_b32 v190, 0
v_mov_b32 v191, 0
v_mov_b32 v48, 0
v_mov_b32 v49, 0
v_mov_b32 v50, 0
v_mov_b32 v51, 0
v_mov_b32 v32, 0xf149f2ca
v_mov_b32 v33, 0xf149f2ca
v_mov_b32 v34, 0xf149f2ca
v_mov_b32 v35, 0xf149f2ca
v_mov_b32 v36, 0xf149f2ca
v_mov_b32 v37, 0xf149f2ca
v_mov_b32 v38, 0xf149f2ca
v_mov_b32 v39, 0xf149f2ca
v_accvgpr_write_b32 a64, 0
v_accvgpr_write_b32 a65, 0
v_accvgpr_write_b32 a66, 0
v_accvgpr_write_b32 a67, 0
v_accvgpr_write_b32 a68, 0
v_accvgpr_write_b32 a69, 0
v_accvgpr_write_b32 a70, 0
v_accvgpr_write_b32 a71, 0
v_accvgpr_write_b32 a72, 0
v_accvgpr_write_b32 a73, 0
v_accvgpr_write_b32 a74, 0
v_accvgpr_write_b32 a75, 0
v_accvgpr_write_b32 a76, 0
v_accvgpr_write_b32 a77, 0
v_accvgpr_write_b32 a78, 0
v_accvgpr_write_b32 a79, 0
v_accvgpr_write_b32 a80, 0
v_accvgpr_write_b32 a81, 0
v_accvgpr_write_b32 a82, 0
v_accvgpr_write_b32 a83, 0
v_accvgpr_write_b32 a84, 0
v_accvgpr_write_b32 a85, 0
v_accvgpr_write_b32 a86, 0
v_accvgpr_write_b32 a87, 0
v_accvgpr_write_b32 a88, 0
v_accvgpr_write_b32 a89, 0
v_accvgpr_write_b32 a90, 0
v_accvgpr_write_b32 a91, 0
v_accvgpr_write_b32 a92, 0
v_accvgpr_write_b32 a93, 0
v_accvgpr_write_b32 a94, 0
v_accvgpr_write_b32 a95, 0
v_mov_b32 v52, 0
v_mov_b32 v53, 0
v_mov_b32 v54, 0
v_mov_b32 v55, 0
v_mov_b32 v40, 0xf149f2ca
v_mov_b32 v41, 0xf149f2ca
v_mov_b32 v42, 0xf149f2ca
v_mov_b32 v43, 0xf149f2ca
v_mov_b32 v44, 0xf149f2ca
v_mov_b32 v45, 0xf149f2ca
v_mov_b32 v46, 0xf149f2ca
v_mov_b32 v47, 0xf149f2ca
v_accvgpr_write_b32 a96, 0
v_accvgpr_write_b32 a97, 0
v_accvgpr_write_b32 a98, 0
v_accvgpr_write_b32 a99, 0
v_accvgpr_write_b32 a100, 0
v_accvgpr_write_b32 a101, 0
v_accvgpr_write_b32 a102, 0
v_accvgpr_write_b32 a103, 0
v_accvgpr_write_b32 a104, 0
v_accvgpr_write_b32 a105, 0
v_accvgpr_write_b32 a106, 0
v_accvgpr_write_b32 a107, 0
v_accvgpr_write_b32 a108, 0
v_accvgpr_write_b32 a109, 0
v_accvgpr_write_b32 a110, 0
v_accvgpr_write_b32 a111, 0
v_accvgpr_write_b32 a112, 0
v_accvgpr_write_b32 a113, 0
v_accvgpr_write_b32 a114, 0
v_accvgpr_write_b32 a115, 0
v_accvgpr_write_b32 a116, 0
v_accvgpr_write_b32 a117, 0
v_accvgpr_write_b32 a118, 0
v_accvgpr_write_b32 a119, 0
v_accvgpr_write_b32 a120, 0
v_accvgpr_write_b32 a121, 0
v_accvgpr_write_b32 a122, 0
v_accvgpr_write_b32 a123, 0
v_accvgpr_write_b32 a124, 0
v_accvgpr_write_b32 a125, 0
v_accvgpr_write_b32 a126, 0
v_accvgpr_write_b32 a127, 0
s_waitcnt vmcnt(7) lgkmcnt(0)
s_barrier
s_add_u32 s28, s24, 0x20000
v_mov_b32 v1, s28
ds_read_b32 v0, v1 offset:32
s_waitcnt lgkmcnt(0)
v_readfirstlane_b32 s99, v0
s_nop 3
s_cmp_lg_u32 s99, 0
s_cbranch_scc1 M_generic_%=
s_lshl_b32 s27, s23, 13
s_mov_b32 s52, s27
s_mov_b32 s53, s27
s_add_u32 s54, s53, 0x1000
s_add_u32 m0, s52, 0
s_nop 0
buffer_load_dwordx4 v4, s[48:51], s53 offen offset:0 sc1 lds
buffer_load_dwordx4 v4, s[48:51], s53 offen offset:1024 sc1 lds
buffer_load_dwordx4 v4, s[48:51], s53 offen offset:2048 sc1 lds
buffer_load_dwordx4 v4, s[48:51], s53 offen offset:3072 sc1 lds
s_add_u32 m0, s52, 0x1000
s_nop 0
buffer_load_dwordx4 v4, s[48:51], s54 offen offset:0 sc1 lds
buffer_load_dwordx4 v4, s[48:51], s54 offen offset:1024 sc1 lds
buffer_load_dwordx4 v4, s[48:51], s54 offen offset:2048 sc1 lds
buffer_load_dwordx4 v4, s[48:51], s54 offen offset:3072 sc1 lds
s_add_u32 s53, s53, 0x8000
s_add_u32 s54, s54, 0x8000
s_add_u32 s52, s52, 0x8000
s_and_b32 s52, s52, 0x1ffff
s_add_u32 m0, s52, 0
s_nop 0
buffer_load_dwordx4 v4, s[48:51], s53 offen offset:0 sc1 lds
buffer_load_dwordx4 v4, s[48:51], s53 offen offset:1024 sc1 lds
buffer_load_dwordx4 v4, s[48:51], s53 offen offset:2048 sc1 lds
buffer_load_dwordx4 v4, s[48:51], s53 offen offset:3072 sc1 lds
s_add_u32 m0, s52, 0x1000
s_nop 0
buffer_load_dwordx4 v4, s[48:51], s54 offen offset:0 sc1 lds
buffer_load_dwordx4 v4, s[48:51], s54 offen offset:1024 sc1 lds
buffer_load_dwordx4 v4, s[48:51], s54 offen offset:2048 sc1 lds
buffer_load_dwordx4 v4, s[48:51], s54 offen offset:3072 sc1 lds
s_add_u32 s53, s53, 0x8000
s_add_u32 s54, s54, 0x8000
s_add_u32 s52, s52, 0x8000
s_and_b32 s52, s52, 0x1ffff
s_add_u32 m0, s52, 0
s_nop 0
buffer_load_dwordx4 v4, s[48:51], s53 offen offset:0 sc1 lds
buffer_load_dwordx4 v4, s[48:51], s53 offen offset:1024 sc1 lds
buffer_load_dwordx4 v4, s[48:51], s53 offen offset:2048 sc1 lds
buffer_load_dwordx4 v4, s[48:51], s53 offen offset:3072 sc1 lds
s_waitcnt vmcnt(12)
s_barrier
s_cmp_lg_u32 s23, 0
s_cbranch_scc1 F_ef_%=
s_and_b32 s27, s22, 7
s_lshl_b32 s27, s27, 5
s_lshr_b32 s28, s22, 3
s_add_u32 s28, s28, s27
s_lshl_b32 s28, s28, 2
s_add_u32 s28, s28, 0x4000
v_mov_b32 v0, 0
v_mov_b32 v1, s98
s_mov_b64 s[92:93], exec
s_mov_b64 exec, 1
buffer_store_dword v1, v0, s[40:43], s28 offen offset:0 sc1
s_mov_b64 exec, s[92:93]
F_ef_%=:
v_mbcnt_lo_u32_b32 v12, -1, 0
v_mbcnt_hi_u32_b32 v12, -1, v12
s_and_b32 s27, s23, 1
s_lshl_b32 s27, s27, 6
v_add_u32 v12, s27, v12
v_lshlrev_b32 v12, 4, v12
v_sub_u32 v13, 0xff0, v12
v_subrev_u32 v14, 16, v13
v_subrev_u32 v15, 0x800, v13
v_subrev_u32 v12, 0x810, v13
buffer_load_dwordx4 a[132:135], v13, s[76:79], 0 offen offset:0
buffer_load_dwordx4 a[128:131], v14, s[76:79], 0 offen offset:0
buffer_load_dwordx4 a[140:143], v15, s[76:79], 0 offen offset:0
buffer_load_dwordx4 a[136:139], v12, s[76:79], 0 offen offset:0
ds_read_b128 a[0:3], v6 offset:0
ds_read_b128 a[4:7], v6 offset:1024
ds_read_b128 a[8:11], v6 offset:2048
ds_read_b128 a[12:15], v6 offset:3072
ds_read_b128 a[16:19], v6 offset:4096
ds_read_b128 a[20:23], v6 offset:5120
ds_read_b128 a[24:27], v6 offset:6144
ds_read_b128 a[28:31], v6 offset:7168
ds_read_b128 a[64:67], v6 offset:16384
ds_read_b128 a[68:71], v6 offset:17408
ds_read_b128 a[72:75], v6 offset:18432
ds_read_b128 a[76:79], v6 offset:19456
v_mov_b32 v5, v6
v_add_u32 v6, 0x8000, v6
v_and_b32 v6, 0x1ffff, v6
s_waitcnt lgkmcnt(0)
s_lshl_b32 s57, s26, 1
s_cmp_eq_u32 s57, 0
s_cbranch_scc1 B_D1_%=
s_waitcnt lgkmcnt(0)
B_L1_%=:
v_mfma_f32_16x16x32_bf16 v[32:35], a[0:3], v[192:195], 0
v_exp_f32 v40, v40
v_mfma_f32_16x16x32_bf16 v[36:39], a[16:19], v[192:195], 0
ds_read_b128 a[80:83], v5 offset:20480
v_add_f32 v22, v22, v40
v_mfma_f32_16x16x32_bf16 v[32:35], a[4:7], v[196:199], v[32:35]
v_exp_f32 v41, v41
v_mfma_f32_16x16x32_bf16 v[36:39], a[20:23], v[196:199], v[36:39]
v_add_f32 v23, v23, v41
v_cvt_pk_bf16_f32 v52, v40, v41
v_mfma_f32_16x16x32_bf16 v[32:35], a[8:11], v[200:203], v[32:35]
s_add_u32 m0, s52, 0x1000
v_exp_f32 v42, v42
v_mfma_f32_16x16x32_bf16 v[36:39], a[24:27], v[200:203], v[36:39]
ds_read_b128 a[84:87], v5 offset:21504
v_add_f32 v22, v22, v42
s_waitcnt lgkmcnt(2)
v_mfma_f32_16x16x32_bf16 v[32:35], a[12:15], v[204:207], v[32:35]
v_exp_f32 v43, v43
v_mfma_f32_16x16x32_bf16 v[36:39], a[28:31], v[204:207], v[36:39]
v_add_f32 v23, v23, v43
v_cvt_pk_bf16_f32 v53, v42, v43
v_mfma_f32_16x16x32_bf16 v[128:131], a[96:99], v[48:51], v[128:131]
v_exp_f32 v44, v44
v_mfma_f32_16x16x32_bf16 v[132:135], a[100:103], v[48:51], v[132:135]
ds_read_b128 a[32:35], v5 offset:8192
v_add_f32 v22, v22, v44
v_mfma_f32_16x16x32_bf16 v[136:139], a[104:107], v[48:51], v[136:139]
v_exp_f32 v45, v45
v_mfma_f32_16x16x32_bf16 v[140:143], a[108:111], v[48:51], v[140:143]
v_add_f32 v23, v23, v45
v_cvt_pk_bf16_f32 v54, v44, v45
v_mfma_f32_16x16x32_bf16 v[144:147], a[112:115], v[48:51], v[144:147]
v_exp_f32 v46, v46
v_mfma_f32_16x16x32_bf16 v[148:151], a[116:119], v[48:51], v[148:151]
ds_read_b128 a[48:51], v5 offset:12288
v_add_f32 v22, v22, v46
v_mfma_f32_16x16x32_bf16 v[152:155], a[120:123], v[48:51], v[152:155]
v_exp_f32 v47, v47
v_mfma_f32_16x16x32_bf16 v[156:159], a[124:127], v[48:51], v[156:159]
v_add_f32 v23, v23, v47
v_cvt_pk_bf16_f32 v55, v46, v47
v_mfma_f32_16x16x32_bf16 v[40:43], a[0:3], v[208:211], 0
ds_read_b128 a[88:91], v5 offset:22528
v_exp_f32 v32, v32
v_mfma_f32_16x16x32_bf16 v[44:47], a[16:19], v[208:211], 0
buffer_load_dwordx4 v4, s[48:51], s54 offen offset:0 sc1 lds
v_add_f32 v16, v16, v32
v_mfma_f32_16x16x32_bf16 v[40:43], a[4:7], v[212:215], v[40:43]
v_exp_f32 v33, v33
v_mfma_f32_16x16x32_bf16 v[44:47], a[20:23], v[212:215], v[44:47]
v_add_f32 v17, v17, v33
v_cvt_pk_bf16_f32 v48, v32, v33
v_mfma_f32_16x16x32_bf16 v[40:43], a[8:11], v[216:219], v[40:43]
v_exp_f32 v34, v34
v_mfma_f32_16x16x32_bf16 v[44:47], a[24:27], v[216:219], v[44:47]
ds_read_b128 a[92:95], v5 offset:23552
v_add_f32 v16, v16, v34
v_mfma_f32_16x16x32_bf16 v[40:43], a[12:15], v[220:223], v[40:43]
v_exp_f32 v35, v35
v_mfma_f32_16x16x32_bf16 v[44:47], a[28:31], v[220:223], v[44:47]
v_add_f32 v17, v17, v35
v_cvt_pk_bf16_f32 v49, v34, v35
v_mfma_f32_16x16x32_bf16 v[160:163], a[96:99], v[52:55], v[160:163]
ds_read_b128 a[36:39], v5 offset:9216
v_exp_f32 v36, v36
v_mfma_f32_16x16x32_bf16 v[164:167], a[100:103], v[52:55], v[164:167]
buffer_load_dwordx4 v4, s[48:51], s54 offen offset:1024 sc1 lds
v_add_f32 v16, v16, v36
v_mfma_f32_16x16x32_bf16 v[168:171], a[104:107], v[52:55], v[168:171]
v_exp_f32 v37, v37
v_mfma_f32_16x16x32_bf16 v[172:175], a[108:111], v[52:55], v[172:175]
v_add_f32 v17, v17, v37
v_cvt_pk_bf16_f32 v50, v36, v37
v_mfma_f32_16x16x32_bf16 v[176:179], a[112:115], v[52:55], v[176:179]
v_exp_f32 v38, v38
v_mfma_f32_16x16x32_bf16 v[180:183], a[116:119], v[52:55], v[180:183]
ds_read_b128 a[52:55], v5 offset:13312
v_add_f32 v16, v16, v38
v_mfma_f32_16x16x32_bf16 v[184:187], a[120:123], v[52:55], v[184:187]
v_exp_f32 v39, v39
v_mfma_f32_16x16x32_bf16 v[188:191], a[124:127], v[52:55], v[188:191]
v_add_f32 v17, v17, v39
v_cvt_pk_bf16_f32 v51, v38, v39
v_mfma_f32_16x16x32_bf16 v[32:35], a[0:3], v[224:227], 0
ds_read_b128 a[40:43], v5 offset:10240
v_exp_f32 v40, v40
v_mfma_f32_16x16x32_bf16 v[36:39], a[16:19], v[224:227], 0
buffer_load_dwordx4 v4, s[48:51], s54 offen offset:2048 sc1 lds
v_add_f32 v20, v20, v40
v_mfma_f32_16x16x32_bf16 v[32:35], a[4:7], v[228:231], v[32:35]
v_exp_f32 v41, v41
v_mfma_f32_16x16x32_bf16 v[36:39], a[20:23], v[228:231], v[36:39]
v_add_f32 v21, v21, v41
v_cvt_pk_bf16_f32 v52, v40, v41
v_mfma_f32_16x16x32_bf16 v[32:35], a[8:11], v[232:235], v[32:35]
v_exp_f32 v42, v42
v_mfma_f32_16x16x32_bf16 v[36:39], a[24:27], v[232:235], v[36:39]
ds_read_b128 a[56:59], v5 offset:14336
v_add_f32 v20, v20, v42
v_mfma_f32_16x16x32_bf16 v[32:35], a[12:15], v[236:239], v[32:35]
v_exp_f32 v43, v43
v_mfma_f32_16x16x32_bf16 v[36:39], a[28:31], v[236:239], v[36:39]
v_add_f32 v21, v21, v43
v_cvt_pk_bf16_f32 v53, v42, v43
v_mfma_f32_16x16x32_bf16 v[64:67], a[64:67], v[48:51], v[64:67]
ds_read_b128 a[96:99], v5 offset:24576
v_exp_f32 v44, v44
v_mfma_f32_16x16x32_bf16 v[68:71], a[68:71], v[48:51], v[68:71]
buffer_load_dwordx4 v4, s[48:51], s54 offen offset:3072 sc1 lds
v_add_f32 v20, v20, v44
v_mfma_f32_16x16x32_bf16 v[72:75], a[72:75], v[48:51], v[72:75]
v_exp_f32 v45, v45
v_mfma_f32_16x16x32_bf16 v[76:79], a[76:79], v[48:51], v[76:79]
v_add_f32 v21, v21, v45
v_cvt_pk_bf16_f32 v54, v44, v45
s_waitcnt lgkmcnt(1)
v_mfma_f32_16x16x32_bf16 v[80:83], a[80:83], v[48:51], v[80:83]
v_exp_f32 v46, v46
v_mfma_f32_16x16x32_bf16 v[84:87], a[84:87], v[48:51], v[84:87]
ds_read_b128 a[100:103], v5 offset:25600
v_add_f32 v20, v20, v46
v_mfma_f32_16x16x32_bf16 v[88:91], a[88:91], v[48:51], v[88:91]
v_exp_f32 v47, v47
v_mfma_f32_16x16x32_bf16 v[92:95], a[92:95], v[48:51], v[92:95]
v_add_f32 v21, v21, v47
v_cvt_pk_bf16_f32 v55, v46, v47
v_mfma_f32_16x16x32_bf16 v[40:43], a[0:3], v[240:243], 0
v_exp_f32 v32, v32
v_mfma_f32_16x16x32_bf16 v[44:47], a[16:19], v[240:243], 0
ds_read_b128 a[44:47], v5 offset:11264
s_add_u32 s53, s53, 0x8000
s_add_u32 s54, s54, 0x8000
v_add_f32 v18, v18, v32
v_mfma_f32_16x16x32_bf16 v[40:43], a[4:7], v[244:247], v[40:43]
v_exp_f32 v33, v33
v_mfma_f32_16x16x32_bf16 v[44:47], a[20:23], v[244:247], v[44:47]
v_add_f32 v19, v19, v33
v_cvt_pk_bf16_f32 v48, v32, v33
v_mfma_f32_16x16x32_bf16 v[40:43], a[8:11], v[248:251], v[40:43]
v_exp_f32 v34, v34
v_mfma_f32_16x16x32_bf16 v[44:47], a[24:27], v[248:251], v[44:47]
ds_read_b128 a[60:63], v5 offset:15360
v_add_f32 v18, v18, v34
v_mfma_f32_16x16x32_bf16 v[40:43], a[12:15], v[252:255], v[40:43]
v_exp_f32 v35, v35
v_mfma_f32_16x16x32_bf16 v[44:47], a[28:31], v[252:255], v[44:47]
v_add_f32 v19, v19, v35
v_cvt_pk_bf16_f32 v49, v34, v35
v_mfma_f32_16x16x32_bf16 v[96:99], a[64:67], v[52:55], v[96:99]
v_exp_f32 v36, v36
v_mfma_f32_16x16x32_bf16 v[100:103], a[68:71], v[52:55], v[100:103]
ds_read_b128 a[104:107], v5 offset:26624
s_add_u32 s52, s52, 0x8000
s_and_b32 s52, s52, 0x1ffff
v_add_f32 v18, v18, v36
v_mfma_f32_16x16x32_bf16 v[104:107], a[72:75], v[52:55], v[104:107]
v_exp_f32 v37, v37
v_mfma_f32_16x16x32_bf16 v[108:111], a[76:79], v[52:55], v[108:111]
v_add_f32 v19, v19, v37
v_cvt_pk_bf16_f32 v50, v36, v37
v_mfma_f32_16x16x32_bf16 v[112:115], a[80:83], v[52:55], v[112:115]
v_exp_f32 v38, v38
v_mfma_f32_16x16x32_bf16 v[116:119], a[84:87], v[52:55], v[116:119]
ds_read_b128 a[108:111], v5 offset:27648
v_add_f32 v18, v18, v38
v_mfma_f32_16x16x32_bf16 v[120:123], a[88:91], v[52:55], v[120:123]
v_exp_f32 v39, v39
v_mfma_f32_16x16x32_bf16 v[124:127], a[92:95], v[52:55], v[124:127]
v_add_f32 v19, v19, v39
v_cvt_pk_bf16_f32 v51, v38, v39
s_waitcnt vmcnt(8)
s_barrier
v_mfma_f32_16x16x32_bf16 v[32:35], a[32:35], v[192:195], 0
v_exp_f32 v40, v40
v_mfma_f32_16x16x32_bf16 v[36:39], a[48:51], v[192:195], 0
ds_read_b128 a[112:115], v5 offset:28672
v_add_f32 v22, v22, v40
v_mfma_f32_16x16x32_bf16 v[32:35], a[36:39], v[196:199], v[32:35]
v_exp_f32 v41, v41
v_mfma_f32_16x16x32_bf16 v[36:39], a[52:55], v[196:199], v[36:39]
v_add_f32 v23, v23, v41
v_cvt_pk_bf16_f32 v52, v40, v41
v_mfma_f32_16x16x32_bf16 v[32:35], a[40:43], v[200:203], v[32:35]
s_add_u32 m0, s52, 0
v_exp_f32 v42, v42
v_mfma_f32_16x16x32_bf16 v[36:39], a[56:59], v[200:203], v[36:39]
ds_read_b128 a[116:119], v5 offset:29696
v_add_f32 v22, v22, v42
s_waitcnt lgkmcnt(2)
v_mfma_f32_16x16x32_bf16 v[32:35], a[44:47], v[204:207], v[32:35]
v_exp_f32 v43, v43
v_mfma_f32_16x16x32_bf16 v[36:39], a[60:63], v[204:207], v[36:39]
v_add_f32 v23, v23, v43
v_cvt_pk_bf16_f32 v53, v42, v43
v_mfma_f32_16x16x32_bf16 v[128:131], a[64:67], v[48:51], v[128:131]
v_exp_f32 v44, v44
v_mfma_f32_16x16x32_bf16 v[132:135], a[68:71], v[48:51], v[132:135]
ds_read_b128 a[0:3], v6 offset:0
v_add_f32 v22, v22, v44
v_mfma_f32_16x16x32_bf16 v[136:139], a[72:75], v[48:51], v[136:139]
v_exp_f32 v45, v45
v_mfma_f32_16x16x32_bf16 v[140:143], a[76:79], v[48:51], v[140:143]
v_add_f32 v23, v23, v45
v_cvt_pk_bf16_f32 v54, v44, v45
v_mfma_f32_16x16x32_bf16 v[144:147], a[80:83], v[48:51], v[144:147]
v_exp_f32 v46, v46
v_mfma_f32_16x16x32_bf16 v[148:151], a[84:87], v[48:51], v[148:151]
ds_read_b128 a[16:19], v6 offset:4096
v_add_f32 v22, v22, v46
v_mfma_f32_16x16x32_bf16 v[152:155], a[88:91], v[48:51], v[152:155]
v_exp_f32 v47, v47
v_mfma_f32_16x16x32_bf16 v[156:159], a[92:95], v[48:51], v[156:159]
v_add_f32 v23, v23, v47
v_cvt_pk_bf16_f32 v55, v46, v47
v_mfma_f32_16x16x32_bf16 v[40:43], a[32:35], v[208:211], 0
ds_read_b128 a[120:123], v5 offset:30720
v_exp_f32 v32, v32
v_mfma_f32_16x16x32_bf16 v[44:47], a[48:51], v[208:211], 0
buffer_load_dwordx4 v4, s[48:51], s53 offen offset:0 sc1 lds
v_add_f32 v16, v16, v32
v_mfma_f32_16x16x32_bf16 v[40:43], a[36:39], v[212:215], v[40:43]
v_exp_f32 v33, v33
v_mfma_f32_16x16x32_bf16 v[44:47], a[52:55], v[212:215], v[44:47]
v_add_f32 v17, v17, v33
v_cvt_pk_bf16_f32 v48, v32, v33
v_mfma_f32_16x16x32_bf16 v[40:43], a[40:43], v[216:219], v[40:43]
v_exp_f32 v34, v34
v_mfma_f32_16x16x32_bf16 v[44:47], a[56:59], v[216:219], v[44:47]
ds_read_b128 a[124:127], v5 offset:31744
v_add_f32 v16, v16, v34
v_mfma_f32_16x16x32_bf16 v[40:43], a[44:47], v[220:223], v[40:43]
v_exp_f32 v35, v35
v_mfma_f32_16x16x32_bf16 v[44:47], a[60:63], v[220:223], v[44:47]
v_add_f32 v17, v17, v35
v_cvt_pk_bf16_f32 v49, v34, v35
v_mfma_f32_16x16x32_bf16 v[160:163], a[64:67], v[52:55], v[160:163]
v_exp_f32 v36, v36
v_mfma_f32_16x16x32_bf16 v[164:167], a[68:71], v[52:55], v[164:167]
ds_read_b128 a[4:7], v6 offset:1024
v_add_f32 v16, v16, v36
v_mfma_f32_16x16x32_bf16 v[168:171], a[72:75], v[52:55], v[168:171]
v_exp_f32 v37, v37
v_mfma_f32_16x16x32_bf16 v[172:175], a[76:79], v[52:55], v[172:175]
v_add_f32 v17, v17, v37
v_cvt_pk_bf16_f32 v50, v36, v37
v_mfma_f32_16x16x32_bf16 v[176:179], a[80:83], v[52:55], v[176:179]
v_exp_f32 v38, v38
v_mfma_f32_16x16x32_bf16 v[180:183], a[84:87], v[52:55], v[180:183]
ds_read_b128 a[20:23], v6 offset:5120
v_add_f32 v16, v16, v38
v_mfma_f32_16x16x32_bf16 v[184:187], a[88:91], v[52:55], v[184:187]
v_exp_f32 v39, v39
v_mfma_f32_16x16x32_bf16 v[188:191], a[92:95], v[52:55], v[188:191]
v_add_f32 v17, v17, v39
v_cvt_pk_bf16_f32 v51, v38, v39
v_mfma_f32_16x16x32_bf16 v[32:35], a[32:35], v[224:227], 0
ds_read_b128 a[8:11], v6 offset:2048
v_exp_f32 v40, v40
v_mfma_f32_16x16x32_bf16 v[36:39], a[48:51], v[224:227], 0
buffer_load_dwordx4 v4, s[48:51], s53 offen offset:1024 sc1 lds
v_add_f32 v20, v20, v40
v_mfma_f32_16x16x32_bf16 v[32:35], a[36:39], v[228:231], v[32:35]
v_exp_f32 v41, v41
v_mfma_f32_16x16x32_bf16 v[36:39], a[52:55], v[228:231], v[36:39]
v_add_f32 v21, v21, v41
v_cvt_pk_bf16_f32 v52, v40, v41
v_mfma_f32_16x16x32_bf16 v[32:35], a[40:43], v[232:235], v[32:35]
v_exp_f32 v42, v42
v_mfma_f32_16x16x32_bf16 v[36:39], a[56:59], v[232:235], v[36:39]
ds_read_b128 a[24:27], v6 offset:6144
v_add_f32 v20, v20, v42
v_mfma_f32_16x16x32_bf16 v[32:35], a[44:47], v[236:239], v[32:35]
v_exp_f32 v43, v43
v_mfma_f32_16x16x32_bf16 v[36:39], a[60:63], v[236:239], v[36:39]
v_add_f32 v21, v21, v43
v_cvt_pk_bf16_f32 v53, v42, v43
v_mfma_f32_16x16x32_bf16 v[64:67], a[96:99], v[48:51], v[64:67]
v_exp_f32 v44, v44
v_mfma_f32_16x16x32_bf16 v[68:71], a[100:103], v[48:51], v[68:71]
ds_read_b128 a[64:67], v6 offset:16384
v_add_f32 v20, v20, v44
v_mfma_f32_16x16x32_bf16 v[72:75], a[104:107], v[48:51], v[72:75]
v_exp_f32 v45, v45
v_mfma_f32_16x16x32_bf16 v[76:79], a[108:111], v[48:51], v[76:79]
v_add_f32 v21, v21, v45
v_cvt_pk_bf16_f32 v54, v44, v45
s_waitcnt lgkmcnt(1)
v_mfma_f32_16x16x32_bf16 v[80:83], a[112:115], v[48:51], v[80:83]
v_exp_f32 v46, v46
v_mfma_f32_16x16x32_bf16 v[84:87], a[116:119], v[48:51], v[84:87]
ds_read_b128 a[68:71], v6 offset:17408
v_add_f32 v20, v20, v46
v_mfma_f32_16x16x32_bf16 v[88:91], a[120:123], v[48:51], v[88:91]
v_exp_f32 v47, v47
v_mfma_f32_16x16x32_bf16 v[92:95], a[124:127], v[48:51], v[92:95]
v_add_f32 v21, v21, v47
v_cvt_pk_bf16_f32 v55, v46, v47
v_mfma_f32_16x16x32_bf16 v[40:43], a[32:35], v[240:243], 0
ds_read_b128 a[12:15], v6 offset:3072
v_exp_f32 v32, v32
v_mfma_f32_16x16x32_bf16 v[44:47], a[48:51], v[240:243], 0
buffer_load_dwordx4 v4, s[48:51], s53 offen offset:2048 sc1 lds
v_add_f32 v18, v18, v32
v_mfma_f32_16x16x32_bf16 v[40:43], a[36:39], v[244:247], v[40:43]
v_exp_f32 v33, v33
v_mfma_f32_16x16x32_bf16 v[44:47], a[52:55], v[244:247], v[44:47]
v_add_f32 v19, v19, v33
v_cvt_pk_bf16_f32 v48, v32, v33
v_mfma_f32_16x16x32_bf16 v[40:43], a[40:43], v[248:251], v[40:43]
v_exp_f32 v34, v34
v_mfma_f32_16x16x32_bf16 v[44:47], a[56:59], v[248:251], v[44:47]
ds_read_b128 a[28:31], v6 offset:7168
v_add_f32 v18, v18, v34
v_mfma_f32_16x16x32_bf16 v[40:43], a[44:47], v[252:255], v[40:43]
v_exp_f32 v35, v35
v_mfma_f32_16x16x32_bf16 v[44:47], a[60:63], v[252:255], v[44:47]
v_add_f32 v19, v19, v35
v_cvt_pk_bf16_f32 v49, v34, v35
v_mfma_f32_16x16x32_bf16 v[96:99], a[96:99], v[52:55], v[96:99]
ds_read_b128 a[72:75], v6 offset:18432
v_exp_f32 v36, v36
v_mfma_f32_16x16x32_bf16 v[100:103], a[100:103], v[52:55], v[100:103]
buffer_load_dwordx4 v4, s[48:51], s53 offen offset:3072 sc1 lds
v_add_f32 v18, v18, v36
v_mfma_f32_16x16x32_bf16 v[104:107], a[104:107], v[52:55], v[104:107]
v_exp_f32 v37, v37
v_mfma_f32_16x16x32_bf16 v[108:111], a[108:111], v[52:55], v[108:111]
v_add_f32 v19, v19, v37
v_cvt_pk_bf16_f32 v50, v36, v37
v_mfma_f32_16x16x32_bf16 v[112:115], a[112:115], v[52:55], v[112:115]
v_exp_f32 v38, v38
v_mfma_f32_16x16x32_bf16 v[116:119], a[116:119], v[52:55], v[116:119]
ds_read_b128 a[76:79], v6 offset:19456
v_add_f32 v18, v18, v38
v_mfma_f32_16x16x32_bf16 v[120:123], a[120:123], v[52:55], v[120:123]
v_exp_f32 v39, v39
v_mfma_f32_16x16x32_bf16 v[124:127], a[124:127], v[52:55], v[124:127]
v_add_f32 v19, v19, v39
v_cvt_pk_bf16_f32 v51, v38, v39
v_mov_b32 v5, v6
v_add_u32 v6, 0x8000, v6
v_and_b32 v6, 0x1ffff, v6
s_sub_u32 s57, s57, 1
s_cmp_lg_u32 s57, 0
s_cbranch_scc1 B_L1_%=
s_waitcnt lgkmcnt(0)
B_D1_%=:
s_cmp_eq_u32 s23, 1
s_cbranch_scc1 B_D1w1_%=
s_cmp_eq_u32 s23, 2
s_cbranch_scc1 B_D1w2_%=
s_cmp_eq_u32 s23, 3
s_cbranch_scc1 B_D1w3_%=
v_mfma_f32_16x16x32_bf16 v[32:35], a[0:3], v[192:195], v[8:11]
ds_read_b128 a[80:83], v5 offset:20480
v_exp_f32 v40, v40
v_mfma_f32_16x16x32_bf16 v[32:35], a[4:7], v[196:199], v[32:35]
v_add_f32 v22, v22, v40
v_mfma_f32_16x16x32_bf16 v[32:35], a[8:11], v[200:203], v[32:35]
s_add_u32 m0, s52, 0x1000
v_exp_f32 v41, v41
v_mfma_f32_16x16x32_bf16 v[32:35], a[12:15], v[204:207], v[32:35]
ds_read_b128 a[84:87], v5 offset:21504
v_add_f32 v23, v23, v41
v_mfma_f32_16x16x32_bf16 v[128:131], a[96:99], v[48:51], v[128:131]
v_exp_f32 v42, v42
v_mfma_f32_16x16x32_bf16 v[132:135], a[100:103], v[48:51], v[132:135]
v_add_f32 v22, v22, v42
v_mfma_f32_16x16x32_bf16 v[136:139], a[104:107], v[48:51], v[136:139]
ds_read_b128 a[32:35], v5 offset:8192
v_exp_f32 v43, v43
v_mfma_f32_16x16x32_bf16 v[140:143], a[108:111], v[48:51], v[140:143]
v_add_f32 v23, v23, v43
v_cvt_pk_bf16_f32 v52, v40, v41
v_mfma_f32_16x16x32_bf16 v[144:147], a[112:115], v[48:51], v[144:147]
v_cvt_pk_bf16_f32 v53, v42, v43
v_mfma_f32_16x16x32_bf16 v[148:151], a[116:119], v[48:51], v[148:151]
ds_read_b128 a[48:51], v5 offset:12288
v_exp_f32 v44, v44
v_mfma_f32_16x16x32_bf16 v[152:155], a[120:123], v[48:51], v[152:155]
v_add_f32 v22, v22, v44
v_mfma_f32_16x16x32_bf16 v[156:159], a[124:127], v[48:51], v[156:159]
v_exp_f32 v45, v45
s_nop 0
v_add_f32 v23, v23, v45
v_exp_f32 v46, v46
s_nop 0
v_add_f32 v22, v22, v46
v_exp_f32 v47, v47
s_nop 0
v_add_f32 v23, v23, v47
v_cvt_pk_bf16_f32 v54, v44, v45
v_cvt_pk_bf16_f32 v55, v46, v47
v_mfma_f32_16x16x32_bf16 v[40:43], a[0:3], v[208:211], 0
ds_read_b128 a[88:91], v5 offset:22528
v_exp_f32 v32, v32
v_mfma_f32_16x16x32_bf16 v[44:47], a[16:19], v[208:211], v[8:11]
v_add_f32 v16, v16, v32
v_mfma_f32_16x16x32_bf16 v[40:43], a[4:7], v[212:215], v[40:43]
buffer_load_dwordx4 v4, s[48:51], s54 offen offset:0 sc1 lds
v_exp_f32 v33, v33
v_mfma_f32_16x16x32_bf16 v[44:47], a[20:23], v[212:215], v[44:47]
v_add_f32 v17, v17, v33
v_mfma_f32_16x16x32_bf16 v[40:43], a[8:11], v[216:219], v[40:43]
v_exp_f32 v34, v34
v_mfma_f32_16x16x32_bf16 v[44:47], a[24:27], v[216:219], v[44:47]
ds_read_b128 a[92:95], v5 offset:23552
v_add_f32 v16, v16, v34
v_mfma_f32_16x16x32_bf16 v[40:43], a[12:15], v[220:223], v[40:43]
v_exp_f32 v35, v35
v_mfma_f32_16x16x32_bf16 v[44:47], a[28:31], v[220:223], v[44:47]
v_add_f32 v17, v17, v35
v_cvt_pk_bf16_f32 v48, v32, v33
v_mfma_f32_16x16x32_bf16 v[160:163], a[96:99], v[52:55], v[160:163]
ds_read_b128 a[36:39], v5 offset:9216
v_cvt_pk_bf16_f32 v49, v34, v35
v_mov_b32 v50, 0
v_mfma_f32_16x16x32_bf16 v[164:167], a[100:103], v[52:55], v[164:167]
v_mov_b32 v51, 0
v_mfma_f32_16x16x32_bf16 v[168:171], a[104:107], v[52:55], v[168:171]
buffer_load_dwordx4 v4, s[48:51], s54 offen offset:1024 sc1 lds
v_mfma_f32_16x16x32_bf16 v[172:175], a[108:111], v[52:55], v[172:175]
v_mfma_f32_16x16x32_bf16 v[176:179], a[112:115], v[52:55], v[176:179]
v_mfma_f32_16x16x32_bf16 v[180:183], a[116:119], v[52:55], v[180:183]
ds_read_b128 a[52:55], v5 offset:13312
v_mfma_f32_16x16x32_bf16 v[184:187], a[120:123], v[52:55], v[184:187]
v_mfma_f32_16x16x32_bf16 v[188:191], a[124:127], v[52:55], v[188:191]
v_mfma_f32_16x16x32_bf16 v[32:35], a[0:3], v[224:227], 0
ds_read_b128 a[40:43], v5 offset:10240
v_exp_f32 v40, v40
v_mfma_f32_16x16x32_bf16 v[36:39], a[16:19], v[224:227], 0
buffer_load_dwordx4 v4, s[48:51], s54 offen offset:2048 sc1 lds
v_add_f32 v20, v20, v40
v_mfma_f32_16x16x32_bf16 v[32:35], a[4:7], v[228:231], v[32:35]
v_exp_f32 v41, v41
v_mfma_f32_16x16x32_bf16 v[36:39], a[20:23], v[228:231], v[36:39]
v_add_f32 v21, v21, v41
v_cvt_pk_bf16_f32 v52, v40, v41
v_mfma_f32_16x16x32_bf16 v[32:35], a[8:11], v[232:235], v[32:35]
v_exp_f32 v42, v42
v_mfma_f32_16x16x32_bf16 v[36:39], a[24:27], v[232:235], v[36:39]
ds_read_b128 a[56:59], v5 offset:14336
v_add_f32 v20, v20, v42
v_mfma_f32_16x16x32_bf16 v[32:35], a[12:15], v[236:239], v[32:35]
v_exp_f32 v43, v43
v_mfma_f32_16x16x32_bf16 v[36:39], a[28:31], v[236:239], v[36:39]
v_add_f32 v21, v21, v43
v_cvt_pk_bf16_f32 v53, v42, v43
v_mfma_f32_16x16x32_bf16 v[64:67], a[64:67], v[48:51], v[64:67]
ds_read_b128 a[96:99], v5 offset:24576
v_exp_f32 v44, v44
v_mfma_f32_16x16x32_bf16 v[68:71], a[68:71], v[48:51], v[68:71]
buffer_load_dwordx4 v4, s[48:51], s54 offen offset:3072 sc1 lds
v_add_f32 v20, v20, v44
v_mfma_f32_16x16x32_bf16 v[72:75], a[72:75], v[48:51], v[72:75]
v_exp_f32 v45, v45
v_mfma_f32_16x16x32_bf16 v[76:79], a[76:79], v[48:51], v[76:79]
v_add_f32 v21, v21, v45
v_cvt_pk_bf16_f32 v54, v44, v45
s_waitcnt lgkmcnt(1)
v_mfma_f32_16x16x32_bf16 v[80:83], a[80:83], v[48:51], v[80:83]
v_exp_f32 v46, v46
v_mfma_f32_16x16x32_bf16 v[84:87], a[84:87], v[48:51], v[84:87]
ds_read_b128 a[100:103], v5 offset:25600
v_add_f32 v20, v20, v46
v_mfma_f32_16x16x32_bf16 v[88:91], a[88:91], v[48:51], v[88:91]
v_exp_f32 v47, v47
v_mfma_f32_16x16x32_bf16 v[92:95], a[92:95], v[48:51], v[92:95]
v_add_f32 v21, v21, v47
v_cvt_pk_bf16_f32 v55, v46, v47
v_mfma_f32_16x16x32_bf16 v[40:43], a[0:3], v[240:243], 0
v_exp_f32 v32, v32
v_mfma_f32_16x16x32_bf16 v[44:47], a[16:19], v[240:243], 0
ds_read_b128 a[44:47], v5 offset:11264
s_add_u32 s53, s53, 0x8000
s_add_u32 s54, s54, 0x8000
v_add_f32 v18, v18, v32
v_mfma_f32_16x16x32_bf16 v[40:43], a[4:7], v[244:247], v[40:43]
v_exp_f32 v33, v33
v_mfma_f32_16x16x32_bf16 v[44:47], a[20:23], v[244:247], v[44:47]
v_add_f32 v19, v19, v33
v_cvt_pk_bf16_f32 v48, v32, v33
v_mfma_f32_16x16x32_bf16 v[40:43], a[8:11], v[248:251], v[40:43]
v_exp_f32 v34, v34
v_mfma_f32_16x16x32_bf16 v[44:47], a[24:27], v[248:251], v[44:47]
ds_read_b128 a[60:63], v5 offset:15360
v_add_f32 v18, v18, v34
v_mfma_f32_16x16x32_bf16 v[40:43], a[12:15], v[252:255], v[40:43]
v_exp_f32 v35, v35
v_mfma_f32_16x16x32_bf16 v[44:47], a[28:31], v[252:255], v[44:47]
v_add_f32 v19, v19, v35
v_cvt_pk_bf16_f32 v49, v34, v35
v_mfma_f32_16x16x32_bf16 v[96:99], a[64:67], v[52:55], v[96:99]
v_exp_f32 v36, v36
v_mfma_f32_16x16x32_bf16 v[100:103], a[68:71], v[52:55], v[100:103]
ds_read_b128 a[104:107], v5 offset:26624
s_add_u32 s52, s52, 0x8000
s_and_b32 s52, s52, 0x1ffff
v_add_f32 v18, v18, v36
v_mfma_f32_16x16x32_bf16 v[104:107], a[72:75], v[52:55], v[104:107]
v_exp_f32 v37, v37
v_mfma_f32_16x16x32_bf16 v[108:111], a[76:79], v[52:55], v[108:111]
v_add_f32 v19, v19, v37
v_cvt_pk_bf16_f32 v50, v36, v37
v_mfma_f32_16x16x32_bf16 v[112:115], a[80:83], v[52:55], v[112:115]
v_exp_f32 v38, v38
v_mfma_f32_16x16x32_bf16 v[116:119], a[84:87], v[52:55], v[116:119]
ds_read_b128 a[108:111], v5 offset:27648
v_add_f32 v18, v18, v38
v_mfma_f32_16x16x32_bf16 v[120:123], a[88:91], v[52:55], v[120:123]
v_exp_f32 v39, v39
v_mfma_f32_16x16x32_bf16 v[124:127], a[92:95], v[52:55], v[124:127]
v_add_f32 v19, v19, v39
v_cvt_pk_bf16_f32 v51, v38, v39
s_waitcnt vmcnt(8)
s_barrier
v_mfma_f32_16x16x32_bf16 v[32:35], a[32:35], v[224:227], 0
ds_read_b128 a[112:115], v5 offset:28672
s_add_u32 m0, s52, 0
v_exp_f32 v40, v40
v_mfma_f32_16x16x32_bf16 v[36:39], a[48:51], v[224:227], 0
ds_read_b128 a[116:119], v5 offset:29696
v_add_f32 v22, v22, v40
v_mfma_f32_16x16x32_bf16 v[32:35], a[36:39], v[228:231], v[32:35]
v_exp_f32 v41, v41
v_mfma_f32_16x16x32_bf16 v[36:39], a[52:55], v[228:231], v[36:39]
v_add_f32 v23, v23, v41
v_cvt_pk_bf16_f32 v52, v40, v41
v_mfma_f32_16x16x32_bf16 v[32:35], a[40:43], v[232:235], v[32:35]
ds_read_b128 a[0:3], v6 offset:0
v_exp_f32 v42, v42
v_mfma_f32_16x16x32_bf16 v[36:39], a[56:59], v[232:235], v[36:39]
ds_read_b128 a[16:19], v6 offset:4096
v_add_f32 v22, v22, v42
s_waitcnt lgkmcnt(3)
v_mfma_f32_16x16x32_bf16 v[32:35], a[44:47], v[236:239], v[32:35]
v_exp_f32 v43, v43
v_mfma_f32_16x16x32_bf16 v[36:39], a[60:63], v[236:239], v[36:39]
v_add_f32 v23, v23, v43
v_cvt_pk_bf16_f32 v53, v42, v43
v_mfma_f32_16x16x32_bf16 v[128:131], a[64:67], v[48:51], v[128:131]
buffer_load_dwordx4 v4, s[48:51], s53 offen offset:0 sc1 lds
v_exp_f32 v44, v44
v_mfma_f32_16x16x32_bf16 v[132:135], a[68:71], v[48:51], v[132:135]
ds_read_b128 a[4:7], v6 offset:1024
v_add_f32 v22, v22, v44
v_mfma_f32_16x16x32_bf16 v[136:139], a[72:75], v[48:51], v[136:139]
v_exp_f32 v45, v45
v_mfma_f32_16x16x32_bf16 v[140:143], a[76:79], v[48:51], v[140:143]
v_add_f32 v23, v23, v45
v_cvt_pk_bf16_f32 v54, v44, v45
v_mfma_f32_16x16x32_bf16 v[144:147], a[80:83], v[48:51], v[144:147]
v_exp_f32 v46, v46
v_mfma_f32_16x16x32_bf16 v[148:151], a[84:87], v[48:51], v[148:151]
ds_read_b128 a[20:23], v6 offset:5120
v_add_f32 v22, v22, v46
v_mfma_f32_16x16x32_bf16 v[152:155], a[88:91], v[48:51], v[152:155]
v_exp_f32 v47, v47
v_mfma_f32_16x16x32_bf16 v[156:159], a[92:95], v[48:51], v[156:159]
v_add_f32 v23, v23, v47
v_cvt_pk_bf16_f32 v55, v46, v47
v_mfma_f32_16x16x32_bf16 v[40:43], a[32:35], v[240:243], 0
ds_read_b128 a[120:123], v5 offset:30720
v_exp_f32 v32, v32
v_mfma_f32_16x16x32_bf16 v[44:47], a[48:51], v[240:243], 0
buffer_load_dwordx4 v4, s[48:51], s53 offen offset:1024 sc1 lds
v_add_f32 v18, v18, v32
v_mfma_f32_16x16x32_bf16 v[40:43], a[36:39], v[244:247], v[40:43]
ds_read_b128 a[124:127], v5 offset:31744
v_exp_f32 v33, v33
v_mfma_f32_16x16x32_bf16 v[44:47], a[52:55], v[244:247], v[44:47]
v_add_f32 v19, v19, v33
v_cvt_pk_bf16_f32 v48, v32, v33
v_mfma_f32_16x16x32_bf16 v[40:43], a[40:43], v[248:251], v[40:43]
ds_read_b128 a[8:11], v6 offset:2048
v_exp_f32 v34, v34
v_mfma_f32_16x16x32_bf16 v[44:47], a[56:59], v[248:251], v[44:47]
buffer_load_dwordx4 v4, s[48:51], s53 offen offset:2048 sc1 lds
v_add_f32 v18, v18, v34
v_mfma_f32_16x16x32_bf16 v[40:43], a[44:47], v[252:255], v[40:43]
v_exp_f32 v35, v35
v_mfma_f32_16x16x32_bf16 v[44:47], a[60:63], v[252:255], v[44:47]
v_add_f32 v19, v19, v35
v_cvt_pk_bf16_f32 v49, v34, v35
v_mfma_f32_16x16x32_bf16 v[160:163], a[64:67], v[52:55], v[160:163]
ds_read_b128 a[24:27], v6 offset:6144
v_exp_f32 v36, v36
v_mfma_f32_16x16x32_bf16 v[164:167], a[68:71], v[52:55], v[164:167]
ds_read_b128 a[12:15], v6 offset:3072
v_add_f32 v18, v18, v36
v_mfma_f32_16x16x32_bf16 v[168:171], a[72:75], v[52:55], v[168:171]
v_exp_f32 v37, v37
v_mfma_f32_16x16x32_bf16 v[172:175], a[76:79], v[52:55], v[172:175]
v_add_f32 v19, v19, v37
v_cvt_pk_bf16_f32 v50, v36, v37
v_mfma_f32_16x16x32_bf16 v[176:179], a[80:83], v[52:55], v[176:179]
buffer_load_dwordx4 v4, s[48:51], s53 offen offset:3072 sc1 lds
v_exp_f32 v38, v38
v_mfma_f32_16x16x32_bf16 v[180:183], a[84:87], v[52:55], v[180:183]
ds_read_b128 a[28:31], v6 offset:7168
v_add_f32 v18, v18, v38
v_mfma_f32_16x16x32_bf16 v[184:187], a[88:91], v[52:55], v[184:187]
v_exp_f32 v39, v39
v_mfma_f32_16x16x32_bf16 v[188:191], a[92:95], v[52:55], v[188:191]
v_add_f32 v19, v19, v39
v_cvt_pk_bf16_f32 v51, v38, v39
v_mov_b32 v5, v6
v_add_u32 v6, 0x8000, v6
v_and_b32 v6, 0x1ffff, v6
s_waitcnt lgkmcnt(1)
v_mfma_f32_16x16x32_bf16 v[32:35], a[0:3], v[224:227], 0
ds_read_b128 a[32:35], v5 offset:8192
s_add_u32 m0, s52, 0x1000
v_exp_f32 v40, v40
v_mfma_f32_16x16x32_bf16 v[36:39], a[16:19], v[224:227], 0
ds_read_b128 a[48:51], v5 offset:12288
v_add_f32 v22, v22, v40
v_mfma_f32_16x16x32_bf16 v[32:35], a[4:7], v[228:231], v[32:35]
ds_read_b128 a[36:39], v5 offset:9216
v_exp_f32 v41, v41
v_mfma_f32_16x16x32_bf16 v[36:39], a[20:23], v[228:231], v[36:39]
v_add_f32 v23, v23, v41
v_cvt_pk_bf16_f32 v52, v40, v41
v_mfma_f32_16x16x32_bf16 v[32:35], a[8:11], v[232:235], v[32:35]
buffer_load_dwordx4 v4, s[48:51], s54 offen offset:0 sc1 lds
v_exp_f32 v42, v42
v_mfma_f32_16x16x32_bf16 v[36:39], a[24:27], v[232:235], v[36:39]
ds_read_b128 a[52:55], v5 offset:13312
v_add_f32 v22, v22, v42
v_mfma_f32_16x16x32_bf16 v[32:35], a[12:15], v[236:239], v[32:35]
v_exp_f32 v43, v43
s_waitcnt lgkmcnt(2)
v_mfma_f32_16x16x32_bf16 v[36:39], a[28:31], v[236:239], v[36:39]
v_add_f32 v23, v23, v43
v_cvt_pk_bf16_f32 v53, v42, v43
v_mfma_f32_16x16x32_bf16 v[128:131], a[96:99], v[48:51], v[128:131]
ds_read_b128 a[40:43], v5 offset:10240
v_exp_f32 v44, v44
v_mfma_f32_16x16x32_bf16 v[132:135], a[100:103], v[48:51], v[132:135]
ds_read_b128 a[56:59], v5 offset:14336
v_add_f32 v22, v22, v44
v_mfma_f32_16x16x32_bf16 v[136:139], a[104:107], v[48:51], v[136:139]
buffer_load_dwordx4 v4, s[48:51], s54 offen offset:1024 sc1 lds
v_exp_f32 v45, v45
v_mfma_f32_16x16x32_bf16 v[140:143], a[108:111], v[48:51], v[140:143]
v_add_f32 v23, v23, v45
v_cvt_pk_bf16_f32 v54, v44, v45
v_mfma_f32_16x16x32_bf16 v[144:147], a[112:115], v[48:51], v[144:147]
ds_read_b128 a[44:47], v5 offset:11264
v_exp_f32 v46, v46
v_mfma_f32_16x16x32_bf16 v[148:151], a[116:119], v[48:51], v[148:151]
ds_read_b128 a[60:63], v5 offset:15360
v_add_f32 v22, v22, v46
v_mfma_f32_16x16x32_bf16 v[152:155], a[120:123], v[48:51], v[152:155]
v_exp_f32 v47, v47
v_mfma_f32_16x16x32_bf16 v[156:159], a[124:127], v[48:51], v[156:159]
v_add_f32 v23, v23, v47
v_cvt_pk_bf16_f32 v55, v46, v47
v_mfma_f32_16x16x32_bf16 v[40:43], a[0:3], v[240:243], 0
ds_read_b128 a[64:67], v5 offset:16384
v_exp_f32 v32, v32
v_mfma_f32_16x16x32_bf16 v[44:47], a[16:19], v[240:243], 0
buffer_load_dwordx4 v4, s[48:51], s54 offen offset:2048 sc1 lds
v_add_f32 v18, v18, v32
v_mfma_f32_16x16x32_bf16 v[40:43], a[4:7], v[244:247], v[40:43]
ds_read_b128 a[68:71], v5 offset:17408
v_exp_f32 v33, v33
v_mfma_f32_16x16x32_bf16 v[44:47], a[20:23], v[244:247], v[44:47]
v_add_f32 v19, v19, v33
v_cvt_pk_bf16_f32 v48, v32, v33
v_mfma_f32_16x16x32_bf16 v[40:43], a[8:11], v[248:251], v[40:43]
ds_read_b128 a[72:75], v5 offset:18432
v_exp_f32 v34, v34
v_mfma_f32_16x16x32_bf16 v[44:47], a[24:27], v[248:251], v[44:47]
buffer_load_dwordx4 v4, s[48:51], s54 offen offset:3072 sc1 lds
v_add_f32 v18, v18, v34
v_mfma_f32_16x16x32_bf16 v[40:43], a[12:15], v[252:255], v[40:43]
v_exp_f32 v35, v35
v_mfma_f32_16x16x32_bf16 v[44:47], a[28:31], v[252:255], v[44:47]
v_add_f32 v19, v19, v35
v_cvt_pk_bf16_f32 v49, v34, v35
v_mfma_f32_16x16x32_bf16 v[160:163], a[96:99], v[52:55], v[160:163]
ds_read_b128 a[76:79], v5 offset:19456
v_exp_f32 v36, v36
v_mfma_f32_16x16x32_bf16 v[164:167], a[100:103], v[52:55], v[164:167]
ds_read_b128 a[80:83], v5 offset:20480
s_add_u32 s53, s53, 0x8000
s_add_u32 s54, s54, 0x8000
v_add_f32 v18, v18, v36
v_mfma_f32_16x16x32_bf16 v[168:171], a[104:107], v[52:55], v[168:171]
ds_read_b128 a[84:87], v5 offset:21504
v_exp_f32 v37, v37
v_mfma_f32_16x16x32_bf16 v[172:175], a[108:111], v[52:55], v[172:175]
v_add_f32 v19, v19, v37
v_cvt_pk_bf16_f32 v50, v36, v37
v_mfma_f32_16x16x32_bf16 v[176:179], a[112:115], v[52:55], v[176:179]
ds_read_b128 a[88:91], v5 offset:22528
s_add_u32 s52, s52, 0x8000
s_and_b32 s52, s52, 0x1ffff
v_exp_f32 v38, v38
v_mfma_f32_16x16x32_bf16 v[180:183], a[116:119], v[52:55], v[180:183]
ds_read_b128 a[92:95], v5 offset:23552
v_add_f32 v18, v18, v38
v_mfma_f32_16x16x32_bf16 v[184:187], a[120:123], v[52:55], v[184:187]
v_exp_f32 v39, v39
v_mfma_f32_16x16x32_bf16 v[188:191], a[124:127], v[52:55], v[188:191]
v_add_f32 v19, v19, v39
v_cvt_pk_bf16_f32 v51, v38, v39
s_waitcnt vmcnt(8)
s_barrier
v_mfma_f32_16x16x32_bf16 v[32:35], a[32:35], v[224:227], 0
ds_read_b128 a[0:3], v6 offset:0
s_add_u32 m0, s52, 0
v_exp_f32 v40, v40
v_mfma_f32_16x16x32_bf16 v[36:39], a[48:51], v[224:227], 0
ds_read_b128 a[16:19], v6 offset:4096
v_add_f32 v22, v22, v40
s_waitcnt lgkmcnt(3)
v_mfma_f32_16x16x32_bf16 v[32:35], a[36:39], v[228:231], v[32:35]
ds_read_b128 a[4:7], v6 offset:1024
v_exp_f32 v41, v41
v_mfma_f32_16x16x32_bf16 v[36:39], a[52:55], v[228:231], v[36:39]
v_add_f32 v23, v23, v41
v_cvt_pk_bf16_f32 v52, v40, v41
v_mfma_f32_16x16x32_bf16 v[32:35], a[40:43], v[232:235], v[32:35]
ds_read_b128 a[20:23], v6 offset:5120
v_exp_f32 v42, v42
v_mfma_f32_16x16x32_bf16 v[36:39], a[56:59], v[232:235], v[36:39]
ds_read_b128 a[8:11], v6 offset:2048
v_add_f32 v22, v22, v42
v_mfma_f32_16x16x32_bf16 v[32:35], a[44:47], v[236:239], v[32:35]
v_exp_f32 v43, v43
v_mfma_f32_16x16x32_bf16 v[36:39], a[60:63], v[236:239], v[36:39]
v_add_f32 v23, v23, v43
v_cvt_pk_bf16_f32 v53, v42, v43
v_mfma_f32_16x16x32_bf16 v[128:131], a[64:67], v[48:51], v[128:131]
buffer_load_dwordx4 v4, s[48:51], s53 offen offset:0 sc1 lds
v_exp_f32 v44, v44
v_mfma_f32_16x16x32_bf16 v[132:135], a[68:71], v[48:51], v[132:135]
ds_read_b128 a[24:27], v6 offset:6144
v_add_f32 v22, v22, v44
v_mfma_f32_16x16x32_bf16 v[136:139], a[72:75], v[48:51], v[136:139]
v_exp_f32 v45, v45
v_mfma_f32_16x16x32_bf16 v[140:143], a[76:79], v[48:51], v[140:143]
v_add_f32 v23, v23, v45
v_cvt_pk_bf16_f32 v54, v44, v45
v_mfma_f32_16x16x32_bf16 v[144:147], a[80:83], v[48:51], v[144:147]
ds_read_b128 a[12:15], v6 offset:3072
v_exp_f32 v46, v46
v_mfma_f32_16x16x32_bf16 v[148:151], a[84:87], v[48:51], v[148:151]
ds_read_b128 a[28:31], v6 offset:7168
v_add_f32 v22, v22, v46
v_mfma_f32_16x16x32_bf16 v[152:155], a[88:91], v[48:51], v[152:155]
v_exp_f32 v47, v47
s_waitcnt lgkmcnt(2)
v_mfma_f32_16x16x32_bf16 v[156:159], a[92:95], v[48:51], v[156:159]
v_add_f32 v23, v23, v47
v_cvt_pk_bf16_f32 v55, v46, v47
v_mfma_f32_16x16x32_bf16 v[40:43], a[32:35], v[240:243], 0
ds_read_b128 a[96:99], v5 offset:24576
v_exp_f32 v32, v32
v_mfma_f32_16x16x32_bf16 v[44:47], a[48:51], v[240:243], 0
buffer_load_dwordx4 v4, s[48:51], s53 offen offset:1024 sc1 lds
v_exp_f32 v33, v33
v_mfma_f32_16x16x32_bf16 v[40:43], a[36:39], v[244:247], v[40:43]
ds_read_b128 a[100:103], v5 offset:25600
v_exp_f32 v34, v34
v_mfma_f32_16x16x32_bf16 v[44:47], a[52:55], v[244:247], v[44:47]
v_exp_f32 v35, v35
v_mfma_f32_16x16x32_bf16 v[40:43], a[40:43], v[248:251], v[40:43]
ds_read_b128 a[104:107], v5 offset:26624
v_cvt_pk_bf16_f32 v48, v32, v33
v_cvt_pk_bf16_f32 v49, v34, v35
v_mfma_f32_16x16x32_bf16 v[44:47], a[56:59], v[248:251], v[44:47]
buffer_load_dwordx4 v4, s[48:51], s53 offen offset:2048 sc1 lds
v_exp_f32 v36, v36
v_mfma_f32_16x16x32_bf16 v[40:43], a[44:47], v[252:255], v[40:43]
v_exp_f32 v37, v37
v_mfma_f32_16x16x32_bf16 v[44:47], a[60:63], v[252:255], v[44:47]
ds_read_b128 a[108:111], v5 offset:27648
v_exp_f32 v38, v38
v_mfma_f32_16x16x32_bf16 v[160:163], a[64:67], v[52:55], v[160:163]
ds_read_b128 a[112:115], v5 offset:28672
v_exp_f32 v39, v39
v_mfma_f32_16x16x32_bf16 v[164:167], a[68:71], v[52:55], v[164:167]
v_cvt_pk_bf16_f32 v50, v36, v37
v_cvt_pk_bf16_f32 v51, v38, v39
v_mfma_f32_16x16x32_bf16 v[168:171], a[72:75], v[52:55], v[168:171]
ds_read_b128 a[116:119], v5 offset:29696
v_mfma_f32_16x16x32_bf16 v[172:175], a[76:79], v[52:55], v[172:175]
buffer_load_dwordx4 v4, s[48:51], s53 offen offset:3072 sc1 lds
v_mfma_f32_16x16x32_bf16 v[176:179], a[80:83], v[52:55], v[176:179]
v_mfma_f32_16x16x32_bf16 v[180:183], a[84:87], v[52:55], v[180:183]
ds_read_b128 a[120:123], v5 offset:30720
v_mfma_f32_16x16x32_bf16 v[184:187], a[88:91], v[52:55], v[184:187]
ds_read_b128 a[124:127], v5 offset:31744
v_mfma_f32_16x16x32_bf16 v[188:191], a[92:95], v[52:55], v[188:191]
v_mov_b32 v5, v6
v_add_u32 v6, 0x8000, v6
v_and_b32 v6, 0x1ffff, v6
s_waitcnt lgkmcnt(0)
v_mbcnt_lo_u32_b32 v12, -1, 0
v_mbcnt_hi_u32_b32 v12, -1, v12
v_lshlrev_b32 v12, 4, v12
s_mov_b32 s27, 0x4000
buffer_load_dwordx4 v[0:3], v12, s[40:43], s27 offen offset:0 sc1
s_nop 7
v_add_f32 v12, v16, v17
v_mov_b32 v13, v12
s_nop 1
v_permlane16_swap_b32 v12, v13
v_add_f32 v12, v12, v13
v_mov_b32 v13, v12
s_nop 1
v_permlane32_swap_b32 v12, v13
v_add_f32 v12, v12, v13
v_rcp_f32 v12, v12
s_nop 0
v_pk_mul_f32 v[64:65], v[64:65], v[12:13] op_sel_hi:[1,0]
v_pk_mul_f32 v[66:67], v[66:67], v[12:13] op_sel_hi:[1,0]
v_pk_mul_f32 v[68:69], v[68:69], v[12:13] op_sel_hi:[1,0]
v_pk_mul_f32 v[70:71], v[70:71], v[12:13] op_sel_hi:[1,0]
v_pk_mul_f32 v[72:73], v[72:73], v[12:13] op_sel_hi:[1,0]
v_pk_mul_f32 v[74:75], v[74:75], v[12:13] op_sel_hi:[1,0]
v_pk_mul_f32 v[76:77], v[76:77], v[12:13] op_sel_hi:[1,0]
v_pk_mul_f32 v[78:79], v[78:79], v[12:13] op_sel_hi:[1,0]
v_pk_mul_f32 v[80:81], v[80:81], v[12:13] op_sel_hi:[1,0]
v_pk_mul_f32 v[82:83], v[82:83], v[12:13] op_sel_hi:[1,0]
v_pk_mul_f32 v[84:85], v[84:85], v[12:13] op_sel_hi:[1,0]
v_pk_mul_f32 v[86:87], v[86:87], v[12:13] op_sel_hi:[1,0]
v_pk_mul_f32 v[88:89], v[88:89], v[12:13] op_sel_hi:[1,0]
v_pk_mul_f32 v[90:91], v[90:91], v[12:13] op_sel_hi:[1,0]
v_pk_mul_f32 v[92:93], v[92:93], v[12:13] op_sel_hi:[1,0]
v_pk_mul_f32 v[94:95], v[94:95], v[12:13] op_sel_hi:[1,0]
v_cvt_pk_bf16_f32 v64, v64, v65
v_cvt_pk_bf16_f32 v65, v66, v67
v_cvt_pk_bf16_f32 v66, v68, v69
v_cvt_pk_bf16_f32 v67, v70, v71
s_add_u32 s27, s55, 0
buffer_store_dwordx4 v[64:67], v4, s[36:39], s27 offen offset:0 sc1
v_cvt_pk_bf16_f32 v72, v72, v73
v_cvt_pk_bf16_f32 v73, v74, v75
v_cvt_pk_bf16_f32 v74, v76, v77
v_cvt_pk_bf16_f32 v75, v78, v79
s_add_u32 s27, s55, 0x1000
buffer_store_dwordx4 v[72:75], v4, s[36:39], s27 offen offset:0 sc1
v_cvt_pk_bf16_f32 v80, v80, v81
v_cvt_pk_bf16_f32 v81, v82, v83
v_cvt_pk_bf16_f32 v82, v84, v85
v_cvt_pk_bf16_f32 v83, v86, v87
s_add_u32 s27, s55, 0x2000
buffer_store_dwordx4 v[80:83], v4, s[36:39], s27 offen offset:0 sc1
v_cvt_pk_bf16_f32 v88, v88, v89
v_cvt_pk_bf16_f32 v89, v90, v91
v_cvt_pk_bf16_f32 v90, v92, v93
v_cvt_pk_bf16_f32 v91, v94, v95
s_add_u32 s27, s55, 0x3000
buffer_store_dwordx4 v[88:91], v4, s[36:39], s27 offen offset:0 sc1
v_add_f32 v12, v20, v21
v_mov_b32 v13, v12
s_nop 1
v_permlane16_swap_b32 v12, v13
v_add_f32 v12, v12, v13
v_mov_b32 v13, v12
s_nop 1
v_permlane32_swap_b32 v12, v13
v_add_f32 v12, v12, v13
v_rcp_f32 v12, v12
s_nop 0
v_pk_mul_f32 v[96:97], v[96:97], v[12:13] op_sel_hi:[1,0]
v_pk_mul_f32 v[98:99], v[98:99], v[12:13] op_sel_hi:[1,0]
v_pk_mul_f32 v[100:101], v[100:101], v[12:13] op_sel_hi:[1,0]
v_pk_mul_f32 v[102:103], v[102:103], v[12:13] op_sel_hi:[1,0]
v_pk_mul_f32 v[104:105], v[104:105], v[12:13] op_sel_hi:[1,0]
v_pk_mul_f32 v[106:107], v[106:107], v[12:13] op_sel_hi:[1,0]
v_pk_mul_f32 v[108:109], v[108:109], v[12:13] op_sel_hi:[1,0]
v_pk_mul_f32 v[110:111], v[110:111], v[12:13] op_sel_hi:[1,0]
v_pk_mul_f32 v[112:113], v[112:113], v[12:13] op_sel_hi:[1,0]
v_pk_mul_f32 v[114:115], v[114:115], v[12:13] op_sel_hi:[1,0]
v_pk_mul_f32 v[116:117], v[116:117], v[12:13] op_sel_hi:[1,0]
v_pk_mul_f32 v[118:119], v[118:119], v[12:13] op_sel_hi:[1,0]
v_pk_mul_f32 v[120:121], v[120:121], v[12:13] op_sel_hi:[1,0]
v_pk_mul_f32 v[122:123], v[122:123], v[12:13] op_sel_hi:[1,0]
v_pk_mul_f32 v[124:125], v[124:125], v[12:13] op_sel_hi:[1,0]
v_pk_mul_f32 v[126:127], v[126:127], v[12:13] op_sel_hi:[1,0]
v_cvt_pk_bf16_f32 v96, v96, v97
v_cvt_pk_bf16_f32 v97, v98, v99
v_cvt_pk_bf16_f32 v98, v100, v101
v_cvt_pk_bf16_f32 v99, v102, v103
s_add_u32 s27, s55, 0
s_add_u32 s27, s27, s69
buffer_store_dwordx4 v[96:99], v4, s[36:39], s27 offen offset:0 sc1
v_cvt_pk_bf16_f32 v104, v104, v105
v_cvt_pk_bf16_f32 v105, v106, v107
v_cvt_pk_bf16_f32 v106, v108, v109
v_cvt_pk_bf16_f32 v107, v110, v111
s_add_u32 s27, s55, 0x1000
s_add_u32 s27, s27, s69
buffer_store_dwordx4 v[104:107], v4, s[36:39], s27 offen offset:0 sc1
v_cvt_pk_bf16_f32 v112, v112, v113
v_cvt_pk_bf16_f32 v113, v114, v115
v_cvt_pk_bf16_f32 v114, v116, v117
v_cvt_pk_bf16_f32 v115, v118, v119
s_add_u32 s27, s55, 0x2000
s_add_u32 s27, s27, s69
buffer_store_dwordx4 v[112:115], v4, s[36:39], s27 offen offset:0 sc1
v_cvt_pk_bf16_f32 v120, v120, v121
v_cvt_pk_bf16_f32 v121, v122, v123
v_cvt_pk_bf16_f32 v122, v124, v125
v_cvt_pk_bf16_f32 v123, v126, v127
s_add_u32 s27, s55, 0x3000
s_add_u32 s27, s27, s69
buffer_store_dwordx4 v[120:123], v4, s[36:39], s27 offen offset:0 sc1
s_nop 1
s_branch B_D1end_%=
B_D1w1_%=:
v_mfma_f32_16x16x32_bf16 v[32:35], a[0:3], v[192:195], 0
v_exp_f32 v40, v40
v_mfma_f32_16x16x32_bf16 v[36:39], a[16:19], v[192:195], 0
ds_read_b128 a[80:83], v5 offset:20480
v_add_f32 v22, v22, v40
v_mfma_f32_16x16x32_bf16 v[32:35], a[4:7], v[196:199], v[32:35]
v_exp_f32 v41, v41
v_mfma_f32_16x16x32_bf16 v[36:39], a[20:23], v[196:199], v[36:39]
v_add_f32 v23, v23, v41
v_cvt_pk_bf16_f32 v52, v40, v41
v_mfma_f32_16x16x32_bf16 v[32:35], a[8:11], v[200:203], v[32:35]
s_add_u32 m0, s52, 0x1000
v_exp_f32 v42, v42
v_mfma_f32_16x16x32_bf16 v[36:39], a[24:27], v[200:203], v[36:39]
ds_read_b128 a[84:87], v5 offset:21504
v_add_f32 v22, v22, v42
v_mfma_f32_16x16x32_bf16 v[32:35], a[12:15], v[204:207], v[32:35]
v_exp_f32 v43, v43
v_mfma_f32_16x16x32_bf16 v[36:39], a[28:31], v[204:207], v[36:39]
v_add_f32 v23, v23, v43
v_cvt_pk_bf16_f32 v53, v42, v43
v_mfma_f32_16x16x32_bf16 v[128:131], a[96:99], v[48:51], v[128:131]
v_exp_f32 v44, v44
v_mfma_f32_16x16x32_bf16 v[132:135], a[100:103], v[48:51], v[132:135]
ds_read_b128 a[32:35], v5 offset:8192
v_add_f32 v22, v22, v44
v_mfma_f32_16x16x32_bf16 v[136:139], a[104:107], v[48:51], v[136:139]
v_exp_f32 v45, v45
v_mfma_f32_16x16x32_bf16 v[140:143], a[108:111], v[48:51], v[140:143]
v_add_f32 v23, v23, v45
v_cvt_pk_bf16_f32 v54, v44, v45
v_mfma_f32_16x16x32_bf16 v[144:147], a[112:115], v[48:51], v[144:147]
v_exp_f32 v46, v46
v_mfma_f32_16x16x32_bf16 v[148:151], a[116:119], v[48:51], v[148:151]
ds_read_b128 a[48:51], v5 offset:12288
v_add_f32 v22, v22, v46
v_mfma_f32_16x16x32_bf16 v[152:155], a[120:123], v[48:51], v[152:155]
v_exp_f32 v47, v47
v_mfma_f32_16x16x32_bf16 v[156:159], a[124:127], v[48:51], v[156:159]
v_add_f32 v23, v23, v47
v_cvt_pk_bf16_f32 v55, v46, v47
v_mfma_f32_16x16x32_bf16 v[40:43], a[0:3], v[208:211], 0
ds_read_b128 a[88:91], v5 offset:22528
v_exp_f32 v32, v32
v_mfma_f32_16x16x32_bf16 v[44:47], a[16:19], v[208:211], 0
buffer_load_dwordx4 v4, s[48:51], s54 offen offset:0 sc1 lds
v_add_f32 v16, v16, v32
v_mfma_f32_16x16x32_bf16 v[40:43], a[4:7], v[212:215], v[40:43]
v_exp_f32 v33, v33
v_mfma_f32_16x16x32_bf16 v[44:47], a[20:23], v[212:215], v[44:47]
v_add_f32 v17, v17, v33
v_cvt_pk_bf16_f32 v48, v32, v33
v_mfma_f32_16x16x32_bf16 v[40:43], a[8:11], v[216:219], v[40:43]
v_exp_f32 v34, v34
v_mfma_f32_16x16x32_bf16 v[44:47], a[24:27], v[216:219], v[44:47]
ds_read_b128 a[92:95], v5 offset:23552
v_add_f32 v16, v16, v34
v_mfma_f32_16x16x32_bf16 v[40:43], a[12:15], v[220:223], v[40:43]
v_exp_f32 v35, v35
v_mfma_f32_16x16x32_bf16 v[44:47], a[28:31], v[220:223], v[44:47]
v_add_f32 v17, v17, v35
v_cvt_pk_bf16_f32 v49, v34, v35
v_mfma_f32_16x16x32_bf16 v[160:163], a[96:99], v[52:55], v[160:163]
ds_read_b128 a[36:39], v5 offset:9216
v_exp_f32 v36, v36
v_mfma_f32_16x16x32_bf16 v[164:167], a[100:103], v[52:55], v[164:167]
buffer_load_dwordx4 v4, s[48:51], s54 offen offset:1024 sc1 lds
v_add_f32 v16, v16, v36
v_mfma_f32_16x16x32_bf16 v[168:171], a[104:107], v[52:55], v[168:171]
v_exp_f32 v37, v37
v_mfma_f32_16x16x32_bf16 v[172:175], a[108:111], v[52:55], v[172:175]
v_add_f32 v17, v17, v37
v_cvt_pk_bf16_f32 v50, v36, v37
v_mfma_f32_16x16x32_bf16 v[176:179], a[112:115], v[52:55], v[176:179]
v_exp_f32 v38, v38
v_mfma_f32_16x16x32_bf16 v[180:183], a[116:119], v[52:55], v[180:183]
ds_read_b128 a[52:55], v5 offset:13312
v_add_f32 v16, v16, v38
v_mfma_f32_16x16x32_bf16 v[184:187], a[120:123], v[52:55], v[184:187]
v_exp_f32 v39, v39
v_mfma_f32_16x16x32_bf16 v[188:191], a[124:127], v[52:55], v[188:191]
v_add_f32 v17, v17, v39
v_cvt_pk_bf16_f32 v51, v38, v39
v_mfma_f32_16x16x32_bf16 v[32:35], a[0:3], v[224:227], 0
ds_read_b128 a[40:43], v5 offset:10240
v_exp_f32 v40, v40
v_mfma_f32_16x16x32_bf16 v[36:39], a[16:19], v[224:227], 0
buffer_load_dwordx4 v4, s[48:51], s54 offen offset:2048 sc1 lds
v_add_f32 v20, v20, v40
v_mfma_f32_16x16x32_bf16 v[32:35], a[4:7], v[228:231], v[32:35]
v_exp_f32 v41, v41
v_mfma_f32_16x16x32_bf16 v[36:39], a[20:23], v[228:231], v[36:39]
v_add_f32 v21, v21, v41
v_cvt_pk_bf16_f32 v52, v40, v41
v_mfma_f32_16x16x32_bf16 v[32:35], a[8:11], v[232:235], v[32:35]
v_exp_f32 v42, v42
v_mfma_f32_16x16x32_bf16 v[36:39], a[24:27], v[232:235], v[36:39]
ds_read_b128 a[56:59], v5 offset:14336
v_add_f32 v20, v20, v42
v_mfma_f32_16x16x32_bf16 v[32:35], a[12:15], v[236:239], v[32:35]
v_exp_f32 v43, v43
v_mfma_f32_16x16x32_bf16 v[36:39], a[28:31], v[236:239], v[36:39]
v_add_f32 v21, v21, v43
v_cvt_pk_bf16_f32 v53, v42, v43
v_mfma_f32_16x16x32_bf16 v[64:67], a[64:67], v[48:51], v[64:67]
ds_read_b128 a[96:99], v5 offset:24576
v_exp_f32 v44, v44
v_mfma_f32_16x16x32_bf16 v[68:71], a[68:71], v[48:51], v[68:71]
buffer_load_dwordx4 v4, s[48:51], s54 offen offset:3072 sc1 lds
v_add_f32 v20, v20, v44
v_mfma_f32_16x16x32_bf16 v[72:75], a[72:75], v[48:51], v[72:75]
v_exp_f32 v45, v45
v_mfma_f32_16x16x32_bf16 v[76:79], a[76:79], v[48:51], v[76:79]
v_add_f32 v21, v21, v45
v_cvt_pk_bf16_f32 v54, v44, v45
s_waitcnt lgkmcnt(1)
v_mfma_f32_16x16x32_bf16 v[80:83], a[80:83], v[48:51], v[80:83]
v_exp_f32 v46, v46
v_mfma_f32_16x16x32_bf16 v[84:87], a[84:87], v[48:51], v[84:87]
ds_read_b128 a[100:103], v5 offset:25600
v_add_f32 v20, v20, v46
v_mfma_f32_16x16x32_bf16 v[88:91], a[88:91], v[48:51], v[88:91]
v_exp_f32 v47, v47
v_mfma_f32_16x16x32_bf16 v[92:95], a[92:95], v[48:51], v[92:95]
v_add_f32 v21, v21, v47
v_cvt_pk_bf16_f32 v55, v46, v47
v_mfma_f32_16x16x32_bf16 v[40:43], a[0:3], v[240:243], 0
v_exp_f32 v32, v32
v_mfma_f32_16x16x32_bf16 v[44:47], a[16:19], v[240:243], 0
ds_read_b128 a[44:47], v5 offset:11264
s_add_u32 s53, s53, 0x8000
s_add_u32 s54, s54, 0x8000
v_add_f32 v18, v18, v32
v_mfma_f32_16x16x32_bf16 v[40:43], a[4:7], v[244:247], v[40:43]
v_exp_f32 v33, v33
v_mfma_f32_16x16x32_bf16 v[44:47], a[20:23], v[244:247], v[44:47]
v_add_f32 v19, v19, v33
v_cvt_pk_bf16_f32 v48, v32, v33
v_mfma_f32_16x16x32_bf16 v[40:43], a[8:11], v[248:251], v[40:43]
v_exp_f32 v34, v34
v_mfma_f32_16x16x32_bf16 v[44:47], a[24:27], v[248:251], v[44:47]
ds_read_b128 a[60:63], v5 offset:15360
v_add_f32 v18, v18, v34
v_mfma_f32_16x16x32_bf16 v[40:43], a[12:15], v[252:255], v[40:43]
v_exp_f32 v35, v35
v_mfma_f32_16x16x32_bf16 v[44:47], a[28:31], v[252:255], v[44:47]
v_add_f32 v19, v19, v35
v_cvt_pk_bf16_f32 v49, v34, v35
v_mfma_f32_16x16x32_bf16 v[96:99], a[64:67], v[52:55], v[96:99]
v_exp_f32 v36, v36
v_mfma_f32_16x16x32_bf16 v[100:103], a[68:71], v[52:55], v[100:103]
ds_read_b128 a[104:107], v5 offset:26624
s_add_u32 s52, s52, 0x8000
s_and_b32 s52, s52, 0x1ffff
v_add_f32 v18, v18, v36
v_mfma_f32_16x16x32_bf16 v[104:107], a[72:75], v[52:55], v[104:107]
v_exp_f32 v37, v37
v_mfma_f32_16x16x32_bf16 v[108:111], a[76:79], v[52:55], v[108:111]
v_add_f32 v19, v19, v37
v_cvt_pk_bf16_f32 v50, v36, v37
v_mfma_f32_16x16x32_bf16 v[112:115], a[80:83], v[52:55], v[112:115]
v_exp_f32 v38, v38
v_mfma_f32_16x16x32_bf16 v[116:119], a[84:87], v[52:55], v[116:119]
ds_read_b128 a[108:111], v5 offset:27648
v_add_f32 v18, v18, v38
v_mfma_f32_16x16x32_bf16 v[120:123], a[88:91], v[52:55], v[120:123]
v_exp_f32 v39, v39
v_mfma_f32_16x16x32_bf16 v[124:127], a[92:95], v[52:55], v[124:127]
v_add_f32 v19, v19, v39
v_cvt_pk_bf16_f32 v51, v38, v39
s_waitcnt vmcnt(8)
s_barrier
v_mfma_f32_16x16x32_bf16 v[32:35], a[32:35], v[192:195], v[8:11]
ds_read_b128 a[112:115], v5 offset:28672
v_exp_f32 v40, v40
v_mfma_f32_16x16x32_bf16 v[32:35], a[36:39], v[196:199], v[32:35]
v_add_f32 v22, v22, v40
v_mfma_f32_16x16x32_bf16 v[32:35], a[40:43], v[200:203], v[32:35]
s_add_u32 m0, s52, 0
v_exp_f32 v41, v41
s_waitcnt lgkmcnt(1)
v_mfma_f32_16x16x32_bf16 v[32:35], a[44:47], v[204:207], v[32:35]
ds_read_b128 a[116:119], v5 offset:29696
v_add_f32 v23, v23, v41
v_mfma_f32_16x16x32_bf16 v[128:131], a[64:67], v[48:51], v[128:131]
v_exp_f32 v42, v42
v_mfma_f32_16x16x32_bf16 v[132:135], a[68:71], v[48:51], v[132:135]
v_add_f32 v22, v22, v42
v_mfma_f32_16x16x32_bf16 v[136:139], a[72:75], v[48:51], v[136:139]
ds_read_b128 a[0:3], v6 offset:0
v_exp_f32 v43, v43
v_mfma_f32_16x16x32_bf16 v[140:143], a[76:79], v[48:51], v[140:143]
v_add_f32 v23, v23, v43
v_cvt_pk_bf16_f32 v52, v40, v41
v_mfma_f32_16x16x32_bf16 v[144:147], a[80:83], v[48:51], v[144:147]
v_cvt_pk_bf16_f32 v53, v42, v43
v_mfma_f32_16x16x32_bf16 v[148:151], a[84:87], v[48:51], v[148:151]
ds_read_b128 a[16:19], v6 offset:4096
v_exp_f32 v44, v44
v_mfma_f32_16x16x32_bf16 v[152:155], a[88:91], v[48:51], v[152:155]
v_add_f32 v22, v22, v44
v_mfma_f32_16x16x32_bf16 v[156:159], a[92:95], v[48:51], v[156:159]
v_exp_f32 v45, v45
s_nop 0
v_add_f32 v23, v23, v45
v_exp_f32 v46, v46
s_nop 0
v_add_f32 v22, v22, v46
v_exp_f32 v47, v47
s_nop 0
v_add_f32 v23, v23, v47
v_cvt_pk_bf16_f32 v54, v44, v45
v_cvt_pk_bf16_f32 v55, v46, v47
v_mfma_f32_16x16x32_bf16 v[40:43], a[32:35], v[208:211], 0
ds_read_b128 a[120:123], v5 offset:30720
v_exp_f32 v32, v32
v_mfma_f32_16x16x32_bf16 v[44:47], a[48:51], v[208:211], v[8:11]
v_add_f32 v16, v16, v32
v_mfma_f32_16x16x32_bf16 v[40:43], a[36:39], v[212:215], v[40:43]
v_exp_f32 v33, v33
v_mfma_f32_16x16x32_bf16 v[44:47], a[52:55], v[212:215], v[44:47]
buffer_load_dwordx4 v4, s[48:51], s53 offen offset:0 sc1 lds
v_add_f32 v17, v17, v33
v_mfma_f32_16x16x32_bf16 v[40:43], a[40:43], v[216:219], v[40:43]
v_exp_f32 v34, v34
v_mfma_f32_16x16x32_bf16 v[44:47], a[56:59], v[216:219], v[44:47]
v_add_f32 v16, v16, v34
v_mfma_f32_16x16x32_bf16 v[40:43], a[44:47], v[220:223], v[40:43]
ds_read_b128 a[124:127], v5 offset:31744
v_exp_f32 v35, v35
v_mfma_f32_16x16x32_bf16 v[44:47], a[60:63], v[220:223], v[44:47]
v_add_f32 v17, v17, v35
v_cvt_pk_bf16_f32 v48, v32, v33
v_mfma_f32_16x16x32_bf16 v[160:163], a[64:67], v[52:55], v[160:163]
v_cvt_pk_bf16_f32 v49, v34, v35
v_mov_b32 v50, 0
v_mfma_f32_16x16x32_bf16 v[164:167], a[68:71], v[52:55], v[164:167]
ds_read_b128 a[4:7], v6 offset:1024
v_mov_b32 v51, 0
v_mfma_f32_16x16x32_bf16 v[168:171], a[72:75], v[52:55], v[168:171]
v_mfma_f32_16x16x32_bf16 v[172:175], a[76:79], v[52:55], v[172:175]
v_mfma_f32_16x16x32_bf16 v[176:179], a[80:83], v[52:55], v[176:179]
ds_read_b128 a[20:23], v6 offset:5120
v_mfma_f32_16x16x32_bf16 v[180:183], a[84:87], v[52:55], v[180:183]
v_mfma_f32_16x16x32_bf16 v[184:187], a[88:91], v[52:55], v[184:187]
v_mfma_f32_16x16x32_bf16 v[188:191], a[92:95], v[52:55], v[188:191]
v_mfma_f32_16x16x32_bf16 v[32:35], a[32:35], v[224:227], 0
ds_read_b128 a[8:11], v6 offset:2048
v_exp_f32 v40, v40
v_mfma_f32_16x16x32_bf16 v[36:39], a[48:51], v[224:227], 0
buffer_load_dwordx4 v4, s[48:51], s53 offen offset:1024 sc1 lds
v_add_f32 v20, v20, v40
v_mfma_f32_16x16x32_bf16 v[32:35], a[36:39], v[228:231], v[32:35]
v_exp_f32 v41, v41
v_mfma_f32_16x16x32_bf16 v[36:39], a[52:55], v[228:231], v[36:39]
v_add_f32 v21, v21, v41
v_cvt_pk_bf16_f32 v52, v40, v41
v_mfma_f32_16x16x32_bf16 v[32:35], a[40:43], v[232:235], v[32:35]
v_exp_f32 v42, v42
v_mfma_f32_16x16x32_bf16 v[36:39], a[56:59], v[232:235], v[36:39]
ds_read_b128 a[24:27], v6 offset:6144
v_add_f32 v20, v20, v42
v_mfma_f32_16x16x32_bf16 v[32:35], a[44:47], v[236:239], v[32:35]
v_exp_f32 v43, v43
v_mfma_f32_16x16x32_bf16 v[36:39], a[60:63], v[236:239], v[36:39]
v_add_f32 v21, v21, v43
v_cvt_pk_bf16_f32 v53, v42, v43
v_mfma_f32_16x16x32_bf16 v[64:67], a[96:99], v[48:51], v[64:67]
v_exp_f32 v44, v44
v_mfma_f32_16x16x32_bf16 v[68:71], a[100:103], v[48:51], v[68:71]
ds_read_b128 a[64:67], v6 offset:16384
v_add_f32 v20, v20, v44
v_mfma_f32_16x16x32_bf16 v[72:75], a[104:107], v[48:51], v[72:75]
v_exp_f32 v45, v45
v_mfma_f32_16x16x32_bf16 v[76:79], a[108:111], v[48:51], v[76:79]
v_add_f32 v21, v21, v45
v_cvt_pk_bf16_f32 v54, v44, v45
s_waitcnt lgkmcnt(1)
v_mfma_f32_16x16x32_bf16 v[80:83], a[112:115], v[48:51], v[80:83]
v_exp_f32 v46, v46
v_mfma_f32_16x16x32_bf16 v[84:87], a[116:119], v[48:51], v[84:87]
ds_read_b128 a[68:71], v6 offset:17408
v_add_f32 v20, v20, v46
v_mfma_f32_16x16x32_bf16 v[88:91], a[120:123], v[48:51], v[88:91]
v_exp_f32 v47, v47
v_mfma_f32_16x16x32_bf16 v[92:95], a[124:127], v[48:51], v[92:95]
v_add_f32 v21, v21, v47
v_cvt_pk_bf16_f32 v55, v46, v47
v_mfma_f32_16x16x32_bf16 v[40:43], a[32:35], v[240:243], 0
ds_read_b128 a[12:15], v6 offset:3072
v_exp_f32 v32, v32
v_mfma_f32_16x16x32_bf16 v[44:47], a[48:51], v[240:243], 0
buffer_load_dwordx4 v4, s[48:51], s53 offen offset:2048 sc1 lds
v_add_f32 v18, v18, v32
v_mfma_f32_16x16x32_bf16 v[40:43], a[36:39], v[244:247], v[40:43]
v_exp_f32 v33, v33
v_mfma_f32_16x16x32_bf16 v[44:47], a[52:55], v[244:247], v[44:47]
v_add_f32 v19, v19, v33
v_cvt_pk_bf16_f32 v48, v32, v33
v_mfma_f32_16x16x32_bf16 v[40:43], a[40:43], v[248:251], v[40:43]
v_exp_f32 v34, v34
v_mfma_f32_16x16x32_bf16 v[44:47], a[56:59], v[248:251], v[44:47]
ds_read_b128 a[28:31], v6 offset:7168
v_add_f32 v18, v18, v34
v_mfma_f32_16x16x32_bf16 v[40:43], a[44:47], v[252:255], v[40:43]
v_exp_f32 v35, v35
v_mfma_f32_16x16x32_bf16 v[44:47], a[60:63], v[252:255], v[44:47]
v_add_f32 v19, v19, v35
v_cvt_pk_bf16_f32 v49, v34, v35
v_mfma_f32_16x16x32_bf16 v[96:99], a[96:99], v[52:55], v[96:99]
ds_read_b128 a[72:75], v6 offset:18432
v_exp_f32 v36, v36
v_mfma_f32_16x16x32_bf16 v[100:103], a[100:103], v[52:55], v[100:103]
buffer_load_dwordx4 v4, s[48:51], s53 offen offset:3072 sc1 lds
v_add_f32 v18, v18, v36
v_mfma_f32_16x16x32_bf16 v[104:107], a[104:107], v[52:55], v[104:107]
v_exp_f32 v37, v37
v_mfma_f32_16x16x32_bf16 v[108:111], a[108:111], v[52:55], v[108:111]
v_add_f32 v19, v19, v37
v_cvt_pk_bf16_f32 v50, v36, v37
v_mfma_f32_16x16x32_bf16 v[112:115], a[112:115], v[52:55], v[112:115]
v_exp_f32 v38, v38
v_mfma_f32_16x16x32_bf16 v[116:119], a[116:119], v[52:55], v[116:119]
ds_read_b128 a[76:79], v6 offset:19456
v_add_f32 v18, v18, v38
v_mfma_f32_16x16x32_bf16 v[120:123], a[120:123], v[52:55], v[120:123]
v_exp_f32 v39, v39
v_mfma_f32_16x16x32_bf16 v[124:127], a[124:127], v[52:55], v[124:127]
v_add_f32 v19, v19, v39
v_cvt_pk_bf16_f32 v51, v38, v39
v_mov_b32 v5, v6
v_add_u32 v6, 0x8000, v6
v_and_b32 v6, 0x1ffff, v6
v_mfma_f32_16x16x32_bf16 v[32:35], a[0:3], v[224:227], 0
ds_read_b128 a[80:83], v5 offset:20480
s_add_u32 m0, s52, 0x1000
v_exp_f32 v40, v40
v_mfma_f32_16x16x32_bf16 v[36:39], a[16:19], v[224:227], 0
ds_read_b128 a[84:87], v5 offset:21504
v_add_f32 v22, v22, v40
v_mfma_f32_16x16x32_bf16 v[32:35], a[4:7], v[228:231], v[32:35]
v_exp_f32 v41, v41
v_mfma_f32_16x16x32_bf16 v[36:39], a[20:23], v[228:231], v[36:39]
v_add_f32 v23, v23, v41
v_cvt_pk_bf16_f32 v52, v40, v41
v_mfma_f32_16x16x32_bf16 v[32:35], a[8:11], v[232:235], v[32:35]
ds_read_b128 a[32:35], v5 offset:8192
v_exp_f32 v42, v42
v_mfma_f32_16x16x32_bf16 v[36:39], a[24:27], v[232:235], v[36:39]
buffer_load_dwordx4 v4, s[48:51], s54 offen offset:0 sc1 lds
v_add_f32 v22, v22, v42
s_waitcnt lgkmcnt(2)
v_mfma_f32_16x16x32_bf16 v[32:35], a[12:15], v[236:239], v[32:35]
v_exp_f32 v43, v43
v_mfma_f32_16x16x32_bf16 v[36:39], a[28:31], v[236:239], v[36:39]
v_add_f32 v23, v23, v43
v_cvt_pk_bf16_f32 v53, v42, v43
v_mfma_f32_16x16x32_bf16 v[128:131], a[96:99], v[48:51], v[128:131]
ds_read_b128 a[48:51], v5 offset:12288
v_exp_f32 v44, v44
v_mfma_f32_16x16x32_bf16 v[132:135], a[100:103], v[48:51], v[132:135]
ds_read_b128 a[36:39], v5 offset:9216
v_add_f32 v22, v22, v44
v_mfma_f32_16x16x32_bf16 v[136:139], a[104:107], v[48:51], v[136:139]
v_exp_f32 v45, v45
v_mfma_f32_16x16x32_bf16 v[140:143], a[108:111], v[48:51], v[140:143]
v_add_f32 v23, v23, v45
v_cvt_pk_bf16_f32 v54, v44, v45
v_mfma_f32_16x16x32_bf16 v[144:147], a[112:115], v[48:51], v[144:147]
buffer_load_dwordx4 v4, s[48:51], s54 offen offset:1024 sc1 lds
v_exp_f32 v46, v46
v_mfma_f32_16x16x32_bf16 v[148:151], a[116:119], v[48:51], v[148:151]
ds_read_b128 a[52:55], v5 offset:13312
v_add_f32 v22, v22, v46
v_mfma_f32_16x16x32_bf16 v[152:155], a[120:123], v[48:51], v[152:155]
v_exp_f32 v47, v47
v_mfma_f32_16x16x32_bf16 v[156:159], a[124:127], v[48:51], v[156:159]
v_add_f32 v23, v23, v47
v_cvt_pk_bf16_f32 v55, v46, v47
v_mfma_f32_16x16x32_bf16 v[40:43], a[0:3], v[240:243], 0
ds_read_b128 a[88:91], v5 offset:22528
v_exp_f32 v32, v32
v_mfma_f32_16x16x32_bf16 v[44:47], a[16:19], v[240:243], 0
buffer_load_dwordx4 v4, s[48:51], s54 offen offset:2048 sc1 lds
v_add_f32 v18, v18, v32
v_mfma_f32_16x16x32_bf16 v[40:43], a[4:7], v[244:247], v[40:43]
v_exp_f32 v33, v33
v_mfma_f32_16x16x32_bf16 v[44:47], a[20:23], v[244:247], v[44:47]
v_add_f32 v19, v19, v33
v_cvt_pk_bf16_f32 v48, v32, v33
v_mfma_f32_16x16x32_bf16 v[40:43], a[8:11], v[248:251], v[40:43]
ds_read_b128 a[92:95], v5 offset:23552
v_exp_f32 v34, v34
v_mfma_f32_16x16x32_bf16 v[44:47], a[24:27], v[248:251], v[44:47]
buffer_load_dwordx4 v4, s[48:51], s54 offen offset:3072 sc1 lds
v_add_f32 v18, v18, v34
v_mfma_f32_16x16x32_bf16 v[40:43], a[12:15], v[252:255], v[40:43]
v_exp_f32 v35, v35
v_mfma_f32_16x16x32_bf16 v[44:47], a[28:31], v[252:255], v[44:47]
v_add_f32 v19, v19, v35
v_cvt_pk_bf16_f32 v49, v34, v35
v_mfma_f32_16x16x32_bf16 v[160:163], a[96:99], v[52:55], v[160:163]
ds_read_b128 a[40:43], v5 offset:10240
v_exp_f32 v36, v36
v_mfma_f32_16x16x32_bf16 v[164:167], a[100:103], v[52:55], v[164:167]
ds_read_b128 a[56:59], v5 offset:14336
s_add_u32 s53, s53, 0x8000
s_add_u32 s54, s54, 0x8000
v_add_f32 v18, v18, v36
v_mfma_f32_16x16x32_bf16 v[168:171], a[104:107], v[52:55], v[168:171]
v_exp_f32 v37, v37
v_mfma_f32_16x16x32_bf16 v[172:175], a[108:111], v[52:55], v[172:175]
v_add_f32 v19, v19, v37
v_cvt_pk_bf16_f32 v50, v36, v37
v_mfma_f32_16x16x32_bf16 v[176:179], a[112:115], v[52:55], v[176:179]
ds_read_b128 a[44:47], v5 offset:11264
s_add_u32 s52, s52, 0x8000
s_and_b32 s52, s52, 0x1ffff
v_exp_f32 v38, v38
v_mfma_f32_16x16x32_bf16 v[180:183], a[116:119], v[52:55], v[180:183]
ds_read_b128 a[60:63], v5 offset:15360
v_add_f32 v18, v18, v38
v_mfma_f32_16x16x32_bf16 v[184:187], a[120:123], v[52:55], v[184:187]
v_exp_f32 v39, v39
v_mfma_f32_16x16x32_bf16 v[188:191], a[124:127], v[52:55], v[188:191]
v_add_f32 v19, v19, v39
v_cvt_pk_bf16_f32 v51, v38, v39
s_waitcnt vmcnt(8)
s_barrier
s_waitcnt lgkmcnt(2)
v_mfma_f32_16x16x32_bf16 v[32:35], a[32:35], v[224:227], 0
ds_read_b128 a[0:3], v6 offset:0
s_add_u32 m0, s52, 0
v_exp_f32 v40, v40
v_mfma_f32_16x16x32_bf16 v[36:39], a[48:51], v[224:227], 0
ds_read_b128 a[16:19], v6 offset:4096
v_add_f32 v22, v22, v40
v_mfma_f32_16x16x32_bf16 v[32:35], a[36:39], v[228:231], v[32:35]
ds_read_b128 a[4:7], v6 offset:1024
v_exp_f32 v41, v41
v_mfma_f32_16x16x32_bf16 v[36:39], a[52:55], v[228:231], v[36:39]
v_add_f32 v23, v23, v41
v_cvt_pk_bf16_f32 v52, v40, v41
v_mfma_f32_16x16x32_bf16 v[32:35], a[40:43], v[232:235], v[32:35]
ds_read_b128 a[20:23], v6 offset:5120
v_exp_f32 v42, v42
v_mfma_f32_16x16x32_bf16 v[36:39], a[56:59], v[232:235], v[36:39]
ds_read_b128 a[8:11], v6 offset:2048
v_add_f32 v22, v22, v42
s_waitcnt lgkmcnt(4)
v_mfma_f32_16x16x32_bf16 v[32:35], a[44:47], v[236:239], v[32:35]
v_exp_f32 v43, v43
v_mfma_f32_16x16x32_bf16 v[36:39], a[60:63], v[236:239], v[36:39]
v_add_f32 v23, v23, v43
v_cvt_pk_bf16_f32 v53, v42, v43
v_mfma_f32_16x16x32_bf16 v[128:131], a[64:67], v[48:51], v[128:131]
buffer_load_dwordx4 v4, s[48:51], s53 offen offset:0 sc1 lds
v_exp_f32 v44, v44
v_mfma_f32_16x16x32_bf16 v[132:135], a[68:71], v[48:51], v[132:135]
ds_read_b128 a[24:27], v6 offset:6144
v_add_f32 v22, v22, v44
v_mfma_f32_16x16x32_bf16 v[136:139], a[72:75], v[48:51], v[136:139]
v_exp_f32 v45, v45
v_mfma_f32_16x16x32_bf16 v[140:143], a[76:79], v[48:51], v[140:143]
v_add_f32 v23, v23, v45
v_cvt_pk_bf16_f32 v54, v44, v45
v_mfma_f32_16x16x32_bf16 v[144:147], a[80:83], v[48:51], v[144:147]
ds_read_b128 a[12:15], v6 offset:3072
v_exp_f32 v46, v46
v_mfma_f32_16x16x32_bf16 v[148:151], a[84:87], v[48:51], v[148:151]
ds_read_b128 a[28:31], v6 offset:7168
v_add_f32 v22, v22, v46
v_mfma_f32_16x16x32_bf16 v[152:155], a[88:91], v[48:51], v[152:155]
v_exp_f32 v47, v47
v_mfma_f32_16x16x32_bf16 v[156:159], a[92:95], v[48:51], v[156:159]
v_add_f32 v23, v23, v47
v_cvt_pk_bf16_f32 v55, v46, v47
v_mfma_f32_16x16x32_bf16 v[40:43], a[32:35], v[240:243], 0
ds_read_b128 a[96:99], v5 offset:24576
v_exp_f32 v32, v32
v_mfma_f32_16x16x32_bf16 v[44:47], a[48:51], v[240:243], 0
buffer_load_dwordx4 v4, s[48:51], s53 offen offset:1024 sc1 lds
v_exp_f32 v33, v33
v_mfma_f32_16x16x32_bf16 v[40:43], a[36:39], v[244:247], v[40:43]
ds_read_b128 a[100:103], v5 offset:25600
v_exp_f32 v34, v34
v_mfma_f32_16x16x32_bf16 v[44:47], a[52:55], v[244:247], v[44:47]
v_exp_f32 v35, v35
v_mfma_f32_16x16x32_bf16 v[40:43], a[40:43], v[248:251], v[40:43]
ds_read_b128 a[104:107], v5 offset:26624
v_cvt_pk_bf16_f32 v48, v32, v33
v_cvt_pk_bf16_f32 v49, v34, v35
v_mfma_f32_16x16x32_bf16 v[44:47], a[56:59], v[248:251], v[44:47]
buffer_load_dwordx4 v4, s[48:51], s53 offen offset:2048 sc1 lds
v_exp_f32 v36, v36
v_mfma_f32_16x16x32_bf16 v[40:43], a[44:47], v[252:255], v[40:43]
v_exp_f32 v37, v37
v_mfma_f32_16x16x32_bf16 v[44:47], a[60:63], v[252:255], v[44:47]
ds_read_b128 a[108:111], v5 offset:27648
v_exp_f32 v38, v38
v_mfma_f32_16x16x32_bf16 v[160:163], a[64:67], v[52:55], v[160:163]
ds_read_b128 a[112:115], v5 offset:28672
v_exp_f32 v39, v39
v_mfma_f32_16x16x32_bf16 v[164:167], a[68:71], v[52:55], v[164:167]
v_cvt_pk_bf16_f32 v50, v36, v37
v_cvt_pk_bf16_f32 v51, v38, v39
v_mfma_f32_16x16x32_bf16 v[168:171], a[72:75], v[52:55], v[168:171]
ds_read_b128 a[116:119], v5 offset:29696
v_mfma_f32_16x16x32_bf16 v[172:175], a[76:79], v[52:55], v[172:175]
buffer_load_dwordx4 v4, s[48:51], s53 offen offset:3072 sc1 lds
v_mfma_f32_16x16x32_bf16 v[176:179], a[80:83], v[52:55], v[176:179]
v_mfma_f32_16x16x32_bf16 v[180:183], a[84:87], v[52:55], v[180:183]
ds_read_b128 a[120:123], v5 offset:30720
v_mfma_f32_16x16x32_bf16 v[184:187], a[88:91], v[52:55], v[184:187]
ds_read_b128 a[124:127], v5 offset:31744
v_mfma_f32_16x16x32_bf16 v[188:191], a[92:95], v[52:55], v[188:191]
v_mov_b32 v5, v6
v_add_u32 v6, 0x8000, v6
v_and_b32 v6, 0x1ffff, v6
s_waitcnt lgkmcnt(0)
v_mbcnt_lo_u32_b32 v12, -1, 0
v_mbcnt_hi_u32_b32 v12, -1, v12
v_lshlrev_b32 v12, 4, v12
s_mov_b32 s27, 0x4000
buffer_load_dwordx4 v[0:3], v12, s[40:43], s27 offen offset:0 sc1
s_nop 7
v_add_f32 v12, v16, v17
v_mov_b32 v13, v12
s_nop 1
v_permlane16_swap_b32 v12, v13
v_add_f32 v12, v12, v13
v_mov_b32 v13, v12
s_nop 1
v_permlane32_swap_b32 v12, v13
v_add_f32 v12, v12, v13
v_rcp_f32 v12, v12
s_nop 0
v_pk_mul_f32 v[64:65], v[64:65], v[12:13] op_sel_hi:[1,0]
v_pk_mul_f32 v[66:67], v[66:67], v[12:13] op_sel_hi:[1,0]
v_pk_mul_f32 v[68:69], v[68:69], v[12:13] op_sel_hi:[1,0]
v_pk_mul_f32 v[70:71], v[70:71], v[12:13] op_sel_hi:[1,0]
v_pk_mul_f32 v[72:73], v[72:73], v[12:13] op_sel_hi:[1,0]
v_pk_mul_f32 v[74:75], v[74:75], v[12:13] op_sel_hi:[1,0]
v_pk_mul_f32 v[76:77], v[76:77], v[12:13] op_sel_hi:[1,0]
v_pk_mul_f32 v[78:79], v[78:79], v[12:13] op_sel_hi:[1,0]
v_pk_mul_f32 v[80:81], v[80:81], v[12:13] op_sel_hi:[1,0]
v_pk_mul_f32 v[82:83], v[82:83], v[12:13] op_sel_hi:[1,0]
v_pk_mul_f32 v[84:85], v[84:85], v[12:13] op_sel_hi:[1,0]
v_pk_mul_f32 v[86:87], v[86:87], v[12:13] op_sel_hi:[1,0]
v_pk_mul_f32 v[88:89], v[88:89], v[12:13] op_sel_hi:[1,0]
v_pk_mul_f32 v[90:91], v[90:91], v[12:13] op_sel_hi:[1,0]
v_pk_mul_f32 v[92:93], v[92:93], v[12:13] op_sel_hi:[1,0]
v_pk_mul_f32 v[94:95], v[94:95], v[12:13] op_sel_hi:[1,0]
v_cvt_pk_bf16_f32 v64, v64, v65
v_cvt_pk_bf16_f32 v65, v66, v67
v_cvt_pk_bf16_f32 v66, v68, v69
v_cvt_pk_bf16_f32 v67, v70, v71
s_add_u32 s27, s55, 0
buffer_store_dwordx4 v[64:67], v4, s[36:39], s27 offen offset:0 sc1
v_cvt_pk_bf16_f32 v72, v72, v73
v_cvt_pk_bf16_f32 v73, v74, v75
v_cvt_pk_bf16_f32 v74, v76, v77
v_cvt_pk_bf16_f32 v75, v78, v79
s_add_u32 s27, s55, 0x1000
buffer_store_dwordx4 v[72:75], v4, s[36:39], s27 offen offset:0 sc1
v_cvt_pk_bf16_f32 v80, v80, v81
v_cvt_pk_bf16_f32 v81, v82, v83
v_cvt_pk_bf16_f32 v82, v84, v85
v_cvt_pk_bf16_f32 v83, v86, v87
s_add_u32 s27, s55, 0x2000
buffer_store_dwordx4 v[80:83], v4, s[36:39], s27 offen offset:0 sc1
v_cvt_pk_bf16_f32 v88, v88, v89
v_cvt_pk_bf16_f32 v89, v90, v91
v_cvt_pk_bf16_f32 v90, v92, v93
v_cvt_pk_bf16_f32 v91, v94, v95
s_add_u32 s27, s55, 0x3000
buffer_store_dwordx4 v[88:91], v4, s[36:39], s27 offen offset:0 sc1
v_add_f32 v12, v20, v21
v_mov_b32 v13, v12
s_nop 1
v_permlane16_swap_b32 v12, v13
v_add_f32 v12, v12, v13
v_mov_b32 v13, v12
s_nop 1
v_permlane32_swap_b32 v12, v13
v_add_f32 v12, v12, v13
v_rcp_f32 v12, v12
s_nop 0
v_pk_mul_f32 v[96:97], v[96:97], v[12:13] op_sel_hi:[1,0]
v_pk_mul_f32 v[98:99], v[98:99], v[12:13] op_sel_hi:[1,0]
v_pk_mul_f32 v[100:101], v[100:101], v[12:13] op_sel_hi:[1,0]
v_pk_mul_f32 v[102:103], v[102:103], v[12:13] op_sel_hi:[1,0]
v_pk_mul_f32 v[104:105], v[104:105], v[12:13] op_sel_hi:[1,0]
v_pk_mul_f32 v[106:107], v[106:107], v[12:13] op_sel_hi:[1,0]
v_pk_mul_f32 v[108:109], v[108:109], v[12:13] op_sel_hi:[1,0]
v_pk_mul_f32 v[110:111], v[110:111], v[12:13] op_sel_hi:[1,0]
v_pk_mul_f32 v[112:113], v[112:113], v[12:13] op_sel_hi:[1,0]
v_pk_mul_f32 v[114:115], v[114:115], v[12:13] op_sel_hi:[1,0]
v_pk_mul_f32 v[116:117], v[116:117], v[12:13] op_sel_hi:[1,0]
v_pk_mul_f32 v[118:119], v[118:119], v[12:13] op_sel_hi:[1,0]
v_pk_mul_f32 v[120:121], v[120:121], v[12:13] op_sel_hi:[1,0]
v_pk_mul_f32 v[122:123], v[122:123], v[12:13] op_sel_hi:[1,0]
v_pk_mul_f32 v[124:125], v[124:125], v[12:13] op_sel_hi:[1,0]
v_pk_mul_f32 v[126:127], v[126:127], v[12:13] op_sel_hi:[1,0]
v_cvt_pk_bf16_f32 v96, v96, v97
v_cvt_pk_bf16_f32 v97, v98, v99
v_cvt_pk_bf16_f32 v98, v100, v101
v_cvt_pk_bf16_f32 v99, v102, v103
s_add_u32 s27, s55, 0
s_add_u32 s27, s27, s69
buffer_store_dwordx4 v[96:99], v4, s[36:39], s27 offen offset:0 sc1
v_cvt_pk_bf16_f32 v104, v104, v105
v_cvt_pk_bf16_f32 v105, v106, v107
v_cvt_pk_bf16_f32 v106, v108, v109
v_cvt_pk_bf16_f32 v107, v110, v111
s_add_u32 s27, s55, 0x1000
s_add_u32 s27, s27, s69
buffer_store_dwordx4 v[104:107], v4, s[36:39], s27 offen offset:0 sc1
v_cvt_pk_bf16_f32 v112, v112, v113
v_cvt_pk_bf16_f32 v113, v114, v115
v_cvt_pk_bf16_f32 v114, v116, v117
v_cvt_pk_bf16_f32 v115, v118, v119
s_add_u32 s27, s55, 0x2000
s_add_u32 s27, s27, s69
buffer_store_dwordx4 v[112:115], v4, s[36:39], s27 offen offset:0 sc1
v_cvt_pk_bf16_f32 v120, v120, v121
v_cvt_pk_bf16_f32 v121, v122, v123
v_cvt_pk_bf16_f32 v122, v124, v125
v_cvt_pk_bf16_f32 v123, v126, v127
s_add_u32 s27, s55, 0x3000
s_add_u32 s27, s27, s69
buffer_store_dwordx4 v[120:123], v4, s[36:39], s27 offen offset:0 sc1
s_nop 1
s_branch B_D1end_%=
B_D1w2_%=:
v_mfma_f32_16x16x32_bf16 v[32:35], a[0:3], v[192:195], 0
v_exp_f32 v40, v40
v_mfma_f32_16x16x32_bf16 v[36:39], a[16:19], v[192:195], 0
ds_read_b128 a[80:83], v5 offset:20480
v_add_f32 v22, v22, v40
v_mfma_f32_16x16x32_bf16 v[32:35], a[4:7], v[196:199], v[32:35]
v_exp_f32 v41, v41
v_mfma_f32_16x16x32_bf16 v[36:39], a[20:23], v[196:199], v[36:39]
v_add_f32 v23, v23, v41
v_cvt_pk_bf16_f32 v52, v40, v41
v_mfma_f32_16x16x32_bf16 v[32:35], a[8:11], v[200:203], v[32:35]
s_add_u32 m0, s52, 0x1000
v_exp_f32 v42, v42
v_mfma_f32_16x16x32_bf16 v[36:39], a[24:27], v[200:203], v[36:39]
ds_read_b128 a[84:87], v5 offset:21504
v_add_f32 v22, v22, v42
v_mfma_f32_16x16x32_bf16 v[32:35], a[12:15], v[204:207], v[32:35]
v_exp_f32 v43, v43
v_mfma_f32_16x16x32_bf16 v[36:39], a[28:31], v[204:207], v[36:39]
v_add_f32 v23, v23, v43
v_cvt_pk_bf16_f32 v53, v42, v43
v_mfma_f32_16x16x32_bf16 v[128:131], a[96:99], v[48:51], v[128:131]
v_exp_f32 v44, v44
v_mfma_f32_16x16x32_bf16 v[132:135], a[100:103], v[48:51], v[132:135]
ds_read_b128 a[32:35], v5 offset:8192
v_add_f32 v22, v22, v44
v_mfma_f32_16x16x32_bf16 v[136:139], a[104:107], v[48:51], v[136:139]
v_exp_f32 v45, v45
v_mfma_f32_16x16x32_bf16 v[140:143], a[108:111], v[48:51], v[140:143]
v_add_f32 v23, v23, v45
v_cvt_pk_bf16_f32 v54, v44, v45
v_mfma_f32_16x16x32_bf16 v[144:147], a[112:115], v[48:51], v[144:147]
v_exp_f32 v46, v46
v_mfma_f32_16x16x32_bf16 v[148:151], a[116:119], v[48:51], v[148:151]
ds_read_b128 a[48:51], v5 offset:12288
v_add_f32 v22, v22, v46
v_mfma_f32_16x16x32_bf16 v[152:155], a[120:123], v[48:51], v[152:155]
v_exp_f32 v47, v47
v_mfma_f32_16x16x32_bf16 v[156:159], a[124:127], v[48:51], v[156:159]
v_add_f32 v23, v23, v47
v_cvt_pk_bf16_f32 v55, v46, v47
v_mfma_f32_16x16x32_bf16 v[40:43], a[0:3], v[208:211], 0
ds_read_b128 a[88:91], v5 offset:22528
v_exp_f32 v32, v32
v_mfma_f32_16x16x32_bf16 v[44:47], a[16:19], v[208:211], 0
buffer_load_dwordx4 v4, s[48:51], s54 offen offset:0 sc1 lds
v_add_f32 v16, v16, v32
v_mfma_f32_16x16x32_bf16 v[40:43], a[4:7], v[212:215], v[40:43]
v_exp_f32 v33, v33
v_mfma_f32_16x16x32_bf16 v[44:47], a[20:23], v[212:215], v[44:47]
v_add_f32 v17, v17, v33
v_cvt_pk_bf16_f32 v48, v32, v33
v_mfma_f32_16x16x32_bf16 v[40:43], a[8:11], v[216:219], v[40:43]
v_exp_f32 v34, v34
v_mfma_f32_16x16x32_bf16 v[44:47], a[24:27], v[216:219], v[44:47]
ds_read_b128 a[92:95], v5 offset:23552
v_add_f32 v16, v16, v34
v_mfma_f32_16x16x32_bf16 v[40:43], a[12:15], v[220:223], v[40:43]
v_exp_f32 v35, v35
v_mfma_f32_16x16x32_bf16 v[44:47], a[28:31], v[220:223], v[44:47]
v_add_f32 v17, v17, v35
v_cvt_pk_bf16_f32 v49, v34, v35
v_mfma_f32_16x16x32_bf16 v[160:163], a[96:99], v[52:55], v[160:163]
ds_read_b128 a[36:39], v5 offset:9216
v_exp_f32 v36, v36
v_mfma_f32_16x16x32_bf16 v[164:167], a[100:103], v[52:55], v[164:167]
buffer_load_dwordx4 v4, s[48:51], s54 offen offset:1024 sc1 lds
v_add_f32 v16, v16, v36
v_mfma_f32_16x16x32_bf16 v[168:171], a[104:107], v[52:55], v[168:171]
v_exp_f32 v37, v37
v_mfma_f32_16x16x32_bf16 v[172:175], a[108:111], v[52:55], v[172:175]
v_add_f32 v17, v17, v37
v_cvt_pk_bf16_f32 v50, v36, v37
v_mfma_f32_16x16x32_bf16 v[176:179], a[112:115], v[52:55], v[176:179]
v_exp_f32 v38, v38
v_mfma_f32_16x16x32_bf16 v[180:183], a[116:119], v[52:55], v[180:183]
ds_read_b128 a[52:55], v5 offset:13312
v_add_f32 v16, v16, v38
v_mfma_f32_16x16x32_bf16 v[184:187], a[120:123], v[52:55], v[184:187]
v_exp_f32 v39, v39
v_mfma_f32_16x16x32_bf16 v[188:191], a[124:127], v[52:55], v[188:191]
v_add_f32 v17, v17, v39
v_cvt_pk_bf16_f32 v51, v38, v39
v_mfma_f32_16x16x32_bf16 v[32:35], a[0:3], v[224:227], 0
ds_read_b128 a[40:43], v5 offset:10240
v_exp_f32 v40, v40
v_mfma_f32_16x16x32_bf16 v[36:39], a[16:19], v[224:227], 0
buffer_load_dwordx4 v4, s[48:51], s54 offen offset:2048 sc1 lds
v_add_f32 v20, v20, v40
v_mfma_f32_16x16x32_bf16 v[32:35], a[4:7], v[228:231], v[32:35]
v_exp_f32 v41, v41
v_mfma_f32_16x16x32_bf16 v[36:39], a[20:23], v[228:231], v[36:39]
v_add_f32 v21, v21, v41
v_cvt_pk_bf16_f32 v52, v40, v41
v_mfma_f32_16x16x32_bf16 v[32:35], a[8:11], v[232:235], v[32:35]
v_exp_f32 v42, v42
v_mfma_f32_16x16x32_bf16 v[36:39], a[24:27], v[232:235], v[36:39]
ds_read_b128 a[56:59], v5 offset:14336
v_add_f32 v20, v20, v42
v_mfma_f32_16x16x32_bf16 v[32:35], a[12:15], v[236:239], v[32:35]
v_exp_f32 v43, v43
v_mfma_f32_16x16x32_bf16 v[36:39], a[28:31], v[236:239], v[36:39]
v_add_f32 v21, v21, v43
v_cvt_pk_bf16_f32 v53, v42, v43
v_mfma_f32_16x16x32_bf16 v[64:67], a[64:67], v[48:51], v[64:67]
ds_read_b128 a[96:99], v5 offset:24576
v_exp_f32 v44, v44
v_mfma_f32_16x16x32_bf16 v[68:71], a[68:71], v[48:51], v[68:71]
buffer_load_dwordx4 v4, s[48:51], s54 offen offset:3072 sc1 lds
v_add_f32 v20, v20, v44
v_mfma_f32_16x16x32_bf16 v[72:75], a[72:75], v[48:51], v[72:75]
v_exp_f32 v45, v45
v_mfma_f32_16x16x32_bf16 v[76:79], a[76:79], v[48:51], v[76:79]
v_add_f32 v21, v21, v45
v_cvt_pk_bf16_f32 v54, v44, v45
s_waitcnt lgkmcnt(1)
v_mfma_f32_16x16x32_bf16 v[80:83], a[80:83], v[48:51], v[80:83]
v_exp_f32 v46, v46
v_mfma_f32_16x16x32_bf16 v[84:87], a[84:87], v[48:51], v[84:87]
ds_read_b128 a[100:103], v5 offset:25600
v_add_f32 v20, v20, v46
v_mfma_f32_16x16x32_bf16 v[88:91], a[88:91], v[48:51], v[88:91]
v_exp_f32 v47, v47
v_mfma_f32_16x16x32_bf16 v[92:95], a[92:95], v[48:51], v[92:95]
v_add_f32 v21, v21, v47
v_cvt_pk_bf16_f32 v55, v46, v47
v_mfma_f32_16x16x32_bf16 v[40:43], a[0:3], v[240:243], 0
v_exp_f32 v32, v32
v_mfma_f32_16x16x32_bf16 v[44:47], a[16:19], v[240:243], 0
ds_read_b128 a[44:47], v5 offset:11264
s_add_u32 s53, s53, 0x8000
s_add_u32 s54, s54, 0x8000
v_add_f32 v18, v18, v32
v_mfma_f32_16x16x32_bf16 v[40:43], a[4:7], v[244:247], v[40:43]
v_exp_f32 v33, v33
v_mfma_f32_16x16x32_bf16 v[44:47], a[20:23], v[244:247], v[44:47]
v_add_f32 v19, v19, v33
v_cvt_pk_bf16_f32 v48, v32, v33
v_mfma_f32_16x16x32_bf16 v[40:43], a[8:11], v[248:251], v[40:43]
v_exp_f32 v34, v34
v_mfma_f32_16x16x32_bf16 v[44:47], a[24:27], v[248:251], v[44:47]
ds_read_b128 a[60:63], v5 offset:15360
v_add_f32 v18, v18, v34
v_mfma_f32_16x16x32_bf16 v[40:43], a[12:15], v[252:255], v[40:43]
v_exp_f32 v35, v35
v_mfma_f32_16x16x32_bf16 v[44:47], a[28:31], v[252:255], v[44:47]
v_add_f32 v19, v19, v35
v_cvt_pk_bf16_f32 v49, v34, v35
v_mfma_f32_16x16x32_bf16 v[96:99], a[64:67], v[52:55], v[96:99]
v_exp_f32 v36, v36
v_mfma_f32_16x16x32_bf16 v[100:103], a[68:71], v[52:55], v[100:103]
ds_read_b128 a[104:107], v5 offset:26624
s_add_u32 s52, s52, 0x8000
s_and_b32 s52, s52, 0x1ffff
v_add_f32 v18, v18, v36
v_mfma_f32_16x16x32_bf16 v[104:107], a[72:75], v[52:55], v[104:107]
v_exp_f32 v37, v37
v_mfma_f32_16x16x32_bf16 v[108:111], a[76:79], v[52:55], v[108:111]
v_add_f32 v19, v19, v37
v_cvt_pk_bf16_f32 v50, v36, v37
v_mfma_f32_16x16x32_bf16 v[112:115], a[80:83], v[52:55], v[112:115]
v_exp_f32 v38, v38
v_mfma_f32_16x16x32_bf16 v[116:119], a[84:87], v[52:55], v[116:119]
ds_read_b128 a[108:111], v5 offset:27648
v_add_f32 v18, v18, v38
v_mfma_f32_16x16x32_bf16 v[120:123], a[88:91], v[52:55], v[120:123]
v_exp_f32 v39, v39
v_mfma_f32_16x16x32_bf16 v[124:127], a[92:95], v[52:55], v[124:127]
v_add_f32 v19, v19, v39
v_cvt_pk_bf16_f32 v51, v38, v39
s_waitcnt vmcnt(8)
s_barrier
v_mfma_f32_16x16x32_bf16 v[32:35], a[32:35], v[192:195], 0
v_exp_f32 v40, v40
v_mfma_f32_16x16x32_bf16 v[36:39], a[48:51], v[192:195], 0
ds_read_b128 a[112:115], v5 offset:28672
v_add_f32 v22, v22, v40
v_mfma_f32_16x16x32_bf16 v[32:35], a[36:39], v[196:199], v[32:35]
v_exp_f32 v41, v41
v_mfma_f32_16x16x32_bf16 v[36:39], a[52:55], v[196:199], v[36:39]
v_add_f32 v23, v23, v41
v_cvt_pk_bf16_f32 v52, v40, v41
v_mfma_f32_16x16x32_bf16 v[32:35], a[40:43], v[200:203], v[32:35]
s_add_u32 m0, s52, 0
v_exp_f32 v42, v42
v_mfma_f32_16x16x32_bf16 v[36:39], a[56:59], v[200:203], v[36:39]
ds_read_b128 a[116:119], v5 offset:29696
v_add_f32 v22, v22, v42
s_waitcnt lgkmcnt(2)
v_mfma_f32_16x16x32_bf16 v[32:35], a[44:47], v[204:207], v[32:35]
v_exp_f32 v43, v43
v_mfma_f32_16x16x32_bf16 v[36:39], a[60:63], v[204:207], v[36:39]
v_add_f32 v23, v23, v43
v_cvt_pk_bf16_f32 v53, v42, v43
v_mfma_f32_16x16x32_bf16 v[128:131], a[64:67], v[48:51], v[128:131]
v_exp_f32 v44, v44
v_mfma_f32_16x16x32_bf16 v[132:135], a[68:71], v[48:51], v[132:135]
ds_read_b128 a[0:3], v6 offset:0
v_add_f32 v22, v22, v44
v_mfma_f32_16x16x32_bf16 v[136:139], a[72:75], v[48:51], v[136:139]
v_exp_f32 v45, v45
v_mfma_f32_16x16x32_bf16 v[140:143], a[76:79], v[48:51], v[140:143]
v_add_f32 v23, v23, v45
v_cvt_pk_bf16_f32 v54, v44, v45
v_mfma_f32_16x16x32_bf16 v[144:147], a[80:83], v[48:51], v[144:147]
v_exp_f32 v46, v46
v_mfma_f32_16x16x32_bf16 v[148:151], a[84:87], v[48:51], v[148:151]
ds_read_b128 a[16:19], v6 offset:4096
v_add_f32 v22, v22, v46
v_mfma_f32_16x16x32_bf16 v[152:155], a[88:91], v[48:51], v[152:155]
v_exp_f32 v47, v47
v_mfma_f32_16x16x32_bf16 v[156:159], a[92:95], v[48:51], v[156:159]
v_add_f32 v23, v23, v47
v_cvt_pk_bf16_f32 v55, v46, v47
v_mfma_f32_16x16x32_bf16 v[40:43], a[32:35], v[208:211], 0
ds_read_b128 a[120:123], v5 offset:30720
v_exp_f32 v32, v32
v_mfma_f32_16x16x32_bf16 v[44:47], a[48:51], v[208:211], 0
buffer_load_dwordx4 v4, s[48:51], s53 offen offset:0 sc1 lds
v_add_f32 v16, v16, v32
v_mfma_f32_16x16x32_bf16 v[40:43], a[36:39], v[212:215], v[40:43]
v_exp_f32 v33, v33
v_mfma_f32_16x16x32_bf16 v[44:47], a[52:55], v[212:215], v[44:47]
v_add_f32 v17, v17, v33
v_cvt_pk_bf16_f32 v48, v32, v33
v_mfma_f32_16x16x32_bf16 v[40:43], a[40:43], v[216:219], v[40:43]
v_exp_f32 v34, v34
v_mfma_f32_16x16x32_bf16 v[44:47], a[56:59], v[216:219], v[44:47]
ds_read_b128 a[124:127], v5 offset:31744
v_add_f32 v16, v16, v34
v_mfma_f32_16x16x32_bf16 v[40:43], a[44:47], v[220:223], v[40:43]
v_exp_f32 v35, v35
v_mfma_f32_16x16x32_bf16 v[44:47], a[60:63], v[220:223], v[44:47]
v_add_f32 v17, v17, v35
v_cvt_pk_bf16_f32 v49, v34, v35
v_mfma_f32_16x16x32_bf16 v[160:163], a[64:67], v[52:55], v[160:163]
v_exp_f32 v36, v36
v_mfma_f32_16x16x32_bf16 v[164:167], a[68:71], v[52:55], v[164:167]
ds_read_b128 a[4:7], v6 offset:1024
v_add_f32 v16, v16, v36
v_mfma_f32_16x16x32_bf16 v[168:171], a[72:75], v[52:55], v[168:171]
v_exp_f32 v37, v37
v_mfma_f32_16x16x32_bf16 v[172:175], a[76:79], v[52:55], v[172:175]
v_add_f32 v17, v17, v37
v_cvt_pk_bf16_f32 v50, v36, v37
v_mfma_f32_16x16x32_bf16 v[176:179], a[80:83], v[52:55], v[176:179]
v_exp_f32 v38, v38
v_mfma_f32_16x16x32_bf16 v[180:183], a[84:87], v[52:55], v[180:183]
ds_read_b128 a[20:23], v6 offset:5120
v_add_f32 v16, v16, v38
v_mfma_f32_16x16x32_bf16 v[184:187], a[88:91], v[52:55], v[184:187]
v_exp_f32 v39, v39
v_mfma_f32_16x16x32_bf16 v[188:191], a[92:95], v[52:55], v[188:191]
v_add_f32 v17, v17, v39
v_cvt_pk_bf16_f32 v51, v38, v39
v_mfma_f32_16x16x32_bf16 v[32:35], a[32:35], v[224:227], 0
ds_read_b128 a[8:11], v6 offset:2048
v_exp_f32 v40, v40
v_mfma_f32_16x16x32_bf16 v[36:39], a[48:51], v[224:227], 0
buffer_load_dwordx4 v4, s[48:51], s53 offen offset:1024 sc1 lds
v_add_f32 v20, v20, v40
v_mfma_f32_16x16x32_bf16 v[32:35], a[36:39], v[228:231], v[32:35]
v_exp_f32 v41, v41
v_mfma_f32_16x16x32_bf16 v[36:39], a[52:55], v[228:231], v[36:39]
v_add_f32 v21, v21, v41
v_cvt_pk_bf16_f32 v52, v40, v41
v_mfma_f32_16x16x32_bf16 v[32:35], a[40:43], v[232:235], v[32:35]
v_exp_f32 v42, v42
v_mfma_f32_16x16x32_bf16 v[36:39], a[56:59], v[232:235], v[36:39]
ds_read_b128 a[24:27], v6 offset:6144
v_add_f32 v20, v20, v42
v_mfma_f32_16x16x32_bf16 v[32:35], a[44:47], v[236:239], v[32:35]
v_exp_f32 v43, v43
v_mfma_f32_16x16x32_bf16 v[36:39], a[60:63], v[236:239], v[36:39]
v_add_f32 v21, v21, v43
v_cvt_pk_bf16_f32 v53, v42, v43
v_mfma_f32_16x16x32_bf16 v[64:67], a[96:99], v[48:51], v[64:67]
v_exp_f32 v44, v44
v_mfma_f32_16x16x32_bf16 v[68:71], a[100:103], v[48:51], v[68:71]
ds_read_b128 a[64:67], v6 offset:16384
v_add_f32 v20, v20, v44
v_mfma_f32_16x16x32_bf16 v[72:75], a[104:107], v[48:51], v[72:75]
v_exp_f32 v45, v45
v_mfma_f32_16x16x32_bf16 v[76:79], a[108:111], v[48:51], v[76:79]
v_add_f32 v21, v21, v45
v_cvt_pk_bf16_f32 v54, v44, v45
s_waitcnt lgkmcnt(1)
v_mfma_f32_16x16x32_bf16 v[80:83], a[112:115], v[48:51], v[80:83]
v_exp_f32 v46, v46
v_mfma_f32_16x16x32_bf16 v[84:87], a[116:119], v[48:51], v[84:87]
ds_read_b128 a[68:71], v6 offset:17408
v_add_f32 v20, v20, v46
v_mfma_f32_16x16x32_bf16 v[88:91], a[120:123], v[48:51], v[88:91]
v_exp_f32 v47, v47
v_mfma_f32_16x16x32_bf16 v[92:95], a[124:127], v[48:51], v[92:95]
v_add_f32 v21, v21, v47
v_cvt_pk_bf16_f32 v55, v46, v47
v_mfma_f32_16x16x32_bf16 v[40:43], a[32:35], v[240:243], 0
ds_read_b128 a[12:15], v6 offset:3072
v_exp_f32 v32, v32
v_mfma_f32_16x16x32_bf16 v[44:47], a[48:51], v[240:243], 0
buffer_load_dwordx4 v4, s[48:51], s53 offen offset:2048 sc1 lds
v_add_f32 v18, v18, v32
v_mfma_f32_16x16x32_bf16 v[40:43], a[36:39], v[244:247], v[40:43]
v_exp_f32 v33, v33
v_mfma_f32_16x16x32_bf16 v[44:47], a[52:55], v[244:247], v[44:47]
v_add_f32 v19, v19, v33
v_cvt_pk_bf16_f32 v48, v32, v33
v_mfma_f32_16x16x32_bf16 v[40:43], a[40:43], v[248:251], v[40:43]
v_exp_f32 v34, v34
v_mfma_f32_16x16x32_bf16 v[44:47], a[56:59], v[248:251], v[44:47]
ds_read_b128 a[28:31], v6 offset:7168
v_add_f32 v18, v18, v34
v_mfma_f32_16x16x32_bf16 v[40:43], a[44:47], v[252:255], v[40:43]
v_exp_f32 v35, v35
v_mfma_f32_16x16x32_bf16 v[44:47], a[60:63], v[252:255], v[44:47]
v_add_f32 v19, v19, v35
v_cvt_pk_bf16_f32 v49, v34, v35
v_mfma_f32_16x16x32_bf16 v[96:99], a[96:99], v[52:55], v[96:99]
ds_read_b128 a[72:75], v6 offset:18432
v_exp_f32 v36, v36
v_mfma_f32_16x16x32_bf16 v[100:103], a[100:103], v[52:55], v[100:103]
buffer_load_dwordx4 v4, s[48:51], s53 offen offset:3072 sc1 lds
v_add_f32 v18, v18, v36
v_mfma_f32_16x16x32_bf16 v[104:107], a[104:107], v[52:55], v[104:107]
v_exp_f32 v37, v37
v_mfma_f32_16x16x32_bf16 v[108:111], a[108:111], v[52:55], v[108:111]
v_add_f32 v19, v19, v37
v_cvt_pk_bf16_f32 v50, v36, v37
v_mfma_f32_16x16x32_bf16 v[112:115], a[112:115], v[52:55], v[112:115]
v_exp_f32 v38, v38
v_mfma_f32_16x16x32_bf16 v[116:119], a[116:119], v[52:55], v[116:119]
ds_read_b128 a[76:79], v6 offset:19456
v_add_f32 v18, v18, v38
v_mfma_f32_16x16x32_bf16 v[120:123], a[120:123], v[52:55], v[120:123]
v_exp_f32 v39, v39
v_mfma_f32_16x16x32_bf16 v[124:127], a[124:127], v[52:55], v[124:127]
v_add_f32 v19, v19, v39
v_cvt_pk_bf16_f32 v51, v38, v39
v_mov_b32 v5, v6
v_add_u32 v6, 0x8000, v6
v_and_b32 v6, 0x1ffff, v6
v_mfma_f32_16x16x32_bf16 v[32:35], a[0:3], v[192:195], v[8:11]
ds_read_b128 a[80:83], v5 offset:20480
v_exp_f32 v40, v40
v_mfma_f32_16x16x32_bf16 v[32:35], a[4:7], v[196:199], v[32:35]
v_add_f32 v22, v22, v40
v_mfma_f32_16x16x32_bf16 v[32:35], a[8:11], v[200:203], v[32:35]
s_add_u32 m0, s52, 0x1000
v_exp_f32 v41, v41
s_waitcnt lgkmcnt(1)
v_mfma_f32_16x16x32_bf16 v[32:35], a[12:15], v[204:207], v[32:35]
ds_read_b128 a[84:87], v5 offset:21504
v_add_f32 v23, v23, v41
v_mfma_f32_16x16x32_bf16 v[128:131], a[96:99], v[48:51], v[128:131]
v_exp_f32 v42, v42
v_mfma_f32_16x16x32_bf16 v[132:135], a[100:103], v[48:51], v[132:135]
v_add_f32 v22, v22, v42
v_mfma_f32_16x16x32_bf16 v[136:139], a[104:107], v[48:51], v[136:139]
ds_read_b128 a[32:35], v5 offset:8192
v_exp_f32 v43, v43
v_mfma_f32_16x16x32_bf16 v[140:143], a[108:111], v[48:51], v[140:143]
v_add_f32 v23, v23, v43
v_cvt_pk_bf16_f32 v52, v40, v41
v_mfma_f32_16x16x32_bf16 v[144:147], a[112:115], v[48:51], v[144:147]
v_cvt_pk_bf16_f32 v53, v42, v43
v_mfma_f32_16x16x32_bf16 v[148:151], a[116:119], v[48:51], v[148:151]
ds_read_b128 a[48:51], v5 offset:12288
v_exp_f32 v44, v44
v_mfma_f32_16x16x32_bf16 v[152:155], a[120:123], v[48:51], v[152:155]
v_add_f32 v22, v22, v44
v_mfma_f32_16x16x32_bf16 v[156:159], a[124:127], v[48:51], v[156:159]
v_exp_f32 v45, v45
s_nop 0
v_add_f32 v23, v23, v45
v_exp_f32 v46, v46
s_nop 0
v_add_f32 v22, v22, v46
v_exp_f32 v47, v47
s_nop 0
v_add_f32 v23, v23, v47
v_cvt_pk_bf16_f32 v54, v44, v45
v_cvt_pk_bf16_f32 v55, v46, v47
v_mfma_f32_16x16x32_bf16 v[40:43], a[0:3], v[208:211], 0
ds_read_b128 a[88:91], v5 offset:22528
v_exp_f32 v32, v32
v_mfma_f32_16x16x32_bf16 v[44:47], a[16:19], v[208:211], v[8:11]
v_add_f32 v16, v16, v32
v_mfma_f32_16x16x32_bf16 v[40:43], a[4:7], v[212:215], v[40:43]
buffer_load_dwordx4 v4, s[48:51], s54 offen offset:0 sc1 lds
v_exp_f32 v33, v33
v_mfma_f32_16x16x32_bf16 v[44:47], a[20:23], v[212:215], v[44:47]
v_add_f32 v17, v17, v33
v_mfma_f32_16x16x32_bf16 v[40:43], a[8:11], v[216:219], v[40:43]
v_exp_f32 v34, v34
v_mfma_f32_16x16x32_bf16 v[44:47], a[24:27], v[216:219], v[44:47]
ds_read_b128 a[92:95], v5 offset:23552
v_add_f32 v16, v16, v34
v_mfma_f32_16x16x32_bf16 v[40:43], a[12:15], v[220:223], v[40:43]
v_exp_f32 v35, v35
v_mfma_f32_16x16x32_bf16 v[44:47], a[28:31], v[220:223], v[44:47]
v_add_f32 v17, v17, v35
v_cvt_pk_bf16_f32 v48, v32, v33
v_mfma_f32_16x16x32_bf16 v[160:163], a[96:99], v[52:55], v[160:163]
ds_read_b128 a[36:39], v5 offset:9216
v_cvt_pk_bf16_f32 v49, v34, v35
v_mov_b32 v50, 0
v_mfma_f32_16x16x32_bf16 v[164:167], a[100:103], v[52:55], v[164:167]
v_mov_b32 v51, 0
v_mfma_f32_16x16x32_bf16 v[168:171], a[104:107], v[52:55], v[168:171]
buffer_load_dwordx4 v4, s[48:51], s54 offen offset:1024 sc1 lds
v_mfma_f32_16x16x32_bf16 v[172:175], a[108:111], v[52:55], v[172:175]
v_mfma_f32_16x16x32_bf16 v[176:179], a[112:115], v[52:55], v[176:179]
v_mfma_f32_16x16x32_bf16 v[180:183], a[116:119], v[52:55], v[180:183]
ds_read_b128 a[52:55], v5 offset:13312
v_mfma_f32_16x16x32_bf16 v[184:187], a[120:123], v[52:55], v[184:187]
v_mfma_f32_16x16x32_bf16 v[188:191], a[124:127], v[52:55], v[188:191]
v_mfma_f32_16x16x32_bf16 v[32:35], a[0:3], v[224:227], 0
ds_read_b128 a[40:43], v5 offset:10240
v_exp_f32 v40, v40
v_mfma_f32_16x16x32_bf16 v[36:39], a[16:19], v[224:227], 0
buffer_load_dwordx4 v4, s[48:51], s54 offen offset:2048 sc1 lds
v_add_f32 v20, v20, v40
v_mfma_f32_16x16x32_bf16 v[32:35], a[4:7], v[228:231], v[32:35]
v_exp_f32 v41, v41
v_mfma_f32_16x16x32_bf16 v[36:39], a[20:23], v[228:231], v[36:39]
v_add_f32 v21, v21, v41
v_cvt_pk_bf16_f32 v52, v40, v41
v_mfma_f32_16x16x32_bf16 v[32:35], a[8:11], v[232:235], v[32:35]
v_exp_f32 v42, v42
v_mfma_f32_16x16x32_bf16 v[36:39], a[24:27], v[232:235], v[36:39]
ds_read_b128 a[56:59], v5 offset:14336
v_add_f32 v20, v20, v42
v_mfma_f32_16x16x32_bf16 v[32:35], a[12:15], v[236:239], v[32:35]
v_exp_f32 v43, v43
v_mfma_f32_16x16x32_bf16 v[36:39], a[28:31], v[236:239], v[36:39]
v_add_f32 v21, v21, v43
v_cvt_pk_bf16_f32 v53, v42, v43
v_mfma_f32_16x16x32_bf16 v[64:67], a[64:67], v[48:51], v[64:67]
ds_read_b128 a[96:99], v5 offset:24576
v_exp_f32 v44, v44
v_mfma_f32_16x16x32_bf16 v[68:71], a[68:71], v[48:51], v[68:71]
buffer_load_dwordx4 v4, s[48:51], s54 offen offset:3072 sc1 lds
v_add_f32 v20, v20, v44
v_mfma_f32_16x16x32_bf16 v[72:75], a[72:75], v[48:51], v[72:75]
v_exp_f32 v45, v45
v_mfma_f32_16x16x32_bf16 v[76:79], a[76:79], v[48:51], v[76:79]
v_add_f32 v21, v21, v45
v_cvt_pk_bf16_f32 v54, v44, v45
s_waitcnt lgkmcnt(1)
v_mfma_f32_16x16x32_bf16 v[80:83], a[80:83], v[48:51], v[80:83]
v_exp_f32 v46, v46
v_mfma_f32_16x16x32_bf16 v[84:87], a[84:87], v[48:51], v[84:87]
ds_read_b128 a[100:103], v5 offset:25600
v_add_f32 v20, v20, v46
v_mfma_f32_16x16x32_bf16 v[88:91], a[88:91], v[48:51], v[88:91]
v_exp_f32 v47, v47
v_mfma_f32_16x16x32_bf16 v[92:95], a[92:95], v[48:51], v[92:95]
v_add_f32 v21, v21, v47
v_cvt_pk_bf16_f32 v55, v46, v47
v_mfma_f32_16x16x32_bf16 v[40:43], a[0:3], v[240:243], 0
v_exp_f32 v32, v32
v_mfma_f32_16x16x32_bf16 v[44:47], a[16:19], v[240:243], 0
ds_read_b128 a[44:47], v5 offset:11264
s_add_u32 s53, s53, 0x8000
s_add_u32 s54, s54, 0x8000
v_add_f32 v18, v18, v32
v_mfma_f32_16x16x32_bf16 v[40:43], a[4:7], v[244:247], v[40:43]
v_exp_f32 v33, v33
v_mfma_f32_16x16x32_bf16 v[44:47], a[20:23], v[244:247], v[44:47]
v_add_f32 v19, v19, v33
v_cvt_pk_bf16_f32 v48, v32, v33
v_mfma_f32_16x16x32_bf16 v[40:43], a[8:11], v[248:251], v[40:43]
v_exp_f32 v34, v34
v_mfma_f32_16x16x32_bf16 v[44:47], a[24:27], v[248:251], v[44:47]
ds_read_b128 a[60:63], v5 offset:15360
v_add_f32 v18, v18, v34
v_mfma_f32_16x16x32_bf16 v[40:43], a[12:15], v[252:255], v[40:43]
v_exp_f32 v35, v35
v_mfma_f32_16x16x32_bf16 v[44:47], a[28:31], v[252:255], v[44:47]
v_add_f32 v19, v19, v35
v_cvt_pk_bf16_f32 v49, v34, v35
v_mfma_f32_16x16x32_bf16 v[96:99], a[64:67], v[52:55], v[96:99]
v_exp_f32 v36, v36
v_mfma_f32_16x16x32_bf16 v[100:103], a[68:71], v[52:55], v[100:103]
ds_read_b128 a[104:107], v5 offset:26624
s_add_u32 s52, s52, 0x8000
s_and_b32 s52, s52, 0x1ffff
v_add_f32 v18, v18, v36
v_mfma_f32_16x16x32_bf16 v[104:107], a[72:75], v[52:55], v[104:107]
v_exp_f32 v37, v37
v_mfma_f32_16x16x32_bf16 v[108:111], a[76:79], v[52:55], v[108:111]
v_add_f32 v19, v19, v37
v_cvt_pk_bf16_f32 v50, v36, v37
v_mfma_f32_16x16x32_bf16 v[112:115], a[80:83], v[52:55], v[112:115]
v_exp_f32 v38, v38
v_mfma_f32_16x16x32_bf16 v[116:119], a[84:87], v[52:55], v[116:119]
ds_read_b128 a[108:111], v5 offset:27648
v_add_f32 v18, v18, v38
v_mfma_f32_16x16x32_bf16 v[120:123], a[88:91], v[52:55], v[120:123]
v_exp_f32 v39, v39
v_mfma_f32_16x16x32_bf16 v[124:127], a[92:95], v[52:55], v[124:127]
v_add_f32 v19, v19, v39
v_cvt_pk_bf16_f32 v51, v38, v39
s_waitcnt vmcnt(8)
s_barrier
v_mfma_f32_16x16x32_bf16 v[32:35], a[32:35], v[224:227], 0
ds_read_b128 a[112:115], v5 offset:28672
s_add_u32 m0, s52, 0
v_exp_f32 v40, v40
v_mfma_f32_16x16x32_bf16 v[36:39], a[48:51], v[224:227], 0
ds_read_b128 a[116:119], v5 offset:29696
v_add_f32 v22, v22, v40
v_mfma_f32_16x16x32_bf16 v[32:35], a[36:39], v[228:231], v[32:35]
v_exp_f32 v41, v41
v_mfma_f32_16x16x32_bf16 v[36:39], a[52:55], v[228:231], v[36:39]
v_add_f32 v23, v23, v41
v_cvt_pk_bf16_f32 v52, v40, v41
v_mfma_f32_16x16x32_bf16 v[32:35], a[40:43], v[232:235], v[32:35]
ds_read_b128 a[0:3], v6 offset:0
v_exp_f32 v42, v42
v_mfma_f32_16x16x32_bf16 v[36:39], a[56:59], v[232:235], v[36:39]
ds_read_b128 a[16:19], v6 offset:4096
v_add_f32 v22, v22, v42
s_waitcnt lgkmcnt(3)
v_mfma_f32_16x16x32_bf16 v[32:35], a[44:47], v[236:239], v[32:35]
v_exp_f32 v43, v43
v_mfma_f32_16x16x32_bf16 v[36:39], a[60:63], v[236:239], v[36:39]
v_add_f32 v23, v23, v43
v_cvt_pk_bf16_f32 v53, v42, v43
v_mfma_f32_16x16x32_bf16 v[128:131], a[64:67], v[48:51], v[128:131]
buffer_load_dwordx4 v4, s[48:51], s53 offen offset:0 sc1 lds
v_exp_f32 v44, v44
v_mfma_f32_16x16x32_bf16 v[132:135], a[68:71], v[48:51], v[132:135]
ds_read_b128 a[4:7], v6 offset:1024
v_add_f32 v22, v22, v44
v_mfma_f32_16x16x32_bf16 v[136:139], a[72:75], v[48:51], v[136:139]
v_exp_f32 v45, v45
v_mfma_f32_16x16x32_bf16 v[140:143], a[76:79], v[48:51], v[140:143]
v_add_f32 v23, v23, v45
v_cvt_pk_bf16_f32 v54, v44, v45
v_mfma_f32_16x16x32_bf16 v[144:147], a[80:83], v[48:51], v[144:147]
v_exp_f32 v46, v46
v_mfma_f32_16x16x32_bf16 v[148:151], a[84:87], v[48:51], v[148:151]
ds_read_b128 a[20:23], v6 offset:5120
v_add_f32 v22, v22, v46
v_mfma_f32_16x16x32_bf16 v[152:155], a[88:91], v[48:51], v[152:155]
v_exp_f32 v47, v47
v_mfma_f32_16x16x32_bf16 v[156:159], a[92:95], v[48:51], v[156:159]
v_add_f32 v23, v23, v47
v_cvt_pk_bf16_f32 v55, v46, v47
v_mfma_f32_16x16x32_bf16 v[40:43], a[32:35], v[240:243], 0
ds_read_b128 a[120:123], v5 offset:30720
v_exp_f32 v32, v32
v_mfma_f32_16x16x32_bf16 v[44:47], a[48:51], v[240:243], 0
buffer_load_dwordx4 v4, s[48:51], s53 offen offset:1024 sc1 lds
v_exp_f32 v33, v33
v_mfma_f32_16x16x32_bf16 v[40:43], a[36:39], v[244:247], v[40:43]
v_exp_f32 v34, v34
v_mfma_f32_16x16x32_bf16 v[44:47], a[52:55], v[244:247], v[44:47]
ds_read_b128 a[124:127], v5 offset:31744
v_exp_f32 v35, v35
v_mfma_f32_16x16x32_bf16 v[40:43], a[40:43], v[248:251], v[40:43]
v_cvt_pk_bf16_f32 v48, v32, v33
v_cvt_pk_bf16_f32 v49, v34, v35
v_mfma_f32_16x16x32_bf16 v[44:47], a[56:59], v[248:251], v[44:47]
ds_read_b128 a[8:11], v6 offset:2048
v_exp_f32 v36, v36
v_mfma_f32_16x16x32_bf16 v[40:43], a[44:47], v[252:255], v[40:43]
v_exp_f32 v37, v37
v_mfma_f32_16x16x32_bf16 v[44:47], a[60:63], v[252:255], v[44:47]
buffer_load_dwordx4 v4, s[48:51], s53 offen offset:2048 sc1 lds
v_exp_f32 v38, v38
v_mfma_f32_16x16x32_bf16 v[160:163], a[64:67], v[52:55], v[160:163]
ds_read_b128 a[24:27], v6 offset:6144
v_exp_f32 v39, v39
v_mfma_f32_16x16x32_bf16 v[164:167], a[68:71], v[52:55], v[164:167]
v_cvt_pk_bf16_f32 v50, v36, v37
v_cvt_pk_bf16_f32 v51, v38, v39
v_mfma_f32_16x16x32_bf16 v[168:171], a[72:75], v[52:55], v[168:171]
ds_read_b128 a[12:15], v6 offset:3072
v_mfma_f32_16x16x32_bf16 v[172:175], a[76:79], v[52:55], v[172:175]
v_mfma_f32_16x16x32_bf16 v[176:179], a[80:83], v[52:55], v[176:179]
buffer_load_dwordx4 v4, s[48:51], s53 offen offset:3072 sc1 lds
v_mfma_f32_16x16x32_bf16 v[180:183], a[84:87], v[52:55], v[180:183]
v_mfma_f32_16x16x32_bf16 v[184:187], a[88:91], v[52:55], v[184:187]
ds_read_b128 a[28:31], v6 offset:7168
v_mfma_f32_16x16x32_bf16 v[188:191], a[92:95], v[52:55], v[188:191]
v_mov_b32 v5, v6
v_add_u32 v6, 0x8000, v6
v_and_b32 v6, 0x1ffff, v6
s_waitcnt lgkmcnt(0)
v_mbcnt_lo_u32_b32 v12, -1, 0
v_mbcnt_hi_u32_b32 v12, -1, v12
v_lshlrev_b32 v12, 4, v12
s_mov_b32 s27, 0x4000
buffer_load_dwordx4 v[0:3], v12, s[40:43], s27 offen offset:0 sc1
s_nop 7
v_add_f32 v12, v16, v17
v_mov_b32 v13, v12
s_nop 1
v_permlane16_swap_b32 v12, v13
v_add_f32 v12, v12, v13
v_mov_b32 v13, v12
s_nop 1
v_permlane32_swap_b32 v12, v13
v_add_f32 v12, v12, v13
v_rcp_f32 v12, v12
s_nop 0
v_pk_mul_f32 v[64:65], v[64:65], v[12:13] op_sel_hi:[1,0]
v_pk_mul_f32 v[66:67], v[66:67], v[12:13] op_sel_hi:[1,0]
v_pk_mul_f32 v[68:69], v[68:69], v[12:13] op_sel_hi:[1,0]
v_pk_mul_f32 v[70:71], v[70:71], v[12:13] op_sel_hi:[1,0]
v_pk_mul_f32 v[72:73], v[72:73], v[12:13] op_sel_hi:[1,0]
v_pk_mul_f32 v[74:75], v[74:75], v[12:13] op_sel_hi:[1,0]
v_pk_mul_f32 v[76:77], v[76:77], v[12:13] op_sel_hi:[1,0]
v_pk_mul_f32 v[78:79], v[78:79], v[12:13] op_sel_hi:[1,0]
v_pk_mul_f32 v[80:81], v[80:81], v[12:13] op_sel_hi:[1,0]
v_pk_mul_f32 v[82:83], v[82:83], v[12:13] op_sel_hi:[1,0]
v_pk_mul_f32 v[84:85], v[84:85], v[12:13] op_sel_hi:[1,0]
v_pk_mul_f32 v[86:87], v[86:87], v[12:13] op_sel_hi:[1,0]
v_pk_mul_f32 v[88:89], v[88:89], v[12:13] op_sel_hi:[1,0]
v_pk_mul_f32 v[90:91], v[90:91], v[12:13] op_sel_hi:[1,0]
v_pk_mul_f32 v[92:93], v[92:93], v[12:13] op_sel_hi:[1,0]
v_pk_mul_f32 v[94:95], v[94:95], v[12:13] op_sel_hi:[1,0]
v_cvt_pk_bf16_f32 v64, v64, v65
v_cvt_pk_bf16_f32 v65, v66, v67
v_cvt_pk_bf16_f32 v66, v68, v69
v_cvt_pk_bf16_f32 v67, v70, v71
s_add_u32 s27, s55, 0
buffer_store_dwordx4 v[64:67], v4, s[36:39], s27 offen offset:0 sc1
v_cvt_pk_bf16_f32 v72, v72, v73
v_cvt_pk_bf16_f32 v73, v74, v75
v_cvt_pk_bf16_f32 v74, v76, v77
v_cvt_pk_bf16_f32 v75, v78, v79
s_add_u32 s27, s55, 0x1000
buffer_store_dwordx4 v[72:75], v4, s[36:39], s27 offen offset:0 sc1
v_cvt_pk_bf16_f32 v80, v80, v81
v_cvt_pk_bf16_f32 v81, v82, v83
v_cvt_pk_bf16_f32 v82, v84, v85
v_cvt_pk_bf16_f32 v83, v86, v87
s_add_u32 s27, s55, 0x2000
buffer_store_dwordx4 v[80:83], v4, s[36:39], s27 offen offset:0 sc1
v_cvt_pk_bf16_f32 v88, v88, v89
v_cvt_pk_bf16_f32 v89, v90, v91
v_cvt_pk_bf16_f32 v90, v92, v93
v_cvt_pk_bf16_f32 v91, v94, v95
s_add_u32 s27, s55, 0x3000
buffer_store_dwordx4 v[88:91], v4, s[36:39], s27 offen offset:0 sc1
v_add_f32 v12, v20, v21
v_mov_b32 v13, v12
s_nop 1
v_permlane16_swap_b32 v12, v13
v_add_f32 v12, v12, v13
v_mov_b32 v13, v12
s_nop 1
v_permlane32_swap_b32 v12, v13
v_add_f32 v12, v12, v13
v_rcp_f32 v12, v12
s_nop 0
v_pk_mul_f32 v[96:97], v[96:97], v[12:13] op_sel_hi:[1,0]
v_pk_mul_f32 v[98:99], v[98:99], v[12:13] op_sel_hi:[1,0]
v_pk_mul_f32 v[100:101], v[100:101], v[12:13] op_sel_hi:[1,0]
v_pk_mul_f32 v[102:103], v[102:103], v[12:13] op_sel_hi:[1,0]
v_pk_mul_f32 v[104:105], v[104:105], v[12:13] op_sel_hi:[1,0]
v_pk_mul_f32 v[106:107], v[106:107], v[12:13] op_sel_hi:[1,0]
v_pk_mul_f32 v[108:109], v[108:109], v[12:13] op_sel_hi:[1,0]
v_pk_mul_f32 v[110:111], v[110:111], v[12:13] op_sel_hi:[1,0]
v_pk_mul_f32 v[112:113], v[112:113], v[12:13] op_sel_hi:[1,0]
v_pk_mul_f32 v[114:115], v[114:115], v[12:13] op_sel_hi:[1,0]
v_pk_mul_f32 v[116:117], v[116:117], v[12:13] op_sel_hi:[1,0]
v_pk_mul_f32 v[118:119], v[118:119], v[12:13] op_sel_hi:[1,0]
v_pk_mul_f32 v[120:121], v[120:121], v[12:13] op_sel_hi:[1,0]
v_pk_mul_f32 v[122:123], v[122:123], v[12:13] op_sel_hi:[1,0]
v_pk_mul_f32 v[124:125], v[124:125], v[12:13] op_sel_hi:[1,0]
v_pk_mul_f32 v[126:127], v[126:127], v[12:13] op_sel_hi:[1,0]
v_cvt_pk_bf16_f32 v96, v96, v97
v_cvt_pk_bf16_f32 v97, v98, v99
v_cvt_pk_bf16_f32 v98, v100, v101
v_cvt_pk_bf16_f32 v99, v102, v103
s_add_u32 s27, s55, 0
s_add_u32 s27, s27, s69
buffer_store_dwordx4 v[96:99], v4, s[36:39], s27 offen offset:0 sc1
v_cvt_pk_bf16_f32 v104, v104, v105
v_cvt_pk_bf16_f32 v105, v106, v107
v_cvt_pk_bf16_f32 v106, v108, v109
v_cvt_pk_bf16_f32 v107, v110, v111
s_add_u32 s27, s55, 0x1000
s_add_u32 s27, s27, s69
buffer_store_dwordx4 v[104:107], v4, s[36:39], s27 offen offset:0 sc1
v_cvt_pk_bf16_f32 v112, v112, v113
v_cvt_pk_bf16_f32 v113, v114, v115
v_cvt_pk_bf16_f32 v114, v116, v117
v_cvt_pk_bf16_f32 v115, v118, v119
s_add_u32 s27, s55, 0x2000
s_add_u32 s27, s27, s69
buffer_store_dwordx4 v[112:115], v4, s[36:39], s27 offen offset:0 sc1
v_cvt_pk_bf16_f32 v120, v120, v121
v_cvt_pk_bf16_f32 v121, v122, v123
v_cvt_pk_bf16_f32 v122, v124, v125
v_cvt_pk_bf16_f32 v123, v126, v127
s_add_u32 s27, s55, 0x3000
s_add_u32 s27, s27, s69
buffer_store_dwordx4 v[120:123], v4, s[36:39], s27 offen offset:0 sc1
s_nop 1
s_branch B_D1end_%=
B_D1w3_%=:
v_mfma_f32_16x16x32_bf16 v[32:35], a[0:3], v[192:195], 0
v_exp_f32 v40, v40
v_mfma_f32_16x16x32_bf16 v[36:39], a[16:19], v[192:195], 0
ds_read_b128 a[80:83], v5 offset:20480
v_add_f32 v22, v22, v40
v_mfma_f32_16x16x32_bf16 v[32:35], a[4:7], v[196:199], v[32:35]
v_exp_f32 v41, v41
v_mfma_f32_16x16x32_bf16 v[36:39], a[20:23], v[196:199], v[36:39]
v_add_f32 v23, v23, v41
v_cvt_pk_bf16_f32 v52, v40, v41
v_mfma_f32_16x16x32_bf16 v[32:35], a[8:11], v[200:203], v[32:35]
s_add_u32 m0, s52, 0x1000
v_exp_f32 v42, v42
v_mfma_f32_16x16x32_bf16 v[36:39], a[24:27], v[200:203], v[36:39]
ds_read_b128 a[84:87], v5 offset:21504
v_add_f32 v22, v22, v42
v_mfma_f32_16x16x32_bf16 v[32:35], a[12:15], v[204:207], v[32:35]
v_exp_f32 v43, v43
v_mfma_f32_16x16x32_bf16 v[36:39], a[28:31], v[204:207], v[36:39]
v_add_f32 v23, v23, v43
v_cvt_pk_bf16_f32 v53, v42, v43
v_mfma_f32_16x16x32_bf16 v[128:131], a[96:99], v[48:51], v[128:131]
v_exp_f32 v44, v44
v_mfma_f32_16x16x32_bf16 v[132:135], a[100:103], v[48:51], v[132:135]
ds_read_b128 a[32:35], v5 offset:8192
v_add_f32 v22, v22, v44
v_mfma_f32_16x16x32_bf16 v[136:139], a[104:107], v[48:51], v[136:139]
v_exp_f32 v45, v45
v_mfma_f32_16x16x32_bf16 v[140:143], a[108:111], v[48:51], v[140:143]
v_add_f32 v23, v23, v45
v_cvt_pk_bf16_f32 v54, v44, v45
v_mfma_f32_16x16x32_bf16 v[144:147], a[112:115], v[48:51], v[144:147]
v_exp_f32 v46, v46
v_mfma_f32_16x16x32_bf16 v[148:151], a[116:119], v[48:51], v[148:151]
ds_read_b128 a[48:51], v5 offset:12288
v_add_f32 v22, v22, v46
v_mfma_f32_16x16x32_bf16 v[152:155], a[120:123], v[48:51], v[152:155]
v_exp_f32 v47, v47
v_mfma_f32_16x16x32_bf16 v[156:159], a[124:127], v[48:51], v[156:159]
v_add_f32 v23, v23, v47
v_cvt_pk_bf16_f32 v55, v46, v47
v_mfma_f32_16x16x32_bf16 v[40:43], a[0:3], v[208:211], 0
ds_read_b128 a[88:91], v5 offset:22528
v_exp_f32 v32, v32
v_mfma_f32_16x16x32_bf16 v[44:47], a[16:19], v[208:211], 0
buffer_load_dwordx4 v4, s[48:51], s54 offen offset:0 sc1 lds
v_add_f32 v16, v16, v32
v_mfma_f32_16x16x32_bf16 v[40:43], a[4:7], v[212:215], v[40:43]
v_exp_f32 v33, v33
v_mfma_f32_16x16x32_bf16 v[44:47], a[20:23], v[212:215], v[44:47]
v_add_f32 v17, v17, v33
v_cvt_pk_bf16_f32 v48, v32, v33
v_mfma_f32_16x16x32_bf16 v[40:43], a[8:11], v[216:219], v[40:43]
v_exp_f32 v34, v34
v_mfma_f32_16x16x32_bf16 v[44:47], a[24:27], v[216:219], v[44:47]
ds_read_b128 a[92:95], v5 offset:23552
v_add_f32 v16, v16, v34
v_mfma_f32_16x16x32_bf16 v[40:43], a[12:15], v[220:223], v[40:43]
v_exp_f32 v35, v35
v_mfma_f32_16x16x32_bf16 v[44:47], a[28:31], v[220:223], v[44:47]
v_add_f32 v17, v17, v35
v_cvt_pk_bf16_f32 v49, v34, v35
v_mfma_f32_16x16x32_bf16 v[160:163], a[96:99], v[52:55], v[160:163]
ds_read_b128 a[36:39], v5 offset:9216
v_exp_f32 v36, v36
v_mfma_f32_16x16x32_bf16 v[164:167], a[100:103], v[52:55], v[164:167]
buffer_load_dwordx4 v4, s[48:51], s54 offen offset:1024 sc1 lds
v_add_f32 v16, v16, v36
v_mfma_f32_16x16x32_bf16 v[168:171], a[104:107], v[52:55], v[168:171]
v_exp_f32 v37, v37
v_mfma_f32_16x16x32_bf16 v[172:175], a[108:111], v[52:55], v[172:175]
v_add_f32 v17, v17, v37
v_cvt_pk_bf16_f32 v50, v36, v37
v_mfma_f32_16x16x32_bf16 v[176:179], a[112:115], v[52:55], v[176:179]
v_exp_f32 v38, v38
v_mfma_f32_16x16x32_bf16 v[180:183], a[116:119], v[52:55], v[180:183]
ds_read_b128 a[52:55], v5 offset:13312
v_add_f32 v16, v16, v38
v_mfma_f32_16x16x32_bf16 v[184:187], a[120:123], v[52:55], v[184:187]
v_exp_f32 v39, v39
v_mfma_f32_16x16x32_bf16 v[188:191], a[124:127], v[52:55], v[188:191]
v_add_f32 v17, v17, v39
v_cvt_pk_bf16_f32 v51, v38, v39
v_mfma_f32_16x16x32_bf16 v[32:35], a[0:3], v[224:227], 0
ds_read_b128 a[40:43], v5 offset:10240
v_exp_f32 v40, v40
v_mfma_f32_16x16x32_bf16 v[36:39], a[16:19], v[224:227], 0
buffer_load_dwordx4 v4, s[48:51], s54 offen offset:2048 sc1 lds
v_add_f32 v20, v20, v40
v_mfma_f32_16x16x32_bf16 v[32:35], a[4:7], v[228:231], v[32:35]
v_exp_f32 v41, v41
v_mfma_f32_16x16x32_bf16 v[36:39], a[20:23], v[228:231], v[36:39]
v_add_f32 v21, v21, v41
v_cvt_pk_bf16_f32 v52, v40, v41
v_mfma_f32_16x16x32_bf16 v[32:35], a[8:11], v[232:235], v[32:35]
v_exp_f32 v42, v42
v_mfma_f32_16x16x32_bf16 v[36:39], a[24:27], v[232:235], v[36:39]
ds_read_b128 a[56:59], v5 offset:14336
v_add_f32 v20, v20, v42
v_mfma_f32_16x16x32_bf16 v[32:35], a[12:15], v[236:239], v[32:35]
v_exp_f32 v43, v43
v_mfma_f32_16x16x32_bf16 v[36:39], a[28:31], v[236:239], v[36:39]
v_add_f32 v21, v21, v43
v_cvt_pk_bf16_f32 v53, v42, v43
v_mfma_f32_16x16x32_bf16 v[64:67], a[64:67], v[48:51], v[64:67]
ds_read_b128 a[96:99], v5 offset:24576
v_exp_f32 v44, v44
v_mfma_f32_16x16x32_bf16 v[68:71], a[68:71], v[48:51], v[68:71]
buffer_load_dwordx4 v4, s[48:51], s54 offen offset:3072 sc1 lds
v_add_f32 v20, v20, v44
v_mfma_f32_16x16x32_bf16 v[72:75], a[72:75], v[48:51], v[72:75]
v_exp_f32 v45, v45
v_mfma_f32_16x16x32_bf16 v[76:79], a[76:79], v[48:51], v[76:79]
v_add_f32 v21, v21, v45
v_cvt_pk_bf16_f32 v54, v44, v45
s_waitcnt lgkmcnt(1)
v_mfma_f32_16x16x32_bf16 v[80:83], a[80:83], v[48:51], v[80:83]
v_exp_f32 v46, v46
v_mfma_f32_16x16x32_bf16 v[84:87], a[84:87], v[48:51], v[84:87]
ds_read_b128 a[100:103], v5 offset:25600
v_add_f32 v20, v20, v46
v_mfma_f32_16x16x32_bf16 v[88:91], a[88:91], v[48:51], v[88:91]
v_exp_f32 v47, v47
v_mfma_f32_16x16x32_bf16 v[92:95], a[92:95], v[48:51], v[92:95]
v_add_f32 v21, v21, v47
v_cvt_pk_bf16_f32 v55, v46, v47
v_mfma_f32_16x16x32_bf16 v[40:43], a[0:3], v[240:243], 0
v_exp_f32 v32, v32
v_mfma_f32_16x16x32_bf16 v[44:47], a[16:19], v[240:243], 0
ds_read_b128 a[44:47], v5 offset:11264
s_add_u32 s53, s53, 0x8000
s_add_u32 s54, s54, 0x8000
v_add_f32 v18, v18, v32
v_mfma_f32_16x16x32_bf16 v[40:43], a[4:7], v[244:247], v[40:43]
v_exp_f32 v33, v33
v_mfma_f32_16x16x32_bf16 v[44:47], a[20:23], v[244:247], v[44:47]
v_add_f32 v19, v19, v33
v_cvt_pk_bf16_f32 v48, v32, v33
v_mfma_f32_16x16x32_bf16 v[40:43], a[8:11], v[248:251], v[40:43]
v_exp_f32 v34, v34
v_mfma_f32_16x16x32_bf16 v[44:47], a[24:27], v[248:251], v[44:47]
ds_read_b128 a[60:63], v5 offset:15360
v_add_f32 v18, v18, v34
v_mfma_f32_16x16x32_bf16 v[40:43], a[12:15], v[252:255], v[40:43]
v_exp_f32 v35, v35
v_mfma_f32_16x16x32_bf16 v[44:47], a[28:31], v[252:255], v[44:47]
v_add_f32 v19, v19, v35
v_cvt_pk_bf16_f32 v49, v34, v35
v_mfma_f32_16x16x32_bf16 v[96:99], a[64:67], v[52:55], v[96:99]
v_exp_f32 v36, v36
v_mfma_f32_16x16x32_bf16 v[100:103], a[68:71], v[52:55], v[100:103]
ds_read_b128 a[104:107], v5 offset:26624
s_add_u32 s52, s52, 0x8000
s_and_b32 s52, s52, 0x1ffff
v_add_f32 v18, v18, v36
v_mfma_f32_16x16x32_bf16 v[104:107], a[72:75], v[52:55], v[104:107]
v_exp_f32 v37, v37
v_mfma_f32_16x16x32_bf16 v[108:111], a[76:79], v[52:55], v[108:111]
v_add_f32 v19, v19, v37
v_cvt_pk_bf16_f32 v50, v36, v37
v_mfma_f32_16x16x32_bf16 v[112:115], a[80:83], v[52:55], v[112:115]
v_exp_f32 v38, v38
v_mfma_f32_16x16x32_bf16 v[116:119], a[84:87], v[52:55], v[116:119]
ds_read_b128 a[108:111], v5 offset:27648
v_add_f32 v18, v18, v38
v_mfma_f32_16x16x32_bf16 v[120:123], a[88:91], v[52:55], v[120:123]
v_exp_f32 v39, v39
v_mfma_f32_16x16x32_bf16 v[124:127], a[92:95], v[52:55], v[124:127]
v_add_f32 v19, v19, v39
v_cvt_pk_bf16_f32 v51, v38, v39
s_waitcnt vmcnt(8)
s_barrier
v_mfma_f32_16x16x32_bf16 v[32:35], a[32:35], v[192:195], 0
v_exp_f32 v40, v40
v_mfma_f32_16x16x32_bf16 v[36:39], a[48:51], v[192:195], 0
ds_read_b128 a[112:115], v5 offset:28672
v_add_f32 v22, v22, v40
v_mfma_f32_16x16x32_bf16 v[32:35], a[36:39], v[196:199], v[32:35]
v_exp_f32 v41, v41
v_mfma_f32_16x16x32_bf16 v[36:39], a[52:55], v[196:199], v[36:39]
v_add_f32 v23, v23, v41
v_cvt_pk_bf16_f32 v52, v40, v41
v_mfma_f32_16x16x32_bf16 v[32:35], a[40:43], v[200:203], v[32:35]
s_add_u32 m0, s52, 0
v_exp_f32 v42, v42
v_mfma_f32_16x16x32_bf16 v[36:39], a[56:59], v[200:203], v[36:39]
ds_read_b128 a[116:119], v5 offset:29696
v_add_f32 v22, v22, v42
s_waitcnt lgkmcnt(2)
v_mfma_f32_16x16x32_bf16 v[32:35], a[44:47], v[204:207], v[32:35]
v_exp_f32 v43, v43
v_mfma_f32_16x16x32_bf16 v[36:39], a[60:63], v[204:207], v[36:39]
v_add_f32 v23, v23, v43
v_cvt_pk_bf16_f32 v53, v42, v43
v_mfma_f32_16x16x32_bf16 v[128:131], a[64:67], v[48:51], v[128:131]
v_exp_f32 v44, v44
v_mfma_f32_16x16x32_bf16 v[132:135], a[68:71], v[48:51], v[132:135]
ds_read_b128 a[0:3], v6 offset:0
v_add_f32 v22, v22, v44
v_mfma_f32_16x16x32_bf16 v[136:139], a[72:75], v[48:51], v[136:139]
v_exp_f32 v45, v45
v_mfma_f32_16x16x32_bf16 v[140:143], a[76:79], v[48:51], v[140:143]
v_add_f32 v23, v23, v45
v_cvt_pk_bf16_f32 v54, v44, v45
v_mfma_f32_16x16x32_bf16 v[144:147], a[80:83], v[48:51], v[144:147]
v_exp_f32 v46, v46
v_mfma_f32_16x16x32_bf16 v[148:151], a[84:87], v[48:51], v[148:151]
ds_read_b128 a[16:19], v6 offset:4096
v_add_f32 v22, v22, v46
v_mfma_f32_16x16x32_bf16 v[152:155], a[88:91], v[48:51], v[152:155]
v_exp_f32 v47, v47
v_mfma_f32_16x16x32_bf16 v[156:159], a[92:95], v[48:51], v[156:159]
v_add_f32 v23, v23, v47
v_cvt_pk_bf16_f32 v55, v46, v47
v_mfma_f32_16x16x32_bf16 v[40:43], a[32:35], v[208:211], 0
ds_read_b128 a[120:123], v5 offset:30720
v_exp_f32 v32, v32
v_mfma_f32_16x16x32_bf16 v[44:47], a[48:51], v[208:211], 0
buffer_load_dwordx4 v4, s[48:51], s53 offen offset:0 sc1 lds
v_add_f32 v16, v16, v32
v_mfma_f32_16x16x32_bf16 v[40:43], a[36:39], v[212:215], v[40:43]
v_exp_f32 v33, v33
v_mfma_f32_16x16x32_bf16 v[44:47], a[52:55], v[212:215], v[44:47]
v_add_f32 v17, v17, v33
v_cvt_pk_bf16_f32 v48, v32, v33
v_mfma_f32_16x16x32_bf16 v[40:43], a[40:43], v[216:219], v[40:43]
v_exp_f32 v34, v34
v_mfma_f32_16x16x32_bf16 v[44:47], a[56:59], v[216:219], v[44:47]
ds_read_b128 a[124:127], v5 offset:31744
v_add_f32 v16, v16, v34
v_mfma_f32_16x16x32_bf16 v[40:43], a[44:47], v[220:223], v[40:43]
v_exp_f32 v35, v35
v_mfma_f32_16x16x32_bf16 v[44:47], a[60:63], v[220:223], v[44:47]
v_add_f32 v17, v17, v35
v_cvt_pk_bf16_f32 v49, v34, v35
v_mfma_f32_16x16x32_bf16 v[160:163], a[64:67], v[52:55], v[160:163]
v_exp_f32 v36, v36
v_mfma_f32_16x16x32_bf16 v[164:167], a[68:71], v[52:55], v[164:167]
ds_read_b128 a[4:7], v6 offset:1024
v_add_f32 v16, v16, v36
v_mfma_f32_16x16x32_bf16 v[168:171], a[72:75], v[52:55], v[168:171]
v_exp_f32 v37, v37
v_mfma_f32_16x16x32_bf16 v[172:175], a[76:79], v[52:55], v[172:175]
v_add_f32 v17, v17, v37
v_cvt_pk_bf16_f32 v50, v36, v37
v_mfma_f32_16x16x32_bf16 v[176:179], a[80:83], v[52:55], v[176:179]
v_exp_f32 v38, v38
v_mfma_f32_16x16x32_bf16 v[180:183], a[84:87], v[52:55], v[180:183]
ds_read_b128 a[20:23], v6 offset:5120
v_add_f32 v16, v16, v38
v_mfma_f32_16x16x32_bf16 v[184:187], a[88:91], v[52:55], v[184:187]
v_exp_f32 v39, v39
v_mfma_f32_16x16x32_bf16 v[188:191], a[92:95], v[52:55], v[188:191]
v_add_f32 v17, v17, v39
v_cvt_pk_bf16_f32 v51, v38, v39
v_mfma_f32_16x16x32_bf16 v[32:35], a[32:35], v[224:227], 0
ds_read_b128 a[8:11], v6 offset:2048
v_exp_f32 v40, v40
v_mfma_f32_16x16x32_bf16 v[36:39], a[48:51], v[224:227], 0
buffer_load_dwordx4 v4, s[48:51], s53 offen offset:1024 sc1 lds
v_add_f32 v20, v20, v40
v_mfma_f32_16x16x32_bf16 v[32:35], a[36:39], v[228:231], v[32:35]
v_exp_f32 v41, v41
v_mfma_f32_16x16x32_bf16 v[36:39], a[52:55], v[228:231], v[36:39]
v_add_f32 v21, v21, v41
v_cvt_pk_bf16_f32 v52, v40, v41
v_mfma_f32_16x16x32_bf16 v[32:35], a[40:43], v[232:235], v[32:35]
v_exp_f32 v42, v42
v_mfma_f32_16x16x32_bf16 v[36:39], a[56:59], v[232:235], v[36:39]
ds_read_b128 a[24:27], v6 offset:6144
v_add_f32 v20, v20, v42
v_mfma_f32_16x16x32_bf16 v[32:35], a[44:47], v[236:239], v[32:35]
v_exp_f32 v43, v43
v_mfma_f32_16x16x32_bf16 v[36:39], a[60:63], v[236:239], v[36:39]
v_add_f32 v21, v21, v43
v_cvt_pk_bf16_f32 v53, v42, v43
v_mfma_f32_16x16x32_bf16 v[64:67], a[96:99], v[48:51], v[64:67]
v_exp_f32 v44, v44
v_mfma_f32_16x16x32_bf16 v[68:71], a[100:103], v[48:51], v[68:71]
ds_read_b128 a[64:67], v6 offset:16384
v_add_f32 v20, v20, v44
v_mfma_f32_16x16x32_bf16 v[72:75], a[104:107], v[48:51], v[72:75]
v_exp_f32 v45, v45
v_mfma_f32_16x16x32_bf16 v[76:79], a[108:111], v[48:51], v[76:79]
v_add_f32 v21, v21, v45
v_cvt_pk_bf16_f32 v54, v44, v45
s_waitcnt lgkmcnt(1)
v_mfma_f32_16x16x32_bf16 v[80:83], a[112:115], v[48:51], v[80:83]
v_exp_f32 v46, v46
v_mfma_f32_16x16x32_bf16 v[84:87], a[116:119], v[48:51], v[84:87]
ds_read_b128 a[68:71], v6 offset:17408
v_add_f32 v20, v20, v46
v_mfma_f32_16x16x32_bf16 v[88:91], a[120:123], v[48:51], v[88:91]
v_exp_f32 v47, v47
v_mfma_f32_16x16x32_bf16 v[92:95], a[124:127], v[48:51], v[92:95]
v_add_f32 v21, v21, v47
v_cvt_pk_bf16_f32 v55, v46, v47
v_mfma_f32_16x16x32_bf16 v[40:43], a[32:35], v[240:243], 0
ds_read_b128 a[12:15], v6 offset:3072
v_exp_f32 v32, v32
v_mfma_f32_16x16x32_bf16 v[44:47], a[48:51], v[240:243], 0
buffer_load_dwordx4 v4, s[48:51], s53 offen offset:2048 sc1 lds
v_add_f32 v18, v18, v32
v_mfma_f32_16x16x32_bf16 v[40:43], a[36:39], v[244:247], v[40:43]
v_exp_f32 v33, v33
v_mfma_f32_16x16x32_bf16 v[44:47], a[52:55], v[244:247], v[44:47]
v_add_f32 v19, v19, v33
v_cvt_pk_bf16_f32 v48, v32, v33
v_mfma_f32_16x16x32_bf16 v[40:43], a[40:43], v[248:251], v[40:43]
v_exp_f32 v34, v34
v_mfma_f32_16x16x32_bf16 v[44:47], a[56:59], v[248:251], v[44:47]
ds_read_b128 a[28:31], v6 offset:7168
v_add_f32 v18, v18, v34
v_mfma_f32_16x16x32_bf16 v[40:43], a[44:47], v[252:255], v[40:43]
v_exp_f32 v35, v35
v_mfma_f32_16x16x32_bf16 v[44:47], a[60:63], v[252:255], v[44:47]
v_add_f32 v19, v19, v35
v_cvt_pk_bf16_f32 v49, v34, v35
v_mfma_f32_16x16x32_bf16 v[96:99], a[96:99], v[52:55], v[96:99]
ds_read_b128 a[72:75], v6 offset:18432
v_exp_f32 v36, v36
v_mfma_f32_16x16x32_bf16 v[100:103], a[100:103], v[52:55], v[100:103]
buffer_load_dwordx4 v4, s[48:51], s53 offen offset:3072 sc1 lds
v_add_f32 v18, v18, v36
v_mfma_f32_16x16x32_bf16 v[104:107], a[104:107], v[52:55], v[104:107]
v_exp_f32 v37, v37
v_mfma_f32_16x16x32_bf16 v[108:111], a[108:111], v[52:55], v[108:111]
v_add_f32 v19, v19, v37
v_cvt_pk_bf16_f32 v50, v36, v37
v_mfma_f32_16x16x32_bf16 v[112:115], a[112:115], v[52:55], v[112:115]
v_exp_f32 v38, v38
v_mfma_f32_16x16x32_bf16 v[116:119], a[116:119], v[52:55], v[116:119]
ds_read_b128 a[76:79], v6 offset:19456
v_add_f32 v18, v18, v38
v_mfma_f32_16x16x32_bf16 v[120:123], a[120:123], v[52:55], v[120:123]
v_exp_f32 v39, v39
v_mfma_f32_16x16x32_bf16 v[124:127], a[124:127], v[52:55], v[124:127]
v_add_f32 v19, v19, v39
v_cvt_pk_bf16_f32 v51, v38, v39
v_mov_b32 v5, v6
v_add_u32 v6, 0x8000, v6
v_and_b32 v6, 0x1ffff, v6
v_mfma_f32_16x16x32_bf16 v[32:35], a[0:3], v[192:195], 0
v_exp_f32 v40, v40
v_mfma_f32_16x16x32_bf16 v[36:39], a[16:19], v[192:195], 0
ds_read_b128 a[80:83], v5 offset:20480
v_add_f32 v22, v22, v40
v_mfma_f32_16x16x32_bf16 v[32:35], a[4:7], v[196:199], v[32:35]
v_exp_f32 v41, v41
v_mfma_f32_16x16x32_bf16 v[36:39], a[20:23], v[196:199], v[36:39]
v_add_f32 v23, v23, v41
v_cvt_pk_bf16_f32 v52, v40, v41
v_mfma_f32_16x16x32_bf16 v[32:35], a[8:11], v[200:203], v[32:35]
s_add_u32 m0, s52, 0x1000
v_exp_f32 v42, v42
v_mfma_f32_16x16x32_bf16 v[36:39], a[24:27], v[200:203], v[36:39]
ds_read_b128 a[84:87], v5 offset:21504
v_add_f32 v22, v22, v42
s_waitcnt lgkmcnt(2)
v_mfma_f32_16x16x32_bf16 v[32:35], a[12:15], v[204:207], v[32:35]
v_exp_f32 v43, v43
v_mfma_f32_16x16x32_bf16 v[36:39], a[28:31], v[204:207], v[36:39]
v_add_f32 v23, v23, v43
v_cvt_pk_bf16_f32 v53, v42, v43
v_mfma_f32_16x16x32_bf16 v[128:131], a[96:99], v[48:51], v[128:131]
v_exp_f32 v44, v44
v_mfma_f32_16x16x32_bf16 v[132:135], a[100:103], v[48:51], v[132:135]
ds_read_b128 a[32:35], v5 offset:8192
v_add_f32 v22, v22, v44
v_mfma_f32_16x16x32_bf16 v[136:139], a[104:107], v[48:51], v[136:139]
v_exp_f32 v45, v45
v_mfma_f32_16x16x32_bf16 v[140:143], a[108:111], v[48:51], v[140:143]
v_add_f32 v23, v23, v45
v_cvt_pk_bf16_f32 v54, v44, v45
v_mfma_f32_16x16x32_bf16 v[144:147], a[112:115], v[48:51], v[144:147]
v_exp_f32 v46, v46
v_mfma_f32_16x16x32_bf16 v[148:151], a[116:119], v[48:51], v[148:151]
ds_read_b128 a[48:51], v5 offset:12288
v_add_f32 v22, v22, v46
v_mfma_f32_16x16x32_bf16 v[152:155], a[120:123], v[48:51], v[152:155]
v_exp_f32 v47, v47
v_mfma_f32_16x16x32_bf16 v[156:159], a[124:127], v[48:51], v[156:159]
v_add_f32 v23, v23, v47
v_cvt_pk_bf16_f32 v55, v46, v47
v_mfma_f32_16x16x32_bf16 v[40:43], a[0:3], v[208:211], 0
ds_read_b128 a[88:91], v5 offset:22528
v_exp_f32 v32, v32
v_mfma_f32_16x16x32_bf16 v[44:47], a[16:19], v[208:211], 0
buffer_load_dwordx4 v4, s[48:51], s54 offen offset:0 sc1 lds
v_add_f32 v16, v16, v32
v_mfma_f32_16x16x32_bf16 v[40:43], a[4:7], v[212:215], v[40:43]
v_exp_f32 v33, v33
v_mfma_f32_16x16x32_bf16 v[44:47], a[20:23], v[212:215], v[44:47]
v_add_f32 v17, v17, v33
v_cvt_pk_bf16_f32 v48, v32, v33
v_mfma_f32_16x16x32_bf16 v[40:43], a[8:11], v[216:219], v[40:43]
v_exp_f32 v34, v34
v_mfma_f32_16x16x32_bf16 v[44:47], a[24:27], v[216:219], v[44:47]
ds_read_b128 a[92:95], v5 offset:23552
v_add_f32 v16, v16, v34
v_mfma_f32_16x16x32_bf16 v[40:43], a[12:15], v[220:223], v[40:43]
v_exp_f32 v35, v35
v_mfma_f32_16x16x32_bf16 v[44:47], a[28:31], v[220:223], v[44:47]
v_add_f32 v17, v17, v35
v_cvt_pk_bf16_f32 v49, v34, v35
v_mfma_f32_16x16x32_bf16 v[160:163], a[96:99], v[52:55], v[160:163]
ds_read_b128 a[36:39], v5 offset:9216
v_exp_f32 v36, v36
v_mfma_f32_16x16x32_bf16 v[164:167], a[100:103], v[52:55], v[164:167]
buffer_load_dwordx4 v4, s[48:51], s54 offen offset:1024 sc1 lds
v_add_f32 v16, v16, v36
v_mfma_f32_16x16x32_bf16 v[168:171], a[104:107], v[52:55], v[168:171]
v_exp_f32 v37, v37
v_mfma_f32_16x16x32_bf16 v[172:175], a[108:111], v[52:55], v[172:175]
v_add_f32 v17, v17, v37
v_cvt_pk_bf16_f32 v50, v36, v37
v_mfma_f32_16x16x32_bf16 v[176:179], a[112:115], v[52:55], v[176:179]
v_exp_f32 v38, v38
v_mfma_f32_16x16x32_bf16 v[180:183], a[116:119], v[52:55], v[180:183]
ds_read_b128 a[52:55], v5 offset:13312
v_add_f32 v16, v16, v38
v_mfma_f32_16x16x32_bf16 v[184:187], a[120:123], v[52:55], v[184:187]
v_exp_f32 v39, v39
v_mfma_f32_16x16x32_bf16 v[188:191], a[124:127], v[52:55], v[188:191]
v_add_f32 v17, v17, v39
v_cvt_pk_bf16_f32 v51, v38, v39
v_mfma_f32_16x16x32_bf16 v[32:35], a[0:3], v[224:227], 0
ds_read_b128 a[40:43], v5 offset:10240
v_exp_f32 v40, v40
v_mfma_f32_16x16x32_bf16 v[36:39], a[16:19], v[224:227], 0
buffer_load_dwordx4 v4, s[48:51], s54 offen offset:2048 sc1 lds
v_add_f32 v20, v20, v40
v_mfma_f32_16x16x32_bf16 v[32:35], a[4:7], v[228:231], v[32:35]
v_exp_f32 v41, v41
v_mfma_f32_16x16x32_bf16 v[36:39], a[20:23], v[228:231], v[36:39]
v_add_f32 v21, v21, v41
v_cvt_pk_bf16_f32 v52, v40, v41
v_mfma_f32_16x16x32_bf16 v[32:35], a[8:11], v[232:235], v[32:35]
v_exp_f32 v42, v42
v_mfma_f32_16x16x32_bf16 v[36:39], a[24:27], v[232:235], v[36:39]
ds_read_b128 a[56:59], v5 offset:14336
v_add_f32 v20, v20, v42
v_mfma_f32_16x16x32_bf16 v[32:35], a[12:15], v[236:239], v[32:35]
v_exp_f32 v43, v43
v_mfma_f32_16x16x32_bf16 v[36:39], a[28:31], v[236:239], v[36:39]
v_add_f32 v21, v21, v43
v_cvt_pk_bf16_f32 v53, v42, v43
v_mfma_f32_16x16x32_bf16 v[64:67], a[64:67], v[48:51], v[64:67]
ds_read_b128 a[96:99], v5 offset:24576
v_exp_f32 v44, v44
v_mfma_f32_16x16x32_bf16 v[68:71], a[68:71], v[48:51], v[68:71]
buffer_load_dwordx4 v4, s[48:51], s54 offen offset:3072 sc1 lds
v_add_f32 v20, v20, v44
v_mfma_f32_16x16x32_bf16 v[72:75], a[72:75], v[48:51], v[72:75]
v_exp_f32 v45, v45
v_mfma_f32_16x16x32_bf16 v[76:79], a[76:79], v[48:51], v[76:79]
v_add_f32 v21, v21, v45
v_cvt_pk_bf16_f32 v54, v44, v45
s_waitcnt lgkmcnt(1)
v_mfma_f32_16x16x32_bf16 v[80:83], a[80:83], v[48:51], v[80:83]
v_exp_f32 v46, v46
v_mfma_f32_16x16x32_bf16 v[84:87], a[84:87], v[48:51], v[84:87]
ds_read_b128 a[100:103], v5 offset:25600
v_add_f32 v20, v20, v46
v_mfma_f32_16x16x32_bf16 v[88:91], a[88:91], v[48:51], v[88:91]
v_exp_f32 v47, v47
v_mfma_f32_16x16x32_bf16 v[92:95], a[92:95], v[48:51], v[92:95]
v_add_f32 v21, v21, v47
v_cvt_pk_bf16_f32 v55, v46, v47
v_mfma_f32_16x16x32_bf16 v[40:43], a[0:3], v[240:243], 0
v_exp_f32 v32, v32
v_mfma_f32_16x16x32_bf16 v[44:47], a[16:19], v[240:243], 0
ds_read_b128 a[44:47], v5 offset:11264
s_add_u32 s53, s53, 0x8000
s_add_u32 s54, s54, 0x8000
v_add_f32 v18, v18, v32
v_mfma_f32_16x16x32_bf16 v[40:43], a[4:7], v[244:247], v[40:43]
v_exp_f32 v33, v33
v_mfma_f32_16x16x32_bf16 v[44:47], a[20:23], v[244:247], v[44:47]
v_add_f32 v19, v19, v33
v_cvt_pk_bf16_f32 v48, v32, v33
v_mfma_f32_16x16x32_bf16 v[40:43], a[8:11], v[248:251], v[40:43]
v_exp_f32 v34, v34
v_mfma_f32_16x16x32_bf16 v[44:47], a[24:27], v[248:251], v[44:47]
ds_read_b128 a[60:63], v5 offset:15360
v_add_f32 v18, v18, v34
v_mfma_f32_16x16x32_bf16 v[40:43], a[12:15], v[252:255], v[40:43]
v_exp_f32 v35, v35
v_mfma_f32_16x16x32_bf16 v[44:47], a[28:31], v[252:255], v[44:47]
v_add_f32 v19, v19, v35
v_cvt_pk_bf16_f32 v49, v34, v35
v_mfma_f32_16x16x32_bf16 v[96:99], a[64:67], v[52:55], v[96:99]
v_exp_f32 v36, v36
v_mfma_f32_16x16x32_bf16 v[100:103], a[68:71], v[52:55], v[100:103]
ds_read_b128 a[104:107], v5 offset:26624
s_add_u32 s52, s52, 0x8000
s_and_b32 s52, s52, 0x1ffff
v_add_f32 v18, v18, v36
v_mfma_f32_16x16x32_bf16 v[104:107], a[72:75], v[52:55], v[104:107]
v_exp_f32 v37, v37
v_mfma_f32_16x16x32_bf16 v[108:111], a[76:79], v[52:55], v[108:111]
v_add_f32 v19, v19, v37
v_cvt_pk_bf16_f32 v50, v36, v37
v_mfma_f32_16x16x32_bf16 v[112:115], a[80:83], v[52:55], v[112:115]
v_exp_f32 v38, v38
v_mfma_f32_16x16x32_bf16 v[116:119], a[84:87], v[52:55], v[116:119]
ds_read_b128 a[108:111], v5 offset:27648
v_add_f32 v18, v18, v38
v_mfma_f32_16x16x32_bf16 v[120:123], a[88:91], v[52:55], v[120:123]
v_exp_f32 v39, v39
v_mfma_f32_16x16x32_bf16 v[124:127], a[92:95], v[52:55], v[124:127]
v_add_f32 v19, v19, v39
v_cvt_pk_bf16_f32 v51, v38, v39
s_waitcnt vmcnt(8)
s_barrier
v_mfma_f32_16x16x32_bf16 v[32:35], a[32:35], v[192:195], v[8:11]
ds_read_b128 a[112:115], v5 offset:28672
v_exp_f32 v40, v40
v_mfma_f32_16x16x32_bf16 v[32:35], a[36:39], v[196:199], v[32:35]
v_add_f32 v22, v22, v40
v_mfma_f32_16x16x32_bf16 v[32:35], a[40:43], v[200:203], v[32:35]
s_add_u32 m0, s52, 0
v_exp_f32 v41, v41
s_waitcnt lgkmcnt(1)
v_mfma_f32_16x16x32_bf16 v[32:35], a[44:47], v[204:207], v[32:35]
ds_read_b128 a[116:119], v5 offset:29696
v_add_f32 v23, v23, v41
v_mfma_f32_16x16x32_bf16 v[128:131], a[64:67], v[48:51], v[128:131]
v_exp_f32 v42, v42
v_mfma_f32_16x16x32_bf16 v[132:135], a[68:71], v[48:51], v[132:135]
v_add_f32 v22, v22, v42
v_mfma_f32_16x16x32_bf16 v[136:139], a[72:75], v[48:51], v[136:139]
ds_read_b128 a[0:3], v6 offset:0
v_exp_f32 v43, v43
v_mfma_f32_16x16x32_bf16 v[140:143], a[76:79], v[48:51], v[140:143]
v_add_f32 v23, v23, v43
v_cvt_pk_bf16_f32 v52, v40, v41
v_mfma_f32_16x16x32_bf16 v[144:147], a[80:83], v[48:51], v[144:147]
v_cvt_pk_bf16_f32 v53, v42, v43
v_mfma_f32_16x16x32_bf16 v[148:151], a[84:87], v[48:51], v[148:151]
ds_read_b128 a[16:19], v6 offset:4096
v_exp_f32 v44, v44
v_mfma_f32_16x16x32_bf16 v[152:155], a[88:91], v[48:51], v[152:155]
v_add_f32 v22, v22, v44
v_mfma_f32_16x16x32_bf16 v[156:159], a[92:95], v[48:51], v[156:159]
v_exp_f32 v45, v45
s_nop 0
v_add_f32 v23, v23, v45
v_exp_f32 v46, v46
s_nop 0
v_add_f32 v22, v22, v46
v_exp_f32 v47, v47
s_nop 0
v_add_f32 v23, v23, v47
v_cvt_pk_bf16_f32 v54, v44, v45
v_cvt_pk_bf16_f32 v55, v46, v47
v_mfma_f32_16x16x32_bf16 v[40:43], a[32:35], v[208:211], 0
ds_read_b128 a[120:123], v5 offset:30720
v_exp_f32 v32, v32
v_mfma_f32_16x16x32_bf16 v[44:47], a[48:51], v[208:211], v[8:11]
v_add_f32 v16, v16, v32
v_mfma_f32_16x16x32_bf16 v[40:43], a[36:39], v[212:215], v[40:43]
v_exp_f32 v33, v33
v_mfma_f32_16x16x32_bf16 v[44:47], a[52:55], v[212:215], v[44:47]
buffer_load_dwordx4 v4, s[48:51], s53 offen offset:0 sc1 lds
v_add_f32 v17, v17, v33
v_mfma_f32_16x16x32_bf16 v[40:43], a[40:43], v[216:219], v[40:43]
v_exp_f32 v34, v34
v_mfma_f32_16x16x32_bf16 v[44:47], a[56:59], v[216:219], v[44:47]
v_add_f32 v16, v16, v34
v_mfma_f32_16x16x32_bf16 v[40:43], a[44:47], v[220:223], v[40:43]
ds_read_b128 a[124:127], v5 offset:31744
v_exp_f32 v35, v35
v_mfma_f32_16x16x32_bf16 v[44:47], a[60:63], v[220:223], v[44:47]
v_add_f32 v17, v17, v35
v_cvt_pk_bf16_f32 v48, v32, v33
v_mfma_f32_16x16x32_bf16 v[160:163], a[64:67], v[52:55], v[160:163]
v_cvt_pk_bf16_f32 v49, v34, v35
v_mov_b32 v50, 0
v_mfma_f32_16x16x32_bf16 v[164:167], a[68:71], v[52:55], v[164:167]
ds_read_b128 a[4:7], v6 offset:1024
v_mov_b32 v51, 0
v_mfma_f32_16x16x32_bf16 v[168:171], a[72:75], v[52:55], v[168:171]
v_mfma_f32_16x16x32_bf16 v[172:175], a[76:79], v[52:55], v[172:175]
v_mfma_f32_16x16x32_bf16 v[176:179], a[80:83], v[52:55], v[176:179]
ds_read_b128 a[20:23], v6 offset:5120
v_mfma_f32_16x16x32_bf16 v[180:183], a[84:87], v[52:55], v[180:183]
v_mfma_f32_16x16x32_bf16 v[184:187], a[88:91], v[52:55], v[184:187]
v_mfma_f32_16x16x32_bf16 v[188:191], a[92:95], v[52:55], v[188:191]
v_mfma_f32_16x16x32_bf16 v[32:35], a[32:35], v[224:227], 0
v_exp_f32 v40, v40
v_mfma_f32_16x16x32_bf16 v[36:39], a[48:51], v[224:227], 0
ds_read_b128 a[8:11], v6 offset:2048
v_add_f32 v20, v20, v40
v_mfma_f32_16x16x32_bf16 v[32:35], a[36:39], v[228:231], v[32:35]
v_exp_f32 v41, v41
v_mfma_f32_16x16x32_bf16 v[36:39], a[52:55], v[228:231], v[36:39]
v_add_f32 v21, v21, v41
v_cvt_pk_bf16_f32 v52, v40, v41
v_mfma_f32_16x16x32_bf16 v[32:35], a[40:43], v[232:235], v[32:35]
v_exp_f32 v42, v42
v_mfma_f32_16x16x32_bf16 v[36:39], a[56:59], v[232:235], v[36:39]
buffer_load_dwordx4 v4, s[48:51], s53 offen offset:1024 sc1 lds
v_add_f32 v20, v20, v42
v_mfma_f32_16x16x32_bf16 v[32:35], a[44:47], v[236:239], v[32:35]
v_exp_f32 v43, v43
v_mfma_f32_16x16x32_bf16 v[36:39], a[60:63], v[236:239], v[36:39]
v_add_f32 v21, v21, v43
v_cvt_pk_bf16_f32 v53, v42, v43
v_mfma_f32_16x16x32_bf16 v[64:67], a[96:99], v[48:51], v[64:67]
v_exp_f32 v44, v44
v_mfma_f32_16x16x32_bf16 v[68:71], a[100:103], v[48:51], v[68:71]
ds_read_b128 a[24:27], v6 offset:6144
v_add_f32 v20, v20, v44
v_mfma_f32_16x16x32_bf16 v[72:75], a[104:107], v[48:51], v[72:75]
v_exp_f32 v45, v45
v_mfma_f32_16x16x32_bf16 v[76:79], a[108:111], v[48:51], v[76:79]
v_add_f32 v21, v21, v45
v_cvt_pk_bf16_f32 v54, v44, v45
s_waitcnt lgkmcnt(1)
v_mfma_f32_16x16x32_bf16 v[80:83], a[112:115], v[48:51], v[80:83]
v_exp_f32 v46, v46
v_mfma_f32_16x16x32_bf16 v[84:87], a[116:119], v[48:51], v[84:87]
v_add_f32 v20, v20, v46
v_mfma_f32_16x16x32_bf16 v[88:91], a[120:123], v[48:51], v[88:91]
v_exp_f32 v47, v47
v_mfma_f32_16x16x32_bf16 v[92:95], a[124:127], v[48:51], v[92:95]
v_add_f32 v21, v21, v47
v_cvt_pk_bf16_f32 v55, v46, v47
v_mfma_f32_16x16x32_bf16 v[40:43], a[32:35], v[240:243], 0
ds_read_b128 a[12:15], v6 offset:3072
v_exp_f32 v32, v32
v_mfma_f32_16x16x32_bf16 v[44:47], a[48:51], v[240:243], 0
v_exp_f32 v33, v33
v_mfma_f32_16x16x32_bf16 v[40:43], a[36:39], v[244:247], v[40:43]
v_exp_f32 v34, v34
v_mfma_f32_16x16x32_bf16 v[44:47], a[52:55], v[244:247], v[44:47]
v_exp_f32 v35, v35
v_mfma_f32_16x16x32_bf16 v[40:43], a[40:43], v[248:251], v[40:43]
buffer_load_dwordx4 v4, s[48:51], s53 offen offset:2048 sc1 lds
v_cvt_pk_bf16_f32 v48, v32, v33
v_cvt_pk_bf16_f32 v49, v34, v35
v_mfma_f32_16x16x32_bf16 v[44:47], a[56:59], v[248:251], v[44:47]
v_exp_f32 v36, v36
v_mfma_f32_16x16x32_bf16 v[40:43], a[44:47], v[252:255], v[40:43]
v_exp_f32 v37, v37
v_mfma_f32_16x16x32_bf16 v[44:47], a[60:63], v[252:255], v[44:47]
v_exp_f32 v38, v38
v_mfma_f32_16x16x32_bf16 v[96:99], a[96:99], v[52:55], v[96:99]
ds_read_b128 a[28:31], v6 offset:7168
v_exp_f32 v39, v39
v_mfma_f32_16x16x32_bf16 v[100:103], a[100:103], v[52:55], v[100:103]
v_cvt_pk_bf16_f32 v50, v36, v37
v_cvt_pk_bf16_f32 v51, v38, v39
v_mfma_f32_16x16x32_bf16 v[104:107], a[104:107], v[52:55], v[104:107]
v_mfma_f32_16x16x32_bf16 v[108:111], a[108:111], v[52:55], v[108:111]
v_mfma_f32_16x16x32_bf16 v[112:115], a[112:115], v[52:55], v[112:115]
buffer_load_dwordx4 v4, s[48:51], s53 offen offset:3072 sc1 lds
v_mfma_f32_16x16x32_bf16 v[116:119], a[116:119], v[52:55], v[116:119]
v_mfma_f32_16x16x32_bf16 v[120:123], a[120:123], v[52:55], v[120:123]
v_mfma_f32_16x16x32_bf16 v[124:127], a[124:127], v[52:55], v[124:127]
v_mov_b32 v5, v6
v_add_u32 v6, 0x8000, v6
v_and_b32 v6, 0x1ffff, v6
s_waitcnt lgkmcnt(0)
v_mbcnt_lo_u32_b32 v12, -1, 0
v_mbcnt_hi_u32_b32 v12, -1, v12
v_lshlrev_b32 v12, 4, v12
s_mov_b32 s27, 0x4000
buffer_load_dwordx4 v[0:3], v12, s[40:43], s27 offen offset:0 sc1
s_nop 7
v_add_f32 v12, v16, v17
v_mov_b32 v13, v12
s_nop 1
v_permlane16_swap_b32 v12, v13
v_add_f32 v12, v12, v13
v_mov_b32 v13, v12
s_nop 1
v_permlane32_swap_b32 v12, v13
v_add_f32 v12, v12, v13
v_rcp_f32 v12, v12
s_nop 0
v_pk_mul_f32 v[64:65], v[64:65], v[12:13] op_sel_hi:[1,0]
v_pk_mul_f32 v[66:67], v[66:67], v[12:13] op_sel_hi:[1,0]
v_pk_mul_f32 v[68:69], v[68:69], v[12:13] op_sel_hi:[1,0]
v_pk_mul_f32 v[70:71], v[70:71], v[12:13] op_sel_hi:[1,0]
v_pk_mul_f32 v[72:73], v[72:73], v[12:13] op_sel_hi:[1,0]
v_pk_mul_f32 v[74:75], v[74:75], v[12:13] op_sel_hi:[1,0]
v_pk_mul_f32 v[76:77], v[76:77], v[12:13] op_sel_hi:[1,0]
v_pk_mul_f32 v[78:79], v[78:79], v[12:13] op_sel_hi:[1,0]
v_pk_mul_f32 v[80:81], v[80:81], v[12:13] op_sel_hi:[1,0]
v_pk_mul_f32 v[82:83], v[82:83], v[12:13] op_sel_hi:[1,0]
v_pk_mul_f32 v[84:85], v[84:85], v[12:13] op_sel_hi:[1,0]
v_pk_mul_f32 v[86:87], v[86:87], v[12:13] op_sel_hi:[1,0]
v_pk_mul_f32 v[88:89], v[88:89], v[12:13] op_sel_hi:[1,0]
v_pk_mul_f32 v[90:91], v[90:91], v[12:13] op_sel_hi:[1,0]
v_pk_mul_f32 v[92:93], v[92:93], v[12:13] op_sel_hi:[1,0]
v_pk_mul_f32 v[94:95], v[94:95], v[12:13] op_sel_hi:[1,0]
v_cvt_pk_bf16_f32 v64, v64, v65
v_cvt_pk_bf16_f32 v65, v66, v67
v_cvt_pk_bf16_f32 v66, v68, v69
v_cvt_pk_bf16_f32 v67, v70, v71
s_add_u32 s27, s55, 0
buffer_store_dwordx4 v[64:67], v4, s[36:39], s27 offen offset:0 sc1
v_cvt_pk_bf16_f32 v72, v72, v73
v_cvt_pk_bf16_f32 v73, v74, v75
v_cvt_pk_bf16_f32 v74, v76, v77
v_cvt_pk_bf16_f32 v75, v78, v79
s_add_u32 s27, s55, 0x1000
buffer_store_dwordx4 v[72:75], v4, s[36:39], s27 offen offset:0 sc1
v_cvt_pk_bf16_f32 v80, v80, v81
v_cvt_pk_bf16_f32 v81, v82, v83
v_cvt_pk_bf16_f32 v82, v84, v85
v_cvt_pk_bf16_f32 v83, v86, v87
s_add_u32 s27, s55, 0x2000
buffer_store_dwordx4 v[80:83], v4, s[36:39], s27 offen offset:0 sc1
v_cvt_pk_bf16_f32 v88, v88, v89
v_cvt_pk_bf16_f32 v89, v90, v91
v_cvt_pk_bf16_f32 v90, v92, v93
v_cvt_pk_bf16_f32 v91, v94, v95
s_add_u32 s27, s55, 0x3000
buffer_store_dwordx4 v[88:91], v4, s[36:39], s27 offen offset:0 sc1
v_add_f32 v12, v20, v21
v_mov_b32 v13, v12
s_nop 1
v_permlane16_swap_b32 v12, v13
v_add_f32 v12, v12, v13
v_mov_b32 v13, v12
s_nop 1
v_permlane32_swap_b32 v12, v13
v_add_f32 v12, v12, v13
v_rcp_f32 v12, v12
s_nop 0
v_pk_mul_f32 v[96:97], v[96:97], v[12:13] op_sel_hi:[1,0]
v_pk_mul_f32 v[98:99], v[98:99], v[12:13] op_sel_hi:[1,0]
v_pk_mul_f32 v[100:101], v[100:101], v[12:13] op_sel_hi:[1,0]
v_pk_mul_f32 v[102:103], v[102:103], v[12:13] op_sel_hi:[1,0]
v_pk_mul_f32 v[104:105], v[104:105], v[12:13] op_sel_hi:[1,0]
v_pk_mul_f32 v[106:107], v[106:107], v[12:13] op_sel_hi:[1,0]
v_pk_mul_f32 v[108:109], v[108:109], v[12:13] op_sel_hi:[1,0]
v_pk_mul_f32 v[110:111], v[110:111], v[12:13] op_sel_hi:[1,0]
v_pk_mul_f32 v[112:113], v[112:113], v[12:13] op_sel_hi:[1,0]
v_pk_mul_f32 v[114:115], v[114:115], v[12:13] op_sel_hi:[1,0]
v_pk_mul_f32 v[116:117], v[116:117], v[12:13] op_sel_hi:[1,0]
v_pk_mul_f32 v[118:119], v[118:119], v[12:13] op_sel_hi:[1,0]
v_pk_mul_f32 v[120:121], v[120:121], v[12:13] op_sel_hi:[1,0]
v_pk_mul_f32 v[122:123], v[122:123], v[12:13] op_sel_hi:[1,0]
v_pk_mul_f32 v[124:125], v[124:125], v[12:13] op_sel_hi:[1,0]
v_pk_mul_f32 v[126:127], v[126:127], v[12:13] op_sel_hi:[1,0]
v_cvt_pk_bf16_f32 v96, v96, v97
v_cvt_pk_bf16_f32 v97, v98, v99
v_cvt_pk_bf16_f32 v98, v100, v101
v_cvt_pk_bf16_f32 v99, v102, v103
s_add_u32 s27, s55, 0
s_add_u32 s27, s27, s69
buffer_store_dwordx4 v[96:99], v4, s[36:39], s27 offen offset:0 sc1
v_cvt_pk_bf16_f32 v104, v104, v105
v_cvt_pk_bf16_f32 v105, v106, v107
v_cvt_pk_bf16_f32 v106, v108, v109
v_cvt_pk_bf16_f32 v107, v110, v111
s_add_u32 s27, s55, 0x1000
s_add_u32 s27, s27, s69
buffer_store_dwordx4 v[104:107], v4, s[36:39], s27 offen offset:0 sc1
v_cvt_pk_bf16_f32 v112, v112, v113
v_cvt_pk_bf16_f32 v113, v114, v115
v_cvt_pk_bf16_f32 v114, v116, v117
v_cvt_pk_bf16_f32 v115, v118, v119
s_add_u32 s27, s55, 0x2000
s_add_u32 s27, s27, s69
buffer_store_dwordx4 v[112:115], v4, s[36:39], s27 offen offset:0 sc1
v_cvt_pk_bf16_f32 v120, v120, v121
v_cvt_pk_bf16_f32 v121, v122, v123
v_cvt_pk_bf16_f32 v122, v124, v125
v_cvt_pk_bf16_f32 v123, v126, v127
s_add_u32 s27, s55, 0x3000
s_add_u32 s27, s27, s69
buffer_store_dwordx4 v[120:123], v4, s[36:39], s27 offen offset:0 sc1
s_nop 1
B_D1end_%=:
s_lshl_b32 s27, s26, 2
s_sub_u32 s57, 28, s27
s_cmp_eq_u32 s57, 0
s_cbranch_scc1 B_D2_%=
B_L2_%=:
v_mfma_f32_16x16x32_bf16 v[32:35], a[0:3], v[224:227], 0
v_exp_f32 v40, v40
s_waitcnt lgkmcnt(3)
v_mfma_f32_16x16x32_bf16 v[36:39], a[16:19], v[224:227], 0
v_exp_f32 v41, v41
v_mfma_f32_16x16x32_bf16 v[32:35], a[4:7], v[228:231], v[32:35]
v_exp_f32 v42, v42
v_mfma_f32_16x16x32_bf16 v[36:39], a[20:23], v[228:231], v[36:39]
ds_read_b128 a[32:35], v5 offset:8192
s_add_u32 m0, s52, 0x1000
v_exp_f32 v43, v43
v_mfma_f32_16x16x32_bf16 v[32:35], a[8:11], v[232:235], v[32:35]
ds_read_b128 a[48:51], v5 offset:12288
v_cvt_pk_bf16_f32 v52, v40, v41
v_mfma_f32_16x16x32_bf16 v[36:39], a[24:27], v[232:235], v[36:39]
v_exp_f32 v44, v44
v_mfma_f32_16x16x32_bf16 v[32:35], a[12:15], v[236:239], v[32:35]
v_exp_f32 v45, v45
v_mfma_f32_16x16x32_bf16 v[36:39], a[28:31], v[236:239], v[36:39]
v_exp_f32 v46, v46
v_mfma_f32_16x16x32_bf16 v[128:131], a[96:99], v[48:51], v[128:131]
v_exp_f32 v47, v47
v_mfma_f32_16x16x32_bf16 v[132:135], a[100:103], v[48:51], v[132:135]
ds_read_b128 a[36:39], v5 offset:9216
v_cvt_pk_bf16_f32 v53, v42, v43
v_mfma_f32_16x16x32_bf16 v[136:139], a[104:107], v[48:51], v[136:139]
buffer_load_dwordx4 v4, s[48:51], s54 offen offset:0 sc1 lds
v_cvt_pk_bf16_f32 v54, v44, v45
v_mfma_f32_16x16x32_bf16 v[140:143], a[108:111], v[48:51], v[140:143]
ds_read_b128 a[52:55], v5 offset:13312
v_cvt_pk_bf16_f32 v55, v46, v47
v_mfma_f32_16x16x32_bf16 v[144:147], a[112:115], v[48:51], v[144:147]
ds_read_b128 a[40:43], v5 offset:10240
s_waitcnt lgkmcnt(3)
v_mfma_f32_16x16x32_bf16 v[148:151], a[116:119], v[48:51], v[148:151]
ds_read_b128 a[56:59], v5 offset:14336
v_mfma_f32_16x16x32_bf16 v[152:155], a[120:123], v[48:51], v[152:155]
buffer_load_dwordx4 v4, s[48:51], s54 offen offset:1024 sc1 lds
v_mfma_f32_16x16x32_bf16 v[156:159], a[124:127], v[48:51], v[156:159]
ds_read_b128 a[44:47], v5 offset:11264
v_mfma_f32_16x16x32_bf16 v[24:27], v[56:59], v[48:51], v[24:27]
ds_read_b128 a[60:63], v5 offset:15360
v_mfma_f32_16x16x32_bf16 v[40:43], a[0:3], v[240:243], 0
v_exp_f32 v32, v32
v_mfma_f32_16x16x32_bf16 v[44:47], a[16:19], v[240:243], 0
v_exp_f32 v33, v33
v_mfma_f32_16x16x32_bf16 v[40:43], a[4:7], v[244:247], v[40:43]
v_exp_f32 v34, v34
v_mfma_f32_16x16x32_bf16 v[44:47], a[20:23], v[244:247], v[44:47]
ds_read_b128 a[64:67], v5 offset:16384
v_exp_f32 v35, v35
v_mfma_f32_16x16x32_bf16 v[40:43], a[8:11], v[248:251], v[40:43]
buffer_load_dwordx4 v4, s[48:51], s54 offen offset:2048 sc1 lds
v_cvt_pk_bf16_f32 v48, v32, v33
v_mfma_f32_16x16x32_bf16 v[44:47], a[24:27], v[248:251], v[44:47]
v_exp_f32 v36, v36
v_mfma_f32_16x16x32_bf16 v[40:43], a[12:15], v[252:255], v[40:43]
v_exp_f32 v37, v37
v_mfma_f32_16x16x32_bf16 v[44:47], a[28:31], v[252:255], v[44:47]
v_exp_f32 v38, v38
v_mfma_f32_16x16x32_bf16 v[160:163], a[96:99], v[52:55], v[160:163]
v_exp_f32 v39, v39
v_mfma_f32_16x16x32_bf16 v[164:167], a[100:103], v[52:55], v[164:167]
ds_read_b128 a[68:71], v5 offset:17408
v_cvt_pk_bf16_f32 v49, v34, v35
v_mfma_f32_16x16x32_bf16 v[168:171], a[104:107], v[52:55], v[168:171]
ds_read_b128 a[72:75], v5 offset:18432
v_cvt_pk_bf16_f32 v50, v36, v37
v_mfma_f32_16x16x32_bf16 v[172:175], a[108:111], v[52:55], v[172:175]
buffer_load_dwordx4 v4, s[48:51], s54 offen offset:3072 sc1 lds
v_cvt_pk_bf16_f32 v51, v38, v39
v_mfma_f32_16x16x32_bf16 v[176:179], a[112:115], v[52:55], v[176:179]
ds_read_b128 a[76:79], v5 offset:19456
v_mfma_f32_16x16x32_bf16 v[180:183], a[116:119], v[52:55], v[180:183]
ds_read_b128 a[80:83], v5 offset:20480
s_add_u32 s53, s53, 0x8000
s_add_u32 s54, s54, 0x8000
v_mfma_f32_16x16x32_bf16 v[184:187], a[120:123], v[52:55], v[184:187]
ds_read_b128 a[84:87], v5 offset:21504
v_mfma_f32_16x16x32_bf16 v[188:191], a[124:127], v[52:55], v[188:191]
ds_read_b128 a[88:91], v5 offset:22528
s_add_u32 s52, s52, 0x8000
s_and_b32 s52, s52, 0x1ffff
v_mfma_f32_16x16x32_bf16 v[28:31], v[56:59], v[52:55], v[28:31]
ds_read_b128 a[92:95], v5 offset:23552
s_waitcnt vmcnt(8)
s_barrier
s_waitcnt lgkmcnt(5)
v_mfma_f32_16x16x32_bf16 v[32:35], a[32:35], v[224:227], 0
v_exp_f32 v40, v40
v_mfma_f32_16x16x32_bf16 v[36:39], a[48:51], v[224:227], 0
v_exp_f32 v41, v41
v_mfma_f32_16x16x32_bf16 v[32:35], a[36:39], v[228:231], v[32:35]
v_exp_f32 v42, v42
v_mfma_f32_16x16x32_bf16 v[36:39], a[52:55], v[228:231], v[36:39]
v_exp_f32 v43, v43
v_mfma_f32_16x16x32_bf16 v[32:35], a[40:43], v[232:235], v[32:35]
ds_read_b128 a[0:3], v6 offset:0
v_cvt_pk_bf16_f32 v52, v40, v41
v_mfma_f32_16x16x32_bf16 v[36:39], a[56:59], v[232:235], v[36:39]
v_exp_f32 v44, v44
v_mfma_f32_16x16x32_bf16 v[32:35], a[44:47], v[236:239], v[32:35]
v_exp_f32 v45, v45
v_mfma_f32_16x16x32_bf16 v[36:39], a[60:63], v[236:239], v[36:39]
v_exp_f32 v46, v46
v_mfma_f32_16x16x32_bf16 v[128:131], a[64:67], v[48:51], v[128:131]
s_add_u32 m0, s52, 0
v_exp_f32 v47, v47
v_mfma_f32_16x16x32_bf16 v[132:135], a[68:71], v[48:51], v[132:135]
ds_read_b128 a[16:19], v6 offset:4096
v_cvt_pk_bf16_f32 v53, v42, v43
v_mfma_f32_16x16x32_bf16 v[136:139], a[72:75], v[48:51], v[136:139]
ds_read_b128 a[4:7], v6 offset:1024
v_cvt_pk_bf16_f32 v54, v44, v45
s_waitcnt lgkmcnt(2)
v_mfma_f32_16x16x32_bf16 v[140:143], a[76:79], v[48:51], v[140:143]
ds_read_b128 a[20:23], v6 offset:5120
v_cvt_pk_bf16_f32 v55, v46, v47
v_mfma_f32_16x16x32_bf16 v[144:147], a[80:83], v[48:51], v[144:147]
ds_read_b128 a[8:11], v6 offset:2048
v_mfma_f32_16x16x32_bf16 v[148:151], a[84:87], v[48:51], v[148:151]
buffer_load_dwordx4 v4, s[48:51], s53 offen offset:0 sc1 lds
v_mfma_f32_16x16x32_bf16 v[152:155], a[88:91], v[48:51], v[152:155]
ds_read_b128 a[24:27], v6 offset:6144
v_mfma_f32_16x16x32_bf16 v[156:159], a[92:95], v[48:51], v[156:159]
ds_read_b128 a[12:15], v6 offset:3072
v_mfma_f32_16x16x32_bf16 v[24:27], v[56:59], v[48:51], v[24:27]
ds_read_b128 a[28:31], v6 offset:7168
v_mfma_f32_16x16x32_bf16 v[40:43], a[32:35], v[240:243], 0
v_exp_f32 v32, v32
v_mfma_f32_16x16x32_bf16 v[44:47], a[48:51], v[240:243], 0
v_exp_f32 v33, v33
v_mfma_f32_16x16x32_bf16 v[40:43], a[36:39], v[244:247], v[40:43]
v_exp_f32 v34, v34
v_mfma_f32_16x16x32_bf16 v[44:47], a[52:55], v[244:247], v[44:47]
ds_read_b128 a[96:99], v5 offset:24576
v_exp_f32 v35, v35
v_mfma_f32_16x16x32_bf16 v[40:43], a[40:43], v[248:251], v[40:43]
buffer_load_dwordx4 v4, s[48:51], s53 offen offset:1024 sc1 lds
v_cvt_pk_bf16_f32 v48, v32, v33
v_mfma_f32_16x16x32_bf16 v[44:47], a[56:59], v[248:251], v[44:47]
v_exp_f32 v36, v36
v_mfma_f32_16x16x32_bf16 v[40:43], a[44:47], v[252:255], v[40:43]
v_exp_f32 v37, v37
v_mfma_f32_16x16x32_bf16 v[44:47], a[60:63], v[252:255], v[44:47]
v_exp_f32 v38, v38
v_mfma_f32_16x16x32_bf16 v[160:163], a[64:67], v[52:55], v[160:163]
ds_read_b128 a[100:103], v5 offset:25600
v_exp_f32 v39, v39
v_mfma_f32_16x16x32_bf16 v[164:167], a[68:71], v[52:55], v[164:167]
ds_read_b128 a[104:107], v5 offset:26624
v_cvt_pk_bf16_f32 v49, v34, v35
v_mfma_f32_16x16x32_bf16 v[168:171], a[72:75], v[52:55], v[168:171]
buffer_load_dwordx4 v4, s[48:51], s53 offen offset:2048 sc1 lds
v_cvt_pk_bf16_f32 v50, v36, v37
v_mfma_f32_16x16x32_bf16 v[172:175], a[76:79], v[52:55], v[172:175]
ds_read_b128 a[108:111], v5 offset:27648
v_cvt_pk_bf16_f32 v51, v38, v39
v_mfma_f32_16x16x32_bf16 v[176:179], a[80:83], v[52:55], v[176:179]
ds_read_b128 a[112:115], v5 offset:28672
v_mfma_f32_16x16x32_bf16 v[180:183], a[84:87], v[52:55], v[180:183]
ds_read_b128 a[116:119], v5 offset:29696
v_mfma_f32_16x16x32_bf16 v[184:187], a[88:91], v[52:55], v[184:187]
buffer_load_dwordx4 v4, s[48:51], s53 offen offset:3072 sc1 lds
v_mfma_f32_16x16x32_bf16 v[188:191], a[92:95], v[52:55], v[188:191]
ds_read_b128 a[120:123], v5 offset:30720
v_mfma_f32_16x16x32_bf16 v[28:31], v[56:59], v[52:55], v[28:31]
ds_read_b128 a[124:127], v5 offset:31744
v_mov_b32 v5, v6
v_add_u32 v6, 0x8000, v6
v_and_b32 v6, 0x1ffff, v6
s_sub_u32 s57, s57, 1
s_cmp_lg_u32 s57, 0
s_cbranch_scc1 B_L2_%=
s_waitcnt lgkmcnt(0)
B_D2_%=:
s_waitcnt vmcnt(8)
v_cmp_ne_u32 s[92:93], s98, v0
v_cmp_ne_u32 vcc, s98, v1
s_nop 1
s_or_b64 s[92:93], s[92:93], vcc
v_cmp_ne_u32 vcc, s98, v2
s_nop 1
s_or_b64 s[92:93], s[92:93], vcc
v_cmp_ne_u32 vcc, s98, v3
s_nop 1
s_or_b64 vcc, s[92:93], vcc
s_nop 1
s_cbranch_vccz M_ef_done_%=
v_mbcnt_lo_u32_b32 v12, -1, 0
v_mbcnt_hi_u32_b32 v12, -1, v12
v_lshlrev_b32 v12, 4, v12
s_mov_b32 s27, 0x4000
s_memrealtime s[94:95]
s_waitcnt lgkmcnt(0)
M_ef_%=:
buffer_load_dwordx4 v[0:3], v12, s[40:43], s27 offen offset:0 sc1
s_waitcnt vmcnt(0)
v_cmp_ne_u32 s[92:93], s98, v0
v_cmp_ne_u32 vcc, s98, v1
s_nop 1
s_or_b64 s[92:93], s[92:93], vcc
v_cmp_ne_u32 vcc, s98, v2
s_nop 1
s_or_b64 s[92:93], s[92:93], vcc
v_cmp_ne_u32 vcc, s98, v3
s_nop 1
s_or_b64 vcc, s[92:93], vcc
s_nop 1
s_cbranch_vccz M_ef_ok_%=
s_sleep 1
s_memrealtime s[96:97]
s_waitcnt lgkmcnt(0)
s_sub_u32 s29, s96, s94
s_cmp_lt_u32 s29, 0x1e8480
s_cbranch_scc1 M_ef_%=
M_ef_ok_%=:
M_ef_done_%=:
v_mbcnt_lo_u32_b32 v12, -1, 0
v_mbcnt_hi_u32_b32 v12, -1, v12
s_and_b32 s27, s23, 1
s_lshl_b32 s27, s27, 6
v_add_u32 v12, s27, v12
v_lshlrev_b32 v12, 4, v12
buffer_load_dwordx4 a[144:147], v12, s[84:87], 0 offen offset:0 sc1
buffer_load_dwordx4 a[148:151], v12, s[84:87], 0 offen offset:2048 sc1
s_add_u32 s60, s66, 0x4510000
s_addc_u32 s61, s67, 0
s_and_b32 s61, s61, 0xffff
s_mov_b32 s62, 0x400000
s_mov_b32 s63, 0x20000
s_and_b32 s27, s22, 7
s_lshr_b32 s28, s22, 3
s_and_b32 s28, s28, 3
s_lshl_b32 s28, s28, 3
s_add_u32 s27, s27, s28
s_lshl_b32 s27, s27, 5
s_lshl_b32 s28, s23, 3
s_add_u32 s27, s27, s28
s_lshl_b32 s58, s27, 14
s_lshl_b32 s27, s23, 20
s_lshr_b32 s28, s22, 5
s_lshl_b32 s28, s28, 12
s_add_u32 s59, s27, s28
s_lshr_b32 s27, s22, 5
s_lshr_b32 s28, s22, 3
s_and_b32 s28, s28, 3
s_lshl_b32 s28, s28, 1
s_add_u32 s27, s27, s28
s_mul_i32 s27, s27, 1
s_and_b32 s68, s27, 7
s_cmp_eq_u32 s23, 1
s_cbranch_scc1 B_D2w1_%=
s_cmp_eq_u32 s23, 2
s_cbranch_scc1 B_D2w2_%=
s_cmp_eq_u32 s23, 3
s_cbranch_scc1 B_D2w3_%=
v_mfma_f32_16x16x32_bf16 v[32:35], a[0:3], v[224:227], v[8:11]
ds_read_b128 a[64:67], v5 offset:16384
v_exp_f32 v40, v40
v_mfma_f32_16x16x32_bf16 v[32:35], a[4:7], v[228:231], v[32:35]
s_add_u32 s30, s68, 0
s_and_b32 s30, s30, 7
s_lshl_b32 s30, s30, 15
s_add_u32 s30, s30, s59
buffer_load_dwordx4 a[152:155], v4, s[60:63], s30 offen offset:0 sc1
v_exp_f32 v41, v41
v_mfma_f32_16x16x32_bf16 v[32:35], a[8:11], v[232:235], v[32:35]
buffer_load_dwordx4 a[156:159], v4, s[60:63], s30 offen offset:1024 sc1
v_exp_f32 v42, v42
v_mfma_f32_16x16x32_bf16 v[32:35], a[12:15], v[236:239], v[32:35]
buffer_load_dwordx4 a[160:163], v4, s[60:63], s30 offen offset:2048 sc1
v_exp_f32 v43, v43
v_mfma_f32_16x16x32_bf16 v[128:131], a[96:99], v[48:51], v[128:131]
ds_read_b128 a[68:71], v5 offset:17408
v_cvt_pk_bf16_f32 v52, v40, v41
v_cvt_pk_bf16_f32 v53, v42, v43
v_mfma_f32_16x16x32_bf16 v[132:135], a[100:103], v[48:51], v[132:135]
buffer_load_dwordx4 a[164:167], v4, s[60:63], s30 offen offset:3072 sc1
v_exp_f32 v44, v44
v_mfma_f32_16x16x32_bf16 v[136:139], a[104:107], v[48:51], v[136:139]
s_add_u32 s30, s68, 1
s_and_b32 s30, s30, 7
s_lshl_b32 s30, s30, 15
s_add_u32 s30, s30, s59
buffer_load_dwordx4 a[168:171], v4, s[60:63], s30 offen offset:0 sc1
v_exp_f32 v45, v45
v_mfma_f32_16x16x32_bf16 v[140:143], a[108:111], v[48:51], v[140:143]
buffer_load_dwordx4 a[172:175], v4, s[60:63], s30 offen offset:1024 sc1
v_exp_f32 v46, v46
v_mfma_f32_16x16x32_bf16 v[144:147], a[112:115], v[48:51], v[144:147]
ds_read_b128 a[72:75], v5 offset:18432
v_exp_f32 v47, v47
v_mfma_f32_16x16x32_bf16 v[148:151], a[116:119], v[48:51], v[148:151]
buffer_load_dwordx4 a[176:179], v4, s[60:63], s30 offen offset:2048 sc1
v_cvt_pk_bf16_f32 v54, v44, v45
v_cvt_pk_bf16_f32 v55, v46, v47
v_mfma_f32_16x16x32_bf16 v[152:155], a[120:123], v[48:51], v[152:155]
buffer_load_dwordx4 a[180:183], v4, s[60:63], s30 offen offset:3072 sc1
v_mfma_f32_16x16x32_bf16 v[156:159], a[124:127], v[48:51], v[156:159]
s_add_u32 s30, s68, 2
s_and_b32 s30, s30, 7
s_lshl_b32 s30, s30, 15
s_add_u32 s30, s30, s59
buffer_load_dwordx4 a[184:187], v4, s[60:63], s30 offen offset:0 sc1
v_mfma_f32_16x16x32_bf16 v[24:27], v[56:59], v[48:51], v[24:27]
ds_read_b128 a[76:79], v5 offset:19456
buffer_load_dwordx4 a[188:191], v4, s[60:63], s30 offen offset:1024 sc1
buffer_load_dwordx4 a[192:195], v4, s[60:63], s30 offen offset:2048 sc1
buffer_load_dwordx4 a[196:199], v4, s[60:63], s30 offen offset:3072 sc1
v_mfma_f32_16x16x32_bf16 v[40:43], a[0:3], v[240:243], 0
ds_read_b128 a[80:83], v5 offset:20480
v_exp_f32 v32, v32
v_mfma_f32_16x16x32_bf16 v[44:47], a[16:19], v[240:243], v[8:11]
s_add_u32 s30, s68, 3
s_and_b32 s30, s30, 7
s_lshl_b32 s30, s30, 15
s_add_u32 s30, s30, s59
buffer_load_dwordx4 a[200:203], v4, s[60:63], s30 offen offset:0 sc1
v_exp_f32 v33, v33
v_mfma_f32_16x16x32_bf16 v[40:43], a[4:7], v[244:247], v[40:43]
buffer_load_dwordx4 a[204:207], v4, s[60:63], s30 offen offset:1024 sc1
v_exp_f32 v34, v34
v_mfma_f32_16x16x32_bf16 v[44:47], a[20:23], v[244:247], v[44:47]
buffer_load_dwordx4 a[208:211], v4, s[60:63], s30 offen offset:2048 sc1
v_exp_f32 v35, v35
v_mfma_f32_16x16x32_bf16 v[40:43], a[8:11], v[248:251], v[40:43]
ds_read_b128 a[84:87], v5 offset:21504
v_cvt_pk_bf16_f32 v48, v32, v33
v_cvt_pk_bf16_f32 v49, v34, v35
v_mfma_f32_16x16x32_bf16 v[44:47], a[24:27], v[248:251], v[44:47]
buffer_load_dwordx4 a[212:215], v4, s[60:63], s30 offen offset:3072 sc1
v_mov_b32 v50, 0
v_mov_b32 v51, 0
v_mfma_f32_16x16x32_bf16 v[40:43], a[12:15], v[252:255], v[40:43]
s_add_u32 s30, s68, 4
s_and_b32 s30, s30, 7
s_lshl_b32 s30, s30, 15
s_add_u32 s30, s30, s59
buffer_load_dwordx4 a[216:219], v4, s[60:63], s30 offen offset:0 sc1
v_mfma_f32_16x16x32_bf16 v[44:47], a[28:31], v[252:255], v[44:47]
buffer_load_dwordx4 a[220:223], v4, s[60:63], s30 offen offset:1024 sc1
v_mfma_f32_16x16x32_bf16 v[160:163], a[96:99], v[52:55], v[160:163]
ds_read_b128 a[88:91], v5 offset:22528
v_mfma_f32_16x16x32_bf16 v[164:167], a[100:103], v[52:55], v[164:167]
buffer_load_dwordx4 a[224:227], v4, s[60:63], s30 offen offset:2048 sc1
v_mfma_f32_16x16x32_bf16 v[168:171], a[104:107], v[52:55], v[168:171]
buffer_load_dwordx4 a[228:231], v4, s[60:63], s30 offen offset:3072 sc1
v_mfma_f32_16x16x32_bf16 v[172:175], a[108:111], v[52:55], v[172:175]
s_add_u32 s30, s68, 5
s_and_b32 s30, s30, 7
s_lshl_b32 s30, s30, 15
s_add_u32 s30, s30, s59
buffer_load_dwordx4 a[232:235], v4, s[60:63], s30 offen offset:0 sc1
v_mfma_f32_16x16x32_bf16 v[176:179], a[112:115], v[52:55], v[176:179]
ds_read_b128 a[92:95], v5 offset:23552
v_mfma_f32_16x16x32_bf16 v[180:183], a[116:119], v[52:55], v[180:183]
buffer_load_dwordx4 a[236:239], v4, s[60:63], s30 offen offset:1024 sc1
v_mfma_f32_16x16x32_bf16 v[184:187], a[120:123], v[52:55], v[184:187]
buffer_load_dwordx4 a[240:243], v4, s[60:63], s30 offen offset:2048 sc1
v_mfma_f32_16x16x32_bf16 v[188:191], a[124:127], v[52:55], v[188:191]
buffer_load_dwordx4 a[244:247], v4, s[60:63], s30 offen offset:3072 sc1
v_mfma_f32_16x16x32_bf16 v[28:31], v[56:59], v[52:55], v[28:31]
s_waitcnt vmcnt(30)
s_barrier
s_waitcnt lgkmcnt(1)
v_mfma_f32_16x16x32_bf16 v[128:131], a[64:67], v[48:51], v[128:131]
v_exp_f32 v40, v40
v_mfma_f32_16x16x32_bf16 v[132:135], a[68:71], v[48:51], v[132:135]
v_exp_f32 v41, v41
v_mfma_f32_16x16x32_bf16 v[136:139], a[72:75], v[48:51], v[136:139]
v_exp_f32 v42, v42
v_mfma_f32_16x16x32_bf16 v[140:143], a[76:79], v[48:51], v[140:143]
v_exp_f32 v43, v43
v_mfma_f32_16x16x32_bf16 v[144:147], a[80:83], v[48:51], v[144:147]
v_cvt_pk_bf16_f32 v52, v40, v41
v_cvt_pk_bf16_f32 v53, v42, v43
v_mfma_f32_16x16x32_bf16 v[148:151], a[84:87], v[48:51], v[148:151]
v_exp_f32 v44, v44
v_mfma_f32_16x16x32_bf16 v[152:155], a[88:91], v[48:51], v[152:155]
v_exp_f32 v45, v45
s_waitcnt lgkmcnt(0)
v_mfma_f32_16x16x32_bf16 v[156:159], a[92:95], v[48:51], v[156:159]
v_exp_f32 v46, v46
v_mfma_f32_16x16x32_bf16 v[24:27], v[56:59], v[48:51], v[24:27]
v_exp_f32 v47, v47
v_cvt_pk_bf16_f32 v54, v44, v45
v_cvt_pk_bf16_f32 v55, v46, v47
s_nop 1
v_mfma_f32_16x16x32_bf16 v[160:163], a[64:67], v[52:55], v[160:163]
v_mfma_f32_16x16x32_bf16 v[164:167], a[68:71], v[52:55], v[164:167]
v_mfma_f32_16x16x32_bf16 v[168:171], a[72:75], v[52:55], v[168:171]
v_mfma_f32_16x16x32_bf16 v[172:175], a[76:79], v[52:55], v[172:175]
v_mfma_f32_16x16x32_bf16 v[176:179], a[80:83], v[52:55], v[176:179]
v_mfma_f32_16x16x32_bf16 v[180:183], a[84:87], v[52:55], v[180:183]
v_mfma_f32_16x16x32_bf16 v[184:187], a[88:91], v[52:55], v[184:187]
v_mfma_f32_16x16x32_bf16 v[188:191], a[92:95], v[52:55], v[188:191]
v_mfma_f32_16x16x32_bf16 v[28:31], v[56:59], v[52:55], v[28:31]
s_waitcnt lgkmcnt(0)
s_nop 7
v_add_f32 v12, v18, v19
v_mov_b32 v13, v12
s_nop 1
v_permlane16_swap_b32 v12, v13
v_add_f32 v12, v12, v13
v_mov_b32 v13, v12
s_nop 1
v_permlane32_swap_b32 v12, v13
v_add_f32 v12, v12, v13
v_add_f32 v12, v12, v24
v_rcp_f32 v12, v12
s_nop 0
v_pk_mul_f32 v[128:129], v[128:129], v[12:13] op_sel_hi:[1,0]
v_pk_mul_f32 v[130:131], v[130:131], v[12:13] op_sel_hi:[1,0]
v_pk_mul_f32 v[132:133], v[132:133], v[12:13] op_sel_hi:[1,0]
v_pk_mul_f32 v[134:135], v[134:135], v[12:13] op_sel_hi:[1,0]
v_pk_mul_f32 v[136:137], v[136:137], v[12:13] op_sel_hi:[1,0]
v_pk_mul_f32 v[138:139], v[138:139], v[12:13] op_sel_hi:[1,0]
v_pk_mul_f32 v[140:141], v[140:141], v[12:13] op_sel_hi:[1,0]
v_pk_mul_f32 v[142:143], v[142:143], v[12:13] op_sel_hi:[1,0]
v_pk_mul_f32 v[144:145], v[144:145], v[12:13] op_sel_hi:[1,0]
v_pk_mul_f32 v[146:147], v[146:147], v[12:13] op_sel_hi:[1,0]
v_pk_mul_f32 v[148:149], v[148:149], v[12:13] op_sel_hi:[1,0]
v_pk_mul_f32 v[150:151], v[150:151], v[12:13] op_sel_hi:[1,0]
v_pk_mul_f32 v[152:153], v[152:153], v[12:13] op_sel_hi:[1,0]
v_pk_mul_f32 v[154:155], v[154:155], v[12:13] op_sel_hi:[1,0]
v_pk_mul_f32 v[156:157], v[156:157], v[12:13] op_sel_hi:[1,0]
v_pk_mul_f32 v[158:159], v[158:159], v[12:13] op_sel_hi:[1,0]
v_cvt_pk_bf16_f32 v128, v128, v129
v_cvt_pk_bf16_f32 v129, v130, v131
v_cvt_pk_bf16_f32 v130, v132, v133
v_cvt_pk_bf16_f32 v131, v134, v135
s_add_u32 s27, s56, 0
buffer_store_dwordx4 v[128:131], v4, s[36:39], s27 offen offset:0 sc1
v_cvt_pk_bf16_f32 v136, v136, v137
v_cvt_pk_bf16_f32 v137, v138, v139
v_cvt_pk_bf16_f32 v138, v140, v141
v_cvt_pk_bf16_f32 v139, v142, v143
s_add_u32 s27, s56, 0x1000
buffer_store_dwordx4 v[136:139], v4, s[36:39], s27 offen offset:0 sc1
v_cvt_pk_bf16_f32 v144, v144, v145
v_cvt_pk_bf16_f32 v145, v146, v147
v_cvt_pk_bf16_f32 v146, v148, v149
v_cvt_pk_bf16_f32 v147, v150, v151
s_add_u32 s27, s56, 0x2000
buffer_store_dwordx4 v[144:147], v4, s[36:39], s27 offen offset:0 sc1
v_cvt_pk_bf16_f32 v152, v152, v153
v_cvt_pk_bf16_f32 v153, v154, v155
v_cvt_pk_bf16_f32 v154, v156, v157
v_cvt_pk_bf16_f32 v155, v158, v159
s_add_u32 s27, s56, 0x3000
buffer_store_dwordx4 v[152:155], v4, s[36:39], s27 offen offset:0 sc1
v_add_f32 v12, v22, v23
v_mov_b32 v13, v12
s_nop 1
v_permlane16_swap_b32 v12, v13
v_add_f32 v12, v12, v13
v_mov_b32 v13, v12
s_nop 1
v_permlane32_swap_b32 v12, v13
v_add_f32 v12, v12, v13
v_add_f32 v12, v12, v28
v_rcp_f32 v12, v12
s_nop 0
v_pk_mul_f32 v[160:161], v[160:161], v[12:13] op_sel_hi:[1,0]
v_pk_mul_f32 v[162:163], v[162:163], v[12:13] op_sel_hi:[1,0]
v_pk_mul_f32 v[164:165], v[164:165], v[12:13] op_sel_hi:[1,0]
v_pk_mul_f32 v[166:167], v[166:167], v[12:13] op_sel_hi:[1,0]
v_pk_mul_f32 v[168:169], v[168:169], v[12:13] op_sel_hi:[1,0]
v_pk_mul_f32 v[170:171], v[170:171], v[12:13] op_sel_hi:[1,0]
v_pk_mul_f32 v[172:173], v[172:173], v[12:13] op_sel_hi:[1,0]
v_pk_mul_f32 v[174:175], v[174:175], v[12:13] op_sel_hi:[1,0]
v_pk_mul_f32 v[176:177], v[176:177], v[12:13] op_sel_hi:[1,0]
v_pk_mul_f32 v[178:179], v[178:179], v[12:13] op_sel_hi:[1,0]
v_pk_mul_f32 v[180:181], v[180:181], v[12:13] op_sel_hi:[1,0]
v_pk_mul_f32 v[182:183], v[182:183], v[12:13] op_sel_hi:[1,0]
v_pk_mul_f32 v[184:185], v[184:185], v[12:13] op_sel_hi:[1,0]
v_pk_mul_f32 v[186:187], v[186:187], v[12:13] op_sel_hi:[1,0]
v_pk_mul_f32 v[188:189], v[188:189], v[12:13] op_sel_hi:[1,0]
v_pk_mul_f32 v[190:191], v[190:191], v[12:13] op_sel_hi:[1,0]
v_cvt_pk_bf16_f32 v160, v160, v161
v_cvt_pk_bf16_f32 v161, v162, v163
v_cvt_pk_bf16_f32 v162, v164, v165
v_cvt_pk_bf16_f32 v163, v166, v167
s_add_u32 s27, s56, 0
s_add_u32 s27, s27, s69
buffer_store_dwordx4 v[160:163], v4, s[36:39], s27 offen offset:0 sc1
v_cvt_pk_bf16_f32 v168, v168, v169
v_cvt_pk_bf16_f32 v169, v170, v171
v_cvt_pk_bf16_f32 v170, v172, v173
v_cvt_pk_bf16_f32 v171, v174, v175
s_add_u32 s27, s56, 0x1000
s_add_u32 s27, s27, s69
buffer_store_dwordx4 v[168:171], v4, s[36:39], s27 offen offset:0 sc1
v_cvt_pk_bf16_f32 v176, v176, v177
v_cvt_pk_bf16_f32 v177, v178, v179
v_cvt_pk_bf16_f32 v178, v180, v181
v_cvt_pk_bf16_f32 v179, v182, v183
s_add_u32 s27, s56, 0x2000
s_add_u32 s27, s27, s69
buffer_store_dwordx4 v[176:179], v4, s[36:39], s27 offen offset:0 sc1
v_cvt_pk_bf16_f32 v184, v184, v185
v_cvt_pk_bf16_f32 v185, v186, v187
v_cvt_pk_bf16_f32 v186, v188, v189
v_cvt_pk_bf16_f32 v187, v190, v191
s_add_u32 s27, s56, 0x3000
s_add_u32 s27, s27, s69
buffer_store_dwordx4 v[184:187], v4, s[36:39], s27 offen offset:0 sc1
s_nop 1
s_branch B_D2end_%=
B_D2w1_%=:
v_mfma_f32_16x16x32_bf16 v[32:35], a[0:3], v[224:227], 0
v_exp_f32 v40, v40
v_mfma_f32_16x16x32_bf16 v[36:39], a[16:19], v[224:227], 0
ds_read_b128 a[32:35], v5 offset:8192
v_exp_f32 v41, v41
v_mfma_f32_16x16x32_bf16 v[32:35], a[4:7], v[228:231], v[32:35]
s_add_u32 s30, s68, 0
s_and_b32 s30, s30, 7
s_lshl_b32 s30, s30, 15
s_add_u32 s30, s30, s59
buffer_load_dwordx4 a[152:155], v4, s[60:63], s30 offen offset:0 sc1
v_exp_f32 v42, v42
v_mfma_f32_16x16x32_bf16 v[36:39], a[20:23], v[228:231], v[36:39]
ds_read_b128 a[48:51], v5 offset:12288
v_exp_f32 v43, v43
v_mfma_f32_16x16x32_bf16 v[32:35], a[8:11], v[232:235], v[32:35]
buffer_load_dwordx4 a[156:159], v4, s[60:63], s30 offen offset:1024 sc1
v_cvt_pk_bf16_f32 v52, v40, v41
v_mfma_f32_16x16x32_bf16 v[36:39], a[24:27], v[232:235], v[36:39]
v_exp_f32 v44, v44
v_mfma_f32_16x16x32_bf16 v[32:35], a[12:15], v[236:239], v[32:35]
ds_read_b128 a[36:39], v5 offset:9216
v_exp_f32 v45, v45
v_mfma_f32_16x16x32_bf16 v[36:39], a[28:31], v[236:239], v[36:39]
v_exp_f32 v46, v46
v_mfma_f32_16x16x32_bf16 v[128:131], a[96:99], v[48:51], v[128:131]
buffer_load_dwordx4 a[160:163], v4, s[60:63], s30 offen offset:2048 sc1
v_exp_f32 v47, v47
v_mfma_f32_16x16x32_bf16 v[132:135], a[100:103], v[48:51], v[132:135]
ds_read_b128 a[52:55], v5 offset:13312
v_cvt_pk_bf16_f32 v53, v42, v43
v_mfma_f32_16x16x32_bf16 v[136:139], a[104:107], v[48:51], v[136:139]
ds_read_b128 a[40:43], v5 offset:10240
v_cvt_pk_bf16_f32 v54, v44, v45
v_mfma_f32_16x16x32_bf16 v[140:143], a[108:111], v[48:51], v[140:143]
buffer_load_dwordx4 a[164:167], v4, s[60:63], s30 offen offset:3072 sc1
v_cvt_pk_bf16_f32 v55, v46, v47
v_mfma_f32_16x16x32_bf16 v[144:147], a[112:115], v[48:51], v[144:147]
ds_read_b128 a[56:59], v5 offset:14336
v_mfma_f32_16x16x32_bf16 v[148:151], a[116:119], v[48:51], v[148:151]
s_add_u32 s30, s68, 1
s_and_b32 s30, s30, 7
s_lshl_b32 s30, s30, 15
s_add_u32 s30, s30, s59
buffer_load_dwordx4 a[168:171], v4, s[60:63], s30 offen offset:0 sc1
v_mfma_f32_16x16x32_bf16 v[152:155], a[120:123], v[48:51], v[152:155]
ds_read_b128 a[44:47], v5 offset:11264
v_mfma_f32_16x16x32_bf16 v[156:159], a[124:127], v[48:51], v[156:159]
buffer_load_dwordx4 a[172:175], v4, s[60:63], s30 offen offset:1024 sc1
v_mfma_f32_16x16x32_bf16 v[24:27], v[56:59], v[48:51], v[24:27]
ds_read_b128 a[60:63], v5 offset:15360
v_mfma_f32_16x16x32_bf16 v[40:43], a[0:3], v[240:243], 0
v_exp_f32 v32, v32
v_mfma_f32_16x16x32_bf16 v[44:47], a[16:19], v[240:243], 0
ds_read_b128 a[64:67], v5 offset:16384
v_exp_f32 v33, v33
v_mfma_f32_16x16x32_bf16 v[40:43], a[4:7], v[244:247], v[40:43]
buffer_load_dwordx4 a[176:179], v4, s[60:63], s30 offen offset:2048 sc1
v_exp_f32 v34, v34
v_mfma_f32_16x16x32_bf16 v[44:47], a[20:23], v[244:247], v[44:47]
ds_read_b128 a[68:71], v5 offset:17408
v_exp_f32 v35, v35
v_mfma_f32_16x16x32_bf16 v[40:43], a[8:11], v[248:251], v[40:43]
buffer_load_dwordx4 a[180:183], v4, s[60:63], s30 offen offset:3072 sc1
v_cvt_pk_bf16_f32 v48, v32, v33
v_mfma_f32_16x16x32_bf16 v[44:47], a[24:27], v[248:251], v[44:47]
v_exp_f32 v36, v36
v_mfma_f32_16x16x32_bf16 v[40:43], a[12:15], v[252:255], v[40:43]
ds_read_b128 a[72:75], v5 offset:18432
v_exp_f32 v37, v37
v_mfma_f32_16x16x32_bf16 v[44:47], a[28:31], v[252:255], v[44:47]
v_exp_f32 v38, v38
v_mfma_f32_16x16x32_bf16 v[160:163], a[96:99], v[52:55], v[160:163]
s_add_u32 s30, s68, 2
s_and_b32 s30, s30, 7
s_lshl_b32 s30, s30, 15
s_add_u32 s30, s30, s59
buffer_load_dwordx4 a[184:187], v4, s[60:63], s30 offen offset:0 sc1
v_exp_f32 v39, v39
v_mfma_f32_16x16x32_bf16 v[164:167], a[100:103], v[52:55], v[164:167]
ds_read_b128 a[76:79], v5 offset:19456
v_cvt_pk_bf16_f32 v49, v34, v35
v_mfma_f32_16x16x32_bf16 v[168:171], a[104:107], v[52:55], v[168:171]
ds_read_b128 a[80:83], v5 offset:20480
v_cvt_pk_bf16_f32 v50, v36, v37
v_mfma_f32_16x16x32_bf16 v[172:175], a[108:111], v[52:55], v[172:175]
buffer_load_dwordx4 a[188:191], v4, s[60:63], s30 offen offset:1024 sc1
v_cvt_pk_bf16_f32 v51, v38, v39
v_mfma_f32_16x16x32_bf16 v[176:179], a[112:115], v[52:55], v[176:179]
ds_read_b128 a[84:87], v5 offset:21504
v_mfma_f32_16x16x32_bf16 v[180:183], a[116:119], v[52:55], v[180:183]
buffer_load_dwordx4 a[192:195], v4, s[60:63], s30 offen offset:2048 sc1
v_mfma_f32_16x16x32_bf16 v[184:187], a[120:123], v[52:55], v[184:187]
ds_read_b128 a[88:91], v5 offset:22528
v_mfma_f32_16x16x32_bf16 v[188:191], a[124:127], v[52:55], v[188:191]
buffer_load_dwordx4 a[196:199], v4, s[60:63], s30 offen offset:3072 sc1
v_mfma_f32_16x16x32_bf16 v[28:31], v[56:59], v[52:55], v[28:31]
ds_read_b128 a[92:95], v5 offset:23552
s_waitcnt vmcnt(18)
s_barrier
s_waitcnt lgkmcnt(3)
v_mfma_f32_16x16x32_bf16 v[32:35], a[32:35], v[224:227], v[8:11]
ds_read_b128 a[96:99], v5 offset:24576
v_exp_f32 v40, v40
v_mfma_f32_16x16x32_bf16 v[32:35], a[36:39], v[228:231], v[32:35]
s_add_u32 s30, s68, 3
s_and_b32 s30, s30, 7
s_lshl_b32 s30, s30, 15
s_add_u32 s30, s30, s59
buffer_load_dwordx4 a[200:203], v4, s[60:63], s30 offen offset:0 sc1
v_exp_f32 v41, v41
v_mfma_f32_16x16x32_bf16 v[32:35], a[40:43], v[232:235], v[32:35]
buffer_load_dwordx4 a[204:207], v4, s[60:63], s30 offen offset:1024 sc1
v_exp_f32 v42, v42
v_mfma_f32_16x16x32_bf16 v[32:35], a[44:47], v[236:239], v[32:35]
ds_read_b128 a[100:103], v5 offset:25600
v_exp_f32 v43, v43
v_mfma_f32_16x16x32_bf16 v[128:131], a[64:67], v[48:51], v[128:131]
v_cvt_pk_bf16_f32 v52, v40, v41
v_cvt_pk_bf16_f32 v53, v42, v43
v_mfma_f32_16x16x32_bf16 v[132:135], a[68:71], v[48:51], v[132:135]
buffer_load_dwordx4 a[208:211], v4, s[60:63], s30 offen offset:2048 sc1
v_exp_f32 v44, v44
v_mfma_f32_16x16x32_bf16 v[136:139], a[72:75], v[48:51], v[136:139]
ds_read_b128 a[104:107], v5 offset:26624
v_exp_f32 v45, v45
v_mfma_f32_16x16x32_bf16 v[140:143], a[76:79], v[48:51], v[140:143]
buffer_load_dwordx4 a[212:215], v4, s[60:63], s30 offen offset:3072 sc1
v_exp_f32 v46, v46
v_mfma_f32_16x16x32_bf16 v[144:147], a[80:83], v[48:51], v[144:147]
v_exp_f32 v47, v47
s_waitcnt lgkmcnt(1)
v_mfma_f32_16x16x32_bf16 v[148:151], a[84:87], v[48:51], v[148:151]
s_add_u32 s30, s68, 4
s_and_b32 s30, s30, 7
s_lshl_b32 s30, s30, 15
s_add_u32 s30, s30, s59
buffer_load_dwordx4 a[216:219], v4, s[60:63], s30 offen offset:0 sc1
v_cvt_pk_bf16_f32 v54, v44, v45
v_cvt_pk_bf16_f32 v55, v46, v47
v_mfma_f32_16x16x32_bf16 v[152:155], a[88:91], v[48:51], v[152:155]
ds_read_b128 a[108:111], v5 offset:27648
v_mfma_f32_16x16x32_bf16 v[156:159], a[92:95], v[48:51], v[156:159]
buffer_load_dwordx4 a[220:223], v4, s[60:63], s30 offen offset:1024 sc1
v_mfma_f32_16x16x32_bf16 v[24:27], v[56:59], v[48:51], v[24:27]
v_mfma_f32_16x16x32_bf16 v[40:43], a[32:35], v[240:243], 0
ds_read_b128 a[112:115], v5 offset:28672
v_exp_f32 v32, v32
v_mfma_f32_16x16x32_bf16 v[44:47], a[48:51], v[240:243], v[8:11]
buffer_load_dwordx4 a[224:227], v4, s[60:63], s30 offen offset:2048 sc1
v_exp_f32 v33, v33
v_mfma_f32_16x16x32_bf16 v[40:43], a[36:39], v[244:247], v[40:43]
v_exp_f32 v34, v34
v_mfma_f32_16x16x32_bf16 v[44:47], a[52:55], v[244:247], v[44:47]
buffer_load_dwordx4 a[228:231], v4, s[60:63], s30 offen offset:3072 sc1
v_exp_f32 v35, v35
v_mfma_f32_16x16x32_bf16 v[40:43], a[40:43], v[248:251], v[40:43]
v_cvt_pk_bf16_f32 v48, v32, v33
v_cvt_pk_bf16_f32 v49, v34, v35
v_mfma_f32_16x16x32_bf16 v[44:47], a[56:59], v[248:251], v[44:47]
ds_read_b128 a[116:119], v5 offset:29696
v_mov_b32 v50, 0
v_mov_b32 v51, 0
v_mfma_f32_16x16x32_bf16 v[40:43], a[44:47], v[252:255], v[40:43]
s_add_u32 s30, s68, 5
s_and_b32 s30, s30, 7
s_lshl_b32 s30, s30, 15
s_add_u32 s30, s30, s59
buffer_load_dwordx4 a[232:235], v4, s[60:63], s30 offen offset:0 sc1
v_mfma_f32_16x16x32_bf16 v[44:47], a[60:63], v[252:255], v[44:47]
v_mfma_f32_16x16x32_bf16 v[160:163], a[64:67], v[52:55], v[160:163]
ds_read_b128 a[120:123], v5 offset:30720
v_mfma_f32_16x16x32_bf16 v[164:167], a[68:71], v[52:55], v[164:167]
v_mfma_f32_16x16x32_bf16 v[168:171], a[72:75], v[52:55], v[168:171]
buffer_load_dwordx4 a[236:239], v4, s[60:63], s30 offen offset:1024 sc1
v_mfma_f32_16x16x32_bf16 v[172:175], a[76:79], v[52:55], v[172:175]
buffer_load_dwordx4 a[240:243], v4, s[60:63], s30 offen offset:2048 sc1
v_mfma_f32_16x16x32_bf16 v[176:179], a[80:83], v[52:55], v[176:179]
v_mfma_f32_16x16x32_bf16 v[180:183], a[84:87], v[52:55], v[180:183]
ds_read_b128 a[124:127], v5 offset:31744
v_mfma_f32_16x16x32_bf16 v[184:187], a[88:91], v[52:55], v[184:187]
v_mfma_f32_16x16x32_bf16 v[188:191], a[92:95], v[52:55], v[188:191]
buffer_load_dwordx4 a[244:247], v4, s[60:63], s30 offen offset:3072 sc1
v_mfma_f32_16x16x32_bf16 v[28:31], v[56:59], v[52:55], v[28:31]
v_mfma_f32_16x16x32_bf16 v[128:131], a[96:99], v[48:51], v[128:131]
v_exp_f32 v40, v40
v_mfma_f32_16x16x32_bf16 v[132:135], a[100:103], v[48:51], v[132:135]
v_exp_f32 v41, v41
s_waitcnt lgkmcnt(0)
v_mfma_f32_16x16x32_bf16 v[136:139], a[104:107], v[48:51], v[136:139]
v_exp_f32 v42, v42
v_mfma_f32_16x16x32_bf16 v[140:143], a[108:111], v[48:51], v[140:143]
v_exp_f32 v43, v43
v_mfma_f32_16x16x32_bf16 v[144:147], a[112:115], v[48:51], v[144:147]
v_cvt_pk_bf16_f32 v52, v40, v41
v_cvt_pk_bf16_f32 v53, v42, v43
v_mfma_f32_16x16x32_bf16 v[148:151], a[116:119], v[48:51], v[148:151]
v_exp_f32 v44, v44
v_mfma_f32_16x16x32_bf16 v[152:155], a[120:123], v[48:51], v[152:155]
v_exp_f32 v45, v45
v_mfma_f32_16x16x32_bf16 v[156:159], a[124:127], v[48:51], v[156:159]
v_exp_f32 v46, v46
v_mfma_f32_16x16x32_bf16 v[24:27], v[56:59], v[48:51], v[24:27]
v_exp_f32 v47, v47
v_cvt_pk_bf16_f32 v54, v44, v45
v_cvt_pk_bf16_f32 v55, v46, v47
s_nop 1
v_mfma_f32_16x16x32_bf16 v[160:163], a[96:99], v[52:55], v[160:163]
v_mfma_f32_16x16x32_bf16 v[164:167], a[100:103], v[52:55], v[164:167]
v_mfma_f32_16x16x32_bf16 v[168:171], a[104:107], v[52:55], v[168:171]
v_mfma_f32_16x16x32_bf16 v[172:175], a[108:111], v[52:55], v[172:175]
v_mfma_f32_16x16x32_bf16 v[176:179], a[112:115], v[52:55], v[176:179]
v_mfma_f32_16x16x32_bf16 v[180:183], a[116:119], v[52:55], v[180:183]
v_mfma_f32_16x16x32_bf16 v[184:187], a[120:123], v[52:55], v[184:187]
v_mfma_f32_16x16x32_bf16 v[188:191], a[124:127], v[52:55], v[188:191]
v_mfma_f32_16x16x32_bf16 v[28:31], v[56:59], v[52:55], v[28:31]
s_waitcnt lgkmcnt(0)
s_nop 7
v_add_f32 v12, v18, v19
v_mov_b32 v13, v12
s_nop 1
v_permlane16_swap_b32 v12, v13
v_add_f32 v12, v12, v13
v_mov_b32 v13, v12
s_nop 1
v_permlane32_swap_b32 v12, v13
v_add_f32 v12, v12, v13
v_add_f32 v12, v12, v24
v_rcp_f32 v12, v12
s_nop 0
v_pk_mul_f32 v[128:129], v[128:129], v[12:13] op_sel_hi:[1,0]
v_pk_mul_f32 v[130:131], v[130:131], v[12:13] op_sel_hi:[1,0]
v_pk_mul_f32 v[132:133], v[132:133], v[12:13] op_sel_hi:[1,0]
v_pk_mul_f32 v[134:135], v[134:135], v[12:13] op_sel_hi:[1,0]
v_pk_mul_f32 v[136:137], v[136:137], v[12:13] op_sel_hi:[1,0]
v_pk_mul_f32 v[138:139], v[138:139], v[12:13] op_sel_hi:[1,0]
v_pk_mul_f32 v[140:141], v[140:141], v[12:13] op_sel_hi:[1,0]
v_pk_mul_f32 v[142:143], v[142:143], v[12:13] op_sel_hi:[1,0]
v_pk_mul_f32 v[144:145], v[144:145], v[12:13] op_sel_hi:[1,0]
v_pk_mul_f32 v[146:147], v[146:147], v[12:13] op_sel_hi:[1,0]
v_pk_mul_f32 v[148:149], v[148:149], v[12:13] op_sel_hi:[1,0]
v_pk_mul_f32 v[150:151], v[150:151], v[12:13] op_sel_hi:[1,0]
v_pk_mul_f32 v[152:153], v[152:153], v[12:13] op_sel_hi:[1,0]
v_pk_mul_f32 v[154:155], v[154:155], v[12:13] op_sel_hi:[1,0]
v_pk_mul_f32 v[156:157], v[156:157], v[12:13] op_sel_hi:[1,0]
v_pk_mul_f32 v[158:159], v[158:159], v[12:13] op_sel_hi:[1,0]
v_cvt_pk_bf16_f32 v128, v128, v129
v_cvt_pk_bf16_f32 v129, v130, v131
v_cvt_pk_bf16_f32 v130, v132, v133
v_cvt_pk_bf16_f32 v131, v134, v135
s_add_u32 s27, s56, 0
buffer_store_dwordx4 v[128:131], v4, s[36:39], s27 offen offset:0 sc1
v_cvt_pk_bf16_f32 v136, v136, v137
v_cvt_pk_bf16_f32 v137, v138, v139
v_cvt_pk_bf16_f32 v138, v140, v141
v_cvt_pk_bf16_f32 v139, v142, v143
s_add_u32 s27, s56, 0x1000
buffer_store_dwordx4 v[136:139], v4, s[36:39], s27 offen offset:0 sc1
v_cvt_pk_bf16_f32 v144, v144, v145
v_cvt_pk_bf16_f32 v145, v146, v147
v_cvt_pk_bf16_f32 v146, v148, v149
v_cvt_pk_bf16_f32 v147, v150, v151
s_add_u32 s27, s56, 0x2000
buffer_store_dwordx4 v[144:147], v4, s[36:39], s27 offen offset:0 sc1
v_cvt_pk_bf16_f32 v152, v152, v153
v_cvt_pk_bf16_f32 v153, v154, v155
v_cvt_pk_bf16_f32 v154, v156, v157
v_cvt_pk_bf16_f32 v155, v158, v159
s_add_u32 s27, s56, 0x3000
buffer_store_dwordx4 v[152:155], v4, s[36:39], s27 offen offset:0 sc1
v_add_f32 v12, v22, v23
v_mov_b32 v13, v12
s_nop 1
v_permlane16_swap_b32 v12, v13
v_add_f32 v12, v12, v13
v_mov_b32 v13, v12
s_nop 1
v_permlane32_swap_b32 v12, v13
v_add_f32 v12, v12, v13
v_add_f32 v12, v12, v28
v_rcp_f32 v12, v12
s_nop 0
v_pk_mul_f32 v[160:161], v[160:161], v[12:13] op_sel_hi:[1,0]
v_pk_mul_f32 v[162:163], v[162:163], v[12:13] op_sel_hi:[1,0]
v_pk_mul_f32 v[164:165], v[164:165], v[12:13] op_sel_hi:[1,0]
v_pk_mul_f32 v[166:167], v[166:167], v[12:13] op_sel_hi:[1,0]
v_pk_mul_f32 v[168:169], v[168:169], v[12:13] op_sel_hi:[1,0]
v_pk_mul_f32 v[170:171], v[170:171], v[12:13] op_sel_hi:[1,0]
v_pk_mul_f32 v[172:173], v[172:173], v[12:13] op_sel_hi:[1,0]
v_pk_mul_f32 v[174:175], v[174:175], v[12:13] op_sel_hi:[1,0]
v_pk_mul_f32 v[176:177], v[176:177], v[12:13] op_sel_hi:[1,0]
v_pk_mul_f32 v[178:179], v[178:179], v[12:13] op_sel_hi:[1,0]
v_pk_mul_f32 v[180:181], v[180:181], v[12:13] op_sel_hi:[1,0]
v_pk_mul_f32 v[182:183], v[182:183], v[12:13] op_sel_hi:[1,0]
v_pk_mul_f32 v[184:185], v[184:185], v[12:13] op_sel_hi:[1,0]
v_pk_mul_f32 v[186:187], v[186:187], v[12:13] op_sel_hi:[1,0]
v_pk_mul_f32 v[188:189], v[188:189], v[12:13] op_sel_hi:[1,0]
v_pk_mul_f32 v[190:191], v[190:191], v[12:13] op_sel_hi:[1,0]
v_cvt_pk_bf16_f32 v160, v160, v161
v_cvt_pk_bf16_f32 v161, v162, v163
v_cvt_pk_bf16_f32 v162, v164, v165
v_cvt_pk_bf16_f32 v163, v166, v167
s_add_u32 s27, s56, 0
s_add_u32 s27, s27, s69
buffer_store_dwordx4 v[160:163], v4, s[36:39], s27 offen offset:0 sc1
v_cvt_pk_bf16_f32 v168, v168, v169
v_cvt_pk_bf16_f32 v169, v170, v171
v_cvt_pk_bf16_f32 v170, v172, v173
v_cvt_pk_bf16_f32 v171, v174, v175
s_add_u32 s27, s56, 0x1000
s_add_u32 s27, s27, s69
buffer_store_dwordx4 v[168:171], v4, s[36:39], s27 offen offset:0 sc1
v_cvt_pk_bf16_f32 v176, v176, v177
v_cvt_pk_bf16_f32 v177, v178, v179
v_cvt_pk_bf16_f32 v178, v180, v181
v_cvt_pk_bf16_f32 v179, v182, v183
s_add_u32 s27, s56, 0x2000
s_add_u32 s27, s27, s69
buffer_store_dwordx4 v[176:179], v4, s[36:39], s27 offen offset:0 sc1
v_cvt_pk_bf16_f32 v184, v184, v185
v_cvt_pk_bf16_f32 v185, v186, v187
v_cvt_pk_bf16_f32 v186, v188, v189
v_cvt_pk_bf16_f32 v187, v190, v191
s_add_u32 s27, s56, 0x3000
s_add_u32 s27, s27, s69
buffer_store_dwordx4 v[184:187], v4, s[36:39], s27 offen offset:0 sc1
s_nop 1
s_branch B_D2end_%=
B_D2w2_%=:
v_mfma_f32_16x16x32_bf16 v[32:35], a[0:3], v[224:227], 0
v_exp_f32 v40, v40
v_mfma_f32_16x16x32_bf16 v[36:39], a[16:19], v[224:227], 0
ds_read_b128 a[32:35], v5 offset:8192
v_exp_f32 v41, v41
v_mfma_f32_16x16x32_bf16 v[32:35], a[4:7], v[228:231], v[32:35]
v_exp_f32 v42, v42
v_mfma_f32_16x16x32_bf16 v[36:39], a[20:23], v[228:231], v[36:39]
s_add_u32 s30, s68, 0
s_and_b32 s30, s30, 7
s_lshl_b32 s30, s30, 15
s_add_u32 s30, s30, s59
buffer_load_dwordx4 a[152:155], v4, s[60:63], s30 offen offset:0 sc1
v_exp_f32 v43, v43
v_mfma_f32_16x16x32_bf16 v[32:35], a[8:11], v[232:235], v[32:35]
ds_read_b128 a[48:51], v5 offset:12288
v_cvt_pk_bf16_f32 v52, v40, v41
v_mfma_f32_16x16x32_bf16 v[36:39], a[24:27], v[232:235], v[36:39]
v_exp_f32 v44, v44
v_mfma_f32_16x16x32_bf16 v[32:35], a[12:15], v[236:239], v[32:35]
v_exp_f32 v45, v45
v_mfma_f32_16x16x32_bf16 v[36:39], a[28:31], v[236:239], v[36:39]
v_exp_f32 v46, v46
v_mfma_f32_16x16x32_bf16 v[128:131], a[96:99], v[48:51], v[128:131]
ds_read_b128 a[36:39], v5 offset:9216
v_exp_f32 v47, v47
v_mfma_f32_16x16x32_bf16 v[132:135], a[100:103], v[48:51], v[132:135]
buffer_load_dwordx4 a[156:159], v4, s[60:63], s30 offen offset:1024 sc1
v_cvt_pk_bf16_f32 v53, v42, v43
v_mfma_f32_16x16x32_bf16 v[136:139], a[104:107], v[48:51], v[136:139]
ds_read_b128 a[52:55], v5 offset:13312
v_cvt_pk_bf16_f32 v54, v44, v45
v_mfma_f32_16x16x32_bf16 v[140:143], a[108:111], v[48:51], v[140:143]
ds_read_b128 a[40:43], v5 offset:10240
v_cvt_pk_bf16_f32 v55, v46, v47
v_mfma_f32_16x16x32_bf16 v[144:147], a[112:115], v[48:51], v[144:147]
buffer_load_dwordx4 a[160:163], v4, s[60:63], s30 offen offset:2048 sc1
v_mfma_f32_16x16x32_bf16 v[148:151], a[116:119], v[48:51], v[148:151]
ds_read_b128 a[56:59], v5 offset:14336
v_mfma_f32_16x16x32_bf16 v[152:155], a[120:123], v[48:51], v[152:155]
ds_read_b128 a[44:47], v5 offset:11264
v_mfma_f32_16x16x32_bf16 v[156:159], a[124:127], v[48:51], v[156:159]
buffer_load_dwordx4 a[164:167], v4, s[60:63], s30 offen offset:3072 sc1
v_mfma_f32_16x16x32_bf16 v[24:27], v[56:59], v[48:51], v[24:27]
ds_read_b128 a[60:63], v5 offset:15360
v_mfma_f32_16x16x32_bf16 v[40:43], a[0:3], v[240:243], 0
v_exp_f32 v32, v32
v_mfma_f32_16x16x32_bf16 v[44:47], a[16:19], v[240:243], 0
ds_read_b128 a[64:67], v5 offset:16384
v_exp_f32 v33, v33
v_mfma_f32_16x16x32_bf16 v[40:43], a[4:7], v[244:247], v[40:43]
v_exp_f32 v34, v34
v_mfma_f32_16x16x32_bf16 v[44:47], a[20:23], v[244:247], v[44:47]
s_add_u32 s30, s68, 1
s_and_b32 s30, s30, 7
s_lshl_b32 s30, s30, 15
s_add_u32 s30, s30, s59
buffer_load_dwordx4 a[168:171], v4, s[60:63], s30 offen offset:0 sc1
v_exp_f32 v35, v35
v_mfma_f32_16x16x32_bf16 v[40:43], a[8:11], v[248:251], v[40:43]
ds_read_b128 a[68:71], v5 offset:17408
v_cvt_pk_bf16_f32 v48, v32, v33
v_mfma_f32_16x16x32_bf16 v[44:47], a[24:27], v[248:251], v[44:47]
v_exp_f32 v36, v36
v_mfma_f32_16x16x32_bf16 v[40:43], a[12:15], v[252:255], v[40:43]
v_exp_f32 v37, v37
v_mfma_f32_16x16x32_bf16 v[44:47], a[28:31], v[252:255], v[44:47]
v_exp_f32 v38, v38
v_mfma_f32_16x16x32_bf16 v[160:163], a[96:99], v[52:55], v[160:163]
ds_read_b128 a[72:75], v5 offset:18432
v_exp_f32 v39, v39
v_mfma_f32_16x16x32_bf16 v[164:167], a[100:103], v[52:55], v[164:167]
buffer_load_dwordx4 a[172:175], v4, s[60:63], s30 offen offset:1024 sc1
v_cvt_pk_bf16_f32 v49, v34, v35
v_mfma_f32_16x16x32_bf16 v[168:171], a[104:107], v[52:55], v[168:171]
ds_read_b128 a[76:79], v5 offset:19456
v_cvt_pk_bf16_f32 v50, v36, v37
v_mfma_f32_16x16x32_bf16 v[172:175], a[108:111], v[52:55], v[172:175]
ds_read_b128 a[80:83], v5 offset:20480
v_cvt_pk_bf16_f32 v51, v38, v39
v_mfma_f32_16x16x32_bf16 v[176:179], a[112:115], v[52:55], v[176:179]
buffer_load_dwordx4 a[176:179], v4, s[60:63], s30 offen offset:2048 sc1
v_mfma_f32_16x16x32_bf16 v[180:183], a[116:119], v[52:55], v[180:183]
ds_read_b128 a[84:87], v5 offset:21504
v_mfma_f32_16x16x32_bf16 v[184:187], a[120:123], v[52:55], v[184:187]
ds_read_b128 a[88:91], v5 offset:22528
v_mfma_f32_16x16x32_bf16 v[188:191], a[124:127], v[52:55], v[188:191]
buffer_load_dwordx4 a[180:183], v4, s[60:63], s30 offen offset:3072 sc1
v_mfma_f32_16x16x32_bf16 v[28:31], v[56:59], v[52:55], v[28:31]
ds_read_b128 a[92:95], v5 offset:23552
s_waitcnt vmcnt(14)
s_barrier
s_waitcnt lgkmcnt(3)
v_mfma_f32_16x16x32_bf16 v[32:35], a[32:35], v[224:227], 0
v_exp_f32 v40, v40
v_mfma_f32_16x16x32_bf16 v[36:39], a[48:51], v[224:227], 0
ds_read_b128 a[0:3], v6 offset:0
v_exp_f32 v41, v41
v_mfma_f32_16x16x32_bf16 v[32:35], a[36:39], v[228:231], v[32:35]
v_exp_f32 v42, v42
v_mfma_f32_16x16x32_bf16 v[36:39], a[52:55], v[228:231], v[36:39]
s_add_u32 s30, s68, 2
s_and_b32 s30, s30, 7
s_lshl_b32 s30, s30, 15
s_add_u32 s30, s30, s59
buffer_load_dwordx4 a[184:187], v4, s[60:63], s30 offen offset:0 sc1
v_exp_f32 v43, v43
v_mfma_f32_16x16x32_bf16 v[32:35], a[40:43], v[232:235], v[32:35]
ds_read_b128 a[16:19], v6 offset:4096
v_cvt_pk_bf16_f32 v52, v40, v41
v_mfma_f32_16x16x32_bf16 v[36:39], a[56:59], v[232:235], v[36:39]
v_exp_f32 v44, v44
v_mfma_f32_16x16x32_bf16 v[32:35], a[44:47], v[236:239], v[32:35]
v_exp_f32 v45, v45
v_mfma_f32_16x16x32_bf16 v[36:39], a[60:63], v[236:239], v[36:39]
v_exp_f32 v46, v46
v_mfma_f32_16x16x32_bf16 v[128:131], a[64:67], v[48:51], v[128:131]
ds_read_b128 a[4:7], v6 offset:1024
v_exp_f32 v47, v47
v_mfma_f32_16x16x32_bf16 v[132:135], a[68:71], v[48:51], v[132:135]
buffer_load_dwordx4 a[188:191], v4, s[60:63], s30 offen offset:1024 sc1
v_cvt_pk_bf16_f32 v53, v42, v43
v_mfma_f32_16x16x32_bf16 v[136:139], a[72:75], v[48:51], v[136:139]
ds_read_b128 a[20:23], v6 offset:5120
v_cvt_pk_bf16_f32 v54, v44, v45
v_mfma_f32_16x16x32_bf16 v[140:143], a[76:79], v[48:51], v[140:143]
ds_read_b128 a[8:11], v6 offset:2048
v_cvt_pk_bf16_f32 v55, v46, v47
v_mfma_f32_16x16x32_bf16 v[144:147], a[80:83], v[48:51], v[144:147]
buffer_load_dwordx4 a[192:195], v4, s[60:63], s30 offen offset:2048 sc1
s_waitcnt lgkmcnt(3)
v_mfma_f32_16x16x32_bf16 v[148:151], a[84:87], v[48:51], v[148:151]
ds_read_b128 a[24:27], v6 offset:6144
v_mfma_f32_16x16x32_bf16 v[152:155], a[88:91], v[48:51], v[152:155]
ds_read_b128 a[12:15], v6 offset:3072
v_mfma_f32_16x16x32_bf16 v[156:159], a[92:95], v[48:51], v[156:159]
buffer_load_dwordx4 a[196:199], v4, s[60:63], s30 offen offset:3072 sc1
v_mfma_f32_16x16x32_bf16 v[24:27], v[56:59], v[48:51], v[24:27]
ds_read_b128 a[28:31], v6 offset:7168
v_mfma_f32_16x16x32_bf16 v[40:43], a[32:35], v[240:243], 0
v_exp_f32 v32, v32
v_mfma_f32_16x16x32_bf16 v[44:47], a[48:51], v[240:243], 0
ds_read_b128 a[96:99], v5 offset:24576
v_exp_f32 v33, v33
v_mfma_f32_16x16x32_bf16 v[40:43], a[36:39], v[244:247], v[40:43]
v_exp_f32 v34, v34
v_mfma_f32_16x16x32_bf16 v[44:47], a[52:55], v[244:247], v[44:47]
s_add_u32 s30, s68, 3
s_and_b32 s30, s30, 7
s_lshl_b32 s30, s30, 15
s_add_u32 s30, s30, s59
buffer_load_dwordx4 a[200:203], v4, s[60:63], s30 offen offset:0 sc1
v_exp_f32 v35, v35
v_mfma_f32_16x16x32_bf16 v[40:43], a[40:43], v[248:251], v[40:43]
ds_read_b128 a[100:103], v5 offset:25600
v_cvt_pk_bf16_f32 v48, v32, v33
v_mfma_f32_16x16x32_bf16 v[44:47], a[56:59], v[248:251], v[44:47]
v_exp_f32 v36, v36
v_mfma_f32_16x16x32_bf16 v[40:43], a[44:47], v[252:255], v[40:43]
v_exp_f32 v37, v37
v_mfma_f32_16x16x32_bf16 v[44:47], a[60:63], v[252:255], v[44:47]
v_exp_f32 v38, v38
v_mfma_f32_16x16x32_bf16 v[160:163], a[64:67], v[52:55], v[160:163]
ds_read_b128 a[104:107], v5 offset:26624
v_exp_f32 v39, v39
v_mfma_f32_16x16x32_bf16 v[164:167], a[68:71], v[52:55], v[164:167]
buffer_load_dwordx4 a[204:207], v4, s[60:63], s30 offen offset:1024 sc1
v_cvt_pk_bf16_f32 v49, v34, v35
v_mfma_f32_16x16x32_bf16 v[168:171], a[72:75], v[52:55], v[168:171]
ds_read_b128 a[108:111], v5 offset:27648
v_cvt_pk_bf16_f32 v50, v36, v37
v_mfma_f32_16x16x32_bf16 v[172:175], a[76:79], v[52:55], v[172:175]
ds_read_b128 a[112:115], v5 offset:28672
v_cvt_pk_bf16_f32 v51, v38, v39
v_mfma_f32_16x16x32_bf16 v[176:179], a[80:83], v[52:55], v[176:179]
buffer_load_dwordx4 a[208:211], v4, s[60:63], s30 offen offset:2048 sc1
v_mfma_f32_16x16x32_bf16 v[180:183], a[84:87], v[52:55], v[180:183]
ds_read_b128 a[116:119], v5 offset:29696
v_mfma_f32_16x16x32_bf16 v[184:187], a[88:91], v[52:55], v[184:187]
ds_read_b128 a[120:123], v5 offset:30720
v_mfma_f32_16x16x32_bf16 v[188:191], a[92:95], v[52:55], v[188:191]
buffer_load_dwordx4 a[212:215], v4, s[60:63], s30 offen offset:3072 sc1
v_mfma_f32_16x16x32_bf16 v[28:31], v[56:59], v[52:55], v[28:31]
ds_read_b128 a[124:127], v5 offset:31744
v_mov_b32 v5, v6
v_add_u32 v6, 0x8000, v6
v_and_b32 v6, 0x1ffff, v6
v_mfma_f32_16x16x32_bf16 v[32:35], a[0:3], v[224:227], v[8:11]
ds_read_b128 a[64:67], v5 offset:16384
v_exp_f32 v40, v40
s_waitcnt lgkmcnt(4)
v_mfma_f32_16x16x32_bf16 v[32:35], a[4:7], v[228:231], v[32:35]
s_add_u32 s30, s68, 4
s_and_b32 s30, s30, 7
s_lshl_b32 s30, s30, 15
s_add_u32 s30, s30, s59
buffer_load_dwordx4 a[216:219], v4, s[60:63], s30 offen offset:0 sc1
v_exp_f32 v41, v41
v_mfma_f32_16x16x32_bf16 v[32:35], a[8:11], v[232:235], v[32:35]
v_exp_f32 v42, v42
v_mfma_f32_16x16x32_bf16 v[32:35], a[12:15], v[236:239], v[32:35]
ds_read_b128 a[68:71], v5 offset:17408
v_exp_f32 v43, v43
v_mfma_f32_16x16x32_bf16 v[128:131], a[96:99], v[48:51], v[128:131]
buffer_load_dwordx4 a[220:223], v4, s[60:63], s30 offen offset:1024 sc1
v_cvt_pk_bf16_f32 v52, v40, v41
v_cvt_pk_bf16_f32 v53, v42, v43
v_mfma_f32_16x16x32_bf16 v[132:135], a[100:103], v[48:51], v[132:135]
v_exp_f32 v44, v44
v_mfma_f32_16x16x32_bf16 v[136:139], a[104:107], v[48:51], v[136:139]
ds_read_b128 a[72:75], v5 offset:18432
v_exp_f32 v45, v45
v_mfma_f32_16x16x32_bf16 v[140:143], a[108:111], v[48:51], v[140:143]
v_exp_f32 v46, v46
v_mfma_f32_16x16x32_bf16 v[144:147], a[112:115], v[48:51], v[144:147]
buffer_load_dwordx4 a[224:227], v4, s[60:63], s30 offen offset:2048 sc1
v_exp_f32 v47, v47
s_waitcnt lgkmcnt(1)
v_mfma_f32_16x16x32_bf16 v[148:151], a[116:119], v[48:51], v[148:151]
ds_read_b128 a[76:79], v5 offset:19456
v_cvt_pk_bf16_f32 v54, v44, v45
v_cvt_pk_bf16_f32 v55, v46, v47
v_mfma_f32_16x16x32_bf16 v[152:155], a[120:123], v[48:51], v[152:155]
v_mfma_f32_16x16x32_bf16 v[156:159], a[124:127], v[48:51], v[156:159]
buffer_load_dwordx4 a[228:231], v4, s[60:63], s30 offen offset:3072 sc1
v_mfma_f32_16x16x32_bf16 v[24:27], v[56:59], v[48:51], v[24:27]
v_mfma_f32_16x16x32_bf16 v[40:43], a[0:3], v[240:243], 0
ds_read_b128 a[80:83], v5 offset:20480
v_exp_f32 v32, v32
v_mfma_f32_16x16x32_bf16 v[44:47], a[16:19], v[240:243], v[8:11]
v_exp_f32 v33, v33
v_mfma_f32_16x16x32_bf16 v[40:43], a[4:7], v[244:247], v[40:43]
s_add_u32 s30, s68, 5
s_and_b32 s30, s30, 7
s_lshl_b32 s30, s30, 15
s_add_u32 s30, s30, s59
buffer_load_dwordx4 a[232:235], v4, s[60:63], s30 offen offset:0 sc1
v_exp_f32 v34, v34
v_mfma_f32_16x16x32_bf16 v[44:47], a[20:23], v[244:247], v[44:47]
v_exp_f32 v35, v35
v_mfma_f32_16x16x32_bf16 v[40:43], a[8:11], v[248:251], v[40:43]
ds_read_b128 a[84:87], v5 offset:21504
v_cvt_pk_bf16_f32 v48, v32, v33
v_cvt_pk_bf16_f32 v49, v34, v35
v_mfma_f32_16x16x32_bf16 v[44:47], a[24:27], v[248:251], v[44:47]
v_mov_b32 v50, 0
v_mov_b32 v51, 0
v_mfma_f32_16x16x32_bf16 v[40:43], a[12:15], v[252:255], v[40:43]
buffer_load_dwordx4 a[236:239], v4, s[60:63], s30 offen offset:1024 sc1
v_mfma_f32_16x16x32_bf16 v[44:47], a[28:31], v[252:255], v[44:47]
v_mfma_f32_16x16x32_bf16 v[160:163], a[96:99], v[52:55], v[160:163]
ds_read_b128 a[88:91], v5 offset:22528
v_mfma_f32_16x16x32_bf16 v[164:167], a[100:103], v[52:55], v[164:167]
v_mfma_f32_16x16x32_bf16 v[168:171], a[104:107], v[52:55], v[168:171]
buffer_load_dwordx4 a[240:243], v4, s[60:63], s30 offen offset:2048 sc1
v_mfma_f32_16x16x32_bf16 v[172:175], a[108:111], v[52:55], v[172:175]
v_mfma_f32_16x16x32_bf16 v[176:179], a[112:115], v[52:55], v[176:179]
ds_read_b128 a[92:95], v5 offset:23552
v_mfma_f32_16x16x32_bf16 v[180:183], a[116:119], v[52:55], v[180:183]
v_mfma_f32_16x16x32_bf16 v[184:187], a[120:123], v[52:55], v[184:187]
buffer_load_dwordx4 a[244:247], v4, s[60:63], s30 offen offset:3072 sc1
v_mfma_f32_16x16x32_bf16 v[188:191], a[124:127], v[52:55], v[188:191]
v_mfma_f32_16x16x32_bf16 v[28:31], v[56:59], v[52:55], v[28:31]
v_mfma_f32_16x16x32_bf16 v[128:131], a[64:67], v[48:51], v[128:131]
v_exp_f32 v40, v40
v_mfma_f32_16x16x32_bf16 v[132:135], a[68:71], v[48:51], v[132:135]
v_exp_f32 v41, v41
s_waitcnt lgkmcnt(0)
v_mfma_f32_16x16x32_bf16 v[136:139], a[72:75], v[48:51], v[136:139]
v_exp_f32 v42, v42
v_mfma_f32_16x16x32_bf16 v[140:143], a[76:79], v[48:51], v[140:143]
v_exp_f32 v43, v43
v_mfma_f32_16x16x32_bf16 v[144:147], a[80:83], v[48:51], v[144:147]
v_cvt_pk_bf16_f32 v52, v40, v41
v_cvt_pk_bf16_f32 v53, v42, v43
v_mfma_f32_16x16x32_bf16 v[148:151], a[84:87], v[48:51], v[148:151]
v_exp_f32 v44, v44
v_mfma_f32_16x16x32_bf16 v[152:155], a[88:91], v[48:51], v[152:155]
v_exp_f32 v45, v45
v_mfma_f32_16x16x32_bf16 v[156:159], a[92:95], v[48:51], v[156:159]
v_exp_f32 v46, v46
v_mfma_f32_16x16x32_bf16 v[24:27], v[56:59], v[48:51], v[24:27]
v_exp_f32 v47, v47
v_cvt_pk_bf16_f32 v54, v44, v45
v_cvt_pk_bf16_f32 v55, v46, v47
s_nop 1
v_mfma_f32_16x16x32_bf16 v[160:163], a[64:67], v[52:55], v[160:163]
v_mfma_f32_16x16x32_bf16 v[164:167], a[68:71], v[52:55], v[164:167]
v_mfma_f32_16x16x32_bf16 v[168:171], a[72:75], v[52:55], v[168:171]
v_mfma_f32_16x16x32_bf16 v[172:175], a[76:79], v[52:55], v[172:175]
v_mfma_f32_16x16x32_bf16 v[176:179], a[80:83], v[52:55], v[176:179]
v_mfma_f32_16x16x32_bf16 v[180:183], a[84:87], v[52:55], v[180:183]
v_mfma_f32_16x16x32_bf16 v[184:187], a[88:91], v[52:55], v[184:187]
v_mfma_f32_16x16x32_bf16 v[188:191], a[92:95], v[52:55], v[188:191]
v_mfma_f32_16x16x32_bf16 v[28:31], v[56:59], v[52:55], v[28:31]
s_waitcnt lgkmcnt(0)
s_nop 7
v_add_f32 v12, v18, v19
v_mov_b32 v13, v12
s_nop 1
v_permlane16_swap_b32 v12, v13
v_add_f32 v12, v12, v13
v_mov_b32 v13, v12
s_nop 1
v_permlane32_swap_b32 v12, v13
v_add_f32 v12, v12, v13
v_add_f32 v12, v12, v24
v_rcp_f32 v12, v12
s_nop 0
v_pk_mul_f32 v[128:129], v[128:129], v[12:13] op_sel_hi:[1,0]
v_pk_mul_f32 v[130:131], v[130:131], v[12:13] op_sel_hi:[1,0]
v_pk_mul_f32 v[132:133], v[132:133], v[12:13] op_sel_hi:[1,0]
v_pk_mul_f32 v[134:135], v[134:135], v[12:13] op_sel_hi:[1,0]
v_pk_mul_f32 v[136:137], v[136:137], v[12:13] op_sel_hi:[1,0]
v_pk_mul_f32 v[138:139], v[138:139], v[12:13] op_sel_hi:[1,0]
v_pk_mul_f32 v[140:141], v[140:141], v[12:13] op_sel_hi:[1,0]
v_pk_mul_f32 v[142:143], v[142:143], v[12:13] op_sel_hi:[1,0]
v_pk_mul_f32 v[144:145], v[144:145], v[12:13] op_sel_hi:[1,0]
v_pk_mul_f32 v[146:147], v[146:147], v[12:13] op_sel_hi:[1,0]
v_pk_mul_f32 v[148:149], v[148:149], v[12:13] op_sel_hi:[1,0]
v_pk_mul_f32 v[150:151], v[150:151], v[12:13] op_sel_hi:[1,0]
v_pk_mul_f32 v[152:153], v[152:153], v[12:13] op_sel_hi:[1,0]
v_pk_mul_f32 v[154:155], v[154:155], v[12:13] op_sel_hi:[1,0]
v_pk_mul_f32 v[156:157], v[156:157], v[12:13] op_sel_hi:[1,0]
v_pk_mul_f32 v[158:159], v[158:159], v[12:13] op_sel_hi:[1,0]
v_cvt_pk_bf16_f32 v128, v128, v129
v_cvt_pk_bf16_f32 v129, v130, v131
v_cvt_pk_bf16_f32 v130, v132, v133
v_cvt_pk_bf16_f32 v131, v134, v135
s_add_u32 s27, s56, 0
buffer_store_dwordx4 v[128:131], v4, s[36:39], s27 offen offset:0 sc1
v_cvt_pk_bf16_f32 v136, v136, v137
v_cvt_pk_bf16_f32 v137, v138, v139
v_cvt_pk_bf16_f32 v138, v140, v141
v_cvt_pk_bf16_f32 v139, v142, v143
s_add_u32 s27, s56, 0x1000
buffer_store_dwordx4 v[136:139], v4, s[36:39], s27 offen offset:0 sc1
v_cvt_pk_bf16_f32 v144, v144, v145
v_cvt_pk_bf16_f32 v145, v146, v147
v_cvt_pk_bf16_f32 v146, v148, v149
v_cvt_pk_bf16_f32 v147, v150, v151
s_add_u32 s27, s56, 0x2000
buffer_store_dwordx4 v[144:147], v4, s[36:39], s27 offen offset:0 sc1
v_cvt_pk_bf16_f32 v152, v152, v153
v_cvt_pk_bf16_f32 v153, v154, v155
v_cvt_pk_bf16_f32 v154, v156, v157
v_cvt_pk_bf16_f32 v155, v158, v159
s_add_u32 s27, s56, 0x3000
buffer_store_dwordx4 v[152:155], v4, s[36:39], s27 offen offset:0 sc1
v_add_f32 v12, v22, v23
v_mov_b32 v13, v12
s_nop 1
v_permlane16_swap_b32 v12, v13
v_add_f32 v12, v12, v13
v_mov_b32 v13, v12
s_nop 1
v_permlane32_swap_b32 v12, v13
v_add_f32 v12, v12, v13
v_add_f32 v12, v12, v28
v_rcp_f32 v12, v12
s_nop 0
v_pk_mul_f32 v[160:161], v[160:161], v[12:13] op_sel_hi:[1,0]
v_pk_mul_f32 v[162:163], v[162:163], v[12:13] op_sel_hi:[1,0]
v_pk_mul_f32 v[164:165], v[164:165], v[12:13] op_sel_hi:[1,0]
v_pk_mul_f32 v[166:167], v[166:167], v[12:13] op_sel_hi:[1,0]
v_pk_mul_f32 v[168:169], v[168:169], v[12:13] op_sel_hi:[1,0]
v_pk_mul_f32 v[170:171], v[170:171], v[12:13] op_sel_hi:[1,0]
v_pk_mul_f32 v[172:173], v[172:173], v[12:13] op_sel_hi:[1,0]
v_pk_mul_f32 v[174:175], v[174:175], v[12:13] op_sel_hi:[1,0]
v_pk_mul_f32 v[176:177], v[176:177], v[12:13] op_sel_hi:[1,0]
v_pk_mul_f32 v[178:179], v[178:179], v[12:13] op_sel_hi:[1,0]
v_pk_mul_f32 v[180:181], v[180:181], v[12:13] op_sel_hi:[1,0]
v_pk_mul_f32 v[182:183], v[182:183], v[12:13] op_sel_hi:[1,0]
v_pk_mul_f32 v[184:185], v[184:185], v[12:13] op_sel_hi:[1,0]
v_pk_mul_f32 v[186:187], v[186:187], v[12:13] op_sel_hi:[1,0]
v_pk_mul_f32 v[188:189], v[188:189], v[12:13] op_sel_hi:[1,0]
v_pk_mul_f32 v[190:191], v[190:191], v[12:13] op_sel_hi:[1,0]
v_cvt_pk_bf16_f32 v160, v160, v161
v_cvt_pk_bf16_f32 v161, v162, v163
v_cvt_pk_bf16_f32 v162, v164, v165
v_cvt_pk_bf16_f32 v163, v166, v167
s_add_u32 s27, s56, 0
s_add_u32 s27, s27, s69
buffer_store_dwordx4 v[160:163], v4, s[36:39], s27 offen offset:0 sc1
v_cvt_pk_bf16_f32 v168, v168, v169
v_cvt_pk_bf16_f32 v169, v170, v171
v_cvt_pk_bf16_f32 v170, v172, v173
v_cvt_pk_bf16_f32 v171, v174, v175
s_add_u32 s27, s56, 0x1000
s_add_u32 s27, s27, s69
buffer_store_dwordx4 v[168:171], v4, s[36:39], s27 offen offset:0 sc1
v_cvt_pk_bf16_f32 v176, v176, v177
v_cvt_pk_bf16_f32 v177, v178, v179
v_cvt_pk_bf16_f32 v178, v180, v181
v_cvt_pk_bf16_f32 v179, v182, v183
s_add_u32 s27, s56, 0x2000
s_add_u32 s27, s27, s69
buffer_store_dwordx4 v[176:179], v4, s[36:39], s27 offen offset:0 sc1
v_cvt_pk_bf16_f32 v184, v184, v185
v_cvt_pk_bf16_f32 v185, v186, v187
v_cvt_pk_bf16_f32 v186, v188, v189
v_cvt_pk_bf16_f32 v187, v190, v191
s_add_u32 s27, s56, 0x3000
s_add_u32 s27, s27, s69
buffer_store_dwordx4 v[184:187], v4, s[36:39], s27 offen offset:0 sc1
s_nop 1
s_branch B_D2end_%=
B_D2w3_%=:
v_mfma_f32_16x16x32_bf16 v[32:35], a[0:3], v[224:227], 0
v_exp_f32 v40, v40
v_mfma_f32_16x16x32_bf16 v[36:39], a[16:19], v[224:227], 0
v_exp_f32 v41, v41
v_mfma_f32_16x16x32_bf16 v[32:35], a[4:7], v[228:231], v[32:35]
v_exp_f32 v42, v42
v_mfma_f32_16x16x32_bf16 v[36:39], a[20:23], v[228:231], v[36:39]
ds_read_b128 a[32:35], v5 offset:8192
v_exp_f32 v43, v43
v_mfma_f32_16x16x32_bf16 v[32:35], a[8:11], v[232:235], v[32:35]
s_add_u32 s30, s68, 0
s_and_b32 s30, s30, 7
s_lshl_b32 s30, s30, 15
s_add_u32 s30, s30, s59
buffer_load_dwordx4 a[152:155], v4, s[60:63], s30 offen offset:0 sc1
v_cvt_pk_bf16_f32 v52, v40, v41
v_mfma_f32_16x16x32_bf16 v[36:39], a[24:27], v[232:235], v[36:39]
v_exp_f32 v44, v44
v_mfma_f32_16x16x32_bf16 v[32:35], a[12:15], v[236:239], v[32:35]
v_exp_f32 v45, v45
v_mfma_f32_16x16x32_bf16 v[36:39], a[28:31], v[236:239], v[36:39]
v_exp_f32 v46, v46
v_mfma_f32_16x16x32_bf16 v[128:131], a[96:99], v[48:51], v[128:131]
ds_read_b128 a[48:51], v5 offset:12288
v_exp_f32 v47, v47
v_mfma_f32_16x16x32_bf16 v[132:135], a[100:103], v[48:51], v[132:135]
ds_read_b128 a[36:39], v5 offset:9216
v_cvt_pk_bf16_f32 v53, v42, v43
v_mfma_f32_16x16x32_bf16 v[136:139], a[104:107], v[48:51], v[136:139]
buffer_load_dwordx4 a[156:159], v4, s[60:63], s30 offen offset:1024 sc1
v_cvt_pk_bf16_f32 v54, v44, v45
v_mfma_f32_16x16x32_bf16 v[140:143], a[108:111], v[48:51], v[140:143]
ds_read_b128 a[52:55], v5 offset:13312
v_cvt_pk_bf16_f32 v55, v46, v47
v_mfma_f32_16x16x32_bf16 v[144:147], a[112:115], v[48:51], v[144:147]
ds_read_b128 a[40:43], v5 offset:10240
v_mfma_f32_16x16x32_bf16 v[148:151], a[116:119], v[48:51], v[148:151]
ds_read_b128 a[56:59], v5 offset:14336
v_mfma_f32_16x16x32_bf16 v[152:155], a[120:123], v[48:51], v[152:155]
buffer_load_dwordx4 a[160:163], v4, s[60:63], s30 offen offset:2048 sc1
v_mfma_f32_16x16x32_bf16 v[156:159], a[124:127], v[48:51], v[156:159]
ds_read_b128 a[44:47], v5 offset:11264
v_mfma_f32_16x16x32_bf16 v[24:27], v[56:59], v[48:51], v[24:27]
ds_read_b128 a[60:63], v5 offset:15360
v_mfma_f32_16x16x32_bf16 v[40:43], a[0:3], v[240:243], 0
v_exp_f32 v32, v32
v_mfma_f32_16x16x32_bf16 v[44:47], a[16:19], v[240:243], 0
v_exp_f32 v33, v33
v_mfma_f32_16x16x32_bf16 v[40:43], a[4:7], v[244:247], v[40:43]
v_exp_f32 v34, v34
v_mfma_f32_16x16x32_bf16 v[44:47], a[20:23], v[244:247], v[44:47]
ds_read_b128 a[64:67], v5 offset:16384
v_exp_f32 v35, v35
v_mfma_f32_16x16x32_bf16 v[40:43], a[8:11], v[248:251], v[40:43]
buffer_load_dwordx4 a[164:167], v4, s[60:63], s30 offen offset:3072 sc1
v_cvt_pk_bf16_f32 v48, v32, v33
v_mfma_f32_16x16x32_bf16 v[44:47], a[24:27], v[248:251], v[44:47]
v_exp_f32 v36, v36
v_mfma_f32_16x16x32_bf16 v[40:43], a[12:15], v[252:255], v[40:43]
v_exp_f32 v37, v37
v_mfma_f32_16x16x32_bf16 v[44:47], a[28:31], v[252:255], v[44:47]
v_exp_f32 v38, v38
v_mfma_f32_16x16x32_bf16 v[160:163], a[96:99], v[52:55], v[160:163]
ds_read_b128 a[68:71], v5 offset:17408
v_exp_f32 v39, v39
v_mfma_f32_16x16x32_bf16 v[164:167], a[100:103], v[52:55], v[164:167]
ds_read_b128 a[72:75], v5 offset:18432
v_cvt_pk_bf16_f32 v49, v34, v35
v_mfma_f32_16x16x32_bf16 v[168:171], a[104:107], v[52:55], v[168:171]
s_add_u32 s30, s68, 1
s_and_b32 s30, s30, 7
s_lshl_b32 s30, s30, 15
s_add_u32 s30, s30, s59
buffer_load_dwordx4 a[168:171], v4, s[60:63], s30 offen offset:0 sc1
v_cvt_pk_bf16_f32 v50, v36, v37
v_mfma_f32_16x16x32_bf16 v[172:175], a[108:111], v[52:55], v[172:175]
ds_read_b128 a[76:79], v5 offset:19456
v_cvt_pk_bf16_f32 v51, v38, v39
v_mfma_f32_16x16x32_bf16 v[176:179], a[112:115], v[52:55], v[176:179]
ds_read_b128 a[80:83], v5 offset:20480
v_mfma_f32_16x16x32_bf16 v[180:183], a[116:119], v[52:55], v[180:183]
ds_read_b128 a[84:87], v5 offset:21504
v_mfma_f32_16x16x32_bf16 v[184:187], a[120:123], v[52:55], v[184:187]
buffer_load_dwordx4 a[172:175], v4, s[60:63], s30 offen offset:1024 sc1
v_mfma_f32_16x16x32_bf16 v[188:191], a[124:127], v[52:55], v[188:191]
ds_read_b128 a[88:91], v5 offset:22528
v_mfma_f32_16x16x32_bf16 v[28:31], v[56:59], v[52:55], v[28:31]
ds_read_b128 a[92:95], v5 offset:23552
s_waitcnt vmcnt(12)
s_barrier
s_waitcnt lgkmcnt(4)
v_mfma_f32_16x16x32_bf16 v[32:35], a[32:35], v[224:227], 0
v_exp_f32 v40, v40
v_mfma_f32_16x16x32_bf16 v[36:39], a[48:51], v[224:227], 0
v_exp_f32 v41, v41
v_mfma_f32_16x16x32_bf16 v[32:35], a[36:39], v[228:231], v[32:35]
v_exp_f32 v42, v42
v_mfma_f32_16x16x32_bf16 v[36:39], a[52:55], v[228:231], v[36:39]
ds_read_b128 a[0:3], v6 offset:0
v_exp_f32 v43, v43
v_mfma_f32_16x16x32_bf16 v[32:35], a[40:43], v[232:235], v[32:35]
buffer_load_dwordx4 a[176:179], v4, s[60:63], s30 offen offset:2048 sc1
v_cvt_pk_bf16_f32 v52, v40, v41
v_mfma_f32_16x16x32_bf16 v[36:39], a[56:59], v[232:235], v[36:39]
v_exp_f32 v44, v44
v_mfma_f32_16x16x32_bf16 v[32:35], a[44:47], v[236:239], v[32:35]
v_exp_f32 v45, v45
v_mfma_f32_16x16x32_bf16 v[36:39], a[60:63], v[236:239], v[36:39]
v_exp_f32 v46, v46
v_mfma_f32_16x16x32_bf16 v[128:131], a[64:67], v[48:51], v[128:131]
ds_read_b128 a[16:19], v6 offset:4096
v_exp_f32 v47, v47
v_mfma_f32_16x16x32_bf16 v[132:135], a[68:71], v[48:51], v[132:135]
ds_read_b128 a[4:7], v6 offset:1024
v_cvt_pk_bf16_f32 v53, v42, v43
v_mfma_f32_16x16x32_bf16 v[136:139], a[72:75], v[48:51], v[136:139]
buffer_load_dwordx4 a[180:183], v4, s[60:63], s30 offen offset:3072 sc1
v_cvt_pk_bf16_f32 v54, v44, v45
v_mfma_f32_16x16x32_bf16 v[140:143], a[76:79], v[48:51], v[140:143]
ds_read_b128 a[20:23], v6 offset:5120
v_cvt_pk_bf16_f32 v55, v46, v47
s_waitcnt lgkmcnt(3)
v_mfma_f32_16x16x32_bf16 v[144:147], a[80:83], v[48:51], v[144:147]
ds_read_b128 a[8:11], v6 offset:2048
v_mfma_f32_16x16x32_bf16 v[148:151], a[84:87], v[48:51], v[148:151]
ds_read_b128 a[24:27], v6 offset:6144
v_mfma_f32_16x16x32_bf16 v[152:155], a[88:91], v[48:51], v[152:155]
s_add_u32 s30, s68, 2
s_and_b32 s30, s30, 7
s_lshl_b32 s30, s30, 15
s_add_u32 s30, s30, s59
buffer_load_dwordx4 a[184:187], v4, s[60:63], s30 offen offset:0 sc1
v_mfma_f32_16x16x32_bf16 v[156:159], a[92:95], v[48:51], v[156:159]
ds_read_b128 a[12:15], v6 offset:3072
v_mfma_f32_16x16x32_bf16 v[24:27], v[56:59], v[48:51], v[24:27]
ds_read_b128 a[28:31], v6 offset:7168
v_mfma_f32_16x16x32_bf16 v[40:43], a[32:35], v[240:243], 0
v_exp_f32 v32, v32
v_mfma_f32_16x16x32_bf16 v[44:47], a[48:51], v[240:243], 0
v_exp_f32 v33, v33
v_mfma_f32_16x16x32_bf16 v[40:43], a[36:39], v[244:247], v[40:43]
v_exp_f32 v34, v34
v_mfma_f32_16x16x32_bf16 v[44:47], a[52:55], v[244:247], v[44:47]
ds_read_b128 a[96:99], v5 offset:24576
v_exp_f32 v35, v35
v_mfma_f32_16x16x32_bf16 v[40:43], a[40:43], v[248:251], v[40:43]
buffer_load_dwordx4 a[188:191], v4, s[60:63], s30 offen offset:1024 sc1
v_cvt_pk_bf16_f32 v48, v32, v33
v_mfma_f32_16x16x32_bf16 v[44:47], a[56:59], v[248:251], v[44:47]
v_exp_f32 v36, v36
v_mfma_f32_16x16x32_bf16 v[40:43], a[44:47], v[252:255], v[40:43]
v_exp_f32 v37, v37
v_mfma_f32_16x16x32_bf16 v[44:47], a[60:63], v[252:255], v[44:47]
v_exp_f32 v38, v38
v_mfma_f32_16x16x32_bf16 v[160:163], a[64:67], v[52:55], v[160:163]
ds_read_b128 a[100:103], v5 offset:25600
v_exp_f32 v39, v39
v_mfma_f32_16x16x32_bf16 v[164:167], a[68:71], v[52:55], v[164:167]
ds_read_b128 a[104:107], v5 offset:26624
v_cvt_pk_bf16_f32 v49, v34, v35
v_mfma_f32_16x16x32_bf16 v[168:171], a[72:75], v[52:55], v[168:171]
buffer_load_dwordx4 a[192:195], v4, s[60:63], s30 offen offset:2048 sc1
v_cvt_pk_bf16_f32 v50, v36, v37
v_mfma_f32_16x16x32_bf16 v[172:175], a[76:79], v[52:55], v[172:175]
ds_read_b128 a[108:111], v5 offset:27648
v_cvt_pk_bf16_f32 v51, v38, v39
v_mfma_f32_16x16x32_bf16 v[176:179], a[80:83], v[52:55], v[176:179]
ds_read_b128 a[112:115], v5 offset:28672
v_mfma_f32_16x16x32_bf16 v[180:183], a[84:87], v[52:55], v[180:183]
ds_read_b128 a[116:119], v5 offset:29696
v_mfma_f32_16x16x32_bf16 v[184:187], a[88:91], v[52:55], v[184:187]
buffer_load_dwordx4 a[196:199], v4, s[60:63], s30 offen offset:3072 sc1
v_mfma_f32_16x16x32_bf16 v[188:191], a[92:95], v[52:55], v[188:191]
ds_read_b128 a[120:123], v5 offset:30720
v_mfma_f32_16x16x32_bf16 v[28:31], v[56:59], v[52:55], v[28:31]
ds_read_b128 a[124:127], v5 offset:31744
v_mov_b32 v5, v6
v_add_u32 v6, 0x8000, v6
v_and_b32 v6, 0x1ffff, v6
v_mfma_f32_16x16x32_bf16 v[32:35], a[0:3], v[224:227], 0
v_exp_f32 v40, v40
s_waitcnt lgkmcnt(3)
v_mfma_f32_16x16x32_bf16 v[36:39], a[16:19], v[224:227], 0
v_exp_f32 v41, v41
v_mfma_f32_16x16x32_bf16 v[32:35], a[4:7], v[228:231], v[32:35]
v_exp_f32 v42, v42
v_mfma_f32_16x16x32_bf16 v[36:39], a[20:23], v[228:231], v[36:39]
ds_read_b128 a[32:35], v5 offset:8192
v_exp_f32 v43, v43
v_mfma_f32_16x16x32_bf16 v[32:35], a[8:11], v[232:235], v[32:35]
s_add_u32 s30, s68, 3
s_and_b32 s30, s30, 7
s_lshl_b32 s30, s30, 15
s_add_u32 s30, s30, s59
buffer_load_dwordx4 a[200:203], v4, s[60:63], s30 offen offset:0 sc1
v_cvt_pk_bf16_f32 v52, v40, v41
v_mfma_f32_16x16x32_bf16 v[36:39], a[24:27], v[232:235], v[36:39]
v_exp_f32 v44, v44
v_mfma_f32_16x16x32_bf16 v[32:35], a[12:15], v[236:239], v[32:35]
v_exp_f32 v45, v45
v_mfma_f32_16x16x32_bf16 v[36:39], a[28:31], v[236:239], v[36:39]
v_exp_f32 v46, v46
v_mfma_f32_16x16x32_bf16 v[128:131], a[96:99], v[48:51], v[128:131]
ds_read_b128 a[48:51], v5 offset:12288
v_exp_f32 v47, v47
v_mfma_f32_16x16x32_bf16 v[132:135], a[100:103], v[48:51], v[132:135]
ds_read_b128 a[36:39], v5 offset:9216
v_cvt_pk_bf16_f32 v53, v42, v43
v_mfma_f32_16x16x32_bf16 v[136:139], a[104:107], v[48:51], v[136:139]
buffer_load_dwordx4 a[204:207], v4, s[60:63], s30 offen offset:1024 sc1
v_cvt_pk_bf16_f32 v54, v44, v45
v_mfma_f32_16x16x32_bf16 v[140:143], a[108:111], v[48:51], v[140:143]
ds_read_b128 a[52:55], v5 offset:13312
v_cvt_pk_bf16_f32 v55, v46, v47
v_mfma_f32_16x16x32_bf16 v[144:147], a[112:115], v[48:51], v[144:147]
ds_read_b128 a[40:43], v5 offset:10240
s_waitcnt lgkmcnt(4)
v_mfma_f32_16x16x32_bf16 v[148:151], a[116:119], v[48:51], v[148:151]
ds_read_b128 a[56:59], v5 offset:14336
v_mfma_f32_16x16x32_bf16 v[152:155], a[120:123], v[48:51], v[152:155]
buffer_load_dwordx4 a[208:211], v4, s[60:63], s30 offen offset:2048 sc1
v_mfma_f32_16x16x32_bf16 v[156:159], a[124:127], v[48:51], v[156:159]
ds_read_b128 a[44:47], v5 offset:11264
v_mfma_f32_16x16x32_bf16 v[24:27], v[56:59], v[48:51], v[24:27]
ds_read_b128 a[60:63], v5 offset:15360
v_mfma_f32_16x16x32_bf16 v[40:43], a[0:3], v[240:243], 0
v_exp_f32 v32, v32
v_mfma_f32_16x16x32_bf16 v[44:47], a[16:19], v[240:243], 0
v_exp_f32 v33, v33
v_mfma_f32_16x16x32_bf16 v[40:43], a[4:7], v[244:247], v[40:43]
v_exp_f32 v34, v34
v_mfma_f32_16x16x32_bf16 v[44:47], a[20:23], v[244:247], v[44:47]
ds_read_b128 a[64:67], v5 offset:16384
v_exp_f32 v35, v35
v_mfma_f32_16x16x32_bf16 v[40:43], a[8:11], v[248:251], v[40:43]
buffer_load_dwordx4 a[212:215], v4, s[60:63], s30 offen offset:3072 sc1
v_cvt_pk_bf16_f32 v48, v32, v33
v_mfma_f32_16x16x32_bf16 v[44:47], a[24:27], v[248:251], v[44:47]
v_exp_f32 v36, v36
v_mfma_f32_16x16x32_bf16 v[40:43], a[12:15], v[252:255], v[40:43]
v_exp_f32 v37, v37
v_mfma_f32_16x16x32_bf16 v[44:47], a[28:31], v[252:255], v[44:47]
v_exp_f32 v38, v38
v_mfma_f32_16x16x32_bf16 v[160:163], a[96:99], v[52:55], v[160:163]
ds_read_b128 a[68:71], v5 offset:17408
v_exp_f32 v39, v39
v_mfma_f32_16x16x32_bf16 v[164:167], a[100:103], v[52:55], v[164:167]
ds_read_b128 a[72:75], v5 offset:18432
v_cvt_pk_bf16_f32 v49, v34, v35
v_mfma_f32_16x16x32_bf16 v[168:171], a[104:107], v[52:55], v[168:171]
s_add_u32 s30, s68, 4
s_and_b32 s30, s30, 7
s_lshl_b32 s30, s30, 15
s_add_u32 s30, s30, s59
buffer_load_dwordx4 a[216:219], v4, s[60:63], s30 offen offset:0 sc1
v_cvt_pk_bf16_f32 v50, v36, v37
v_mfma_f32_16x16x32_bf16 v[172:175], a[108:111], v[52:55], v[172:175]
ds_read_b128 a[76:79], v5 offset:19456
v_cvt_pk_bf16_f32 v51, v38, v39
v_mfma_f32_16x16x32_bf16 v[176:179], a[112:115], v[52:55], v[176:179]
ds_read_b128 a[80:83], v5 offset:20480
v_mfma_f32_16x16x32_bf16 v[180:183], a[116:119], v[52:55], v[180:183]
ds_read_b128 a[84:87], v5 offset:21504
v_mfma_f32_16x16x32_bf16 v[184:187], a[120:123], v[52:55], v[184:187]
buffer_load_dwordx4 a[220:223], v4, s[60:63], s30 offen offset:1024 sc1
v_mfma_f32_16x16x32_bf16 v[188:191], a[124:127], v[52:55], v[188:191]
ds_read_b128 a[88:91], v5 offset:22528
v_mfma_f32_16x16x32_bf16 v[28:31], v[56:59], v[52:55], v[28:31]
ds_read_b128 a[92:95], v5 offset:23552
v_mfma_f32_16x16x32_bf16 v[32:35], a[32:35], v[224:227], v[8:11]
ds_read_b128 a[96:99], v5 offset:24576
v_exp_f32 v40, v40
s_waitcnt lgkmcnt(4)
v_mfma_f32_16x16x32_bf16 v[32:35], a[36:39], v[228:231], v[32:35]
buffer_load_dwordx4 a[224:227], v4, s[60:63], s30 offen offset:2048 sc1
v_exp_f32 v41, v41
v_mfma_f32_16x16x32_bf16 v[32:35], a[40:43], v[232:235], v[32:35]
v_exp_f32 v42, v42
v_mfma_f32_16x16x32_bf16 v[32:35], a[44:47], v[236:239], v[32:35]
ds_read_b128 a[100:103], v5 offset:25600
v_exp_f32 v43, v43
v_mfma_f32_16x16x32_bf16 v[128:131], a[64:67], v[48:51], v[128:131]
v_cvt_pk_bf16_f32 v52, v40, v41
v_cvt_pk_bf16_f32 v53, v42, v43
v_mfma_f32_16x16x32_bf16 v[132:135], a[68:71], v[48:51], v[132:135]
buffer_load_dwordx4 a[228:231], v4, s[60:63], s30 offen offset:3072 sc1
v_exp_f32 v44, v44
v_mfma_f32_16x16x32_bf16 v[136:139], a[72:75], v[48:51], v[136:139]
v_exp_f32 v45, v45
v_mfma_f32_16x16x32_bf16 v[140:143], a[76:79], v[48:51], v[140:143]
ds_read_b128 a[104:107], v5 offset:26624
v_exp_f32 v46, v46
v_mfma_f32_16x16x32_bf16 v[144:147], a[80:83], v[48:51], v[144:147]
v_exp_f32 v47, v47
s_waitcnt lgkmcnt(1)
v_mfma_f32_16x16x32_bf16 v[148:151], a[84:87], v[48:51], v[148:151]
s_add_u32 s30, s68, 5
s_and_b32 s30, s30, 7
s_lshl_b32 s30, s30, 15
s_add_u32 s30, s30, s59
buffer_load_dwordx4 a[232:235], v4, s[60:63], s30 offen offset:0 sc1
v_cvt_pk_bf16_f32 v54, v44, v45
v_cvt_pk_bf16_f32 v55, v46, v47
v_mfma_f32_16x16x32_bf16 v[152:155], a[88:91], v[48:51], v[152:155]
v_mfma_f32_16x16x32_bf16 v[156:159], a[92:95], v[48:51], v[156:159]
ds_read_b128 a[108:111], v5 offset:27648
v_mfma_f32_16x16x32_bf16 v[24:27], v[56:59], v[48:51], v[24:27]
v_mfma_f32_16x16x32_bf16 v[40:43], a[32:35], v[240:243], 0
ds_read_b128 a[112:115], v5 offset:28672
v_exp_f32 v32, v32
v_mfma_f32_16x16x32_bf16 v[44:47], a[48:51], v[240:243], v[8:11]
v_exp_f32 v33, v33
v_mfma_f32_16x16x32_bf16 v[40:43], a[36:39], v[244:247], v[40:43]
buffer_load_dwordx4 a[236:239], v4, s[60:63], s30 offen offset:1024 sc1
v_exp_f32 v34, v34
v_mfma_f32_16x16x32_bf16 v[44:47], a[52:55], v[244:247], v[44:47]
v_exp_f32 v35, v35
v_mfma_f32_16x16x32_bf16 v[40:43], a[40:43], v[248:251], v[40:43]
ds_read_b128 a[116:119], v5 offset:29696
v_cvt_pk_bf16_f32 v48, v32, v33
v_cvt_pk_bf16_f32 v49, v34, v35
v_mfma_f32_16x16x32_bf16 v[44:47], a[56:59], v[248:251], v[44:47]
v_mov_b32 v50, 0
v_mov_b32 v51, 0
v_mfma_f32_16x16x32_bf16 v[40:43], a[44:47], v[252:255], v[40:43]
v_mfma_f32_16x16x32_bf16 v[44:47], a[60:63], v[252:255], v[44:47]
buffer_load_dwordx4 a[240:243], v4, s[60:63], s30 offen offset:2048 sc1
v_mfma_f32_16x16x32_bf16 v[160:163], a[64:67], v[52:55], v[160:163]
v_mfma_f32_16x16x32_bf16 v[164:167], a[68:71], v[52:55], v[164:167]
ds_read_b128 a[120:123], v5 offset:30720
v_mfma_f32_16x16x32_bf16 v[168:171], a[72:75], v[52:55], v[168:171]
v_mfma_f32_16x16x32_bf16 v[172:175], a[76:79], v[52:55], v[172:175]
v_mfma_f32_16x16x32_bf16 v[176:179], a[80:83], v[52:55], v[176:179]
buffer_load_dwordx4 a[244:247], v4, s[60:63], s30 offen offset:3072 sc1
v_mfma_f32_16x16x32_bf16 v[180:183], a[84:87], v[52:55], v[180:183]
v_mfma_f32_16x16x32_bf16 v[184:187], a[88:91], v[52:55], v[184:187]
ds_read_b128 a[124:127], v5 offset:31744
v_mfma_f32_16x16x32_bf16 v[188:191], a[92:95], v[52:55], v[188:191]
v_mfma_f32_16x16x32_bf16 v[28:31], v[56:59], v[52:55], v[28:31]
v_mfma_f32_16x16x32_bf16 v[128:131], a[96:99], v[48:51], v[128:131]
v_exp_f32 v40, v40
v_mfma_f32_16x16x32_bf16 v[132:135], a[100:103], v[48:51], v[132:135]
v_exp_f32 v41, v41
s_waitcnt lgkmcnt(1)
v_mfma_f32_16x16x32_bf16 v[136:139], a[104:107], v[48:51], v[136:139]
v_exp_f32 v42, v42
v_mfma_f32_16x16x32_bf16 v[140:143], a[108:111], v[48:51], v[140:143]
v_exp_f32 v43, v43
v_mfma_f32_16x16x32_bf16 v[144:147], a[112:115], v[48:51], v[144:147]
v_cvt_pk_bf16_f32 v52, v40, v41
v_cvt_pk_bf16_f32 v53, v42, v43
v_mfma_f32_16x16x32_bf16 v[148:151], a[116:119], v[48:51], v[148:151]
v_exp_f32 v44, v44
v_mfma_f32_16x16x32_bf16 v[152:155], a[120:123], v[48:51], v[152:155]
v_exp_f32 v45, v45
s_waitcnt lgkmcnt(0)
v_mfma_f32_16x16x32_bf16 v[156:159], a[124:127], v[48:51], v[156:159]
v_exp_f32 v46, v46
v_mfma_f32_16x16x32_bf16 v[24:27], v[56:59], v[48:51], v[24:27]
v_exp_f32 v47, v47
v_cvt_pk_bf16_f32 v54, v44, v45
v_cvt_pk_bf16_f32 v55, v46, v47
s_nop 1
v_mfma_f32_16x16x32_bf16 v[160:163], a[96:99], v[52:55], v[160:163]
v_mfma_f32_16x16x32_bf16 v[164:167], a[100:103], v[52:55], v[164:167]
v_mfma_f32_16x16x32_bf16 v[168:171], a[104:107], v[52:55], v[168:171]
v_mfma_f32_16x16x32_bf16 v[172:175], a[108:111], v[52:55], v[172:175]
v_mfma_f32_16x16x32_bf16 v[176:179], a[112:115], v[52:55], v[176:179]
v_mfma_f32_16x16x32_bf16 v[180:183], a[116:119], v[52:55], v[180:183]
v_mfma_f32_16x16x32_bf16 v[184:187], a[120:123], v[52:55], v[184:187]
v_mfma_f32_16x16x32_bf16 v[188:191], a[124:127], v[52:55], v[188:191]
v_mfma_f32_16x16x32_bf16 v[28:31], v[56:59], v[52:55], v[28:31]
s_waitcnt lgkmcnt(0)
s_nop 7
v_add_f32 v12, v18, v19
v_mov_b32 v13, v12
s_nop 1
v_permlane16_swap_b32 v12, v13
v_add_f32 v12, v12, v13
v_mov_b32 v13, v12
s_nop 1
v_permlane32_swap_b32 v12, v13
v_add_f32 v12, v12, v13
v_add_f32 v12, v12, v24
v_rcp_f32 v12, v12
s_nop 0
v_pk_mul_f32 v[128:129], v[128:129], v[12:13] op_sel_hi:[1,0]
v_pk_mul_f32 v[130:131], v[130:131], v[12:13] op_sel_hi:[1,0]
v_pk_mul_f32 v[132:133], v[132:133], v[12:13] op_sel_hi:[1,0]
v_pk_mul_f32 v[134:135], v[134:135], v[12:13] op_sel_hi:[1,0]
v_pk_mul_f32 v[136:137], v[136:137], v[12:13] op_sel_hi:[1,0]
v_pk_mul_f32 v[138:139], v[138:139], v[12:13] op_sel_hi:[1,0]
v_pk_mul_f32 v[140:141], v[140:141], v[12:13] op_sel_hi:[1,0]
v_pk_mul_f32 v[142:143], v[142:143], v[12:13] op_sel_hi:[1,0]
v_pk_mul_f32 v[144:145], v[144:145], v[12:13] op_sel_hi:[1,0]
v_pk_mul_f32 v[146:147], v[146:147], v[12:13] op_sel_hi:[1,0]
v_pk_mul_f32 v[148:149], v[148:149], v[12:13] op_sel_hi:[1,0]
v_pk_mul_f32 v[150:151], v[150:151], v[12:13] op_sel_hi:[1,0]
v_pk_mul_f32 v[152:153], v[152:153], v[12:13] op_sel_hi:[1,0]
v_pk_mul_f32 v[154:155], v[154:155], v[12:13] op_sel_hi:[1,0]
v_pk_mul_f32 v[156:157], v[156:157], v[12:13] op_sel_hi:[1,0]
v_pk_mul_f32 v[158:159], v[158:159], v[12:13] op_sel_hi:[1,0]
v_cvt_pk_bf16_f32 v128, v128, v129
v_cvt_pk_bf16_f32 v129, v130, v131
v_cvt_pk_bf16_f32 v130, v132, v133
v_cvt_pk_bf16_f32 v131, v134, v135
s_add_u32 s27, s56, 0
buffer_store_dwordx4 v[128:131], v4, s[36:39], s27 offen offset:0 sc1
v_cvt_pk_bf16_f32 v136, v136, v137
v_cvt_pk_bf16_f32 v137, v138, v139
v_cvt_pk_bf16_f32 v138, v140, v141
v_cvt_pk_bf16_f32 v139, v142, v143
s_add_u32 s27, s56, 0x1000
buffer_store_dwordx4 v[136:139], v4, s[36:39], s27 offen offset:0 sc1
v_cvt_pk_bf16_f32 v144, v144, v145
v_cvt_pk_bf16_f32 v145, v146, v147
v_cvt_pk_bf16_f32 v146, v148, v149
v_cvt_pk_bf16_f32 v147, v150, v151
s_add_u32 s27, s56, 0x2000
buffer_store_dwordx4 v[144:147], v4, s[36:39], s27 offen offset:0 sc1
v_cvt_pk_bf16_f32 v152, v152, v153
v_cvt_pk_bf16_f32 v153, v154, v155
v_cvt_pk_bf16_f32 v154, v156, v157
v_cvt_pk_bf16_f32 v155, v158, v159
s_add_u32 s27, s56, 0x3000
buffer_store_dwordx4 v[152:155], v4, s[36:39], s27 offen offset:0 sc1
v_add_f32 v12, v22, v23
v_mov_b32 v13, v12
s_nop 1
v_permlane16_swap_b32 v12, v13
v_add_f32 v12, v12, v13
v_mov_b32 v13, v12
s_nop 1
v_permlane32_swap_b32 v12, v13
v_add_f32 v12, v12, v13
v_add_f32 v12, v12, v28
v_rcp_f32 v12, v12
s_nop 0
v_pk_mul_f32 v[160:161], v[160:161], v[12:13] op_sel_hi:[1,0]
v_pk_mul_f32 v[162:163], v[162:163], v[12:13] op_sel_hi:[1,0]
v_pk_mul_f32 v[164:165], v[164:165], v[12:13] op_sel_hi:[1,0]
v_pk_mul_f32 v[166:167], v[166:167], v[12:13] op_sel_hi:[1,0]
v_pk_mul_f32 v[168:169], v[168:169], v[12:13] op_sel_hi:[1,0]
v_pk_mul_f32 v[170:171], v[170:171], v[12:13] op_sel_hi:[1,0]
v_pk_mul_f32 v[172:173], v[172:173], v[12:13] op_sel_hi:[1,0]
v_pk_mul_f32 v[174:175], v[174:175], v[12:13] op_sel_hi:[1,0]
v_pk_mul_f32 v[176:177], v[176:177], v[12:13] op_sel_hi:[1,0]
v_pk_mul_f32 v[178:179], v[178:179], v[12:13] op_sel_hi:[1,0]
v_pk_mul_f32 v[180:181], v[180:181], v[12:13] op_sel_hi:[1,0]
v_pk_mul_f32 v[182:183], v[182:183], v[12:13] op_sel_hi:[1,0]
v_pk_mul_f32 v[184:185], v[184:185], v[12:13] op_sel_hi:[1,0]
v_pk_mul_f32 v[186:187], v[186:187], v[12:13] op_sel_hi:[1,0]
v_pk_mul_f32 v[188:189], v[188:189], v[12:13] op_sel_hi:[1,0]
v_pk_mul_f32 v[190:191], v[190:191], v[12:13] op_sel_hi:[1,0]
v_cvt_pk_bf16_f32 v160, v160, v161
v_cvt_pk_bf16_f32 v161, v162, v163
v_cvt_pk_bf16_f32 v162, v164, v165
v_cvt_pk_bf16_f32 v163, v166, v167
s_add_u32 s27, s56, 0
s_add_u32 s27, s27, s69
buffer_store_dwordx4 v[160:163], v4, s[36:39], s27 offen offset:0 sc1
v_cvt_pk_bf16_f32 v168, v168, v169
v_cvt_pk_bf16_f32 v169, v170, v171
v_cvt_pk_bf16_f32 v170, v172, v173
v_cvt_pk_bf16_f32 v171, v174, v175
s_add_u32 s27, s56, 0x1000
s_add_u32 s27, s27, s69
buffer_store_dwordx4 v[168:171], v4, s[36:39], s27 offen offset:0 sc1
v_cvt_pk_bf16_f32 v176, v176, v177
v_cvt_pk_bf16_f32 v177, v178, v179
v_cvt_pk_bf16_f32 v178, v180, v181
v_cvt_pk_bf16_f32 v179, v182, v183
s_add_u32 s27, s56, 0x2000
s_add_u32 s27, s27, s69
buffer_store_dwordx4 v[176:179], v4, s[36:39], s27 offen offset:0 sc1
v_cvt_pk_bf16_f32 v184, v184, v185
v_cvt_pk_bf16_f32 v185, v186, v187
v_cvt_pk_bf16_f32 v186, v188, v189
v_cvt_pk_bf16_f32 v187, v190, v191
s_add_u32 s27, s56, 0x3000
s_add_u32 s27, s27, s69
buffer_store_dwordx4 v[184:187], v4, s[36:39], s27 offen offset:0 sc1
s_nop 1
B_D2end_%=:
s_branch N_pref_%=
M_generic_%=:
s_load_dwordx2 s[66:67], s[20:21], 0x68
s_waitcnt lgkmcnt(0)
s_lshl_b32 s27, s25, 19
s_add_u32 s27, s27, 0x2100000
s_add_u32 s66, s66, s27
s_addc_u32 s67, s67, 0
s_mov_b32 s36, s66
s_and_b32 s37, s67, 0xffff
s_mov_b32 s38, 0x80000
s_mov_b32 s39, 0x20000
s_lshl_b32 s27, s26, 3
s_lshl_b32 s28, s23, 1
s_add_u32 s27, s27, s28
s_add_u32 s27, s27, 0
s_lshl_b32 s27, s27, 12
buffer_store_dwordx4 v[192:195], v4, s[36:39], s27 offen offset:0
buffer_store_dwordx4 v[196:199], v4, s[36:39], s27 offen offset:1024
buffer_store_dwordx4 v[200:203], v4, s[36:39], s27 offen offset:2048
buffer_store_dwordx4 v[204:207], v4, s[36:39], s27 offen offset:3072
s_lshl_b32 s27, s26, 3
s_lshl_b32 s28, s23, 1
s_add_u32 s27, s27, s28
s_add_u32 s27, s27, 1
s_lshl_b32 s27, s27, 12
buffer_store_dwordx4 v[208:211], v4, s[36:39], s27 offen offset:0
buffer_store_dwordx4 v[212:215], v4, s[36:39], s27 offen offset:1024
buffer_store_dwordx4 v[216:219], v4, s[36:39], s27 offen offset:2048
buffer_store_dwordx4 v[220:223], v4, s[36:39], s27 offen offset:3072
s_sub_u32 s27, 15, s26
s_lshl_b32 s27, s27, 3
s_lshl_b32 s28, s23, 1
s_add_u32 s27, s27, s28
s_add_u32 s27, s27, 0
s_lshl_b32 s27, s27, 12
buffer_store_dwordx4 v[224:227], v4, s[36:39], s27 offen offset:0
buffer_store_dwordx4 v[228:231], v4, s[36:39], s27 offen offset:1024
buffer_store_dwordx4 v[232:235], v4, s[36:39], s27 offen offset:2048
buffer_store_dwordx4 v[236:239], v4, s[36:39], s27 offen offset:3072
s_sub_u32 s27, 15, s26
s_lshl_b32 s27, s27, 3
s_lshl_b32 s28, s23, 1
s_add_u32 s27, s27, s28
s_add_u32 s27, s27, 1
s_lshl_b32 s27, s27, 12
buffer_store_dwordx4 v[240:243], v4, s[36:39], s27 offen offset:0
buffer_store_dwordx4 v[244:247], v4, s[36:39], s27 offen offset:1024
buffer_store_dwordx4 v[248:251], v4, s[36:39], s27 offen offset:2048
buffer_store_dwordx4 v[252:255], v4, s[36:39], s27 offen offset:3072
s_waitcnt vmcnt(0)
s_barrier
s_cmp_lg_u32 s23, 0
s_cbranch_scc1 F_efg_%=
s_and_b32 s27, s22, 7
s_lshl_b32 s27, s27, 5
s_lshr_b32 s28, s22, 3
s_add_u32 s28, s28, s27
s_lshl_b32 s28, s28, 2
s_add_u32 s28, s28, 0x4000
v_mov_b32 v0, 0
v_mov_b32 v1, s98
s_mov_b64 s[92:93], exec
s_mov_b64 exec, 1
buffer_store_dword v1, v0, s[40:43], s28 offen offset:0 sc1
s_mov_b64 exec, s[92:93]
F_efg_%=:
s_branch FUSED_exit_%=
N_resume_%=:
s_mov_b64 s[20:21], %2
s_mov_b32 s22, %3
s_mov_b32 s23, %4
s_mov_b32 s24, %5
s_and_b32 s27, s22, 7
s_lshl_b32 s27, s27, 2
s_lshr_b32 s28, s22, 3
s_and_b32 s28, s28, 3
s_add_u32 s25, s27, s28
s_lshr_b32 s26, s22, 5
s_load_dwordx2 s[64:65], s[20:21], 0x20
s_load_dwordx2 s[66:67], s[20:21], 0x68
s_load_dwordx2 s[88:89], s[20:21], 0x0
s_load_dwordx2 s[90:91], s[20:21], 0x60
s_load_dwordx2 s[80:81], s[20:21], 0x28
s_waitcnt lgkmcnt(0)
s_and_b32 s27, s22, 7
s_lshl_b32 s27, s27, 6
s_lshr_b32 s28, s22, 3
s_lshl_b32 s28, s28, 1
s_add_u32 s27, s27, s28
s_lshl_b32 s27, s27, 1
s_add_u32 s80, s80, s27
s_addc_u32 s81, s81, 0
s_load_dword s74, s[80:81], 0x0
s_mov_b32 s40, s66
s_and_b32 s41, s67, 0xffff
s_mov_b32 s42, 0x8000000
s_mov_b32 s43, 0x20000
s_mov_b32 s66, s66
s_mov_b32 s67, s67
s_waitcnt lgkmcnt(0)
v_mbcnt_lo_u32_b32 v0, -1, 0
v_mbcnt_hi_u32_b32 v0, -1, v0
v_lshlrev_b32 v4, 4, v0
s_and_b32 s27, s22, 7
s_lshl_b32 s27, s27, 6
s_lshr_b32 s28, s22, 3
s_lshl_b32 s28, s28, 1
s_add_u32 s27, s27, s28
s_lshr_b32 s28, s23, 1
s_add_u32 s27, s27, s28
s_lshl_b32 s27, s27, 12
s_add_u32 s60, s64, s27
s_addc_u32 s61, s65, 0
s_mov_b32 s76, s60
s_and_b32 s77, s61, 0xffff
s_mov_b32 s78, 0x1000
s_mov_b32 s79, 0x20000
s_add_u32 s27, s27, 0x4100000
s_add_u32 s62, s66, s27
s_addc_u32 s63, s67, 0
s_mov_b32 s84, s62
s_and_b32 s85, s63, 0xffff
s_mov_b32 s86, 0x1000
s_mov_b32 s87, 0x20000
v_mbcnt_lo_u32_b32 v12, -1, 0
v_mbcnt_hi_u32_b32 v12, -1, v12
v_lshlrev_b32 v12, 4, v12
s_mov_b32 s27, 0x4000
buffer_load_dwordx4 v[0:3], v12, s[40:43], s27 offen offset:0 sc1
s_waitcnt vmcnt(0)
v_cmp_ne_u32 s[92:93], s98, v0
v_cmp_ne_u32 vcc, s98, v1
s_nop 1
s_or_b64 s[92:93], s[92:93], vcc
v_cmp_ne_u32 vcc, s98, v2
s_nop 1
s_or_b64 s[92:93], s[92:93], vcc
v_cmp_ne_u32 vcc, s98, v3
s_nop 1
s_or_b64 vcc, s[92:93], vcc
s_nop 1
s_cbranch_vccz N_ef_done_%=
v_mbcnt_lo_u32_b32 v12, -1, 0
v_mbcnt_hi_u32_b32 v12, -1, v12
v_lshlrev_b32 v12, 4, v12
s_mov_b32 s27, 0x4000
s_memrealtime s[94:95]
s_waitcnt lgkmcnt(0)
N_ef_%=:
buffer_load_dwordx4 v[0:3], v12, s[40:43], s27 offen offset:0 sc1
s_waitcnt vmcnt(0)
v_cmp_ne_u32 s[92:93], s98, v0
v_cmp_ne_u32 vcc, s98, v1
s_nop 1
s_or_b64 s[92:93], s[92:93], vcc
v_cmp_ne_u32 vcc, s98, v2
s_nop 1
s_or_b64 s[92:93], s[92:93], vcc
v_cmp_ne_u32 vcc, s98, v3
s_nop 1
s_or_b64 vcc, s[92:93], vcc
s_nop 1
s_cbranch_vccz N_ef_ok_%=
s_sleep 1
s_memrealtime s[96:97]
s_waitcnt lgkmcnt(0)
s_sub_u32 s29, s96, s94
s_cmp_lt_u32 s29, 0x1e8480
s_cbranch_scc1 N_ef_%=
N_ef_ok_%=:
N_ef_done_%=:
v_mbcnt_lo_u32_b32 v12, -1, 0
v_mbcnt_hi_u32_b32 v12, -1, v12
s_and_b32 s27, s23, 1
s_lshl_b32 s27, s27, 6
v_add_u32 v12, s27, v12
v_lshlrev_b32 v12, 4, v12
v_sub_u32 v13, 0xff0, v12
v_subrev_u32 v14, 16, v13
v_subrev_u32 v15, 0x800, v13
v_subrev_u32 v12, 0x810, v13
buffer_load_dwordx4 a[132:135], v13, s[76:79], 0 offen offset:0
buffer_load_dwordx4 a[128:131], v14, s[76:79], 0 offen offset:0
buffer_load_dwordx4 a[140:143], v15, s[76:79], 0 offen offset:0
buffer_load_dwordx4 a[136:139], v12, s[76:79], 0 offen offset:0
v_mbcnt_lo_u32_b32 v12, -1, 0
v_mbcnt_hi_u32_b32 v12, -1, v12
s_and_b32 s27, s23, 1
s_lshl_b32 s27, s27, 6
v_add_u32 v12, s27, v12
v_lshlrev_b32 v12, 4, v12
buffer_load_dwordx4 a[144:147], v12, s[84:87], 0 offen offset:0 sc1
buffer_load_dwordx4 a[148:151], v12, s[84:87], 0 offen offset:2048 sc1
s_add_u32 s60, s66, 0x4510000
s_addc_u32 s61, s67, 0
s_and_b32 s61, s61, 0xffff
s_mov_b32 s62, 0x400000
s_mov_b32 s63, 0x20000
s_and_b32 s27, s22, 7
s_lshr_b32 s28, s22, 3
s_and_b32 s28, s28, 3
s_lshl_b32 s28, s28, 3
s_add_u32 s27, s27, s28
s_lshl_b32 s27, s27, 5
s_lshl_b32 s28, s23, 3
s_add_u32 s27, s27, s28
s_lshl_b32 s58, s27, 14
s_lshl_b32 s27, s23, 20
s_lshr_b32 s28, s22, 5
s_lshl_b32 s28, s28, 12
s_add_u32 s59, s27, s28
s_lshr_b32 s27, s22, 5
s_lshr_b32 s28, s22, 3
s_and_b32 s28, s28, 3
s_lshl_b32 s28, s28, 1
s_add_u32 s27, s27, s28
s_mul_i32 s27, s27, 1
s_and_b32 s68, s27, 7
s_add_u32 s30, s68, 0
s_and_b32 s30, s30, 7
s_lshl_b32 s30, s30, 15
s_add_u32 s30, s30, s59
buffer_load_dwordx4 a[152:155], v4, s[60:63], s30 offen offset:0 sc1
buffer_load_dwordx4 a[156:159], v4, s[60:63], s30 offen offset:1024 sc1
buffer_load_dwordx4 a[160:163], v4, s[60:63], s30 offen offset:2048 sc1
buffer_load_dwordx4 a[164:167], v4, s[60:63], s30 offen offset:3072 sc1
s_add_u32 s30, s68, 1
s_and_b32 s30, s30, 7
s_lshl_b32 s30, s30, 15
s_add_u32 s30, s30, s59
buffer_load_dwordx4 a[168:171], v4, s[60:63], s30 offen offset:0 sc1
buffer_load_dwordx4 a[172:175], v4, s[60:63], s30 offen offset:1024 sc1
buffer_load_dwordx4 a[176:179], v4, s[60:63], s30 offen offset:2048 sc1
buffer_load_dwordx4 a[180:183], v4, s[60:63], s30 offen offset:3072 sc1
s_add_u32 s30, s68, 2
s_and_b32 s30, s30, 7
s_lshl_b32 s30, s30, 15
s_add_u32 s30, s30, s59
buffer_load_dwordx4 a[184:187], v4, s[60:63], s30 offen offset:0 sc1
buffer_load_dwordx4 a[188:191], v4, s[60:63], s30 offen offset:1024 sc1
buffer_load_dwordx4 a[192:195], v4, s[60:63], s30 offen offset:2048 sc1
buffer_load_dwordx4 a[196:199], v4, s[60:63], s30 offen offset:3072 sc1
s_add_u32 s30, s68, 3
s_and_b32 s30, s30, 7
s_lshl_b32 s30, s30, 15
s_add_u32 s30, s30, s59
buffer_load_dwordx4 a[200:203], v4, s[60:63], s30 offen offset:0 sc1
buffer_load_dwordx4 a[204:207], v4, s[60:63], s30 offen offset:1024 sc1
buffer_load_dwordx4 a[208:211], v4, s[60:63], s30 offen offset:2048 sc1
buffer_load_dwordx4 a[212:215], v4, s[60:63], s30 offen offset:3072 sc1
s_add_u32 s30, s68, 4
s_and_b32 s30, s30, 7
s_lshl_b32 s30, s30, 15
s_add_u32 s30, s30, s59
buffer_load_dwordx4 a[216:219], v4, s[60:63], s30 offen offset:0 sc1
buffer_load_dwordx4 a[220:223], v4, s[60:63], s30 offen offset:1024 sc1
buffer_load_dwordx4 a[224:227], v4, s[60:63], s30 offen offset:2048 sc1
buffer_load_dwordx4 a[228:231], v4, s[60:63], s30 offen offset:3072 sc1
s_add_u32 s30, s68, 5
s_and_b32 s30, s30, 7
s_lshl_b32 s30, s30, 15
s_add_u32 s30, s30, s59
buffer_load_dwordx4 a[232:235], v4, s[60:63], s30 offen offset:0 sc1
buffer_load_dwordx4 a[236:239], v4, s[60:63], s30 offen offset:1024 sc1
buffer_load_dwordx4 a[240:243], v4, s[60:63], s30 offen offset:2048 sc1
buffer_load_dwordx4 a[244:247], v4, s[60:63], s30 offen offset:3072 sc1
s_waitcnt vmcnt(0)
s_mov_b32 s99, 0
N_pref_%=:
s_and_b32 s27, s22, 7
s_lshl_b32 s27, s27, 6
s_lshr_b32 s28, s22, 3
s_lshl_b32 s28, s28, 1
s_add_u32 s27, s27, s28
s_lshr_b32 s28, s23, 1
s_add_u32 s29, s27, s28
s_lshl_b32 s29, s29, 12
s_add_u32 s29, s29, 0x4300000
s_add_u32 s44, s66, s29
s_addc_u32 s45, s67, 0
s_and_b32 s45, s45, 0xffff
s_mov_b32 s46, 0x1000
s_mov_b32 s47, 0x20000
s_mul_i32 s52, s28, 0xa400
s_add_u32 s52, s52, s24
s_mov_b32 s76, s88
s_and_b32 s77, s89, 0xffff
s_mov_b32 s78, 0x200000
s_mov_b32 s79, 0x20000
s_mov_b32 s94, s90
s_mov_b32 s95, s91
s_mov_b32 s84, s94
s_and_b32 s85, s95, 0xffff
s_mov_b32 s86, 0x200000
s_mov_b32 s87, 0x20000
s_add_u32 s36, s66, 0x3100000
s_addc_u32 s37, s67, 0
s_and_b32 s37, s37, 0xffff
s_mov_b32 s38, 0x1000000
s_mov_b32 s39, 0x20000
s_lshl_b32 s28, s74, 16
s_and_b32 s53, s74, 0xffff0000
s_lshr_b32 s27, s23, 1
s_cmp_eq_u32 s27, 0
s_cselect_b32 s53, s28, s53
s_waitcnt vmcnt(32)
s_barrier
v_mbcnt_lo_u32_b32 v3, -1, 0
v_mbcnt_hi_u32_b32 v3, -1, v3
s_and_b32 s27, s23, 1
s_lshl_b32 s27, s27, 6
v_add_u32 v3, s27, v3
v_mov_b32 v15, 0xffff
v_lshlrev_b32 v14, 4, v3
v_add_u32 v14, s52, v14
v_accvgpr_read_b32 v20, a128
v_accvgpr_read_b32 v24, a132
v_accvgpr_read_b32 v21, a129
v_accvgpr_read_b32 v25, a133
v_accvgpr_read_b32 v22, a130
v_accvgpr_read_b32 v26, a134
v_accvgpr_read_b32 v23, a131
v_accvgpr_read_b32 v27, a135
v_alignbit_b32 v52, v27, v27, 16
v_alignbit_b32 v53, v26, v26, 16
v_alignbit_b32 v54, v25, v25, 16
v_alignbit_b32 v55, v24, v24, 16
ds_write_b128 v14, v[52:55] offset:0
v_bfi_b32 v56, v15, v27, v26
v_bfi_b32 v57, v15, v26, v25
v_bfi_b32 v58, v15, v25, v24
v_bfi_b32 v59, v15, v24, v23
ds_write_b128 v14, v[56:59] offset:4288
v_alignbit_b32 v52, v26, v26, 16
v_alignbit_b32 v53, v25, v25, 16
v_alignbit_b32 v54, v24, v24, 16
v_alignbit_b32 v55, v23, v23, 16
ds_write_b128 v14, v[52:55] offset:8576
v_bfi_b32 v56, v15, v26, v25
v_bfi_b32 v57, v15, v25, v24
v_bfi_b32 v58, v15, v24, v23
v_bfi_b32 v59, v15, v23, v22
ds_write_b128 v14, v[56:59] offset:12864
v_alignbit_b32 v52, v25, v25, 16
v_alignbit_b32 v53, v24, v24, 16
v_alignbit_b32 v54, v23, v23, 16
v_alignbit_b32 v55, v22, v22, 16
ds_write_b128 v14, v[52:55] offset:17152
v_bfi_b32 v56, v15, v25, v24
v_bfi_b32 v57, v15, v24, v23
v_bfi_b32 v58, v15, v23, v22
v_bfi_b32 v59, v15, v22, v21
ds_write_b128 v14, v[56:59] offset:21440
v_alignbit_b32 v52, v24, v24, 16
v_alignbit_b32 v53, v23, v23, 16
v_alignbit_b32 v54, v22, v22, 16
v_alignbit_b32 v55, v21, v21, 16
ds_write_b128 v14, v[52:55] offset:25728
v_bfi_b32 v56, v15, v24, v23
v_bfi_b32 v57, v15, v23, v22
v_bfi_b32 v58, v15, v22, v21
v_bfi_b32 v59, v15, v21, v20
ds_write_b128 v14, v[56:59] offset:30016
v_accvgpr_read_b32 v20, a136
v_accvgpr_read_b32 v24, a140
v_accvgpr_read_b32 v21, a137
v_accvgpr_read_b32 v25, a141
v_accvgpr_read_b32 v22, a138
v_accvgpr_read_b32 v26, a142
v_accvgpr_read_b32 v23, a139
v_accvgpr_read_b32 v27, a143
v_alignbit_b32 v52, v27, v27, 16
v_alignbit_b32 v53, v26, v26, 16
v_alignbit_b32 v54, v25, v25, 16
v_alignbit_b32 v55, v24, v24, 16
ds_write_b128 v14, v[52:55] offset:2048
v_bfi_b32 v56, v15, v27, v26
v_bfi_b32 v57, v15, v26, v25
v_bfi_b32 v58, v15, v25, v24
v_bfi_b32 v59, v15, v24, v23
ds_write_b128 v14, v[56:59] offset:6336
v_alignbit_b32 v52, v26, v26, 16
v_alignbit_b32 v53, v25, v25, 16
v_alignbit_b32 v54, v24, v24, 16
v_alignbit_b32 v55, v23, v23, 16
ds_write_b128 v14, v[52:55] offset:10624
v_bfi_b32 v56, v15, v26, v25
v_bfi_b32 v57, v15, v25, v24
v_bfi_b32 v58, v15, v24, v23
v_bfi_b32 v59, v15, v23, v22
ds_write_b128 v14, v[56:59] offset:14912
v_alignbit_b32 v52, v25, v25, 16
v_alignbit_b32 v53, v24, v24, 16
v_alignbit_b32 v54, v23, v23, 16
v_alignbit_b32 v55, v22, v22, 16
ds_write_b128 v14, v[52:55] offset:19200
v_bfi_b32 v56, v15, v25, v24
v_bfi_b32 v57, v15, v24, v23
v_bfi_b32 v58, v15, v23, v22
v_bfi_b32 v59, v15, v22, v21
ds_write_b128 v14, v[56:59] offset:23488
v_alignbit_b32 v52, v24, v24, 16
v_alignbit_b32 v53, v23, v23, 16
v_alignbit_b32 v54, v22, v22, 16
v_alignbit_b32 v55, v21, v21, 16
ds_write_b128 v14, v[52:55] offset:27776
v_bfi_b32 v56, v15, v24, v23
v_bfi_b32 v57, v15, v23, v22
v_bfi_b32 v58, v15, v22, v21
v_bfi_b32 v59, v15, v21, v20
ds_write_b128 v14, v[56:59] offset:32064
v_mov_b32 v28, 0
v_mov_b32 v29, 0
v_mov_b32 v30, 0
v_mov_b32 v31, 0
v_and_b32 v16, 63, v3
v_lshrrev_b32 v17, 3, v16
v_mul_u32_u24 v17, 0x10c0, v17
v_and_b32 v16, 7, v16
v_lshl_add_u32 v17, v16, 4, v17
v_add_u32 v17, s52, v17
ds_write_b128 v17, v[28:31] offset:4096
v_lshrrev_b32 v16, 3, v3
v_mul_u32_u24 v16, 0xa0, v16
v_and_b32 v17, 7, v3
v_lshl_add_u32 v16, v17, 4, v16
v_add_u32 v16, s52, v16
v_accvgpr_read_b32 v44, a144
v_accvgpr_read_b32 v45, a145
v_accvgpr_read_b32 v46, a146
v_accvgpr_read_b32 v47, a147
ds_write_b128 v16, v[44:47] offset:36864
v_accvgpr_read_b32 v48, a148
v_accvgpr_read_b32 v49, a149
v_accvgpr_read_b32 v50, a150
v_accvgpr_read_b32 v51, a151
ds_write_b128 v16, v[48:51] offset:39424
ds_write_b128 v16, v[28:31] offset:34304
v_and_b32 v16, 63, v3
v_and_b32 v17, 7, v16
v_sub_u32 v17, 7, v17
v_mul_u32_u24 v17, 0x10c0, v17
v_lshrrev_b32 v18, 4, v16
v_bfe_u32 v19, v16, 3, 1
v_sub_u32 v18, v18, v19
v_add_u32 v18, 1, v18
v_lshl_add_u32 v12, v18, 4, v17
v_add_u32 v12, s52, v12
v_and_b32 v17, 15, v16
v_mul_u32_u24 v17, 0xa0, v17
v_lshrrev_b32 v18, 4, v16
v_lshl_add_u32 v13, v18, 4, v17
v_add_u32 v13, 0x7ca0, v13
v_add_u32 v13, s52, v13
s_waitcnt vmcnt(0) lgkmcnt(0)
s_barrier
s_cmp_lg_u32 s23, 0
s_cbranch_scc1 F_ctx_%=
s_lshl_b32 s28, s26, 5
s_add_u32 s28, s28, s25
s_lshl_b32 s28, s28, 2
s_add_u32 s28, s28, 0x2000
v_mov_b32 v0, 0
v_mov_b32 v1, s98
s_mov_b64 s[92:93], exec
s_mov_b64 exec, 1
buffer_store_dword v1, v0, s[40:43], s28 offen offset:0 sc1
s_mov_b64 exec, s[92:93]
F_ctx_%=:
s_and_b32 s27, s23, 1
s_cmp_eq_u32 s27, 1
s_cbranch_scc1 N_h1_%=
ds_read_b128 v[136:139], v12 offset:4128
ds_read_b128 v[140:143], v12 offset:4096
ds_read_b128 v[64:67], v12 offset:4064
ds_read_b128 v[68:71], v12 offset:4032
ds_read_b128 v[72:75], v12 offset:4000
ds_read_b128 v[76:79], v12 offset:3968
ds_read_b128 v[144:147], v13 offset:4960
ds_read_b128 v[148:151], v13 offset:5024
ds_read_b128 v[152:155], v13 offset:7520
ds_read_b128 v[156:159], v13 offset:7584
s_waitcnt lgkmcnt(3)
v_mfma_f32_16x16x32_bf16 a[0:3], v[64:67], v[144:147], 0
s_add_u32 s30, s68, 6
s_and_b32 s30, s30, 7
s_lshl_b32 s30, s30, 15
s_add_u32 s30, s30, s59
buffer_load_dwordx4 a[64:67], v4, s[60:63], s30 offen offset:0 sc1
v_mfma_f32_16x16x32_bf16 a[8:11], v[68:71], v[144:147], 0
ds_read_b128 v[80:83], v12 offset:3936
v_mfma_f32_16x16x32_bf16 a[16:19], v[72:75], v[144:147], 0
v_mfma_f32_16x16x32_bf16 a[24:27], v[76:79], v[144:147], 0
ds_read_b128 v[84:87], v12 offset:3904
s_waitcnt lgkmcnt(2)
v_mfma_f32_16x16x32_bf16 a[0:3], v[136:139], v[148:151], a[0:3]
v_mfma_f32_16x16x32_bf16 a[8:11], v[140:143], v[148:151], a[8:11]
ds_read_b128 v[88:91], v12 offset:3872
buffer_load_dwordx4 a[68:71], v4, s[60:63], s30 offen offset:1024 sc1
v_mfma_f32_16x16x32_bf16 a[16:19], v[64:67], v[148:151], a[16:19]
ds_read_b128 v[92:95], v12 offset:3840
v_mfma_f32_16x16x32_bf16 a[24:27], v[68:71], v[148:151], a[24:27]
v_mfma_f32_16x16x32_bf16 a[4:7], v[64:67], v[152:155], 0
ds_read_b128 v[160:163], v13 offset:4800
v_mfma_f32_16x16x32_bf16 a[12:15], v[68:71], v[152:155], 0
v_mfma_f32_16x16x32_bf16 a[20:23], v[72:75], v[152:155], 0
ds_read_b128 v[164:167], v13 offset:4864
buffer_load_dwordx4 a[72:75], v4, s[60:63], s30 offen offset:2048 sc1
v_mfma_f32_16x16x32_bf16 a[28:31], v[76:79], v[152:155], 0
v_mfma_f32_16x16x32_bf16 a[4:7], v[136:139], v[156:159], a[4:7]
ds_read_b128 v[168:171], v13 offset:7360
v_mfma_f32_16x16x32_bf16 a[12:15], v[140:143], v[156:159], a[12:15]
ds_read_b128 v[172:175], v13 offset:7424
v_mfma_f32_16x16x32_bf16 a[20:23], v[64:67], v[156:159], a[20:23]
v_mfma_f32_16x16x32_bf16 a[28:31], v[68:71], v[156:159], a[28:31]
s_waitcnt lgkmcnt(2)
v_mfma_f32_16x16x32_bf16 a[0:3], v[80:83], v[160:163], a[0:3]
buffer_load_dwordx4 a[76:79], v4, s[60:63], s30 offen offset:3072 sc1
v_mfma_f32_16x16x32_bf16 a[8:11], v[84:87], v[160:163], a[8:11]
ds_read_b128 v[96:99], v12 offset:3808
v_mfma_f32_16x16x32_bf16 a[16:19], v[88:91], v[160:163], a[16:19]
v_mfma_f32_16x16x32_bf16 a[24:27], v[92:95], v[160:163], a[24:27]
ds_read_b128 v[100:103], v12 offset:3776
v_mfma_f32_16x16x32_bf16 a[0:3], v[72:75], v[164:167], a[0:3]
v_mfma_f32_16x16x32_bf16 a[8:11], v[76:79], v[164:167], a[8:11]
ds_read_b128 v[104:107], v12 offset:3744
s_add_u32 s30, s68, 7
s_and_b32 s30, s30, 7
s_lshl_b32 s30, s30, 15
s_add_u32 s30, s30, s59
buffer_load_dwordx4 a[80:83], v4, s[60:63], s30 offen offset:0 sc1
v_mfma_f32_16x16x32_bf16 a[16:19], v[80:83], v[164:167], a[16:19]
ds_read_b128 v[108:111], v12 offset:3712
v_mfma_f32_16x16x32_bf16 a[24:27], v[84:87], v[164:167], a[24:27]
s_waitcnt lgkmcnt(2)
v_mfma_f32_16x16x32_bf16 a[4:7], v[80:83], v[168:171], a[4:7]
ds_read_b128 v[144:147], v13 offset:4640
v_mfma_f32_16x16x32_bf16 a[12:15], v[84:87], v[168:171], a[12:15]
v_mfma_f32_16x16x32_bf16 a[20:23], v[88:91], v[168:171], a[20:23]
ds_read_b128 v[148:151], v13 offset:4704
buffer_load_dwordx4 a[84:87], v4, s[60:63], s30 offen offset:1024 sc1
v_mfma_f32_16x16x32_bf16 a[28:31], v[92:95], v[168:171], a[28:31]
v_mfma_f32_16x16x32_bf16 a[4:7], v[72:75], v[172:175], a[4:7]
ds_read_b128 v[152:155], v13 offset:7200
v_mfma_f32_16x16x32_bf16 a[12:15], v[76:79], v[172:175], a[12:15]
ds_read_b128 v[156:159], v13 offset:7264
v_mfma_f32_16x16x32_bf16 a[20:23], v[80:83], v[172:175], a[20:23]
v_mfma_f32_16x16x32_bf16 a[28:31], v[84:87], v[172:175], a[28:31]
s_waitcnt lgkmcnt(2)
v_mfma_f32_16x16x32_bf16 a[0:3], v[96:99], v[144:147], a[0:3]
buffer_load_dwordx4 a[88:91], v4, s[60:63], s30 offen offset:2048 sc1
v_mfma_f32_16x16x32_bf16 a[8:11], v[100:103], v[144:147], a[8:11]
ds_read_b128 v[112:115], v12 offset:3680
v_mfma_f32_16x16x32_bf16 a[16:19], v[104:107], v[144:147], a[16:19]
v_mfma_f32_16x16x32_bf16 a[24:27], v[108:111], v[144:147], a[24:27]
ds_read_b128 v[116:119], v12 offset:3648
v_mfma_f32_16x16x32_bf16 a[0:3], v[88:91], v[148:151], a[0:3]
v_mfma_f32_16x16x32_bf16 a[8:11], v[92:95], v[148:151], a[8:11]
ds_read_b128 v[120:123], v12 offset:3616
buffer_load_dwordx4 a[92:95], v4, s[60:63], s30 offen offset:3072 sc1
v_mfma_f32_16x16x32_bf16 a[16:19], v[96:99], v[148:151], a[16:19]
ds_read_b128 v[124:127], v12 offset:3584
v_mfma_f32_16x16x32_bf16 a[24:27], v[100:103], v[148:151], a[24:27]
s_waitcnt lgkmcnt(2)
v_mfma_f32_16x16x32_bf16 a[4:7], v[96:99], v[152:155], a[4:7]
ds_read_b128 v[160:163], v13 offset:4480
v_mfma_f32_16x16x32_bf16 a[12:15], v[100:103], v[152:155], a[12:15]
v_mfma_f32_16x16x32_bf16 a[20:23], v[104:107], v[152:155], a[20:23]
ds_read_b128 v[164:167], v13 offset:4544
s_add_u32 s30, s68, 0
s_and_b32 s30, s30, 7
s_or_b32 s30, s30, 8
s_lshl_b32 s30, s30, 15
s_add_u32 s30, s30, s59
buffer_load_dwordx4 a[96:99], v4, s[60:63], s30 offen offset:0 sc1
v_mfma_f32_16x16x32_bf16 a[28:31], v[108:111], v[152:155], a[28:31]
v_mfma_f32_16x16x32_bf16 a[4:7], v[88:91], v[156:159], a[4:7]
ds_read_b128 v[168:171], v13 offset:7040
v_mfma_f32_16x16x32_bf16 a[12:15], v[92:95], v[156:159], a[12:15]
ds_read_b128 v[172:175], v13 offset:7104
v_mfma_f32_16x16x32_bf16 a[20:23], v[96:99], v[156:159], a[20:23]
v_mfma_f32_16x16x32_bf16 a[28:31], v[100:103], v[156:159], a[28:31]
s_waitcnt lgkmcnt(2)
v_mfma_f32_16x16x32_bf16 a[0:3], v[112:115], v[160:163], a[0:3]
buffer_load_dwordx4 a[100:103], v4, s[60:63], s30 offen offset:1024 sc1
v_mfma_f32_16x16x32_bf16 a[8:11], v[116:119], v[160:163], a[8:11]
ds_read_b128 v[128:131], v12 offset:3552
v_mfma_f32_16x16x32_bf16 a[16:19], v[120:123], v[160:163], a[16:19]
v_mfma_f32_16x16x32_bf16 a[24:27], v[124:127], v[160:163], a[24:27]
ds_read_b128 v[132:135], v12 offset:3520
v_mfma_f32_16x16x32_bf16 a[0:3], v[104:107], v[164:167], a[0:3]
v_mfma_f32_16x16x32_bf16 a[8:11], v[108:111], v[164:167], a[8:11]
ds_read_b128 v[136:139], v12 offset:3488
buffer_load_dwordx4 a[104:107], v4, s[60:63], s30 offen offset:2048 sc1
v_mfma_f32_16x16x32_bf16 a[16:19], v[112:115], v[164:167], a[16:19]
ds_read_b128 v[140:143], v12 offset:3456
v_mfma_f32_16x16x32_bf16 a[24:27], v[116:119], v[164:167], a[24:27]
s_waitcnt lgkmcnt(2)
v_mfma_f32_16x16x32_bf16 a[4:7], v[112:115], v[168:171], a[4:7]
ds_read_b128 v[144:147], v13 offset:4320
v_mfma_f32_16x16x32_bf16 a[12:15], v[116:119], v[168:171], a[12:15]
v_mfma_f32_16x16x32_bf16 a[20:23], v[120:123], v[168:171], a[20:23]
ds_read_b128 v[148:151], v13 offset:4384
buffer_load_dwordx4 a[108:111], v4, s[60:63], s30 offen offset:3072 sc1
v_mfma_f32_16x16x32_bf16 a[28:31], v[124:127], v[168:171], a[28:31]
v_mfma_f32_16x16x32_bf16 a[4:7], v[104:107], v[172:175], a[4:7]
ds_read_b128 v[152:155], v13 offset:6880
v_mfma_f32_16x16x32_bf16 a[12:15], v[108:111], v[172:175], a[12:15]
ds_read_b128 v[156:159], v13 offset:6944
v_mfma_f32_16x16x32_bf16 a[20:23], v[112:115], v[172:175], a[20:23]
v_mfma_f32_16x16x32_bf16 a[28:31], v[116:119], v[172:175], a[28:31]
s_waitcnt lgkmcnt(2)
v_mfma_f32_16x16x32_bf16 a[0:3], v[128:131], v[144:147], a[0:3]
s_add_u32 s30, s68, 1
s_and_b32 s30, s30, 7
s_or_b32 s30, s30, 8
s_lshl_b32 s30, s30, 15
s_add_u32 s30, s30, s59
buffer_load_dwordx4 a[112:115], v4, s[60:63], s30 offen offset:0 sc1
v_mfma_f32_16x16x32_bf16 a[8:11], v[132:135], v[144:147], a[8:11]
ds_read_b128 v[64:67], v12 offset:3424
v_mfma_f32_16x16x32_bf16 a[16:19], v[136:139], v[144:147], a[16:19]
v_mfma_f32_16x16x32_bf16 a[24:27], v[140:143], v[144:147], a[24:27]
ds_read_b128 v[68:71], v12 offset:3392
v_mfma_f32_16x16x32_bf16 a[0:3], v[120:123], v[148:151], a[0:3]
v_mfma_f32_16x16x32_bf16 a[8:11], v[124:127], v[148:151], a[8:11]
ds_read_b128 v[72:75], v12 offset:3360
buffer_load_dwordx4 a[116:119], v4, s[60:63], s30 offen offset:1024 sc1
v_mfma_f32_16x16x32_bf16 a[16:19], v[128:131], v[148:151], a[16:19]
ds_read_b128 v[76:79], v12 offset:3328
v_mfma_f32_16x16x32_bf16 a[24:27], v[132:135], v[148:151], a[24:27]
s_waitcnt lgkmcnt(2)
v_mfma_f32_16x16x32_bf16 a[4:7], v[128:131], v[152:155], a[4:7]
ds_read_b128 v[160:163], v13 offset:4160
v_mfma_f32_16x16x32_bf16 a[12:15], v[132:135], v[152:155], a[12:15]
v_mfma_f32_16x16x32_bf16 a[20:23], v[136:139], v[152:155], a[20:23]
ds_read_b128 v[164:167], v13 offset:4224
buffer_load_dwordx4 a[120:123], v4, s[60:63], s30 offen offset:2048 sc1
v_mfma_f32_16x16x32_bf16 a[28:31], v[140:143], v[152:155], a[28:31]
v_mfma_f32_16x16x32_bf16 a[4:7], v[120:123], v[156:159], a[4:7]
ds_read_b128 v[168:171], v13 offset:6720
v_mfma_f32_16x16x32_bf16 a[12:15], v[124:127], v[156:159], a[12:15]
ds_read_b128 v[172:175], v13 offset:6784
v_mfma_f32_16x16x32_bf16 a[20:23], v[128:131], v[156:159], a[20:23]
v_mfma_f32_16x16x32_bf16 a[28:31], v[132:135], v[156:159], a[28:31]
s_waitcnt lgkmcnt(2)
v_mfma_f32_16x16x32_bf16 a[0:3], v[64:67], v[160:163], a[0:3]
buffer_load_dwordx4 a[124:127], v4, s[60:63], s30 offen offset:3072 sc1
v_mfma_f32_16x16x32_bf16 a[8:11], v[68:71], v[160:163], a[8:11]
ds_read_b128 v[80:83], v12 offset:3296
v_mfma_f32_16x16x32_bf16 a[16:19], v[72:75], v[160:163], a[16:19]
v_mfma_f32_16x16x32_bf16 a[24:27], v[76:79], v[160:163], a[24:27]
ds_read_b128 v[84:87], v12 offset:3264
v_mfma_f32_16x16x32_bf16 a[0:3], v[136:139], v[164:167], a[0:3]
v_mfma_f32_16x16x32_bf16 a[8:11], v[140:143], v[164:167], a[8:11]
ds_read_b128 v[88:91], v12 offset:3232
s_add_u32 s30, s68, 2
s_and_b32 s30, s30, 7
s_or_b32 s30, s30, 8
s_lshl_b32 s30, s30, 15
s_add_u32 s30, s30, s59
buffer_load_dwordx4 a[248:251], v4, s[60:63], s30 offen offset:0 sc1
v_mfma_f32_16x16x32_bf16 a[16:19], v[64:67], v[164:167], a[16:19]
ds_read_b128 v[92:95], v12 offset:3200
v_mfma_f32_16x16x32_bf16 a[24:27], v[68:71], v[164:167], a[24:27]
s_waitcnt lgkmcnt(2)
v_mfma_f32_16x16x32_bf16 a[4:7], v[64:67], v[168:171], a[4:7]
ds_read_b128 v[144:147], v13 offset:4000
v_mfma_f32_16x16x32_bf16 a[12:15], v[68:71], v[168:171], a[12:15]
v_mfma_f32_16x16x32_bf16 a[20:23], v[72:75], v[168:171], a[20:23]
ds_read_b128 v[148:151], v13 offset:4064
buffer_load_dwordx4 a[252:255], v4, s[60:63], s30 offen offset:1024 sc1
v_mfma_f32_16x16x32_bf16 a[28:31], v[76:79], v[168:171], a[28:31]
v_mfma_f32_16x16x32_bf16 a[4:7], v[136:139], v[172:175], a[4:7]
ds_read_b128 v[152:155], v13 offset:6560
v_mfma_f32_16x16x32_bf16 a[12:15], v[140:143], v[172:175], a[12:15]
ds_read_b128 v[156:159], v13 offset:6624
v_mfma_f32_16x16x32_bf16 a[20:23], v[64:67], v[172:175], a[20:23]
v_mfma_f32_16x16x32_bf16 a[28:31], v[68:71], v[172:175], a[28:31]
s_waitcnt lgkmcnt(2)
v_mfma_f32_16x16x32_bf16 a[0:3], v[80:83], v[144:147], a[0:3]
buffer_load_dwordx4 v[176:179], v4, s[60:63], s30 offen offset:2048 sc1
v_mfma_f32_16x16x32_bf16 a[8:11], v[84:87], v[144:147], a[8:11]
ds_read_b128 v[96:99], v12 offset:3168
v_mfma_f32_16x16x32_bf16 a[16:19], v[88:91], v[144:147], a[16:19]
v_mfma_f32_16x16x32_bf16 a[24:27], v[92:95], v[144:147], a[24:27]
ds_read_b128 v[100:103], v12 offset:3136
v_mfma_f32_16x16x32_bf16 a[0:3], v[72:75], v[148:151], a[0:3]
v_mfma_f32_16x16x32_bf16 a[8:11], v[76:79], v[148:151], a[8:11]
ds_read_b128 v[104:107], v12 offset:3104
buffer_load_dwordx4 v[180:183], v4, s[60:63], s30 offen offset:3072 sc1
v_mfma_f32_16x16x32_bf16 a[16:19], v[80:83], v[148:151], a[16:19]
ds_read_b128 v[108:111], v12 offset:3072
v_mfma_f32_16x16x32_bf16 a[24:27], v[84:87], v[148:151], a[24:27]
s_waitcnt lgkmcnt(2)
v_mfma_f32_16x16x32_bf16 a[4:7], v[80:83], v[152:155], a[4:7]
ds_read_b128 v[160:163], v13 offset:3840
v_mfma_f32_16x16x32_bf16 a[12:15], v[84:87], v[152:155], a[12:15]
v_mfma_f32_16x16x32_bf16 a[20:23], v[88:91], v[152:155], a[20:23]
ds_read_b128 v[164:167], v13 offset:3904
s_add_u32 s30, s68, 3
s_and_b32 s30, s30, 7
s_or_b32 s30, s30, 8
s_lshl_b32 s30, s30, 15
s_add_u32 s30, s30, s59
buffer_load_dwordx4 v[184:187], v4, s[60:63], s30 offen offset:0 sc1
v_mfma_f32_16x16x32_bf16 a[28:31], v[92:95], v[152:155], a[28:31]
v_mfma_f32_16x16x32_bf16 a[4:7], v[72:75], v[156:159], a[4:7]
ds_read_b128 v[168:171], v13 offset:6400
v_mfma_f32_16x16x32_bf16 a[12:15], v[76:79], v[156:159], a[12:15]
ds_read_b128 v[172:175], v13 offset:6464
v_mfma_f32_16x16x32_bf16 a[20:23], v[80:83], v[156:159], a[20:23]
v_mfma_f32_16x16x32_bf16 a[28:31], v[84:87], v[156:159], a[28:31]
s_waitcnt lgkmcnt(2)
v_mfma_f32_16x16x32_bf16 a[0:3], v[96:99], v[160:163], a[0:3]
buffer_load_dwordx4 v[188:191], v4, s[60:63], s30 offen offset:1024 sc1
v_mfma_f32_16x16x32_bf16 a[8:11], v[100:103], v[160:163], a[8:11]
ds_read_b128 v[112:115], v12 offset:3040
v_mfma_f32_16x16x32_bf16 a[16:19], v[104:107], v[160:163], a[16:19]
v_mfma_f32_16x16x32_bf16 a[24:27], v[108:111], v[160:163], a[24:27]
ds_read_b128 v[116:119], v12 offset:3008
v_mfma_f32_16x16x32_bf16 a[0:3], v[88:91], v[164:167], a[0:3]
v_mfma_f32_16x16x32_bf16 a[8:11], v[92:95], v[164:167], a[8:11]
ds_read_b128 v[120:123], v12 offset:2976
buffer_load_dwordx4 v[192:195], v4, s[60:63], s30 offen offset:2048 sc1
v_mfma_f32_16x16x32_bf16 a[16:19], v[96:99], v[164:167], a[16:19]
ds_read_b128 v[124:127], v12 offset:2944
v_mfma_f32_16x16x32_bf16 a[24:27], v[100:103], v[164:167], a[24:27]
s_waitcnt lgkmcnt(2)
v_mfma_f32_16x16x32_bf16 a[4:7], v[96:99], v[168:171], a[4:7]
ds_read_b128 v[144:147], v13 offset:3680
v_mfma_f32_16x16x32_bf16 a[12:15], v[100:103], v[168:171], a[12:15]
v_mfma_f32_16x16x32_bf16 a[20:23], v[104:107], v[168:171], a[20:23]
ds_read_b128 v[148:151], v13 offset:3744
buffer_load_dwordx4 v[196:199], v4, s[60:63], s30 offen offset:3072 sc1
v_mfma_f32_16x16x32_bf16 a[28:31], v[108:111], v[168:171], a[28:31]
v_mfma_f32_16x16x32_bf16 a[4:7], v[88:91], v[172:175], a[4:7]
ds_read_b128 v[152:155], v13 offset:6240
v_mfma_f32_16x16x32_bf16 a[12:15], v[92:95], v[172:175], a[12:15]
ds_read_b128 v[156:159], v13 offset:6304
v_mfma_f32_16x16x32_bf16 a[20:23], v[96:99], v[172:175], a[20:23]
v_mfma_f32_16x16x32_bf16 a[28:31], v[100:103], v[172:175], a[28:31]
s_waitcnt lgkmcnt(2)
v_mfma_f32_16x16x32_bf16 a[0:3], v[112:115], v[144:147], a[0:3]
s_add_u32 s30, s68, 4
s_and_b32 s30, s30, 7
s_or_b32 s30, s30, 8
s_lshl_b32 s30, s30, 15
s_add_u32 s30, s30, s59
buffer_load_dwordx4 v[200:203], v4, s[60:63], s30 offen offset:0 sc1
v_mfma_f32_16x16x32_bf16 a[8:11], v[116:119], v[144:147], a[8:11]
ds_read_b128 v[128:131], v12 offset:2912
v_mfma_f32_16x16x32_bf16 a[16:19], v[120:123], v[144:147], a[16:19]
v_mfma_f32_16x16x32_bf16 a[24:27], v[124:127], v[144:147], a[24:27]
ds_read_b128 v[132:135], v12 offset:2880
v_mfma_f32_16x16x32_bf16 a[0:3], v[104:107], v[148:151], a[0:3]
v_mfma_f32_16x16x32_bf16 a[8:11], v[108:111], v[148:151], a[8:11]
ds_read_b128 v[136:139], v12 offset:2848
buffer_load_dwordx4 v[204:207], v4, s[60:63], s30 offen offset:1024 sc1
v_mfma_f32_16x16x32_bf16 a[16:19], v[112:115], v[148:151], a[16:19]
ds_read_b128 v[140:143], v12 offset:2816
v_mfma_f32_16x16x32_bf16 a[24:27], v[116:119], v[148:151], a[24:27]
s_waitcnt lgkmcnt(2)
v_mfma_f32_16x16x32_bf16 a[4:7], v[112:115], v[152:155], a[4:7]
ds_read_b128 v[160:163], v13 offset:3520
v_mfma_f32_16x16x32_bf16 a[12:15], v[116:119], v[152:155], a[12:15]
v_mfma_f32_16x16x32_bf16 a[20:23], v[120:123], v[152:155], a[20:23]
ds_read_b128 v[164:167], v13 offset:3584
buffer_load_dwordx4 v[208:211], v4, s[60:63], s30 offen offset:2048 sc1
v_mfma_f32_16x16x32_bf16 a[28:31], v[124:127], v[152:155], a[28:31]
v_mfma_f32_16x16x32_bf16 a[4:7], v[104:107], v[156:159], a[4:7]
ds_read_b128 v[168:171], v13 offset:6080
v_mfma_f32_16x16x32_bf16 a[12:15], v[108:111], v[156:159], a[12:15]
ds_read_b128 v[172:175], v13 offset:6144
v_mfma_f32_16x16x32_bf16 a[20:23], v[112:115], v[156:159], a[20:23]
v_mfma_f32_16x16x32_bf16 a[28:31], v[116:119], v[156:159], a[28:31]
s_waitcnt lgkmcnt(2)
v_mfma_f32_16x16x32_bf16 a[0:3], v[128:131], v[160:163], a[0:3]
buffer_load_dwordx4 v[212:215], v4, s[60:63], s30 offen offset:3072 sc1
v_mfma_f32_16x16x32_bf16 a[8:11], v[132:135], v[160:163], a[8:11]
ds_read_b128 v[64:67], v12 offset:2784
v_mfma_f32_16x16x32_bf16 a[16:19], v[136:139], v[160:163], a[16:19]
v_mfma_f32_16x16x32_bf16 a[24:27], v[140:143], v[160:163], a[24:27]
ds_read_b128 v[68:71], v12 offset:2752
v_mfma_f32_16x16x32_bf16 a[0:3], v[120:123], v[164:167], a[0:3]
v_mfma_f32_16x16x32_bf16 a[8:11], v[124:127], v[164:167], a[8:11]
ds_read_b128 v[72:75], v12 offset:2720
s_add_u32 s30, s68, 5
s_and_b32 s30, s30, 7
s_or_b32 s30, s30, 8
s_lshl_b32 s30, s30, 15
s_add_u32 s30, s30, s59
buffer_load_dwordx4 v[216:219], v4, s[60:63], s30 offen offset:0 sc1
v_mfma_f32_16x16x32_bf16 a[16:19], v[128:131], v[164:167], a[16:19]
ds_read_b128 v[76:79], v12 offset:2688
v_mfma_f32_16x16x32_bf16 a[24:27], v[132:135], v[164:167], a[24:27]
s_waitcnt lgkmcnt(2)
v_mfma_f32_16x16x32_bf16 a[4:7], v[128:131], v[168:171], a[4:7]
ds_read_b128 v[144:147], v13 offset:3360
v_mfma_f32_16x16x32_bf16 a[12:15], v[132:135], v[168:171], a[12:15]
v_mfma_f32_16x16x32_bf16 a[20:23], v[136:139], v[168:171], a[20:23]
ds_read_b128 v[148:151], v13 offset:3424
buffer_load_dwordx4 v[220:223], v4, s[60:63], s30 offen offset:1024 sc1
v_mfma_f32_16x16x32_bf16 a[28:31], v[140:143], v[168:171], a[28:31]
v_mfma_f32_16x16x32_bf16 a[4:7], v[120:123], v[172:175], a[4:7]
ds_read_b128 v[152:155], v13 offset:5920
v_mfma_f32_16x16x32_bf16 a[12:15], v[124:127], v[172:175], a[12:15]
ds_read_b128 v[156:159], v13 offset:5984
v_mfma_f32_16x16x32_bf16 a[20:23], v[128:131], v[172:175], a[20:23]
v_mfma_f32_16x16x32_bf16 a[28:31], v[132:135], v[172:175], a[28:31]
s_waitcnt lgkmcnt(2)
v_mfma_f32_16x16x32_bf16 a[0:3], v[64:67], v[144:147], a[0:3]
buffer_load_dwordx4 v[224:227], v4, s[60:63], s30 offen offset:2048 sc1
v_mfma_f32_16x16x32_bf16 a[8:11], v[68:71], v[144:147], a[8:11]
ds_read_b128 v[80:83], v12 offset:2656
v_mfma_f32_16x16x32_bf16 a[16:19], v[72:75], v[144:147], a[16:19]
v_mfma_f32_16x16x32_bf16 a[24:27], v[76:79], v[144:147], a[24:27]
ds_read_b128 v[84:87], v12 offset:2624
v_mfma_f32_16x16x32_bf16 a[0:3], v[136:139], v[148:151], a[0:3]
v_mfma_f32_16x16x32_bf16 a[8:11], v[140:143], v[148:151], a[8:11]
ds_read_b128 v[88:91], v12 offset:2592
buffer_load_dwordx4 v[228:231], v4, s[60:63], s30 offen offset:3072 sc1
v_mfma_f32_16x16x32_bf16 a[16:19], v[64:67], v[148:151], a[16:19]
ds_read_b128 v[92:95], v12 offset:2560
v_mfma_f32_16x16x32_bf16 a[24:27], v[68:71], v[148:151], a[24:27]
s_waitcnt lgkmcnt(2)
v_mfma_f32_16x16x32_bf16 a[4:7], v[64:67], v[152:155], a[4:7]
ds_read_b128 v[160:163], v13 offset:3200
v_mfma_f32_16x16x32_bf16 a[12:15], v[68:71], v[152:155], a[12:15]
v_mfma_f32_16x16x32_bf16 a[20:23], v[72:75], v[152:155], a[20:23]
ds_read_b128 v[164:167], v13 offset:3264
s_add_u32 s30, s68, 6
s_and_b32 s30, s30, 7
s_or_b32 s30, s30, 8
s_lshl_b32 s30, s30, 15
s_add_u32 s30, s30, s59
buffer_load_dwordx4 v[232:235], v4, s[60:63], s30 offen offset:0 sc1
v_mfma_f32_16x16x32_bf16 a[28:31], v[76:79], v[152:155], a[28:31]
v_mfma_f32_16x16x32_bf16 a[4:7], v[136:139], v[156:159], a[4:7]
ds_read_b128 v[168:171], v13 offset:5760
v_mfma_f32_16x16x32_bf16 a[12:15], v[140:143], v[156:159], a[12:15]
ds_read_b128 v[172:175], v13 offset:5824
v_mfma_f32_16x16x32_bf16 a[20:23], v[64:67], v[156:159], a[20:23]
v_mfma_f32_16x16x32_bf16 a[28:31], v[68:71], v[156:159], a[28:31]
s_waitcnt lgkmcnt(2)
v_mfma_f32_16x16x32_bf16 a[0:3], v[80:83], v[160:163], a[0:3]
buffer_load_dwordx4 v[236:239], v4, s[60:63], s30 offen offset:1024 sc1
v_mfma_f32_16x16x32_bf16 a[8:11], v[84:87], v[160:163], a[8:11]
v_mfma_f32_16x16x32_bf16 a[16:19], v[88:91], v[160:163], a[16:19]
v_mfma_f32_16x16x32_bf16 a[24:27], v[92:95], v[160:163], a[24:27]
v_mfma_f32_16x16x32_bf16 a[0:3], v[72:75], v[164:167], a[0:3]
v_mfma_f32_16x16x32_bf16 a[8:11], v[76:79], v[164:167], a[8:11]
buffer_load_dwordx4 v[240:243], v4, s[60:63], s30 offen offset:2048 sc1
v_mfma_f32_16x16x32_bf16 a[16:19], v[80:83], v[164:167], a[16:19]
v_mfma_f32_16x16x32_bf16 a[24:27], v[84:87], v[164:167], a[24:27]
s_branch N_hj_%=
N_h1_%=:
ds_read_b128 v[88:91], v12 offset:2592
ds_read_b128 v[92:95], v12 offset:2560
ds_read_b128 v[96:99], v12 offset:2528
ds_read_b128 v[100:103], v12 offset:2496
ds_read_b128 v[104:107], v12 offset:2464
ds_read_b128 v[108:111], v12 offset:2432
ds_read_b128 v[144:147], v13 offset:3040
ds_read_b128 v[148:151], v13 offset:3104
ds_read_b128 v[152:155], v13 offset:5600
ds_read_b128 v[156:159], v13 offset:5664
s_waitcnt lgkmcnt(3)
v_mfma_f32_16x16x32_bf16 a[0:3], v[96:99], v[144:147], 0
s_add_u32 s30, s68, 6
s_and_b32 s30, s30, 7
s_lshl_b32 s30, s30, 15
s_add_u32 s30, s30, s59
buffer_load_dwordx4 a[64:67], v4, s[60:63], s30 offen offset:0 sc1
v_mfma_f32_16x16x32_bf16 a[8:11], v[100:103], v[144:147], 0
ds_read_b128 v[112:115], v12 offset:2400
v_mfma_f32_16x16x32_bf16 a[16:19], v[104:107], v[144:147], 0
v_mfma_f32_16x16x32_bf16 a[24:27], v[108:111], v[144:147], 0
ds_read_b128 v[116:119], v12 offset:2368
s_waitcnt lgkmcnt(2)
v_mfma_f32_16x16x32_bf16 a[0:3], v[88:91], v[148:151], a[0:3]
v_mfma_f32_16x16x32_bf16 a[8:11], v[92:95], v[148:151], a[8:11]
ds_read_b128 v[120:123], v12 offset:2336
buffer_load_dwordx4 a[68:71], v4, s[60:63], s30 offen offset:1024 sc1
v_mfma_f32_16x16x32_bf16 a[16:19], v[96:99], v[148:151], a[16:19]
ds_read_b128 v[124:127], v12 offset:2304
v_mfma_f32_16x16x32_bf16 a[24:27], v[100:103], v[148:151], a[24:27]
v_mfma_f32_16x16x32_bf16 a[4:7], v[96:99], v[152:155], 0
ds_read_b128 v[160:163], v13 offset:2880
v_mfma_f32_16x16x32_bf16 a[12:15], v[100:103], v[152:155], 0
v_mfma_f32_16x16x32_bf16 a[20:23], v[104:107], v[152:155], 0
ds_read_b128 v[164:167], v13 offset:2944
buffer_load_dwordx4 a[72:75], v4, s[60:63], s30 offen offset:2048 sc1
v_mfma_f32_16x16x32_bf16 a[28:31], v[108:111], v[152:155], 0
v_mfma_f32_16x16x32_bf16 a[4:7], v[88:91], v[156:159], a[4:7]
ds_read_b128 v[168:171], v13 offset:5440
v_mfma_f32_16x16x32_bf16 a[12:15], v[92:95], v[156:159], a[12:15]
ds_read_b128 v[172:175], v13 offset:5504
v_mfma_f32_16x16x32_bf16 a[20:23], v[96:99], v[156:159], a[20:23]
v_mfma_f32_16x16x32_bf16 a[28:31], v[100:103], v[156:159], a[28:31]
s_waitcnt lgkmcnt(2)
v_mfma_f32_16x16x32_bf16 a[0:3], v[112:115], v[160:163], a[0:3]
buffer_load_dwordx4 a[76:79], v4, s[60:63], s30 offen offset:3072 sc1
v_mfma_f32_16x16x32_bf16 a[8:11], v[116:119], v[160:163], a[8:11]
ds_read_b128 v[128:131], v12 offset:2272
v_mfma_f32_16x16x32_bf16 a[16:19], v[120:123], v[160:163], a[16:19]
v_mfma_f32_16x16x32_bf16 a[24:27], v[124:127], v[160:163], a[24:27]
ds_read_b128 v[132:135], v12 offset:2240
v_mfma_f32_16x16x32_bf16 a[0:3], v[104:107], v[164:167], a[0:3]
v_mfma_f32_16x16x32_bf16 a[8:11], v[108:111], v[164:167], a[8:11]
ds_read_b128 v[136:139], v12 offset:2208
s_add_u32 s30, s68, 7
s_and_b32 s30, s30, 7
s_lshl_b32 s30, s30, 15
s_add_u32 s30, s30, s59
buffer_load_dwordx4 a[80:83], v4, s[60:63], s30 offen offset:0 sc1
v_mfma_f32_16x16x32_bf16 a[16:19], v[112:115], v[164:167], a[16:19]
ds_read_b128 v[140:143], v12 offset:2176
v_mfma_f32_16x16x32_bf16 a[24:27], v[116:119], v[164:167], a[24:27]
s_waitcnt lgkmcnt(2)
v_mfma_f32_16x16x32_bf16 a[4:7], v[112:115], v[168:171], a[4:7]
ds_read_b128 v[144:147], v13 offset:2720
v_mfma_f32_16x16x32_bf16 a[12:15], v[116:119], v[168:171], a[12:15]
v_mfma_f32_16x16x32_bf16 a[20:23], v[120:123], v[168:171], a[20:23]
ds_read_b128 v[148:151], v13 offset:2784
buffer_load_dwordx4 a[84:87], v4, s[60:63], s30 offen offset:1024 sc1
v_mfma_f32_16x16x32_bf16 a[28:31], v[124:127], v[168:171], a[28:31]
v_mfma_f32_16x16x32_bf16 a[4:7], v[104:107], v[172:175], a[4:7]
ds_read_b128 v[152:155], v13 offset:5280
v_mfma_f32_16x16x32_bf16 a[12:15], v[108:111], v[172:175], a[12:15]
ds_read_b128 v[156:159], v13 offset:5344
v_mfma_f32_16x16x32_bf16 a[20:23], v[112:115], v[172:175], a[20:23]
v_mfma_f32_16x16x32_bf16 a[28:31], v[116:119], v[172:175], a[28:31]
s_waitcnt lgkmcnt(2)
v_mfma_f32_16x16x32_bf16 a[0:3], v[128:131], v[144:147], a[0:3]
buffer_load_dwordx4 a[88:91], v4, s[60:63], s30 offen offset:2048 sc1
v_mfma_f32_16x16x32_bf16 a[8:11], v[132:135], v[144:147], a[8:11]
ds_read_b128 v[64:67], v12 offset:2144
v_mfma_f32_16x16x32_bf16 a[16:19], v[136:139], v[144:147], a[16:19]
v_mfma_f32_16x16x32_bf16 a[24:27], v[140:143], v[144:147], a[24:27]
ds_read_b128 v[68:71], v12 offset:2112
v_mfma_f32_16x16x32_bf16 a[0:3], v[120:123], v[148:151], a[0:3]
v_mfma_f32_16x16x32_bf16 a[8:11], v[124:127], v[148:151], a[8:11]
ds_read_b128 v[72:75], v12 offset:2080
buffer_load_dwordx4 a[92:95], v4, s[60:63], s30 offen offset:3072 sc1
v_mfma_f32_16x16x32_bf16 a[16:19], v[128:131], v[148:151], a[16:19]
ds_read_b128 v[76:79], v12 offset:2048
v_mfma_f32_16x16x32_bf16 a[24:27], v[132:135], v[148:151], a[24:27]
s_waitcnt lgkmcnt(2)
v_mfma_f32_16x16x32_bf16 a[4:7], v[128:131], v[152:155], a[4:7]
ds_read_b128 v[160:163], v13 offset:2560
v_mfma_f32_16x16x32_bf16 a[12:15], v[132:135], v[152:155], a[12:15]
v_mfma_f32_16x16x32_bf16 a[20:23], v[136:139], v[152:155], a[20:23]
ds_read_b128 v[164:167], v13 offset:2624
s_add_u32 s30, s68, 0
s_and_b32 s30, s30, 7
s_or_b32 s30, s30, 8
s_lshl_b32 s30, s30, 15
s_add_u32 s30, s30, s59
buffer_load_dwordx4 a[96:99], v4, s[60:63], s30 offen offset:0 sc1
v_mfma_f32_16x16x32_bf16 a[28:31], v[140:143], v[152:155], a[28:31]
v_mfma_f32_16x16x32_bf16 a[4:7], v[120:123], v[156:159], a[4:7]
ds_read_b128 v[168:171], v13 offset:5120
v_mfma_f32_16x16x32_bf16 a[12:15], v[124:127], v[156:159], a[12:15]
ds_read_b128 v[172:175], v13 offset:5184
v_mfma_f32_16x16x32_bf16 a[20:23], v[128:131], v[156:159], a[20:23]
v_mfma_f32_16x16x32_bf16 a[28:31], v[132:135], v[156:159], a[28:31]
s_waitcnt lgkmcnt(2)
v_mfma_f32_16x16x32_bf16 a[0:3], v[64:67], v[160:163], a[0:3]
buffer_load_dwordx4 a[100:103], v4, s[60:63], s30 offen offset:1024 sc1
v_mfma_f32_16x16x32_bf16 a[8:11], v[68:71], v[160:163], a[8:11]
ds_read_b128 v[80:83], v12 offset:2016
v_mfma_f32_16x16x32_bf16 a[16:19], v[72:75], v[160:163], a[16:19]
ds_read_b128 v[84:87], v12 offset:1984
v_mfma_f32_16x16x32_bf16 a[24:27], v[76:79], v[160:163], a[24:27]
ds_read_b128 v[88:91], v12 offset:1952
v_mfma_f32_16x16x32_bf16 a[0:3], v[136:139], v[164:167], a[0:3]
ds_read_b128 v[92:95], v12 offset:1920
v_mfma_f32_16x16x32_bf16 a[8:11], v[140:143], v[164:167], a[8:11]
ds_read_b128 v[144:147], v13 offset:4960
buffer_load_dwordx4 a[104:107], v4, s[60:63], s30 offen offset:2048 sc1
v_mfma_f32_16x16x32_bf16 a[16:19], v[64:67], v[164:167], a[16:19]
ds_read_b128 v[148:151], v13 offset:5024
v_mfma_f32_16x16x32_bf16 a[24:27], v[68:71], v[164:167], a[24:27]
s_waitcnt lgkmcnt(3)
v_mfma_f32_16x16x32_bf16 a[4:7], v[64:67], v[168:171], a[4:7]
ds_read_b128 v[96:99], v12 offset:1888
v_mfma_f32_16x16x32_bf16 a[12:15], v[68:71], v[168:171], a[12:15]
ds_read_b128 v[100:103], v12 offset:1856
v_mfma_f32_16x16x32_bf16 a[20:23], v[72:75], v[168:171], a[20:23]
ds_read_b128 v[104:107], v12 offset:1824
buffer_load_dwordx4 a[108:111], v4, s[60:63], s30 offen offset:3072 sc1
v_mfma_f32_16x16x32_bf16 a[28:31], v[76:79], v[168:171], a[28:31]
ds_read_b128 v[108:111], v12 offset:1792
v_mfma_f32_16x16x32_bf16 a[4:7], v[136:139], v[172:175], a[4:7]
ds_read_b128 v[152:155], v13 offset:4800
v_mfma_f32_16x16x32_bf16 a[12:15], v[140:143], v[172:175], a[12:15]
ds_read_b128 v[156:159], v13 offset:4864
v_mfma_f32_16x16x32_bf16 a[20:23], v[64:67], v[172:175], a[20:23]
v_mfma_f32_16x16x32_bf16 a[28:31], v[68:71], v[172:175], a[28:31]
s_waitcnt lgkmcnt(2)
v_mfma_f32_16x16x32_bf16 a[4:7], v[80:83], v[144:147], a[4:7]
s_add_u32 s30, s68, 1
s_and_b32 s30, s30, 7
s_or_b32 s30, s30, 8
s_lshl_b32 s30, s30, 15
s_add_u32 s30, s30, s59
buffer_load_dwordx4 a[112:115], v4, s[60:63], s30 offen offset:0 sc1
v_mfma_f32_16x16x32_bf16 a[12:15], v[84:87], v[144:147], a[12:15]
ds_read_b128 v[112:115], v12 offset:1760
v_mfma_f32_16x16x32_bf16 a[20:23], v[88:91], v[144:147], a[20:23]
ds_read_b128 v[116:119], v12 offset:1728
v_mfma_f32_16x16x32_bf16 a[28:31], v[92:95], v[144:147], a[28:31]
ds_read_b128 v[120:123], v12 offset:1696
v_mfma_f32_16x16x32_bf16 a[4:7], v[72:75], v[148:151], a[4:7]
ds_read_b128 v[124:127], v12 offset:1664
v_mfma_f32_16x16x32_bf16 a[12:15], v[76:79], v[148:151], a[12:15]
ds_read_b128 v[160:163], v13 offset:4640
buffer_load_dwordx4 a[116:119], v4, s[60:63], s30 offen offset:1024 sc1
v_mfma_f32_16x16x32_bf16 a[20:23], v[80:83], v[148:151], a[20:23]
ds_read_b128 v[164:167], v13 offset:4704
v_mfma_f32_16x16x32_bf16 a[28:31], v[84:87], v[148:151], a[28:31]
s_waitcnt lgkmcnt(3)
v_mfma_f32_16x16x32_bf16 a[4:7], v[96:99], v[152:155], a[4:7]
ds_read_b128 v[128:131], v12 offset:1632
v_mfma_f32_16x16x32_bf16 a[12:15], v[100:103], v[152:155], a[12:15]
ds_read_b128 v[132:135], v12 offset:1600
v_mfma_f32_16x16x32_bf16 a[20:23], v[104:107], v[152:155], a[20:23]
ds_read_b128 v[136:139], v12 offset:1568
buffer_load_dwordx4 a[120:123], v4, s[60:63], s30 offen offset:2048 sc1
v_mfma_f32_16x16x32_bf16 a[28:31], v[108:111], v[152:155], a[28:31]
ds_read_b128 v[140:143], v12 offset:1536
v_mfma_f32_16x16x32_bf16 a[4:7], v[88:91], v[156:159], a[4:7]
ds_read_b128 v[168:171], v13 offset:4480
v_mfma_f32_16x16x32_bf16 a[12:15], v[92:95], v[156:159], a[12:15]
ds_read_b128 v[172:175], v13 offset:4544
v_mfma_f32_16x16x32_bf16 a[20:23], v[96:99], v[156:159], a[20:23]
v_mfma_f32_16x16x32_bf16 a[28:31], v[100:103], v[156:159], a[28:31]
s_waitcnt lgkmcnt(2)
v_mfma_f32_16x16x32_bf16 a[4:7], v[112:115], v[160:163], a[4:7]
buffer_load_dwordx4 a[124:127], v4, s[60:63], s30 offen offset:3072 sc1
v_mfma_f32_16x16x32_bf16 a[12:15], v[116:119], v[160:163], a[12:15]
ds_read_b128 v[64:67], v12 offset:1504
v_mfma_f32_16x16x32_bf16 a[20:23], v[120:123], v[160:163], a[20:23]
ds_read_b128 v[68:71], v12 offset:1472
v_mfma_f32_16x16x32_bf16 a[28:31], v[124:127], v[160:163], a[28:31]
ds_read_b128 v[72:75], v12 offset:1440
v_mfma_f32_16x16x32_bf16 a[4:7], v[104:107], v[164:167], a[4:7]
ds_read_b128 v[76:79], v12 offset:1408
v_mfma_f32_16x16x32_bf16 a[12:15], v[108:111], v[164:167], a[12:15]
ds_read_b128 v[144:147], v13 offset:4320
s_add_u32 s30, s68, 2
s_and_b32 s30, s30, 7
s_or_b32 s30, s30, 8
s_lshl_b32 s30, s30, 15
s_add_u32 s30, s30, s59
buffer_load_dwordx4 a[248:251], v4, s[60:63], s30 offen offset:0 sc1
v_mfma_f32_16x16x32_bf16 a[20:23], v[112:115], v[164:167], a[20:23]
ds_read_b128 v[148:151], v13 offset:4384
v_mfma_f32_16x16x32_bf16 a[28:31], v[116:119], v[164:167], a[28:31]
s_waitcnt lgkmcnt(3)
v_mfma_f32_16x16x32_bf16 a[4:7], v[128:131], v[168:171], a[4:7]
ds_read_b128 v[80:83], v12 offset:1376
v_mfma_f32_16x16x32_bf16 a[12:15], v[132:135], v[168:171], a[12:15]
ds_read_b128 v[84:87], v12 offset:1344
v_mfma_f32_16x16x32_bf16 a[20:23], v[136:139], v[168:171], a[20:23]
ds_read_b128 v[88:91], v12 offset:1312
buffer_load_dwordx4 a[252:255], v4, s[60:63], s30 offen offset:1024 sc1
v_mfma_f32_16x16x32_bf16 a[28:31], v[140:143], v[168:171], a[28:31]
ds_read_b128 v[92:95], v12 offset:1280
v_mfma_f32_16x16x32_bf16 a[4:7], v[120:123], v[172:175], a[4:7]
ds_read_b128 v[152:155], v13 offset:4160
v_mfma_f32_16x16x32_bf16 a[12:15], v[124:127], v[172:175], a[12:15]
ds_read_b128 v[156:159], v13 offset:4224
v_mfma_f32_16x16x32_bf16 a[20:23], v[128:131], v[172:175], a[20:23]
v_mfma_f32_16x16x32_bf16 a[28:31], v[132:135], v[172:175], a[28:31]
s_waitcnt lgkmcnt(2)
v_mfma_f32_16x16x32_bf16 a[4:7], v[64:67], v[144:147], a[4:7]
buffer_load_dwordx4 v[176:179], v4, s[60:63], s30 offen offset:2048 sc1
v_mfma_f32_16x16x32_bf16 a[12:15], v[68:71], v[144:147], a[12:15]
ds_read_b128 v[96:99], v12 offset:1248
v_mfma_f32_16x16x32_bf16 a[20:23], v[72:75], v[144:147], a[20:23]
ds_read_b128 v[100:103], v12 offset:1216
v_mfma_f32_16x16x32_bf16 a[28:31], v[76:79], v[144:147], a[28:31]
ds_read_b128 v[104:107], v12 offset:1184
v_mfma_f32_16x16x32_bf16 a[4:7], v[136:139], v[148:151], a[4:7]
ds_read_b128 v[108:111], v12 offset:1152
v_mfma_f32_16x16x32_bf16 a[12:15], v[140:143], v[148:151], a[12:15]
ds_read_b128 v[160:163], v13 offset:4000
buffer_load_dwordx4 v[180:183], v4, s[60:63], s30 offen offset:3072 sc1
v_mfma_f32_16x16x32_bf16 a[20:23], v[64:67], v[148:151], a[20:23]
ds_read_b128 v[164:167], v13 offset:4064
v_mfma_f32_16x16x32_bf16 a[28:31], v[68:71], v[148:151], a[28:31]
s_waitcnt lgkmcnt(3)
v_mfma_f32_16x16x32_bf16 a[4:7], v[80:83], v[152:155], a[4:7]
ds_read_b128 v[112:115], v12 offset:1120
v_mfma_f32_16x16x32_bf16 a[12:15], v[84:87], v[152:155], a[12:15]
ds_read_b128 v[116:119], v12 offset:1088
v_mfma_f32_16x16x32_bf16 a[20:23], v[88:91], v[152:155], a[20:23]
ds_read_b128 v[120:123], v12 offset:1056
s_add_u32 s30, s68, 3
s_and_b32 s30, s30, 7
s_or_b32 s30, s30, 8
s_lshl_b32 s30, s30, 15
s_add_u32 s30, s30, s59
buffer_load_dwordx4 v[184:187], v4, s[60:63], s30 offen offset:0 sc1
v_mfma_f32_16x16x32_bf16 a[28:31], v[92:95], v[152:155], a[28:31]
ds_read_b128 v[124:127], v12 offset:1024
v_mfma_f32_16x16x32_bf16 a[4:7], v[72:75], v[156:159], a[4:7]
ds_read_b128 v[168:171], v13 offset:3840
v_mfma_f32_16x16x32_bf16 a[12:15], v[76:79], v[156:159], a[12:15]
ds_read_b128 v[172:175], v13 offset:3904
v_mfma_f32_16x16x32_bf16 a[20:23], v[80:83], v[156:159], a[20:23]
v_mfma_f32_16x16x32_bf16 a[28:31], v[84:87], v[156:159], a[28:31]
s_waitcnt lgkmcnt(2)
v_mfma_f32_16x16x32_bf16 a[4:7], v[96:99], v[160:163], a[4:7]
buffer_load_dwordx4 v[188:191], v4, s[60:63], s30 offen offset:1024 sc1
v_mfma_f32_16x16x32_bf16 a[12:15], v[100:103], v[160:163], a[12:15]
ds_read_b128 v[128:131], v12 offset:992
v_mfma_f32_16x16x32_bf16 a[20:23], v[104:107], v[160:163], a[20:23]
ds_read_b128 v[132:135], v12 offset:960
v_mfma_f32_16x16x32_bf16 a[28:31], v[108:111], v[160:163], a[28:31]
ds_read_b128 v[136:139], v12 offset:928
v_mfma_f32_16x16x32_bf16 a[4:7], v[88:91], v[164:167], a[4:7]
ds_read_b128 v[140:143], v12 offset:896
v_mfma_f32_16x16x32_bf16 a[12:15], v[92:95], v[164:167], a[12:15]
ds_read_b128 v[144:147], v13 offset:3680
buffer_load_dwordx4 v[192:195], v4, s[60:63], s30 offen offset:2048 sc1
v_mfma_f32_16x16x32_bf16 a[20:23], v[96:99], v[164:167], a[20:23]
ds_read_b128 v[148:151], v13 offset:3744
v_mfma_f32_16x16x32_bf16 a[28:31], v[100:103], v[164:167], a[28:31]
s_waitcnt lgkmcnt(3)
v_mfma_f32_16x16x32_bf16 a[4:7], v[112:115], v[168:171], a[4:7]
ds_read_b128 v[64:67], v12 offset:864
v_mfma_f32_16x16x32_bf16 a[12:15], v[116:119], v[168:171], a[12:15]
ds_read_b128 v[68:71], v12 offset:832
v_mfma_f32_16x16x32_bf16 a[20:23], v[120:123], v[168:171], a[20:23]
ds_read_b128 v[72:75], v12 offset:800
buffer_load_dwordx4 v[196:199], v4, s[60:63], s30 offen offset:3072 sc1
v_mfma_f32_16x16x32_bf16 a[28:31], v[124:127], v[168:171], a[28:31]
ds_read_b128 v[76:79], v12 offset:768
v_mfma_f32_16x16x32_bf16 a[4:7], v[104:107], v[172:175], a[4:7]
ds_read_b128 v[152:155], v13 offset:3520
v_mfma_f32_16x16x32_bf16 a[12:15], v[108:111], v[172:175], a[12:15]
ds_read_b128 v[156:159], v13 offset:3584
v_mfma_f32_16x16x32_bf16 a[20:23], v[112:115], v[172:175], a[20:23]
v_mfma_f32_16x16x32_bf16 a[28:31], v[116:119], v[172:175], a[28:31]
s_waitcnt lgkmcnt(2)
v_mfma_f32_16x16x32_bf16 a[4:7], v[128:131], v[144:147], a[4:7]
s_add_u32 s30, s68, 4
s_and_b32 s30, s30, 7
s_or_b32 s30, s30, 8
s_lshl_b32 s30, s30, 15
s_add_u32 s30, s30, s59
buffer_load_dwordx4 v[200:203], v4, s[60:63], s30 offen offset:0 sc1
v_mfma_f32_16x16x32_bf16 a[12:15], v[132:135], v[144:147], a[12:15]
ds_read_b128 v[80:83], v12 offset:736
v_mfma_f32_16x16x32_bf16 a[20:23], v[136:139], v[144:147], a[20:23]
ds_read_b128 v[84:87], v12 offset:704
v_mfma_f32_16x16x32_bf16 a[28:31], v[140:143], v[144:147], a[28:31]
ds_read_b128 v[88:91], v12 offset:672
v_mfma_f32_16x16x32_bf16 a[4:7], v[120:123], v[148:151], a[4:7]
ds_read_b128 v[92:95], v12 offset:640
v_mfma_f32_16x16x32_bf16 a[12:15], v[124:127], v[148:151], a[12:15]
ds_read_b128 v[160:163], v13 offset:3360
buffer_load_dwordx4 v[204:207], v4, s[60:63], s30 offen offset:1024 sc1
v_mfma_f32_16x16x32_bf16 a[20:23], v[128:131], v[148:151], a[20:23]
ds_read_b128 v[164:167], v13 offset:3424
v_mfma_f32_16x16x32_bf16 a[28:31], v[132:135], v[148:151], a[28:31]
s_waitcnt lgkmcnt(3)
v_mfma_f32_16x16x32_bf16 a[4:7], v[64:67], v[152:155], a[4:7]
ds_read_b128 v[96:99], v12 offset:608
v_mfma_f32_16x16x32_bf16 a[12:15], v[68:71], v[152:155], a[12:15]
ds_read_b128 v[100:103], v12 offset:576
v_mfma_f32_16x16x32_bf16 a[20:23], v[72:75], v[152:155], a[20:23]
ds_read_b128 v[104:107], v12 offset:544
buffer_load_dwordx4 v[208:211], v4, s[60:63], s30 offen offset:2048 sc1
v_mfma_f32_16x16x32_bf16 a[28:31], v[76:79], v[152:155], a[28:31]
ds_read_b128 v[108:111], v12 offset:512
v_mfma_f32_16x16x32_bf16 a[4:7], v[136:139], v[156:159], a[4:7]
ds_read_b128 v[168:171], v13 offset:3200
v_mfma_f32_16x16x32_bf16 a[12:15], v[140:143], v[156:159], a[12:15]
ds_read_b128 v[172:175], v13 offset:3264
v_mfma_f32_16x16x32_bf16 a[20:23], v[64:67], v[156:159], a[20:23]
v_mfma_f32_16x16x32_bf16 a[28:31], v[68:71], v[156:159], a[28:31]
s_waitcnt lgkmcnt(2)
v_mfma_f32_16x16x32_bf16 a[4:7], v[80:83], v[160:163], a[4:7]
buffer_load_dwordx4 v[212:215], v4, s[60:63], s30 offen offset:3072 sc1
v_mfma_f32_16x16x32_bf16 a[12:15], v[84:87], v[160:163], a[12:15]
ds_read_b128 v[112:115], v12 offset:480
v_mfma_f32_16x16x32_bf16 a[20:23], v[88:91], v[160:163], a[20:23]
ds_read_b128 v[116:119], v12 offset:448
v_mfma_f32_16x16x32_bf16 a[28:31], v[92:95], v[160:163], a[28:31]
ds_read_b128 v[120:123], v12 offset:416
v_mfma_f32_16x16x32_bf16 a[4:7], v[72:75], v[164:167], a[4:7]
ds_read_b128 v[124:127], v12 offset:384
v_mfma_f32_16x16x32_bf16 a[12:15], v[76:79], v[164:167], a[12:15]
ds_read_b128 v[144:147], v13 offset:3040
s_add_u32 s30, s68, 5
s_and_b32 s30, s30, 7
s_or_b32 s30, s30, 8
s_lshl_b32 s30, s30, 15
s_add_u32 s30, s30, s59
buffer_load_dwordx4 v[216:219], v4, s[60:63], s30 offen offset:0 sc1
v_mfma_f32_16x16x32_bf16 a[20:23], v[80:83], v[164:167], a[20:23]
ds_read_b128 v[148:151], v13 offset:3104
v_mfma_f32_16x16x32_bf16 a[28:31], v[84:87], v[164:167], a[28:31]
s_waitcnt lgkmcnt(3)
v_mfma_f32_16x16x32_bf16 a[4:7], v[96:99], v[168:171], a[4:7]
ds_read_b128 v[128:131], v12 offset:352
v_mfma_f32_16x16x32_bf16 a[12:15], v[100:103], v[168:171], a[12:15]
ds_read_b128 v[132:135], v12 offset:320
v_mfma_f32_16x16x32_bf16 a[20:23], v[104:107], v[168:171], a[20:23]
ds_read_b128 v[136:139], v12 offset:288
buffer_load_dwordx4 v[220:223], v4, s[60:63], s30 offen offset:1024 sc1
v_mfma_f32_16x16x32_bf16 a[28:31], v[108:111], v[168:171], a[28:31]
ds_read_b128 v[140:143], v12 offset:256
v_mfma_f32_16x16x32_bf16 a[4:7], v[88:91], v[172:175], a[4:7]
ds_read_b128 v[152:155], v13 offset:2880
v_mfma_f32_16x16x32_bf16 a[12:15], v[92:95], v[172:175], a[12:15]
ds_read_b128 v[156:159], v13 offset:2944
v_mfma_f32_16x16x32_bf16 a[20:23], v[96:99], v[172:175], a[20:23]
v_mfma_f32_16x16x32_bf16 a[28:31], v[100:103], v[172:175], a[28:31]
s_waitcnt lgkmcnt(2)
v_mfma_f32_16x16x32_bf16 a[4:7], v[112:115], v[144:147], a[4:7]
buffer_load_dwordx4 v[224:227], v4, s[60:63], s30 offen offset:2048 sc1
v_mfma_f32_16x16x32_bf16 a[12:15], v[116:119], v[144:147], a[12:15]
ds_read_b128 v[64:67], v12 offset:224
v_mfma_f32_16x16x32_bf16 a[20:23], v[120:123], v[144:147], a[20:23]
ds_read_b128 v[68:71], v12 offset:192
v_mfma_f32_16x16x32_bf16 a[28:31], v[124:127], v[144:147], a[28:31]
ds_read_b128 v[72:75], v12 offset:160
v_mfma_f32_16x16x32_bf16 a[4:7], v[104:107], v[148:151], a[4:7]
ds_read_b128 v[76:79], v12 offset:128
v_mfma_f32_16x16x32_bf16 a[12:15], v[108:111], v[148:151], a[12:15]
ds_read_b128 v[160:163], v13 offset:2720
buffer_load_dwordx4 v[228:231], v4, s[60:63], s30 offen offset:3072 sc1
v_mfma_f32_16x16x32_bf16 a[20:23], v[112:115], v[148:151], a[20:23]
ds_read_b128 v[164:167], v13 offset:2784
v_mfma_f32_16x16x32_bf16 a[28:31], v[116:119], v[148:151], a[28:31]
s_waitcnt lgkmcnt(3)
v_mfma_f32_16x16x32_bf16 a[4:7], v[128:131], v[152:155], a[4:7]
ds_read_b128 v[80:83], v12 offset:96
v_mfma_f32_16x16x32_bf16 a[12:15], v[132:135], v[152:155], a[12:15]
ds_read_b128 v[84:87], v12 offset:64
v_mfma_f32_16x16x32_bf16 a[20:23], v[136:139], v[152:155], a[20:23]
ds_read_b128 v[88:91], v12 offset:32
s_add_u32 s30, s68, 6
s_and_b32 s30, s30, 7
s_or_b32 s30, s30, 8
s_lshl_b32 s30, s30, 15
s_add_u32 s30, s30, s59
buffer_load_dwordx4 v[232:235], v4, s[60:63], s30 offen offset:0 sc1
v_mfma_f32_16x16x32_bf16 a[28:31], v[140:143], v[152:155], a[28:31]
ds_read_b128 v[92:95], v12 offset:0
v_mfma_f32_16x16x32_bf16 a[4:7], v[120:123], v[156:159], a[4:7]
ds_read_b128 v[168:171], v13 offset:2560
v_mfma_f32_16x16x32_bf16 a[12:15], v[124:127], v[156:159], a[12:15]
ds_read_b128 v[172:175], v13 offset:2624
v_mfma_f32_16x16x32_bf16 a[20:23], v[128:131], v[156:159], a[20:23]
v_mfma_f32_16x16x32_bf16 a[28:31], v[132:135], v[156:159], a[28:31]
s_waitcnt lgkmcnt(2)
v_mfma_f32_16x16x32_bf16 a[4:7], v[64:67], v[160:163], a[4:7]
buffer_load_dwordx4 v[236:239], v4, s[60:63], s30 offen offset:1024 sc1
v_mfma_f32_16x16x32_bf16 a[12:15], v[68:71], v[160:163], a[12:15]
v_mfma_f32_16x16x32_bf16 a[20:23], v[72:75], v[160:163], a[20:23]
v_mfma_f32_16x16x32_bf16 a[28:31], v[76:79], v[160:163], a[28:31]
v_mfma_f32_16x16x32_bf16 a[4:7], v[136:139], v[164:167], a[4:7]
v_mfma_f32_16x16x32_bf16 a[12:15], v[140:143], v[164:167], a[12:15]
buffer_load_dwordx4 v[240:243], v4, s[60:63], s30 offen offset:2048 sc1
v_mfma_f32_16x16x32_bf16 a[20:23], v[64:67], v[164:167], a[20:23]
v_mfma_f32_16x16x32_bf16 a[28:31], v[68:71], v[164:167], a[28:31]
N_hj_%=:
s_waitcnt lgkmcnt(0)
v_mfma_f32_16x16x32_bf16 a[4:7], v[80:83], v[168:171], a[4:7]
v_mfma_f32_16x16x32_bf16 a[12:15], v[84:87], v[168:171], a[12:15]
v_mfma_f32_16x16x32_bf16 a[20:23], v[88:91], v[168:171], a[20:23]
buffer_load_dwordx4 v[244:247], v4, s[60:63], s30 offen offset:3072 sc1
v_mfma_f32_16x16x32_bf16 a[28:31], v[92:95], v[168:171], a[28:31]
v_mfma_f32_16x16x32_bf16 a[4:7], v[72:75], v[172:175], a[4:7]
v_mfma_f32_16x16x32_bf16 a[12:15], v[76:79], v[172:175], a[12:15]
v_mfma_f32_16x16x32_bf16 a[20:23], v[80:83], v[172:175], a[20:23]
v_mfma_f32_16x16x32_bf16 a[28:31], v[84:87], v[172:175], a[28:31]
s_nop 7
s_barrier
v_and_b32 v16, 63, v3
v_and_b32 v17, 15, v16
v_mul_u32_u24 v17, 0x110, v17
v_lshrrev_b32 v18, 4, v16
v_lshl_add_u32 v17, v18, 4, v17
s_and_b32 s27, s23, 1
s_mul_i32 s27, s27, 0x2200
s_add_u32 s27, s27, s52
v_add_u32 v17, s27, v17
ds_write_b128 v17, a[0:3] offset:0
ds_write_b128 v17, a[4:7] offset:4352
ds_write_b128 v17, a[8:11] offset:64
ds_write_b128 v17, a[12:15] offset:4416
ds_write_b128 v17, a[16:19] offset:128
ds_write_b128 v17, a[20:23] offset:4480
ds_write_b128 v17, a[24:27] offset:192
ds_write_b128 v17, a[28:31] offset:4544
s_waitcnt lgkmcnt(0)
s_barrier
v_lshrrev_b32 v17, 3, v3
v_mul_u32_u24 v17, 0x110, v17
v_and_b32 v18, 7, v3
v_lshl_add_u32 v17, v18, 5, v17
v_add_u32 v17, s52, v17
ds_read_b128 v[64:67], v17 offset:0
ds_read_b128 v[68:71], v17 offset:16
ds_read_b128 v[72:75], v17 offset:8704
ds_read_b128 v[76:79], v17 offset:8720
ds_read_b128 v[80:83], v17 offset:4352
ds_read_b128 v[84:87], v17 offset:4368
ds_read_b128 v[88:91], v17 offset:13056
ds_read_b128 v[92:95], v17 offset:13072
s_waitcnt lgkmcnt(0)
v_add_f32 v64, v64, v72
v_add_f32 v65, v65, v73
v_add_f32 v66, v66, v74
v_add_f32 v67, v67, v75
v_add_f32 v68, v68, v76
v_add_f32 v69, v69, v77
v_add_f32 v70, v70, v78
v_add_f32 v71, v71, v79
v_lshlrev_b32 v20, 16, v44
v_fmac_f32 v64, s53, v20
v_and_b32 v21, 0xffff0000, v44
v_fmac_f32 v65, s53, v21
v_lshlrev_b32 v20, 16, v45
v_fmac_f32 v66, s53, v20
v_and_b32 v21, 0xffff0000, v45
v_fmac_f32 v67, s53, v21
v_lshlrev_b32 v20, 16, v46
v_fmac_f32 v68, s53, v20
v_and_b32 v21, 0xffff0000, v46
v_fmac_f32 v69, s53, v21
v_lshlrev_b32 v20, 16, v47
v_fmac_f32 v70, s53, v20
v_and_b32 v21, 0xffff0000, v47
v_fmac_f32 v71, s53, v21
v_cvt_pk_bf16_f32 v24, v64, v65
v_cvt_pk_bf16_f32 v25, v66, v67
v_cvt_pk_bf16_f32 v26, v68, v69
v_cvt_pk_bf16_f32 v27, v70, v71
v_add_f32 v80, v80, v88
v_add_f32 v81, v81, v89
v_add_f32 v82, v82, v90
v_add_f32 v83, v83, v91
v_add_f32 v84, v84, v92
v_add_f32 v85, v85, v93
v_add_f32 v86, v86, v94
v_add_f32 v87, v87, v95
v_lshlrev_b32 v20, 16, v48
v_fmac_f32 v80, s53, v20
v_and_b32 v21, 0xffff0000, v48
v_fmac_f32 v81, s53, v21
v_lshlrev_b32 v20, 16, v49
v_fmac_f32 v82, s53, v20
v_and_b32 v21, 0xffff0000, v49
v_fmac_f32 v83, s53, v21
v_lshlrev_b32 v20, 16, v50
v_fmac_f32 v84, s53, v20
v_and_b32 v21, 0xffff0000, v50
v_fmac_f32 v85, s53, v21
v_lshlrev_b32 v20, 16, v51
v_fmac_f32 v86, s53, v20
v_and_b32 v21, 0xffff0000, v51
v_fmac_f32 v87, s53, v21
v_cvt_pk_bf16_f32 v28, v80, v81
v_cvt_pk_bf16_f32 v29, v82, v83
v_cvt_pk_bf16_f32 v30, v84, v85
v_cvt_pk_bf16_f32 v31, v86, v87
v_lshlrev_b32 v17, 4, v3
buffer_store_dwordx4 v[24:27], v17, s[44:47], 0 offen offset:0 sc1
buffer_store_dwordx4 v[28:31], v17, s[44:47], 0 offen offset:2048 sc1
s_nop 1
s_cmp_lg_u32 s23, 0
s_cbranch_scc1 N_w0_%=
s_and_b32 s27, s22, 7
s_lshr_b32 s28, s22, 3
s_and_b32 s28, s28, 3
s_lshl_b32 s28, s28, 3
s_add_u32 s27, s27, s28
s_lshr_b32 s27, s27, 1
s_sub_u32 s28, 15, s27
s_min_u32 s27, s27, s28
s_lshl_b32 s27, s27, 7
s_add_u32 s27, s27, 0x2000
v_mbcnt_lo_u32_b32 v0, -1, 0
v_mbcnt_hi_u32_b32 v0, -1, v0
v_and_b32 v0, 31, v0
v_lshlrev_b32 v1, 2, v0
s_memrealtime s[94:95]
s_waitcnt lgkmcnt(0)
N_poll_%=:
buffer_load_dword v2, v1, s[40:43], s27 offen offset:0 sc1
s_waitcnt vmcnt(0)
v_cmp_ne_u32 vcc, s98, v2
s_nop 1
s_cbranch_vccz N_poll_ok_%=
s_sleep 1
s_memrealtime s[96:97]
s_waitcnt lgkmcnt(0)
s_sub_u32 s29, s96, s94
s_cmp_lt_u32 s29, 0x1e8480
s_cbranch_scc1 N_poll_%=
N_poll_ok_%=:
N_w0_%=:
s_barrier
s_add_u32 s30, s68, 0
s_and_b32 s30, s30, 7
s_lshl_b32 s30, s30, 12
s_add_u32 s30, s30, s58
buffer_load_dwordx4 v[64:67], v4, s[36:39], s30 offen offset:0 sc1
buffer_load_dwordx4 v[68:71], v4, s[36:39], s30 offen offset:1024 sc1
buffer_load_dwordx4 v[72:75], v4, s[36:39], s30 offen offset:2048 sc1
buffer_load_dwordx4 v[76:79], v4, s[36:39], s30 offen offset:3072 sc1
s_add_u32 s30, s68, 1
s_and_b32 s30, s30, 7
s_lshl_b32 s30, s30, 12
s_add_u32 s30, s30, s58
buffer_load_dwordx4 v[80:83], v4, s[36:39], s30 offen offset:0 sc1
buffer_load_dwordx4 v[84:87], v4, s[36:39], s30 offen offset:1024 sc1
buffer_load_dwordx4 v[88:91], v4, s[36:39], s30 offen offset:2048 sc1
buffer_load_dwordx4 v[92:95], v4, s[36:39], s30 offen offset:3072 sc1
s_add_u32 s30, s68, 2
s_and_b32 s30, s30, 7
s_lshl_b32 s30, s30, 12
s_add_u32 s30, s30, s58
buffer_load_dwordx4 v[96:99], v4, s[36:39], s30 offen offset:0 sc1
buffer_load_dwordx4 v[100:103], v4, s[36:39], s30 offen offset:1024 sc1
buffer_load_dwordx4 v[104:107], v4, s[36:39], s30 offen offset:2048 sc1
buffer_load_dwordx4 v[108:111], v4, s[36:39], s30 offen offset:3072 sc1
s_add_u32 s30, s68, 3
s_and_b32 s30, s30, 7
s_lshl_b32 s30, s30, 12
s_add_u32 s30, s30, s58
buffer_load_dwordx4 v[112:115], v4, s[36:39], s30 offen offset:0 sc1
buffer_load_dwordx4 v[116:119], v4, s[36:39], s30 offen offset:1024 sc1
buffer_load_dwordx4 v[120:123], v4, s[36:39], s30 offen offset:2048 sc1
buffer_load_dwordx4 v[124:127], v4, s[36:39], s30 offen offset:3072 sc1
s_add_u32 s30, s68, 4
s_and_b32 s30, s30, 7
s_lshl_b32 s30, s30, 12
s_add_u32 s30, s30, s58
buffer_load_dwordx4 v[128:131], v4, s[36:39], s30 offen offset:0 sc1
buffer_load_dwordx4 v[132:135], v4, s[36:39], s30 offen offset:1024 sc1
buffer_load_dwordx4 v[136:139], v4, s[36:39], s30 offen offset:2048 sc1
buffer_load_dwordx4 v[140:143], v4, s[36:39], s30 offen offset:3072 sc1
s_add_u32 s30, s68, 5
s_and_b32 s30, s30, 7
s_lshl_b32 s30, s30, 12
s_add_u32 s30, s30, s58
buffer_load_dwordx4 v[144:147], v4, s[36:39], s30 offen offset:0 sc1
buffer_load_dwordx4 v[148:151], v4, s[36:39], s30 offen offset:1024 sc1
buffer_load_dwordx4 v[152:155], v4, s[36:39], s30 offen offset:2048 sc1
buffer_load_dwordx4 v[156:159], v4, s[36:39], s30 offen offset:3072 sc1
s_waitcnt vmcnt(23)
v_mfma_f32_16x16x32_bf16 a[0:3], a[152:155], v[64:67], 0
s_waitcnt vmcnt(22)
v_mfma_f32_16x16x32_bf16 a[4:7], a[152:155], v[68:71], 0
s_waitcnt vmcnt(21)
v_mfma_f32_16x16x32_bf16 a[8:11], a[152:155], v[72:75], 0
s_waitcnt vmcnt(20)
v_mfma_f32_16x16x32_bf16 a[12:15], a[152:155], v[76:79], 0
v_mfma_f32_16x16x32_bf16 a[16:19], a[156:159], v[64:67], 0
v_mfma_f32_16x16x32_bf16 a[20:23], a[156:159], v[68:71], 0
v_mfma_f32_16x16x32_bf16 a[24:27], a[156:159], v[72:75], 0
v_mfma_f32_16x16x32_bf16 a[28:31], a[156:159], v[76:79], 0
v_mfma_f32_16x16x32_bf16 a[32:35], a[160:163], v[64:67], 0
v_mfma_f32_16x16x32_bf16 a[36:39], a[160:163], v[68:71], 0
v_mfma_f32_16x16x32_bf16 a[40:43], a[160:163], v[72:75], 0
v_mfma_f32_16x16x32_bf16 a[44:47], a[160:163], v[76:79], 0
v_mfma_f32_16x16x32_bf16 a[48:51], a[164:167], v[64:67], 0
v_mfma_f32_16x16x32_bf16 a[52:55], a[164:167], v[68:71], 0
v_mfma_f32_16x16x32_bf16 a[56:59], a[164:167], v[72:75], 0
v_mfma_f32_16x16x32_bf16 a[60:63], a[164:167], v[76:79], 0
s_waitcnt vmcnt(19)
v_mfma_f32_16x16x32_bf16 a[0:3], a[168:171], v[80:83], a[0:3]
s_waitcnt vmcnt(18)
v_mfma_f32_16x16x32_bf16 a[4:7], a[168:171], v[84:87], a[4:7]
s_add_u32 s30, s68, 6
s_and_b32 s30, s30, 7
s_lshl_b32 s30, s30, 12
s_add_u32 s30, s30, s58
buffer_load_dwordx4 v[64:67], v4, s[36:39], s30 offen offset:0 sc1
s_waitcnt vmcnt(18)
v_mfma_f32_16x16x32_bf16 a[8:11], a[168:171], v[88:91], a[8:11]
buffer_load_dwordx4 v[68:71], v4, s[36:39], s30 offen offset:1024 sc1
s_waitcnt vmcnt(18)
v_mfma_f32_16x16x32_bf16 a[12:15], a[168:171], v[92:95], a[12:15]
buffer_load_dwordx4 v[72:75], v4, s[36:39], s30 offen offset:2048 sc1
v_mfma_f32_16x16x32_bf16 a[16:19], a[172:175], v[80:83], a[16:19]
buffer_load_dwordx4 v[76:79], v4, s[36:39], s30 offen offset:3072 sc1
v_mfma_f32_16x16x32_bf16 a[20:23], a[172:175], v[84:87], a[20:23]
s_add_u32 s30, s68, 7
s_and_b32 s30, s30, 7
s_or_b32 s30, s30, 8
s_lshl_b32 s30, s30, 15
s_add_u32 s30, s30, s59
buffer_load_dwordx4 a[152:155], v4, s[60:63], s30 offen offset:0 sc1
v_mfma_f32_16x16x32_bf16 a[24:27], a[172:175], v[88:91], a[24:27]
buffer_load_dwordx4 a[156:159], v4, s[60:63], s30 offen offset:1024 sc1
v_mfma_f32_16x16x32_bf16 a[28:31], a[172:175], v[92:95], a[28:31]
buffer_load_dwordx4 a[160:163], v4, s[60:63], s30 offen offset:2048 sc1
v_mfma_f32_16x16x32_bf16 a[32:35], a[176:179], v[80:83], a[32:35]
buffer_load_dwordx4 a[164:167], v4, s[60:63], s30 offen offset:3072 sc1
v_mfma_f32_16x16x32_bf16 a[36:39], a[176:179], v[84:87], a[36:39]
v_mfma_f32_16x16x32_bf16 a[40:43], a[176:179], v[88:91], a[40:43]
v_mfma_f32_16x16x32_bf16 a[44:47], a[176:179], v[92:95], a[44:47]
v_mfma_f32_16x16x32_bf16 a[48:51], a[180:183], v[80:83], a[48:51]
v_mfma_f32_16x16x32_bf16 a[52:55], a[180:183], v[84:87], a[52:55]
v_mfma_f32_16x16x32_bf16 a[56:59], a[180:183], v[88:91], a[56:59]
v_mfma_f32_16x16x32_bf16 a[60:63], a[180:183], v[92:95], a[60:63]
s_waitcnt vmcnt(23)
v_mfma_f32_16x16x32_bf16 a[0:3], a[184:187], v[96:99], a[0:3]
s_waitcnt vmcnt(22)
v_mfma_f32_16x16x32_bf16 a[4:7], a[184:187], v[100:103], a[4:7]
s_add_u32 s30, s68, 7
s_and_b32 s30, s30, 7
s_lshl_b32 s30, s30, 12
s_add_u32 s30, s30, s58
buffer_load_dwordx4 v[80:83], v4, s[36:39], s30 offen offset:0 sc1
s_waitcnt vmcnt(22)
v_mfma_f32_16x16x32_bf16 a[8:11], a[184:187], v[104:107], a[8:11]
buffer_load_dwordx4 v[84:87], v4, s[36:39], s30 offen offset:1024 sc1
s_waitcnt vmcnt(22)
v_mfma_f32_16x16x32_bf16 a[12:15], a[184:187], v[108:111], a[12:15]
buffer_load_dwordx4 v[88:91], v4, s[36:39], s30 offen offset:2048 sc1
v_mfma_f32_16x16x32_bf16 a[16:19], a[188:191], v[96:99], a[16:19]
buffer_load_dwordx4 v[92:95], v4, s[36:39], s30 offen offset:3072 sc1
v_mfma_f32_16x16x32_bf16 a[20:23], a[188:191], v[100:103], a[20:23]
s_add_u32 s30, s68, 0
s_and_b32 s30, s30, 7
s_or_b32 s30, s30, 16
s_lshl_b32 s30, s30, 15
s_add_u32 s30, s30, s59
buffer_load_dwordx4 a[168:171], v4, s[60:63], s30 offen offset:0 sc1
v_mfma_f32_16x16x32_bf16 a[24:27], a[188:191], v[104:107], a[24:27]
buffer_load_dwordx4 a[172:175], v4, s[60:63], s30 offen offset:1024 sc1
v_mfma_f32_16x16x32_bf16 a[28:31], a[188:191], v[108:111], a[28:31]
buffer_load_dwordx4 a[176:179], v4, s[60:63], s30 offen offset:2048 sc1
v_mfma_f32_16x16x32_bf16 a[32:35], a[192:195], v[96:99], a[32:35]
buffer_load_dwordx4 a[180:183], v4, s[60:63], s30 offen offset:3072 sc1
v_mfma_f32_16x16x32_bf16 a[36:39], a[192:195], v[100:103], a[36:39]
v_mfma_f32_16x16x32_bf16 a[40:43], a[192:195], v[104:107], a[40:43]
v_mfma_f32_16x16x32_bf16 a[44:47], a[192:195], v[108:111], a[44:47]
v_mfma_f32_16x16x32_bf16 a[48:51], a[196:199], v[96:99], a[48:51]
v_mfma_f32_16x16x32_bf16 a[52:55], a[196:199], v[100:103], a[52:55]
v_mfma_f32_16x16x32_bf16 a[56:59], a[196:199], v[104:107], a[56:59]
v_mfma_f32_16x16x32_bf16 a[60:63], a[196:199], v[108:111], a[60:63]
s_waitcnt vmcnt(27)
v_mfma_f32_16x16x32_bf16 a[0:3], a[200:203], v[112:115], a[0:3]
s_waitcnt vmcnt(26)
v_mfma_f32_16x16x32_bf16 a[4:7], a[200:203], v[116:119], a[4:7]
s_add_u32 s30, s68, 0
s_and_b32 s30, s30, 7
s_or_b32 s30, s30, 8
s_lshl_b32 s30, s30, 12
s_add_u32 s30, s30, s58
buffer_load_dwordx4 v[96:99], v4, s[36:39], s30 offen offset:0 sc1
s_waitcnt vmcnt(26)
v_mfma_f32_16x16x32_bf16 a[8:11], a[200:203], v[120:123], a[8:11]
buffer_load_dwordx4 v[100:103], v4, s[36:39], s30 offen offset:1024 sc1
s_waitcnt vmcnt(26)
v_mfma_f32_16x16x32_bf16 a[12:15], a[200:203], v[124:127], a[12:15]
buffer_load_dwordx4 v[104:107], v4, s[36:39], s30 offen offset:2048 sc1
v_mfma_f32_16x16x32_bf16 a[16:19], a[204:207], v[112:115], a[16:19]
buffer_load_dwordx4 v[108:111], v4, s[36:39], s30 offen offset:3072 sc1
v_mfma_f32_16x16x32_bf16 a[20:23], a[204:207], v[116:119], a[20:23]
s_add_u32 s30, s68, 1
s_and_b32 s30, s30, 7
s_or_b32 s30, s30, 16
s_lshl_b32 s30, s30, 15
s_add_u32 s30, s30, s59
buffer_load_dwordx4 a[184:187], v4, s[60:63], s30 offen offset:0 sc1
v_mfma_f32_16x16x32_bf16 a[24:27], a[204:207], v[120:123], a[24:27]
buffer_load_dwordx4 a[188:191], v4, s[60:63], s30 offen offset:1024 sc1
v_mfma_f32_16x16x32_bf16 a[28:31], a[204:207], v[124:127], a[28:31]
buffer_load_dwordx4 a[192:195], v4, s[60:63], s30 offen offset:2048 sc1
v_mfma_f32_16x16x32_bf16 a[32:35], a[208:211], v[112:115], a[32:35]
buffer_load_dwordx4 a[196:199], v4, s[60:63], s30 offen offset:3072 sc1
v_mfma_f32_16x16x32_bf16 a[36:39], a[208:211], v[116:119], a[36:39]
v_mfma_f32_16x16x32_bf16 a[40:43], a[208:211], v[120:123], a[40:43]
v_mfma_f32_16x16x32_bf16 a[44:47], a[208:211], v[124:127], a[44:47]
v_mfma_f32_16x16x32_bf16 a[48:51], a[212:215], v[112:115], a[48:51]
v_mfma_f32_16x16x32_bf16 a[52:55], a[212:215], v[116:119], a[52:55]
v_mfma_f32_16x16x32_bf16 a[56:59], a[212:215], v[120:123], a[56:59]
v_mfma_f32_16x16x32_bf16 a[60:63], a[212:215], v[124:127], a[60:63]
s_waitcnt vmcnt(31)
v_mfma_f32_16x16x32_bf16 a[0:3], a[216:219], v[128:131], a[0:3]
s_waitcnt vmcnt(30)
v_mfma_f32_16x16x32_bf16 a[4:7], a[216:219], v[132:135], a[4:7]
s_add_u32 s30, s68, 1
s_and_b32 s30, s30, 7
s_or_b32 s30, s30, 8
s_lshl_b32 s30, s30, 12
s_add_u32 s30, s30, s58
buffer_load_dwordx4 v[112:115], v4, s[36:39], s30 offen offset:0 sc1
s_waitcnt vmcnt(30)
v_mfma_f32_16x16x32_bf16 a[8:11], a[216:219], v[136:139], a[8:11]
buffer_load_dwordx4 v[116:119], v4, s[36:39], s30 offen offset:1024 sc1
s_waitcnt vmcnt(30)
v_mfma_f32_16x16x32_bf16 a[12:15], a[216:219], v[140:143], a[12:15]
buffer_load_dwordx4 v[120:123], v4, s[36:39], s30 offen offset:2048 sc1
v_mfma_f32_16x16x32_bf16 a[16:19], a[220:223], v[128:131], a[16:19]
buffer_load_dwordx4 v[124:127], v4, s[36:39], s30 offen offset:3072 sc1
v_mfma_f32_16x16x32_bf16 a[20:23], a[220:223], v[132:135], a[20:23]
s_add_u32 s30, s68, 2
s_and_b32 s30, s30, 7
s_or_b32 s30, s30, 16
s_lshl_b32 s30, s30, 15
s_add_u32 s30, s30, s59
buffer_load_dwordx4 a[200:203], v4, s[60:63], s30 offen offset:0 sc1
v_mfma_f32_16x16x32_bf16 a[24:27], a[220:223], v[136:139], a[24:27]
buffer_load_dwordx4 a[204:207], v4, s[60:63], s30 offen offset:1024 sc1
v_mfma_f32_16x16x32_bf16 a[28:31], a[220:223], v[140:143], a[28:31]
buffer_load_dwordx4 a[208:211], v4, s[60:63], s30 offen offset:2048 sc1
v_mfma_f32_16x16x32_bf16 a[32:35], a[224:227], v[128:131], a[32:35]
buffer_load_dwordx4 a[212:215], v4, s[60:63], s30 offen offset:3072 sc1
v_mfma_f32_16x16x32_bf16 a[36:39], a[224:227], v[132:135], a[36:39]
s_waitcnt vmcnt(56)
s_barrier
s_cmp_lg_u32 s23, 0
s_cbranch_scc1 F_y_%=
s_and_b32 s27, s22, 7
s_lshl_b32 s27, s27, 5
s_lshr_b32 s28, s22, 3
s_add_u32 s28, s28, s27
s_lshl_b32 s28, s28, 2
s_add_u32 s28, s28, 0x3000
v_mov_b32 v0, 0
v_mov_b32 v1, s98
s_mov_b64 s[92:93], exec
s_mov_b64 exec, 1
buffer_store_dword v1, v0, s[40:43], s28 offen offset:0 sc1
s_mov_b64 exec, s[92:93]
F_y_%=:
v_mfma_f32_16x16x32_bf16 a[40:43], a[224:227], v[136:139], a[40:43]
v_mfma_f32_16x16x32_bf16 a[44:47], a[224:227], v[140:143], a[44:47]
v_mfma_f32_16x16x32_bf16 a[48:51], a[228:231], v[128:131], a[48:51]
v_mfma_f32_16x16x32_bf16 a[52:55], a[228:231], v[132:135], a[52:55]
v_mfma_f32_16x16x32_bf16 a[56:59], a[228:231], v[136:139], a[56:59]
v_mfma_f32_16x16x32_bf16 a[60:63], a[228:231], v[140:143], a[60:63]
s_waitcnt vmcnt(35)
v_mfma_f32_16x16x32_bf16 a[0:3], a[232:235], v[144:147], a[0:3]
s_waitcnt vmcnt(34)
v_mfma_f32_16x16x32_bf16 a[4:7], a[232:235], v[148:151], a[4:7]
s_add_u32 s30, s68, 2
s_and_b32 s30, s30, 7
s_or_b32 s30, s30, 8
s_lshl_b32 s30, s30, 12
s_add_u32 s30, s30, s58
buffer_load_dwordx4 v[128:131], v4, s[36:39], s30 offen offset:0 sc1
s_waitcnt vmcnt(34)
v_mfma_f32_16x16x32_bf16 a[8:11], a[232:235], v[152:155], a[8:11]
buffer_load_dwordx4 v[132:135], v4, s[36:39], s30 offen offset:1024 sc1
s_waitcnt vmcnt(34)
v_mfma_f32_16x16x32_bf16 a[12:15], a[232:235], v[156:159], a[12:15]
buffer_load_dwordx4 v[136:139], v4, s[36:39], s30 offen offset:2048 sc1
v_mfma_f32_16x16x32_bf16 a[16:19], a[236:239], v[144:147], a[16:19]
buffer_load_dwordx4 v[140:143], v4, s[36:39], s30 offen offset:3072 sc1
v_mfma_f32_16x16x32_bf16 a[20:23], a[236:239], v[148:151], a[20:23]
s_add_u32 s30, s68, 3
s_and_b32 s30, s30, 7
s_or_b32 s30, s30, 16
s_lshl_b32 s30, s30, 15
s_add_u32 s30, s30, s59
buffer_load_dwordx4 a[216:219], v4, s[60:63], s30 offen offset:0 sc1
v_mfma_f32_16x16x32_bf16 a[24:27], a[236:239], v[152:155], a[24:27]
buffer_load_dwordx4 a[220:223], v4, s[60:63], s30 offen offset:1024 sc1
v_mfma_f32_16x16x32_bf16 a[28:31], a[236:239], v[156:159], a[28:31]
buffer_load_dwordx4 a[224:227], v4, s[60:63], s30 offen offset:2048 sc1
v_mfma_f32_16x16x32_bf16 a[32:35], a[240:243], v[144:147], a[32:35]
buffer_load_dwordx4 a[228:231], v4, s[60:63], s30 offen offset:3072 sc1
v_mfma_f32_16x16x32_bf16 a[36:39], a[240:243], v[148:151], a[36:39]
v_mfma_f32_16x16x32_bf16 a[40:43], a[240:243], v[152:155], a[40:43]
v_mfma_f32_16x16x32_bf16 a[44:47], a[240:243], v[156:159], a[44:47]
v_mfma_f32_16x16x32_bf16 a[48:51], a[244:247], v[144:147], a[48:51]
v_mfma_f32_16x16x32_bf16 a[52:55], a[244:247], v[148:151], a[52:55]
v_mfma_f32_16x16x32_bf16 a[56:59], a[244:247], v[152:155], a[56:59]
v_mfma_f32_16x16x32_bf16 a[60:63], a[244:247], v[156:159], a[60:63]
s_waitcnt vmcnt(39)
v_mfma_f32_16x16x32_bf16 a[0:3], a[64:67], v[64:67], a[0:3]
s_waitcnt vmcnt(38)
v_mfma_f32_16x16x32_bf16 a[4:7], a[64:67], v[68:71], a[4:7]
s_add_u32 s30, s68, 3
s_and_b32 s30, s30, 7
s_or_b32 s30, s30, 8
s_lshl_b32 s30, s30, 12
s_add_u32 s30, s30, s58
buffer_load_dwordx4 v[144:147], v4, s[36:39], s30 offen offset:0 sc1
s_waitcnt vmcnt(38)
v_mfma_f32_16x16x32_bf16 a[8:11], a[64:67], v[72:75], a[8:11]
buffer_load_dwordx4 v[148:151], v4, s[36:39], s30 offen offset:1024 sc1
s_waitcnt vmcnt(38)
v_mfma_f32_16x16x32_bf16 a[12:15], a[64:67], v[76:79], a[12:15]
buffer_load_dwordx4 v[152:155], v4, s[36:39], s30 offen offset:2048 sc1
v_mfma_f32_16x16x32_bf16 a[16:19], a[68:71], v[64:67], a[16:19]
buffer_load_dwordx4 v[156:159], v4, s[36:39], s30 offen offset:3072 sc1
v_mfma_f32_16x16x32_bf16 a[20:23], a[68:71], v[68:71], a[20:23]
s_add_u32 s30, s68, 4
s_and_b32 s30, s30, 7
s_or_b32 s30, s30, 16
s_lshl_b32 s30, s30, 15
s_add_u32 s30, s30, s59
buffer_load_dwordx4 a[232:235], v4, s[60:63], s30 offen offset:0 sc1
v_mfma_f32_16x16x32_bf16 a[24:27], a[68:71], v[72:75], a[24:27]
buffer_load_dwordx4 a[236:239], v4, s[60:63], s30 offen offset:1024 sc1
v_mfma_f32_16x16x32_bf16 a[28:31], a[68:71], v[76:79], a[28:31]
buffer_load_dwordx4 a[240:243], v4, s[60:63], s30 offen offset:2048 sc1
v_mfma_f32_16x16x32_bf16 a[32:35], a[72:75], v[64:67], a[32:35]
buffer_load_dwordx4 a[244:247], v4, s[60:63], s30 offen offset:3072 sc1
v_mfma_f32_16x16x32_bf16 a[36:39], a[72:75], v[68:71], a[36:39]
v_mfma_f32_16x16x32_bf16 a[40:43], a[72:75], v[72:75], a[40:43]
v_mfma_f32_16x16x32_bf16 a[44:47], a[72:75], v[76:79], a[44:47]
v_mfma_f32_16x16x32_bf16 a[48:51], a[76:79], v[64:67], a[48:51]
v_mfma_f32_16x16x32_bf16 a[52:55], a[76:79], v[68:71], a[52:55]
v_mfma_f32_16x16x32_bf16 a[56:59], a[76:79], v[72:75], a[56:59]
v_mfma_f32_16x16x32_bf16 a[60:63], a[76:79], v[76:79], a[60:63]
s_waitcnt vmcnt(39)
v_mfma_f32_16x16x32_bf16 a[0:3], a[80:83], v[80:83], a[0:3]
s_waitcnt vmcnt(38)
v_mfma_f32_16x16x32_bf16 a[4:7], a[80:83], v[84:87], a[4:7]
s_add_u32 s30, s68, 4
s_and_b32 s30, s30, 7
s_or_b32 s30, s30, 8
s_lshl_b32 s30, s30, 12
s_add_u32 s30, s30, s58
buffer_load_dwordx4 v[64:67], v4, s[36:39], s30 offen offset:0 sc1
s_waitcnt vmcnt(38)
v_mfma_f32_16x16x32_bf16 a[8:11], a[80:83], v[88:91], a[8:11]
buffer_load_dwordx4 v[68:71], v4, s[36:39], s30 offen offset:1024 sc1
s_waitcnt vmcnt(38)
v_mfma_f32_16x16x32_bf16 a[12:15], a[80:83], v[92:95], a[12:15]
buffer_load_dwordx4 v[72:75], v4, s[36:39], s30 offen offset:2048 sc1
v_mfma_f32_16x16x32_bf16 a[16:19], a[84:87], v[80:83], a[16:19]
buffer_load_dwordx4 v[76:79], v4, s[36:39], s30 offen offset:3072 sc1
v_mfma_f32_16x16x32_bf16 a[20:23], a[84:87], v[84:87], a[20:23]
s_add_u32 s30, s68, 5
s_and_b32 s30, s30, 7
s_or_b32 s30, s30, 16
s_lshl_b32 s30, s30, 15
s_add_u32 s30, s30, s59
buffer_load_dwordx4 a[64:67], v4, s[60:63], s30 offen offset:0 sc1
v_mfma_f32_16x16x32_bf16 a[24:27], a[84:87], v[88:91], a[24:27]
buffer_load_dwordx4 a[68:71], v4, s[60:63], s30 offen offset:1024 sc1
v_mfma_f32_16x16x32_bf16 a[28:31], a[84:87], v[92:95], a[28:31]
buffer_load_dwordx4 a[72:75], v4, s[60:63], s30 offen offset:2048 sc1
v_mfma_f32_16x16x32_bf16 a[32:35], a[88:91], v[80:83], a[32:35]
buffer_load_dwordx4 a[76:79], v4, s[60:63], s30 offen offset:3072 sc1
v_mfma_f32_16x16x32_bf16 a[36:39], a[88:91], v[84:87], a[36:39]
v_mfma_f32_16x16x32_bf16 a[40:43], a[88:91], v[88:91], a[40:43]
v_mfma_f32_16x16x32_bf16 a[44:47], a[88:91], v[92:95], a[44:47]
v_mfma_f32_16x16x32_bf16 a[48:51], a[92:95], v[80:83], a[48:51]
v_mfma_f32_16x16x32_bf16 a[52:55], a[92:95], v[84:87], a[52:55]
v_mfma_f32_16x16x32_bf16 a[56:59], a[92:95], v[88:91], a[56:59]
v_mfma_f32_16x16x32_bf16 a[60:63], a[92:95], v[92:95], a[60:63]
s_waitcnt vmcnt(39)
v_mfma_f32_16x16x32_bf16 a[0:3], a[96:99], v[96:99], a[0:3]
s_waitcnt vmcnt(38)
v_mfma_f32_16x16x32_bf16 a[4:7], a[96:99], v[100:103], a[4:7]
s_add_u32 s30, s68, 5
s_and_b32 s30, s30, 7
s_or_b32 s30, s30, 8
s_lshl_b32 s30, s30, 12
s_add_u32 s30, s30, s58
buffer_load_dwordx4 v[80:83], v4, s[36:39], s30 offen offset:0 sc1
s_waitcnt vmcnt(38)
v_mfma_f32_16x16x32_bf16 a[8:11], a[96:99], v[104:107], a[8:11]
buffer_load_dwordx4 v[84:87], v4, s[36:39], s30 offen offset:1024 sc1
s_waitcnt vmcnt(38)
v_mfma_f32_16x16x32_bf16 a[12:15], a[96:99], v[108:111], a[12:15]
buffer_load_dwordx4 v[88:91], v4, s[36:39], s30 offen offset:2048 sc1
v_mfma_f32_16x16x32_bf16 a[16:19], a[100:103], v[96:99], a[16:19]
buffer_load_dwordx4 v[92:95], v4, s[36:39], s30 offen offset:3072 sc1
v_mfma_f32_16x16x32_bf16 a[20:23], a[100:103], v[100:103], a[20:23]
s_add_u32 s30, s68, 6
s_and_b32 s30, s30, 7
s_or_b32 s30, s30, 16
s_lshl_b32 s30, s30, 15
s_add_u32 s30, s30, s59
buffer_load_dwordx4 a[80:83], v4, s[60:63], s30 offen offset:0 sc1
v_mfma_f32_16x16x32_bf16 a[24:27], a[100:103], v[104:107], a[24:27]
buffer_load_dwordx4 a[84:87], v4, s[60:63], s30 offen offset:1024 sc1
v_mfma_f32_16x16x32_bf16 a[28:31], a[100:103], v[108:111], a[28:31]
buffer_load_dwordx4 a[88:91], v4, s[60:63], s30 offen offset:2048 sc1
v_mfma_f32_16x16x32_bf16 a[32:35], a[104:107], v[96:99], a[32:35]
buffer_load_dwordx4 a[92:95], v4, s[60:63], s30 offen offset:3072 sc1
v_mfma_f32_16x16x32_bf16 a[36:39], a[104:107], v[100:103], a[36:39]
v_mfma_f32_16x16x32_bf16 a[40:43], a[104:107], v[104:107], a[40:43]
v_mfma_f32_16x16x32_bf16 a[44:47], a[104:107], v[108:111], a[44:47]
v_mfma_f32_16x16x32_bf16 a[48:51], a[108:111], v[96:99], a[48:51]
v_mfma_f32_16x16x32_bf16 a[52:55], a[108:111], v[100:103], a[52:55]
v_mfma_f32_16x16x32_bf16 a[56:59], a[108:111], v[104:107], a[56:59]
v_mfma_f32_16x16x32_bf16 a[60:63], a[108:111], v[108:111], a[60:63]
s_waitcnt vmcnt(39)
v_mfma_f32_16x16x32_bf16 a[0:3], a[112:115], v[112:115], a[0:3]
s_waitcnt vmcnt(38)
v_mfma_f32_16x16x32_bf16 a[4:7], a[112:115], v[116:119], a[4:7]
s_add_u32 s30, s68, 6
s_and_b32 s30, s30, 7
s_or_b32 s30, s30, 8
s_lshl_b32 s30, s30, 12
s_add_u32 s30, s30, s58
buffer_load_dwordx4 v[96:99], v4, s[36:39], s30 offen offset:0 sc1
s_waitcnt vmcnt(38)
v_mfma_f32_16x16x32_bf16 a[8:11], a[112:115], v[120:123], a[8:11]
buffer_load_dwordx4 v[100:103], v4, s[36:39], s30 offen offset:1024 sc1
s_waitcnt vmcnt(38)
v_mfma_f32_16x16x32_bf16 a[12:15], a[112:115], v[124:127], a[12:15]
buffer_load_dwordx4 v[104:107], v4, s[36:39], s30 offen offset:2048 sc1
v_mfma_f32_16x16x32_bf16 a[16:19], a[116:119], v[112:115], a[16:19]
buffer_load_dwordx4 v[108:111], v4, s[36:39], s30 offen offset:3072 sc1
v_mfma_f32_16x16x32_bf16 a[20:23], a[116:119], v[116:119], a[20:23]
s_add_u32 s30, s68, 7
s_and_b32 s30, s30, 7
s_or_b32 s30, s30, 16
s_lshl_b32 s30, s30, 15
s_add_u32 s30, s30, s59
buffer_load_dwordx4 a[96:99], v4, s[60:63], s30 offen offset:0 sc1
v_mfma_f32_16x16x32_bf16 a[24:27], a[116:119], v[120:123], a[24:27]
buffer_load_dwordx4 a[100:103], v4, s[60:63], s30 offen offset:1024 sc1
v_mfma_f32_16x16x32_bf16 a[28:31], a[116:119], v[124:127], a[28:31]
buffer_load_dwordx4 a[104:107], v4, s[60:63], s30 offen offset:2048 sc1
v_mfma_f32_16x16x32_bf16 a[32:35], a[120:123], v[112:115], a[32:35]
buffer_load_dwordx4 a[108:111], v4, s[60:63], s30 offen offset:3072 sc1
v_mfma_f32_16x16x32_bf16 a[36:39], a[120:123], v[116:119], a[36:39]
v_mfma_f32_16x16x32_bf16 a[40:43], a[120:123], v[120:123], a[40:43]
v_mfma_f32_16x16x32_bf16 a[44:47], a[120:123], v[124:127], a[44:47]
v_mfma_f32_16x16x32_bf16 a[48:51], a[124:127], v[112:115], a[48:51]
v_mfma_f32_16x16x32_bf16 a[52:55], a[124:127], v[116:119], a[52:55]
v_mfma_f32_16x16x32_bf16 a[56:59], a[124:127], v[120:123], a[56:59]
v_mfma_f32_16x16x32_bf16 a[60:63], a[124:127], v[124:127], a[60:63]
s_waitcnt vmcnt(39)
v_mfma_f32_16x16x32_bf16 a[0:3], a[248:251], v[128:131], a[0:3]
s_waitcnt vmcnt(38)
v_mfma_f32_16x16x32_bf16 a[4:7], a[248:251], v[132:135], a[4:7]
s_add_u32 s30, s68, 7
s_and_b32 s30, s30, 7
s_or_b32 s30, s30, 8
s_lshl_b32 s30, s30, 12
s_add_u32 s30, s30, s58
buffer_load_dwordx4 v[112:115], v4, s[36:39], s30 offen offset:0 sc1
s_waitcnt vmcnt(38)
v_mfma_f32_16x16x32_bf16 a[8:11], a[248:251], v[136:139], a[8:11]
buffer_load_dwordx4 v[116:119], v4, s[36:39], s30 offen offset:1024 sc1
s_waitcnt vmcnt(38)
v_mfma_f32_16x16x32_bf16 a[12:15], a[248:251], v[140:143], a[12:15]
buffer_load_dwordx4 v[120:123], v4, s[36:39], s30 offen offset:2048 sc1
v_mfma_f32_16x16x32_bf16 a[16:19], a[252:255], v[128:131], a[16:19]
buffer_load_dwordx4 v[124:127], v4, s[36:39], s30 offen offset:3072 sc1
v_mfma_f32_16x16x32_bf16 a[20:23], a[252:255], v[132:135], a[20:23]
s_add_u32 s30, s68, 0
s_and_b32 s30, s30, 7
s_or_b32 s30, s30, 24
s_lshl_b32 s30, s30, 15
s_add_u32 s30, s30, s59
buffer_load_dwordx4 a[112:115], v4, s[60:63], s30 offen offset:0 sc1
v_mfma_f32_16x16x32_bf16 a[24:27], a[252:255], v[136:139], a[24:27]
buffer_load_dwordx4 a[116:119], v4, s[60:63], s30 offen offset:1024 sc1
v_mfma_f32_16x16x32_bf16 a[28:31], a[252:255], v[140:143], a[28:31]
buffer_load_dwordx4 a[120:123], v4, s[60:63], s30 offen offset:2048 sc1
v_mfma_f32_16x16x32_bf16 a[32:35], v[176:179], v[128:131], a[32:35]
buffer_load_dwordx4 a[124:127], v4, s[60:63], s30 offen offset:3072 sc1
v_mfma_f32_16x16x32_bf16 a[36:39], v[176:179], v[132:135], a[36:39]
v_mfma_f32_16x16x32_bf16 a[40:43], v[176:179], v[136:139], a[40:43]
v_mfma_f32_16x16x32_bf16 a[44:47], v[176:179], v[140:143], a[44:47]
v_mfma_f32_16x16x32_bf16 a[48:51], v[180:183], v[128:131], a[48:51]
v_mfma_f32_16x16x32_bf16 a[52:55], v[180:183], v[132:135], a[52:55]
v_mfma_f32_16x16x32_bf16 a[56:59], v[180:183], v[136:139], a[56:59]
v_mfma_f32_16x16x32_bf16 a[60:63], v[180:183], v[140:143], a[60:63]
s_waitcnt vmcnt(39)
v_mfma_f32_16x16x32_bf16 a[0:3], v[184:187], v[144:147], a[0:3]
s_waitcnt vmcnt(38)
v_mfma_f32_16x16x32_bf16 a[4:7], v[184:187], v[148:151], a[4:7]
s_add_u32 s30, s68, 0
s_and_b32 s30, s30, 7
s_or_b32 s30, s30, 16
s_lshl_b32 s30, s30, 12
s_add_u32 s30, s30, s58
buffer_load_dwordx4 v[128:131], v4, s[36:39], s30 offen offset:0 sc1
s_waitcnt vmcnt(38)
v_mfma_f32_16x16x32_bf16 a[8:11], v[184:187], v[152:155], a[8:11]
buffer_load_dwordx4 v[132:135], v4, s[36:39], s30 offen offset:1024 sc1
s_waitcnt vmcnt(38)
v_mfma_f32_16x16x32_bf16 a[12:15], v[184:187], v[156:159], a[12:15]
buffer_load_dwordx4 v[136:139], v4, s[36:39], s30 offen offset:2048 sc1
v_mfma_f32_16x16x32_bf16 a[16:19], v[188:191], v[144:147], a[16:19]
buffer_load_dwordx4 v[140:143], v4, s[36:39], s30 offen offset:3072 sc1
v_mfma_f32_16x16x32_bf16 a[20:23], v[188:191], v[148:151], a[20:23]
s_add_u32 s30, s68, 1
s_and_b32 s30, s30, 7
s_or_b32 s30, s30, 24
s_lshl_b32 s30, s30, 15
s_add_u32 s30, s30, s59
buffer_load_dwordx4 a[248:251], v4, s[60:63], s30 offen offset:0 sc1
v_mfma_f32_16x16x32_bf16 a[24:27], v[188:191], v[152:155], a[24:27]
buffer_load_dwordx4 a[252:255], v4, s[60:63], s30 offen offset:1024 sc1
v_mfma_f32_16x16x32_bf16 a[28:31], v[188:191], v[156:159], a[28:31]
buffer_load_dwordx4 v[176:179], v4, s[60:63], s30 offen offset:2048 sc1
v_mfma_f32_16x16x32_bf16 a[32:35], v[192:195], v[144:147], a[32:35]
buffer_load_dwordx4 v[180:183], v4, s[60:63], s30 offen offset:3072 sc1
v_mfma_f32_16x16x32_bf16 a[36:39], v[192:195], v[148:151], a[36:39]
v_mfma_f32_16x16x32_bf16 a[40:43], v[192:195], v[152:155], a[40:43]
v_mfma_f32_16x16x32_bf16 a[44:47], v[192:195], v[156:159], a[44:47]
v_mfma_f32_16x16x32_bf16 a[48:51], v[196:199], v[144:147], a[48:51]
v_mfma_f32_16x16x32_bf16 a[52:55], v[196:199], v[148:151], a[52:55]
v_mfma_f32_16x16x32_bf16 a[56:59], v[196:199], v[152:155], a[56:59]
v_mfma_f32_16x16x32_bf16 a[60:63], v[196:199], v[156:159], a[60:63]
s_waitcnt vmcnt(39)
v_mfma_f32_16x16x32_bf16 a[0:3], v[200:203], v[64:67], a[0:3]
s_waitcnt vmcnt(38)
v_mfma_f32_16x16x32_bf16 a[4:7], v[200:203], v[68:71], a[4:7]
s_add_u32 s30, s68, 1
s_and_b32 s30, s30, 7
s_or_b32 s30, s30, 16
s_lshl_b32 s30, s30, 12
s_add_u32 s30, s30, s58
buffer_load_dwordx4 v[144:147], v4, s[36:39], s30 offen offset:0 sc1
s_waitcnt vmcnt(38)
v_mfma_f32_16x16x32_bf16 a[8:11], v[200:203], v[72:75], a[8:11]
buffer_load_dwordx4 v[148:151], v4, s[36:39], s30 offen offset:1024 sc1
s_waitcnt vmcnt(38)
v_mfma_f32_16x16x32_bf16 a[12:15], v[200:203], v[76:79], a[12:15]
buffer_load_dwordx4 v[152:155], v4, s[36:39], s30 offen offset:2048 sc1
v_mfma_f32_16x16x32_bf16 a[16:19], v[204:207], v[64:67], a[16:19]
buffer_load_dwordx4 v[156:159], v4, s[36:39], s30 offen offset:3072 sc1
v_mfma_f32_16x16x32_bf16 a[20:23], v[204:207], v[68:71], a[20:23]
s_add_u32 s30, s68, 2
s_and_b32 s30, s30, 7
s_or_b32 s30, s30, 24
s_lshl_b32 s30, s30, 15
s_add_u32 s30, s30, s59
buffer_load_dwordx4 v[184:187], v4, s[60:63], s30 offen offset:0 sc1
v_mfma_f32_16x16x32_bf16 a[24:27], v[204:207], v[72:75], a[24:27]
buffer_load_dwordx4 v[188:191], v4, s[60:63], s30 offen offset:1024 sc1
v_mfma_f32_16x16x32_bf16 a[28:31], v[204:207], v[76:79], a[28:31]
buffer_load_dwordx4 v[192:195], v4, s[60:63], s30 offen offset:2048 sc1
v_mfma_f32_16x16x32_bf16 a[32:35], v[208:211], v[64:67], a[32:35]
buffer_load_dwordx4 v[196:199], v4, s[60:63], s30 offen offset:3072 sc1
v_mfma_f32_16x16x32_bf16 a[36:39], v[208:211], v[68:71], a[36:39]
v_mfma_f32_16x16x32_bf16 a[40:43], v[208:211], v[72:75], a[40:43]
v_mfma_f32_16x16x32_bf16 a[44:47], v[208:211], v[76:79], a[44:47]
v_mfma_f32_16x16x32_bf16 a[48:51], v[212:215], v[64:67], a[48:51]
v_mfma_f32_16x16x32_bf16 a[52:55], v[212:215], v[68:71], a[52:55]
v_mfma_f32_16x16x32_bf16 a[56:59], v[212:215], v[72:75], a[56:59]
v_mfma_f32_16x16x32_bf16 a[60:63], v[212:215], v[76:79], a[60:63]
s_waitcnt vmcnt(39)
v_mfma_f32_16x16x32_bf16 a[0:3], v[216:219], v[80:83], a[0:3]
s_waitcnt vmcnt(38)
v_mfma_f32_16x16x32_bf16 a[4:7], v[216:219], v[84:87], a[4:7]
s_add_u32 s30, s68, 2
s_and_b32 s30, s30, 7
s_or_b32 s30, s30, 16
s_lshl_b32 s30, s30, 12
s_add_u32 s30, s30, s58
buffer_load_dwordx4 v[64:67], v4, s[36:39], s30 offen offset:0 sc1
s_waitcnt vmcnt(38)
v_mfma_f32_16x16x32_bf16 a[8:11], v[216:219], v[88:91], a[8:11]
buffer_load_dwordx4 v[68:71], v4, s[36:39], s30 offen offset:1024 sc1
s_waitcnt vmcnt(38)
v_mfma_f32_16x16x32_bf16 a[12:15], v[216:219], v[92:95], a[12:15]
buffer_load_dwordx4 v[72:75], v4, s[36:39], s30 offen offset:2048 sc1
v_mfma_f32_16x16x32_bf16 a[16:19], v[220:223], v[80:83], a[16:19]
buffer_load_dwordx4 v[76:79], v4, s[36:39], s30 offen offset:3072 sc1
v_mfma_f32_16x16x32_bf16 a[20:23], v[220:223], v[84:87], a[20:23]
s_add_u32 s30, s68, 3
s_and_b32 s30, s30, 7
s_or_b32 s30, s30, 24
s_lshl_b32 s30, s30, 15
s_add_u32 s30, s30, s59
buffer_load_dwordx4 v[200:203], v4, s[60:63], s30 offen offset:0 sc1
v_mfma_f32_16x16x32_bf16 a[24:27], v[220:223], v[88:91], a[24:27]
buffer_load_dwordx4 v[204:207], v4, s[60:63], s30 offen offset:1024 sc1
v_mfma_f32_16x16x32_bf16 a[28:31], v[220:223], v[92:95], a[28:31]
buffer_load_dwordx4 v[208:211], v4, s[60:63], s30 offen offset:2048 sc1
v_mfma_f32_16x16x32_bf16 a[32:35], v[224:227], v[80:83], a[32:35]
buffer_load_dwordx4 v[212:215], v4, s[60:63], s30 offen offset:3072 sc1
v_mfma_f32_16x16x32_bf16 a[36:39], v[224:227], v[84:87], a[36:39]
v_mfma_f32_16x16x32_bf16 a[40:43], v[224:227], v[88:91], a[40:43]
v_mfma_f32_16x16x32_bf16 a[44:47], v[224:227], v[92:95], a[44:47]
v_mfma_f32_16x16x32_bf16 a[48:51], v[228:231], v[80:83], a[48:51]
v_mfma_f32_16x16x32_bf16 a[52:55], v[228:231], v[84:87], a[52:55]
v_mfma_f32_16x16x32_bf16 a[56:59], v[228:231], v[88:91], a[56:59]
v_mfma_f32_16x16x32_bf16 a[60:63], v[228:231], v[92:95], a[60:63]
s_waitcnt vmcnt(39)
v_mfma_f32_16x16x32_bf16 a[0:3], v[232:235], v[96:99], a[0:3]
s_waitcnt vmcnt(38)
v_mfma_f32_16x16x32_bf16 a[4:7], v[232:235], v[100:103], a[4:7]
s_add_u32 s30, s68, 3
s_and_b32 s30, s30, 7
s_or_b32 s30, s30, 16
s_lshl_b32 s30, s30, 12
s_add_u32 s30, s30, s58
buffer_load_dwordx4 v[80:83], v4, s[36:39], s30 offen offset:0 sc1
s_waitcnt vmcnt(38)
v_mfma_f32_16x16x32_bf16 a[8:11], v[232:235], v[104:107], a[8:11]
buffer_load_dwordx4 v[84:87], v4, s[36:39], s30 offen offset:1024 sc1
s_waitcnt vmcnt(38)
v_mfma_f32_16x16x32_bf16 a[12:15], v[232:235], v[108:111], a[12:15]
buffer_load_dwordx4 v[88:91], v4, s[36:39], s30 offen offset:2048 sc1
v_mfma_f32_16x16x32_bf16 a[16:19], v[236:239], v[96:99], a[16:19]
buffer_load_dwordx4 v[92:95], v4, s[36:39], s30 offen offset:3072 sc1
v_mfma_f32_16x16x32_bf16 a[20:23], v[236:239], v[100:103], a[20:23]
s_add_u32 s30, s68, 4
s_and_b32 s30, s30, 7
s_or_b32 s30, s30, 24
s_lshl_b32 s30, s30, 15
s_add_u32 s30, s30, s59
buffer_load_dwordx4 v[216:219], v4, s[60:63], s30 offen offset:0 sc1
v_mfma_f32_16x16x32_bf16 a[24:27], v[236:239], v[104:107], a[24:27]
buffer_load_dwordx4 v[220:223], v4, s[60:63], s30 offen offset:1024 sc1
v_mfma_f32_16x16x32_bf16 a[28:31], v[236:239], v[108:111], a[28:31]
buffer_load_dwordx4 v[224:227], v4, s[60:63], s30 offen offset:2048 sc1
v_mfma_f32_16x16x32_bf16 a[32:35], v[240:243], v[96:99], a[32:35]
buffer_load_dwordx4 v[228:231], v4, s[60:63], s30 offen offset:3072 sc1
v_mfma_f32_16x16x32_bf16 a[36:39], v[240:243], v[100:103], a[36:39]
v_mfma_f32_16x16x32_bf16 a[40:43], v[240:243], v[104:107], a[40:43]
v_mfma_f32_16x16x32_bf16 a[44:47], v[240:243], v[108:111], a[44:47]
v_mfma_f32_16x16x32_bf16 a[48:51], v[244:247], v[96:99], a[48:51]
v_mfma_f32_16x16x32_bf16 a[52:55], v[244:247], v[100:103], a[52:55]
v_mfma_f32_16x16x32_bf16 a[56:59], v[244:247], v[104:107], a[56:59]
v_mfma_f32_16x16x32_bf16 a[60:63], v[244:247], v[108:111], a[60:63]
s_waitcnt vmcnt(39)
v_mfma_f32_16x16x32_bf16 a[0:3], a[152:155], v[112:115], a[0:3]
s_waitcnt vmcnt(38)
v_mfma_f32_16x16x32_bf16 a[4:7], a[152:155], v[116:119], a[4:7]
s_add_u32 s30, s68, 4
s_and_b32 s30, s30, 7
s_or_b32 s30, s30, 16
s_lshl_b32 s30, s30, 12
s_add_u32 s30, s30, s58
buffer_load_dwordx4 v[96:99], v4, s[36:39], s30 offen offset:0 sc1
s_waitcnt vmcnt(38)
v_mfma_f32_16x16x32_bf16 a[8:11], a[152:155], v[120:123], a[8:11]
buffer_load_dwordx4 v[100:103], v4, s[36:39], s30 offen offset:1024 sc1
s_waitcnt vmcnt(38)
v_mfma_f32_16x16x32_bf16 a[12:15], a[152:155], v[124:127], a[12:15]
buffer_load_dwordx4 v[104:107], v4, s[36:39], s30 offen offset:2048 sc1
v_mfma_f32_16x16x32_bf16 a[16:19], a[156:159], v[112:115], a[16:19]
buffer_load_dwordx4 v[108:111], v4, s[36:39], s30 offen offset:3072 sc1
v_mfma_f32_16x16x32_bf16 a[20:23], a[156:159], v[116:119], a[20:23]
s_add_u32 s30, s68, 5
s_and_b32 s30, s30, 7
s_or_b32 s30, s30, 24
s_lshl_b32 s30, s30, 15
s_add_u32 s30, s30, s59
buffer_load_dwordx4 v[232:235], v4, s[60:63], s30 offen offset:0 sc1
v_mfma_f32_16x16x32_bf16 a[24:27], a[156:159], v[120:123], a[24:27]
buffer_load_dwordx4 v[236:239], v4, s[60:63], s30 offen offset:1024 sc1
v_mfma_f32_16x16x32_bf16 a[28:31], a[156:159], v[124:127], a[28:31]
buffer_load_dwordx4 v[240:243], v4, s[60:63], s30 offen offset:2048 sc1
v_mfma_f32_16x16x32_bf16 a[32:35], a[160:163], v[112:115], a[32:35]
buffer_load_dwordx4 v[244:247], v4, s[60:63], s30 offen offset:3072 sc1
v_mfma_f32_16x16x32_bf16 a[36:39], a[160:163], v[116:119], a[36:39]
v_mfma_f32_16x16x32_bf16 a[40:43], a[160:163], v[120:123], a[40:43]
v_mfma_f32_16x16x32_bf16 a[44:47], a[160:163], v[124:127], a[44:47]
v_mfma_f32_16x16x32_bf16 a[48:51], a[164:167], v[112:115], a[48:51]
v_mfma_f32_16x16x32_bf16 a[52:55], a[164:167], v[116:119], a[52:55]
v_mfma_f32_16x16x32_bf16 a[56:59], a[164:167], v[120:123], a[56:59]
v_mfma_f32_16x16x32_bf16 a[60:63], a[164:167], v[124:127], a[60:63]
s_waitcnt vmcnt(39)
v_mfma_f32_16x16x32_bf16 a[0:3], a[168:171], v[128:131], a[0:3]
s_waitcnt vmcnt(38)
v_mfma_f32_16x16x32_bf16 a[4:7], a[168:171], v[132:135], a[4:7]
s_add_u32 s30, s68, 5
s_and_b32 s30, s30, 7
s_or_b32 s30, s30, 16
s_lshl_b32 s30, s30, 12
s_add_u32 s30, s30, s58
buffer_load_dwordx4 v[112:115], v4, s[36:39], s30 offen offset:0 sc1
s_waitcnt vmcnt(38)
v_mfma_f32_16x16x32_bf16 a[8:11], a[168:171], v[136:139], a[8:11]
buffer_load_dwordx4 v[116:119], v4, s[36:39], s30 offen offset:1024 sc1
s_waitcnt vmcnt(38)
v_mfma_f32_16x16x32_bf16 a[12:15], a[168:171], v[140:143], a[12:15]
buffer_load_dwordx4 v[120:123], v4, s[36:39], s30 offen offset:2048 sc1
v_mfma_f32_16x16x32_bf16 a[16:19], a[172:175], v[128:131], a[16:19]
buffer_load_dwordx4 v[124:127], v4, s[36:39], s30 offen offset:3072 sc1
v_mfma_f32_16x16x32_bf16 a[20:23], a[172:175], v[132:135], a[20:23]
s_add_u32 s30, s68, 6
s_and_b32 s30, s30, 7
s_or_b32 s30, s30, 24
s_lshl_b32 s30, s30, 15
s_add_u32 s30, s30, s59
buffer_load_dwordx4 a[152:155], v4, s[60:63], s30 offen offset:0 sc1
v_mfma_f32_16x16x32_bf16 a[24:27], a[172:175], v[136:139], a[24:27]
buffer_load_dwordx4 a[156:159], v4, s[60:63], s30 offen offset:1024 sc1
v_mfma_f32_16x16x32_bf16 a[28:31], a[172:175], v[140:143], a[28:31]
buffer_load_dwordx4 a[160:163], v4, s[60:63], s30 offen offset:2048 sc1
v_mfma_f32_16x16x32_bf16 a[32:35], a[176:179], v[128:131], a[32:35]
buffer_load_dwordx4 a[164:167], v4, s[60:63], s30 offen offset:3072 sc1
v_mfma_f32_16x16x32_bf16 a[36:39], a[176:179], v[132:135], a[36:39]
v_mfma_f32_16x16x32_bf16 a[40:43], a[176:179], v[136:139], a[40:43]
v_mfma_f32_16x16x32_bf16 a[44:47], a[176:179], v[140:143], a[44:47]
v_mfma_f32_16x16x32_bf16 a[48:51], a[180:183], v[128:131], a[48:51]
v_mfma_f32_16x16x32_bf16 a[52:55], a[180:183], v[132:135], a[52:55]
v_mfma_f32_16x16x32_bf16 a[56:59], a[180:183], v[136:139], a[56:59]
v_mfma_f32_16x16x32_bf16 a[60:63], a[180:183], v[140:143], a[60:63]
s_waitcnt vmcnt(39)
v_mfma_f32_16x16x32_bf16 a[0:3], a[184:187], v[144:147], a[0:3]
s_waitcnt vmcnt(38)
v_mfma_f32_16x16x32_bf16 a[4:7], a[184:187], v[148:151], a[4:7]
s_add_u32 s30, s68, 6
s_and_b32 s30, s30, 7
s_or_b32 s30, s30, 16
s_lshl_b32 s30, s30, 12
s_add_u32 s30, s30, s58
buffer_load_dwordx4 v[128:131], v4, s[36:39], s30 offen offset:0 sc1
s_waitcnt vmcnt(38)
v_mfma_f32_16x16x32_bf16 a[8:11], a[184:187], v[152:155], a[8:11]
buffer_load_dwordx4 v[132:135], v4, s[36:39], s30 offen offset:1024 sc1
s_waitcnt vmcnt(38)
v_mfma_f32_16x16x32_bf16 a[12:15], a[184:187], v[156:159], a[12:15]
buffer_load_dwordx4 v[136:139], v4, s[36:39], s30 offen offset:2048 sc1
v_mfma_f32_16x16x32_bf16 a[16:19], a[188:191], v[144:147], a[16:19]
buffer_load_dwordx4 v[140:143], v4, s[36:39], s30 offen offset:3072 sc1
v_mfma_f32_16x16x32_bf16 a[20:23], a[188:191], v[148:151], a[20:23]
s_add_u32 s30, s68, 7
s_and_b32 s30, s30, 7
s_or_b32 s30, s30, 24
s_lshl_b32 s30, s30, 15
s_add_u32 s30, s30, s59
buffer_load_dwordx4 a[168:171], v4, s[60:63], s30 offen offset:0 sc1
v_mfma_f32_16x16x32_bf16 a[24:27], a[188:191], v[152:155], a[24:27]
buffer_load_dwordx4 a[172:175], v4, s[60:63], s30 offen offset:1024 sc1
v_mfma_f32_16x16x32_bf16 a[28:31], a[188:191], v[156:159], a[28:31]
buffer_load_dwordx4 a[176:179], v4, s[60:63], s30 offen offset:2048 sc1
v_mfma_f32_16x16x32_bf16 a[32:35], a[192:195], v[144:147], a[32:35]
buffer_load_dwordx4 a[180:183], v4, s[60:63], s30 offen offset:3072 sc1
v_mfma_f32_16x16x32_bf16 a[36:39], a[192:195], v[148:151], a[36:39]
v_mfma_f32_16x16x32_bf16 a[40:43], a[192:195], v[152:155], a[40:43]
v_mfma_f32_16x16x32_bf16 a[44:47], a[192:195], v[156:159], a[44:47]
v_mfma_f32_16x16x32_bf16 a[48:51], a[196:199], v[144:147], a[48:51]
v_mfma_f32_16x16x32_bf16 a[52:55], a[196:199], v[148:151], a[52:55]
v_mfma_f32_16x16x32_bf16 a[56:59], a[196:199], v[152:155], a[56:59]
v_mfma_f32_16x16x32_bf16 a[60:63], a[196:199], v[156:159], a[60:63]
s_waitcnt vmcnt(39)
v_mfma_f32_16x16x32_bf16 a[0:3], a[200:203], v[64:67], a[0:3]
s_waitcnt vmcnt(38)
v_mfma_f32_16x16x32_bf16 a[4:7], a[200:203], v[68:71], a[4:7]
s_add_u32 s30, s68, 7
s_and_b32 s30, s30, 7
s_or_b32 s30, s30, 16
s_lshl_b32 s30, s30, 12
s_add_u32 s30, s30, s58
buffer_load_dwordx4 v[144:147], v4, s[36:39], s30 offen offset:0 sc1
s_waitcnt vmcnt(38)
v_mfma_f32_16x16x32_bf16 a[8:11], a[200:203], v[72:75], a[8:11]
buffer_load_dwordx4 v[148:151], v4, s[36:39], s30 offen offset:1024 sc1
s_waitcnt vmcnt(38)
v_mfma_f32_16x16x32_bf16 a[12:15], a[200:203], v[76:79], a[12:15]
buffer_load_dwordx4 v[152:155], v4, s[36:39], s30 offen offset:2048 sc1
v_mfma_f32_16x16x32_bf16 a[16:19], a[204:207], v[64:67], a[16:19]
buffer_load_dwordx4 v[156:159], v4, s[36:39], s30 offen offset:3072 sc1
v_mfma_f32_16x16x32_bf16 a[20:23], a[204:207], v[68:71], a[20:23]
v_mfma_f32_16x16x32_bf16 a[24:27], a[204:207], v[72:75], a[24:27]
v_mfma_f32_16x16x32_bf16 a[28:31], a[204:207], v[76:79], a[28:31]
v_mfma_f32_16x16x32_bf16 a[32:35], a[208:211], v[64:67], a[32:35]
v_mfma_f32_16x16x32_bf16 a[36:39], a[208:211], v[68:71], a[36:39]
v_mfma_f32_16x16x32_bf16 a[40:43], a[208:211], v[72:75], a[40:43]
v_mfma_f32_16x16x32_bf16 a[44:47], a[208:211], v[76:79], a[44:47]
v_mfma_f32_16x16x32_bf16 a[48:51], a[212:215], v[64:67], a[48:51]
v_mfma_f32_16x16x32_bf16 a[52:55], a[212:215], v[68:71], a[52:55]
v_mfma_f32_16x16x32_bf16 a[56:59], a[212:215], v[72:75], a[56:59]
v_mfma_f32_16x16x32_bf16 a[60:63], a[212:215], v[76:79], a[60:63]
s_waitcnt vmcnt(35)
v_mfma_f32_16x16x32_bf16 a[0:3], a[216:219], v[80:83], a[0:3]
s_waitcnt vmcnt(34)
v_mfma_f32_16x16x32_bf16 a[4:7], a[216:219], v[84:87], a[4:7]
s_add_u32 s30, s68, 0
s_and_b32 s30, s30, 7
s_or_b32 s30, s30, 24
s_lshl_b32 s30, s30, 12
s_add_u32 s30, s30, s58
buffer_load_dwordx4 v[64:67], v4, s[36:39], s30 offen offset:0 sc1
s_waitcnt vmcnt(34)
v_mfma_f32_16x16x32_bf16 a[8:11], a[216:219], v[88:91], a[8:11]
buffer_load_dwordx4 v[68:71], v4, s[36:39], s30 offen offset:1024 sc1
s_waitcnt vmcnt(34)
v_mfma_f32_16x16x32_bf16 a[12:15], a[216:219], v[92:95], a[12:15]
buffer_load_dwordx4 v[72:75], v4, s[36:39], s30 offen offset:2048 sc1
v_mfma_f32_16x16x32_bf16 a[16:19], a[220:223], v[80:83], a[16:19]
buffer_load_dwordx4 v[76:79], v4, s[36:39], s30 offen offset:3072 sc1
v_mfma_f32_16x16x32_bf16 a[20:23], a[220:223], v[84:87], a[20:23]
v_lshrrev_b32 v48, 4, v4
v_and_b32 v49, 15, v48
s_and_b32 s27, s22, 7
s_lshr_b32 s28, s22, 3
s_and_b32 s28, s28, 3
s_lshl_b32 s28, s28, 3
s_add_u32 s27, s27, s28
s_lshl_b32 s27, s27, 6
s_lshl_b32 s28, s23, 4
s_add_u32 s27, s27, s28
v_add_u32 v49, s27, v49
v_mfma_f32_16x16x32_bf16 a[24:27], a[220:223], v[88:91], a[24:27]
v_lshlrev_b32 v51, 2, v49
s_mov_b32 s27, 0x4500000
buffer_load_dword v28, v51, s[40:43], s27 offen offset:0 sc1
v_mfma_f32_16x16x32_bf16 a[28:31], a[220:223], v[92:95], a[28:31]
v_lshlrev_b32 v49, 10, v49
v_lshrrev_b32 v50, 4, v48
v_lshl_add_u32 v49, v50, 4, v49
s_lshr_b32 s27, s22, 5
s_lshl_b32 s27, s27, 7
v_add_u32 v52, s27, v49
v_mfma_f32_16x16x32_bf16 a[32:35], a[224:227], v[80:83], a[32:35]
buffer_load_dwordx4 v[20:23], v52, s[76:79], 0 offen offset:0
v_mfma_f32_16x16x32_bf16 a[36:39], a[224:227], v[84:87], a[36:39]
buffer_load_dwordx4 v[24:27], v52, s[76:79], 0 offen offset:64
v_mfma_f32_16x16x32_bf16 a[40:43], a[224:227], v[88:91], a[40:43]
v_mfma_f32_16x16x32_bf16 a[44:47], a[224:227], v[92:95], a[44:47]
v_mfma_f32_16x16x32_bf16 a[48:51], a[228:231], v[80:83], a[48:51]
v_mfma_f32_16x16x32_bf16 a[52:55], a[228:231], v[84:87], a[52:55]
v_mfma_f32_16x16x32_bf16 a[56:59], a[228:231], v[88:91], a[56:59]
v_mfma_f32_16x16x32_bf16 a[60:63], a[228:231], v[92:95], a[60:63]
s_waitcnt vmcnt(34)
v_mfma_f32_16x16x32_bf16 a[0:3], a[232:235], v[96:99], a[0:3]
s_waitcnt vmcnt(33)
v_mfma_f32_16x16x32_bf16 a[4:7], a[232:235], v[100:103], a[4:7]
s_add_u32 s30, s68, 1
s_and_b32 s30, s30, 7
s_or_b32 s30, s30, 24
s_lshl_b32 s30, s30, 12
s_add_u32 s30, s30, s58
buffer_load_dwordx4 v[80:83], v4, s[36:39], s30 offen offset:0 sc1
s_waitcnt vmcnt(33)
v_mfma_f32_16x16x32_bf16 a[8:11], a[232:235], v[104:107], a[8:11]
buffer_load_dwordx4 v[84:87], v4, s[36:39], s30 offen offset:1024 sc1
s_waitcnt vmcnt(33)
v_mfma_f32_16x16x32_bf16 a[12:15], a[232:235], v[108:111], a[12:15]
buffer_load_dwordx4 v[88:91], v4, s[36:39], s30 offen offset:2048 sc1
v_mfma_f32_16x16x32_bf16 a[16:19], a[236:239], v[96:99], a[16:19]
buffer_load_dwordx4 v[92:95], v4, s[36:39], s30 offen offset:3072 sc1
v_mfma_f32_16x16x32_bf16 a[20:23], a[236:239], v[100:103], a[20:23]
v_mfma_f32_16x16x32_bf16 a[24:27], a[236:239], v[104:107], a[24:27]
v_mfma_f32_16x16x32_bf16 a[28:31], a[236:239], v[108:111], a[28:31]
v_mfma_f32_16x16x32_bf16 a[32:35], a[240:243], v[96:99], a[32:35]
v_mfma_f32_16x16x32_bf16 a[36:39], a[240:243], v[100:103], a[36:39]
v_mfma_f32_16x16x32_bf16 a[40:43], a[240:243], v[104:107], a[40:43]
v_mfma_f32_16x16x32_bf16 a[44:47], a[240:243], v[108:111], a[44:47]
v_mfma_f32_16x16x32_bf16 a[48:51], a[244:247], v[96:99], a[48:51]
v_mfma_f32_16x16x32_bf16 a[52:55], a[244:247], v[100:103], a[52:55]
v_mfma_f32_16x16x32_bf16 a[56:59], a[244:247], v[104:107], a[56:59]
v_mfma_f32_16x16x32_bf16 a[60:63], a[244:247], v[108:111], a[60:63]
s_waitcnt vmcnt(30)
v_mfma_f32_16x16x32_bf16 a[0:3], a[64:67], v[112:115], a[0:3]
s_waitcnt vmcnt(29)
v_mfma_f32_16x16x32_bf16 a[4:7], a[64:67], v[116:119], a[4:7]
s_add_u32 s30, s68, 2
s_and_b32 s30, s30, 7
s_or_b32 s30, s30, 24
s_lshl_b32 s30, s30, 12
s_add_u32 s30, s30, s58
buffer_load_dwordx4 v[96:99], v4, s[36:39], s30 offen offset:0 sc1
s_waitcnt vmcnt(29)
v_mfma_f32_16x16x32_bf16 a[8:11], a[64:67], v[120:123], a[8:11]
buffer_load_dwordx4 v[100:103], v4, s[36:39], s30 offen offset:1024 sc1
s_waitcnt vmcnt(29)
v_mfma_f32_16x16x32_bf16 a[12:15], a[64:67], v[124:127], a[12:15]
buffer_load_dwordx4 v[104:107], v4, s[36:39], s30 offen offset:2048 sc1
v_mfma_f32_16x16x32_bf16 a[16:19], a[68:71], v[112:115], a[16:19]
buffer_load_dwordx4 v[108:111], v4, s[36:39], s30 offen offset:3072 sc1
v_mfma_f32_16x16x32_bf16 a[20:23], a[68:71], v[116:119], a[20:23]
v_lshrrev_b32 v53, 4, v4
v_and_b32 v53, 31, v53
v_lshlrev_b32 v53, 2, v53
s_lshr_b32 s27, s22, 5
s_lshl_b32 s27, s27, 7
s_add_u32 s56, s27, 0x3000
buffer_load_dword v29, v53, s[40:43], s56 offen offset:0 sc1
v_mfma_f32_16x16x32_bf16 a[24:27], a[68:71], v[120:123], a[24:27]
v_mfma_f32_16x16x32_bf16 a[28:31], a[68:71], v[124:127], a[28:31]
v_mfma_f32_16x16x32_bf16 a[32:35], a[72:75], v[112:115], a[32:35]
v_mfma_f32_16x16x32_bf16 a[36:39], a[72:75], v[116:119], a[36:39]
v_mfma_f32_16x16x32_bf16 a[40:43], a[72:75], v[120:123], a[40:43]
v_mfma_f32_16x16x32_bf16 a[44:47], a[72:75], v[124:127], a[44:47]
v_mfma_f32_16x16x32_bf16 a[48:51], a[76:79], v[112:115], a[48:51]
v_mfma_f32_16x16x32_bf16 a[52:55], a[76:79], v[116:119], a[52:55]
v_mfma_f32_16x16x32_bf16 a[56:59], a[76:79], v[120:123], a[56:59]
v_mfma_f32_16x16x32_bf16 a[60:63], a[76:79], v[124:127], a[60:63]
s_waitcnt vmcnt(27)
v_mfma_f32_16x16x32_bf16 a[0:3], a[80:83], v[128:131], a[0:3]
s_waitcnt vmcnt(26)
v_mfma_f32_16x16x32_bf16 a[4:7], a[80:83], v[132:135], a[4:7]
s_add_u32 s30, s68, 3
s_and_b32 s30, s30, 7
s_or_b32 s30, s30, 24
s_lshl_b32 s30, s30, 12
s_add_u32 s30, s30, s58
buffer_load_dwordx4 v[112:115], v4, s[36:39], s30 offen offset:0 sc1
s_waitcnt vmcnt(26)
v_mfma_f32_16x16x32_bf16 a[8:11], a[80:83], v[136:139], a[8:11]
buffer_load_dwordx4 v[116:119], v4, s[36:39], s30 offen offset:1024 sc1
s_waitcnt vmcnt(26)
v_mfma_f32_16x16x32_bf16 a[12:15], a[80:83], v[140:143], a[12:15]
buffer_load_dwordx4 v[120:123], v4, s[36:39], s30 offen offset:2048 sc1
v_mfma_f32_16x16x32_bf16 a[16:19], a[84:87], v[128:131], a[16:19]
buffer_load_dwordx4 v[124:127], v4, s[36:39], s30 offen offset:3072 sc1
v_mfma_f32_16x16x32_bf16 a[20:23], a[84:87], v[132:135], a[20:23]
v_mfma_f32_16x16x32_bf16 a[24:27], a[84:87], v[136:139], a[24:27]
v_mfma_f32_16x16x32_bf16 a[28:31], a[84:87], v[140:143], a[28:31]
v_mfma_f32_16x16x32_bf16 a[32:35], a[88:91], v[128:131], a[32:35]
v_mfma_f32_16x16x32_bf16 a[36:39], a[88:91], v[132:135], a[36:39]
v_mfma_f32_16x16x32_bf16 a[40:43], a[88:91], v[136:139], a[40:43]
v_mfma_f32_16x16x32_bf16 a[44:47], a[88:91], v[140:143], a[44:47]
v_mfma_f32_16x16x32_bf16 a[48:51], a[92:95], v[128:131], a[48:51]
v_mfma_f32_16x16x32_bf16 a[52:55], a[92:95], v[132:135], a[52:55]
v_mfma_f32_16x16x32_bf16 a[56:59], a[92:95], v[136:139], a[56:59]
v_mfma_f32_16x16x32_bf16 a[60:63], a[92:95], v[140:143], a[60:63]
s_waitcnt vmcnt(23)
v_mfma_f32_16x16x32_bf16 a[0:3], a[96:99], v[144:147], a[0:3]
s_waitcnt vmcnt(22)
v_mfma_f32_16x16x32_bf16 a[4:7], a[96:99], v[148:151], a[4:7]
s_add_u32 s30, s68, 4
s_and_b32 s30, s30, 7
s_or_b32 s30, s30, 24
s_lshl_b32 s30, s30, 12
s_add_u32 s30, s30, s58
buffer_load_dwordx4 v[128:131], v4, s[36:39], s30 offen offset:0 sc1
s_waitcnt vmcnt(22)
v_mfma_f32_16x16x32_bf16 a[8:11], a[96:99], v[152:155], a[8:11]
buffer_load_dwordx4 v[132:135], v4, s[36:39], s30 offen offset:1024 sc1
s_waitcnt vmcnt(22)
v_mfma_f32_16x16x32_bf16 a[12:15], a[96:99], v[156:159], a[12:15]
buffer_load_dwordx4 v[136:139], v4, s[36:39], s30 offen offset:2048 sc1
v_mfma_f32_16x16x32_bf16 a[16:19], a[100:103], v[144:147], a[16:19]
buffer_load_dwordx4 v[140:143], v4, s[36:39], s30 offen offset:3072 sc1
v_mfma_f32_16x16x32_bf16 a[20:23], a[100:103], v[148:151], a[20:23]
v_mfma_f32_16x16x32_bf16 a[24:27], a[100:103], v[152:155], a[24:27]
v_mfma_f32_16x16x32_bf16 a[28:31], a[100:103], v[156:159], a[28:31]
v_mfma_f32_16x16x32_bf16 a[32:35], a[104:107], v[144:147], a[32:35]
v_mfma_f32_16x16x32_bf16 a[36:39], a[104:107], v[148:151], a[36:39]
v_mfma_f32_16x16x32_bf16 a[40:43], a[104:107], v[152:155], a[40:43]
v_mfma_f32_16x16x32_bf16 a[44:47], a[104:107], v[156:159], a[44:47]
v_mfma_f32_16x16x32_bf16 a[48:51], a[108:111], v[144:147], a[48:51]
v_mfma_f32_16x16x32_bf16 a[52:55], a[108:111], v[148:151], a[52:55]
v_mfma_f32_16x16x32_bf16 a[56:59], a[108:111], v[152:155], a[56:59]
v_mfma_f32_16x16x32_bf16 a[60:63], a[108:111], v[156:159], a[60:63]
s_waitcnt vmcnt(23)
v_mfma_f32_16x16x32_bf16 a[0:3], a[112:115], v[64:67], a[0:3]
s_waitcnt vmcnt(22)
v_mfma_f32_16x16x32_bf16 a[4:7], a[112:115], v[68:71], a[4:7]
s_add_u32 s30, s68, 5
s_and_b32 s30, s30, 7
s_or_b32 s30, s30, 24
s_lshl_b32 s30, s30, 12
s_add_u32 s30, s30, s58
buffer_load_dwordx4 v[144:147], v4, s[36:39], s30 offen offset:0 sc1
s_waitcnt vmcnt(22)
v_mfma_f32_16x16x32_bf16 a[8:11], a[112:115], v[72:75], a[8:11]
buffer_load_dwordx4 v[148:151], v4, s[36:39], s30 offen offset:1024 sc1
s_waitcnt vmcnt(22)
v_mfma_f32_16x16x32_bf16 a[12:15], a[112:115], v[76:79], a[12:15]
buffer_load_dwordx4 v[152:155], v4, s[36:39], s30 offen offset:2048 sc1
v_mfma_f32_16x16x32_bf16 a[16:19], a[116:119], v[64:67], a[16:19]
buffer_load_dwordx4 v[156:159], v4, s[36:39], s30 offen offset:3072 sc1
v_mfma_f32_16x16x32_bf16 a[20:23], a[116:119], v[68:71], a[20:23]
s_waitcnt vmcnt(12)
v_cmp_ne_u32 vcc, s98, v29
s_nop 1
s_cbranch_vccz N_y_done_%=
s_memrealtime s[94:95]
s_waitcnt lgkmcnt(0)
N_ypoll_%=:
buffer_load_dword v29, v53, s[40:43], s56 offen offset:0 sc1
s_waitcnt vmcnt(0)
v_cmp_ne_u32 vcc, s98, v29
s_nop 1
s_cbranch_vccz N_ypoll_ok_%=
s_sleep 1
s_memrealtime s[96:97]
s_waitcnt lgkmcnt(0)
s_sub_u32 s29, s96, s94
s_cmp_lt_u32 s29, 0x1e8480
s_cbranch_scc1 N_ypoll_%=
N_ypoll_ok_%=:
N_y_done_%=:
v_mfma_f32_16x16x32_bf16 a[24:27], a[116:119], v[72:75], a[24:27]
v_lshrrev_b32 v54, 4, v4
v_lshrrev_b32 v55, 3, v54
v_lshlrev_b32 v55, 12, v55
v_and_b32 v54, 7, v54
v_lshl_add_u32 v55, v54, 4, v55
s_lshr_b32 s27, s22, 5
s_lshl_b32 s27, s27, 6
s_lshl_b32 s28, s23, 4
s_add_u32 s27, s27, s28
s_lshl_b32 s27, s27, 12
s_and_b32 s28, s22, 7
s_lshr_b32 s29, s22, 3
s_and_b32 s29, s29, 3
s_lshl_b32 s29, s29, 3
s_add_u32 s28, s28, s29
s_lshl_b32 s28, s28, 7
s_add_u32 s27, s27, s28
s_add_u32 s27, s27, 0x4300000
buffer_load_dwordx4 v[32:35], v55, s[40:43], s27 offen offset:0 sc1
s_add_u32 s27, s27, 0x8000
buffer_load_dwordx4 v[36:39], v55, s[40:43], s27 offen offset:0 sc1
v_mfma_f32_16x16x32_bf16 a[28:31], a[116:119], v[76:79], a[28:31]
v_mfma_f32_16x16x32_bf16 a[32:35], a[120:123], v[64:67], a[32:35]
v_mfma_f32_16x16x32_bf16 a[36:39], a[120:123], v[68:71], a[36:39]
v_mfma_f32_16x16x32_bf16 a[40:43], a[120:123], v[72:75], a[40:43]
v_mfma_f32_16x16x32_bf16 a[44:47], a[120:123], v[76:79], a[44:47]
v_mfma_f32_16x16x32_bf16 a[48:51], a[124:127], v[64:67], a[48:51]
v_mfma_f32_16x16x32_bf16 a[52:55], a[124:127], v[68:71], a[52:55]
v_mfma_f32_16x16x32_bf16 a[56:59], a[124:127], v[72:75], a[56:59]
v_mfma_f32_16x16x32_bf16 a[60:63], a[124:127], v[76:79], a[60:63]
v_mfma_f32_16x16x32_bf16 a[0:3], a[248:251], v[80:83], a[0:3]
v_mfma_f32_16x16x32_bf16 a[4:7], a[248:251], v[84:87], a[4:7]
s_add_u32 s30, s68, 6
s_and_b32 s30, s30, 7
s_or_b32 s30, s30, 24
s_lshl_b32 s30, s30, 12
s_add_u32 s30, s30, s58
buffer_load_dwordx4 v[64:67], v4, s[36:39], s30 offen offset:0 sc1
v_mfma_f32_16x16x32_bf16 a[8:11], a[248:251], v[88:91], a[8:11]
buffer_load_dwordx4 v[68:71], v4, s[36:39], s30 offen offset:1024 sc1
v_mfma_f32_16x16x32_bf16 a[12:15], a[248:251], v[92:95], a[12:15]
buffer_load_dwordx4 v[72:75], v4, s[36:39], s30 offen offset:2048 sc1
v_mfma_f32_16x16x32_bf16 a[16:19], a[252:255], v[80:83], a[16:19]
buffer_load_dwordx4 v[76:79], v4, s[36:39], s30 offen offset:3072 sc1
v_mfma_f32_16x16x32_bf16 a[20:23], a[252:255], v[84:87], a[20:23]
v_mfma_f32_16x16x32_bf16 a[24:27], a[252:255], v[88:91], a[24:27]
v_mfma_f32_16x16x32_bf16 a[28:31], a[252:255], v[92:95], a[28:31]
v_mfma_f32_16x16x32_bf16 a[32:35], v[176:179], v[80:83], a[32:35]
v_mfma_f32_16x16x32_bf16 a[36:39], v[176:179], v[84:87], a[36:39]
v_mfma_f32_16x16x32_bf16 a[40:43], v[176:179], v[88:91], a[40:43]
v_mfma_f32_16x16x32_bf16 a[44:47], v[176:179], v[92:95], a[44:47]
v_mfma_f32_16x16x32_bf16 a[48:51], v[180:183], v[80:83], a[48:51]
v_mfma_f32_16x16x32_bf16 a[52:55], v[180:183], v[84:87], a[52:55]
v_mfma_f32_16x16x32_bf16 a[56:59], v[180:183], v[88:91], a[56:59]
v_mfma_f32_16x16x32_bf16 a[60:63], v[180:183], v[92:95], a[60:63]
v_mfma_f32_16x16x32_bf16 a[0:3], v[184:187], v[96:99], a[0:3]
v_mfma_f32_16x16x32_bf16 a[4:7], v[184:187], v[100:103], a[4:7]
s_add_u32 s30, s68, 7
s_and_b32 s30, s30, 7
s_or_b32 s30, s30, 24
s_lshl_b32 s30, s30, 12
s_add_u32 s30, s30, s58
buffer_load_dwordx4 v[80:83], v4, s[36:39], s30 offen offset:0 sc1
v_mfma_f32_16x16x32_bf16 a[8:11], v[184:187], v[104:107], a[8:11]
buffer_load_dwordx4 v[84:87], v4, s[36:39], s30 offen offset:1024 sc1
v_mfma_f32_16x16x32_bf16 a[12:15], v[184:187], v[108:111], a[12:15]
buffer_load_dwordx4 v[88:91], v4, s[36:39], s30 offen offset:2048 sc1
v_mfma_f32_16x16x32_bf16 a[16:19], v[188:191], v[96:99], a[16:19]
buffer_load_dwordx4 v[92:95], v4, s[36:39], s30 offen offset:3072 sc1
v_mfma_f32_16x16x32_bf16 a[20:23], v[188:191], v[100:103], a[20:23]
v_mfma_f32_16x16x32_bf16 a[24:27], v[188:191], v[104:107], a[24:27]
v_mfma_f32_16x16x32_bf16 a[28:31], v[188:191], v[108:111], a[28:31]
v_mfma_f32_16x16x32_bf16 a[32:35], v[192:195], v[96:99], a[32:35]
v_mfma_f32_16x16x32_bf16 a[36:39], v[192:195], v[100:103], a[36:39]
v_mfma_f32_16x16x32_bf16 a[40:43], v[192:195], v[104:107], a[40:43]
v_mfma_f32_16x16x32_bf16 a[44:47], v[192:195], v[108:111], a[44:47]
v_mfma_f32_16x16x32_bf16 a[48:51], v[196:199], v[96:99], a[48:51]
v_mfma_f32_16x16x32_bf16 a[52:55], v[196:199], v[100:103], a[52:55]
v_mfma_f32_16x16x32_bf16 a[56:59], v[196:199], v[104:107], a[56:59]
v_mfma_f32_16x16x32_bf16 a[60:63], v[196:199], v[108:111], a[60:63]
s_waitcnt vmcnt(21)
v_mfma_f32_16x16x32_bf16 a[0:3], v[200:203], v[112:115], a[0:3]
s_waitcnt vmcnt(20)
v_mfma_f32_16x16x32_bf16 a[4:7], v[200:203], v[116:119], a[4:7]
s_waitcnt vmcnt(19)
v_mfma_f32_16x16x32_bf16 a[8:11], v[200:203], v[120:123], a[8:11]
s_waitcnt vmcnt(18)
v_mfma_f32_16x16x32_bf16 a[12:15], v[200:203], v[124:127], a[12:15]
v_mfma_f32_16x16x32_bf16 a[16:19], v[204:207], v[112:115], a[16:19]
v_mfma_f32_16x16x32_bf16 a[20:23], v[204:207], v[116:119], a[20:23]
v_mfma_f32_16x16x32_bf16 a[24:27], v[204:207], v[120:123], a[24:27]
v_mfma_f32_16x16x32_bf16 a[28:31], v[204:207], v[124:127], a[28:31]
v_mfma_f32_16x16x32_bf16 a[32:35], v[208:211], v[112:115], a[32:35]
v_mfma_f32_16x16x32_bf16 a[36:39], v[208:211], v[116:119], a[36:39]
v_mfma_f32_16x16x32_bf16 a[40:43], v[208:211], v[120:123], a[40:43]
v_mfma_f32_16x16x32_bf16 a[44:47], v[208:211], v[124:127], a[44:47]
v_mfma_f32_16x16x32_bf16 a[48:51], v[212:215], v[112:115], a[48:51]
v_mfma_f32_16x16x32_bf16 a[52:55], v[212:215], v[116:119], a[52:55]
v_mfma_f32_16x16x32_bf16 a[56:59], v[212:215], v[120:123], a[56:59]
v_mfma_f32_16x16x32_bf16 a[60:63], v[212:215], v[124:127], a[60:63]
s_waitcnt vmcnt(17)
v_mfma_f32_16x16x32_bf16 a[0:3], v[216:219], v[128:131], a[0:3]
s_waitcnt vmcnt(16)
v_mfma_f32_16x16x32_bf16 a[4:7], v[216:219], v[132:135], a[4:7]
v_lshrrev_b32 v0, 4, v4
v_lshrrev_b32 v2, 3, v0
s_lshl_b32 s27, s23, 4
v_add_u32 v2, s27, v2
v_mul_u32_u24 v2, 0x90, v2
v_and_b32 v3, 7, v0
v_lshl_add_u32 v2, v3, 4, v2
v_add_u32 v2, s24, v2
v_add_u32 v2, 0x10000, v2
s_waitcnt vmcnt(9)
ds_write_b128 v2, v[32:35] offset:0
s_waitcnt vmcnt(8)
ds_write_b128 v2, v[36:39] offset:1152
v_mfma_f32_16x16x32_bf16 a[8:11], v[216:219], v[136:139], a[8:11]
v_mfma_f32_16x16x32_bf16 a[12:15], v[216:219], v[140:143], a[12:15]
v_mfma_f32_16x16x32_bf16 a[16:19], v[220:223], v[128:131], a[16:19]
v_mfma_f32_16x16x32_bf16 a[20:23], v[220:223], v[132:135], a[20:23]
v_mfma_f32_16x16x32_bf16 a[24:27], v[220:223], v[136:139], a[24:27]
v_mfma_f32_16x16x32_bf16 a[28:31], v[220:223], v[140:143], a[28:31]
v_mfma_f32_16x16x32_bf16 a[32:35], v[224:227], v[128:131], a[32:35]
v_mfma_f32_16x16x32_bf16 a[36:39], v[224:227], v[132:135], a[36:39]
v_mfma_f32_16x16x32_bf16 a[40:43], v[224:227], v[136:139], a[40:43]
v_mfma_f32_16x16x32_bf16 a[44:47], v[224:227], v[140:143], a[44:47]
v_mfma_f32_16x16x32_bf16 a[48:51], v[228:231], v[128:131], a[48:51]
v_mfma_f32_16x16x32_bf16 a[52:55], v[228:231], v[132:135], a[52:55]
v_mfma_f32_16x16x32_bf16 a[56:59], v[228:231], v[136:139], a[56:59]
v_mfma_f32_16x16x32_bf16 a[60:63], v[228:231], v[140:143], a[60:63]
v_mfma_f32_16x16x32_bf16 a[0:3], v[232:235], v[144:147], a[0:3]
v_mfma_f32_16x16x32_bf16 a[4:7], v[232:235], v[148:151], a[4:7]
s_waitcnt lgkmcnt(0)
s_barrier
v_lshrrev_b32 v2, 4, v0
v_lshlrev_b32 v2, 3, v2
v_bfe_u32 v3, v0, 2, 2
v_add_u32 v2, v2, v3
v_mul_u32_u24 v2, 0x90, v2
v_and_b32 v3, 3, v0
v_lshl_add_u32 v2, v3, 3, v2
s_lshl_b32 s27, s23, 5
s_add_u32 s27, s27, s24
v_add_u32 v2, s27, v2
v_add_u32 v2, 0x10000, v2
ds_read_b64_tr_b16 v[40:41], v2 offset:0
ds_read_b64_tr_b16 v[42:43], v2 offset:576
ds_read_b64_tr_b16 v[44:45], v2 offset:4608
ds_read_b64_tr_b16 v[46:47], v2 offset:5184
v_mfma_f32_16x16x32_bf16 a[8:11], v[232:235], v[152:155], a[8:11]
v_mfma_f32_16x16x32_bf16 a[12:15], v[232:235], v[156:159], a[12:15]
v_mfma_f32_16x16x32_bf16 a[16:19], v[236:239], v[144:147], a[16:19]
v_mfma_f32_16x16x32_bf16 a[20:23], v[236:239], v[148:151], a[20:23]
v_mfma_f32_16x16x32_bf16 a[24:27], v[236:239], v[152:155], a[24:27]
v_mfma_f32_16x16x32_bf16 a[28:31], v[236:239], v[156:159], a[28:31]
v_mfma_f32_16x16x32_bf16 a[32:35], v[240:243], v[144:147], a[32:35]
v_mfma_f32_16x16x32_bf16 a[36:39], v[240:243], v[148:151], a[36:39]
v_mfma_f32_16x16x32_bf16 a[40:43], v[240:243], v[152:155], a[40:43]
v_mfma_f32_16x16x32_bf16 a[44:47], v[240:243], v[156:159], a[44:47]
v_mfma_f32_16x16x32_bf16 a[48:51], v[244:247], v[144:147], a[48:51]
v_mfma_f32_16x16x32_bf16 a[52:55], v[244:247], v[148:151], a[52:55]
v_mfma_f32_16x16x32_bf16 a[56:59], v[244:247], v[152:155], a[56:59]
v_mfma_f32_16x16x32_bf16 a[60:63], v[244:247], v[156:159], a[60:63]
s_waitcnt vmcnt(7)
v_mfma_f32_16x16x32_bf16 a[0:3], a[152:155], v[64:67], a[0:3]
s_waitcnt vmcnt(6)
v_mfma_f32_16x16x32_bf16 a[4:7], a[152:155], v[68:71], a[4:7]
s_mov_b32 s31, 0xffff0000
s_waitcnt vmcnt(5)
v_mfma_f32_16x16x32_bf16 a[8:11], a[152:155], v[72:75], a[8:11]
v_lshlrev_b32 v160, 16, v20
s_waitcnt lgkmcnt(0)
v_lshlrev_b32 v7, 16, v40
s_waitcnt vmcnt(4)
v_mfma_f32_16x16x32_bf16 a[12:15], a[152:155], v[76:79], a[12:15]
v_mul_f32 v160, v160, v7
v_mul_f32 v160, v160, v28
v_mfma_f32_16x16x32_bf16 a[16:19], a[156:159], v[64:67], a[16:19]
v_and_b32 v161, s31, v20
v_and_b32 v7, s31, v40
v_mfma_f32_16x16x32_bf16 a[20:23], a[156:159], v[68:71], a[20:23]
v_mul_f32 v161, v161, v7
v_mul_f32 v161, v161, v28
v_mfma_f32_16x16x32_bf16 a[24:27], a[156:159], v[72:75], a[24:27]
v_lshlrev_b32 v162, 16, v21
v_lshlrev_b32 v7, 16, v41
v_mfma_f32_16x16x32_bf16 a[28:31], a[156:159], v[76:79], a[28:31]
v_mul_f32 v162, v162, v7
v_mul_f32 v162, v162, v28
v_mfma_f32_16x16x32_bf16 a[32:35], a[160:163], v[64:67], a[32:35]
v_and_b32 v163, s31, v21
v_and_b32 v7, s31, v41
v_mfma_f32_16x16x32_bf16 a[36:39], a[160:163], v[68:71], a[36:39]
v_mul_f32 v163, v163, v7
v_mul_f32 v163, v163, v28
v_mfma_f32_16x16x32_bf16 a[40:43], a[160:163], v[72:75], a[40:43]
v_lshlrev_b32 v164, 16, v22
v_lshlrev_b32 v7, 16, v42
v_mfma_f32_16x16x32_bf16 a[44:47], a[160:163], v[76:79], a[44:47]
v_mul_f32 v164, v164, v7
v_mul_f32 v164, v164, v28
v_mfma_f32_16x16x32_bf16 a[48:51], a[164:167], v[64:67], a[48:51]
v_and_b32 v165, s31, v22
v_and_b32 v7, s31, v42
v_mfma_f32_16x16x32_bf16 a[52:55], a[164:167], v[68:71], a[52:55]
v_mul_f32 v165, v165, v7
v_mul_f32 v165, v165, v28
v_mfma_f32_16x16x32_bf16 a[56:59], a[164:167], v[72:75], a[56:59]
v_lshlrev_b32 v166, 16, v23
v_lshlrev_b32 v7, 16, v43
v_mfma_f32_16x16x32_bf16 a[60:63], a[164:167], v[76:79], a[60:63]
v_mul_f32 v166, v166, v7
v_mul_f32 v166, v166, v28
v_and_b32 v167, s31, v23
v_and_b32 v7, s31, v43
v_mul_f32 v167, v167, v7
v_mul_f32 v167, v167, v28
s_waitcnt vmcnt(3)
v_mfma_f32_16x16x32_bf16 a[0:3], a[168:171], v[80:83], a[0:3]
s_waitcnt vmcnt(2)
v_mfma_f32_16x16x32_bf16 a[4:7], a[168:171], v[84:87], a[4:7]
v_lshlrev_b32 v168, 16, v24
v_lshlrev_b32 v7, 16, v44
s_waitcnt vmcnt(1)
v_mfma_f32_16x16x32_bf16 a[8:11], a[168:171], v[88:91], a[8:11]
v_mul_f32 v168, v168, v7
v_mul_f32 v168, v168, v28
s_waitcnt vmcnt(0)
v_mfma_f32_16x16x32_bf16 a[12:15], a[168:171], v[92:95], a[12:15]
v_and_b32 v169, s31, v24
v_and_b32 v7, s31, v44
v_mfma_f32_16x16x32_bf16 a[16:19], a[172:175], v[80:83], a[16:19]
v_mul_f32 v169, v169, v7
v_mul_f32 v169, v169, v28
v_mfma_f32_16x16x32_bf16 a[20:23], a[172:175], v[84:87], a[20:23]
v_lshlrev_b32 v170, 16, v25
v_lshlrev_b32 v7, 16, v45
v_mfma_f32_16x16x32_bf16 a[24:27], a[172:175], v[88:91], a[24:27]
v_mul_f32 v170, v170, v7
v_mul_f32 v170, v170, v28
v_mfma_f32_16x16x32_bf16 a[28:31], a[172:175], v[92:95], a[28:31]
v_and_b32 v171, s31, v25
v_and_b32 v7, s31, v45
v_mfma_f32_16x16x32_bf16 a[32:35], a[176:179], v[80:83], a[32:35]
v_mul_f32 v171, v171, v7
v_mul_f32 v171, v171, v28
v_mfma_f32_16x16x32_bf16 a[36:39], a[176:179], v[84:87], a[36:39]
v_lshlrev_b32 v172, 16, v26
v_lshlrev_b32 v7, 16, v46
v_mfma_f32_16x16x32_bf16 a[40:43], a[176:179], v[88:91], a[40:43]
v_mul_f32 v172, v172, v7
v_mul_f32 v172, v172, v28
v_mfma_f32_16x16x32_bf16 a[44:47], a[176:179], v[92:95], a[44:47]
v_and_b32 v173, s31, v26
v_and_b32 v7, s31, v46
v_mfma_f32_16x16x32_bf16 a[48:51], a[180:183], v[80:83], a[48:51]
v_mul_f32 v173, v173, v7
v_mul_f32 v173, v173, v28
v_mfma_f32_16x16x32_bf16 a[52:55], a[180:183], v[84:87], a[52:55]
v_lshlrev_b32 v174, 16, v27
v_lshlrev_b32 v7, 16, v47
v_mfma_f32_16x16x32_bf16 a[56:59], a[180:183], v[88:91], a[56:59]
v_mul_f32 v174, v174, v7
v_mul_f32 v174, v174, v28
v_mfma_f32_16x16x32_bf16 a[60:63], a[180:183], v[92:95], a[60:63]
v_and_b32 v175, s31, v27
v_and_b32 v7, s31, v47
v_mul_f32 v175, v175, v7
v_mul_f32 v175, v175, v28
s_nop 7
s_lshl_b32 s27, s23, 14
s_add_u32 s27, s27, s24
v_add_u32 v1, s27, v4
ds_write_b128 v1, a[0:3] offset:0
ds_write_b128 v1, a[4:7] offset:1024
ds_write_b128 v1, a[8:11] offset:2048
ds_write_b128 v1, a[12:15] offset:3072
ds_write_b128 v1, a[16:19] offset:4096
ds_write_b128 v1, a[20:23] offset:5120
ds_write_b128 v1, a[24:27] offset:6144
ds_write_b128 v1, a[28:31] offset:7168
ds_write_b128 v1, a[32:35] offset:8192
ds_write_b128 v1, a[36:39] offset:9216
ds_write_b128 v1, a[40:43] offset:10240
ds_write_b128 v1, a[44:47] offset:11264
ds_write_b128 v1, a[48:51] offset:12288
ds_write_b128 v1, a[52:55] offset:13312
ds_write_b128 v1, a[56:59] offset:14336
ds_write_b128 v1, a[60:63] offset:15360
v_sub_f32 v5, 1.0, v28
s_lshl_b32 s27, s23, 10
s_add_u32 s27, s27, s24
v_add_u32 v1, s27, v4
s_waitcnt lgkmcnt(0)
s_barrier
ds_read_b128 v[64:67], v1 offset:0
ds_read_b128 v[68:71], v1 offset:16384
ds_read_b128 v[72:75], v1 offset:32768
ds_read_b128 v[76:79], v1 offset:49152
ds_read_b128 v[80:83], v1 offset:4096
ds_read_b128 v[84:87], v1 offset:20480
ds_read_b128 v[88:91], v1 offset:36864
ds_read_b128 v[92:95], v1 offset:53248
ds_read_b128 v[96:99], v1 offset:8192
ds_read_b128 v[100:103], v1 offset:24576
ds_read_b128 v[104:107], v1 offset:40960
ds_read_b128 v[108:111], v1 offset:57344
ds_read_b128 v[112:115], v1 offset:12288
ds_read_b128 v[116:119], v1 offset:28672
ds_read_b128 v[120:123], v1 offset:45056
ds_read_b128 v[124:127], v1 offset:61440
s_waitcnt lgkmcnt(14)
v_add_f32 v64, v64, v68
v_add_f32 v65, v65, v69
v_add_f32 v66, v66, v70
v_add_f32 v67, v67, v71
s_waitcnt lgkmcnt(12)
v_add_f32 v72, v72, v76
v_add_f32 v73, v73, v77
v_add_f32 v74, v74, v78
v_add_f32 v75, v75, v79
v_add_f32 v64, v64, v72
v_add_f32 v65, v65, v73
v_add_f32 v66, v66, v74
v_add_f32 v67, v67, v75
v_fma_f32 v128, v5, v64, v160
v_fma_f32 v129, v5, v65, v161
v_fma_f32 v130, v5, v66, v162
v_fma_f32 v131, v5, v67, v163
s_waitcnt lgkmcnt(10)
v_add_f32 v80, v80, v84
v_add_f32 v81, v81, v85
v_add_f32 v82, v82, v86
v_add_f32 v83, v83, v87
s_waitcnt lgkmcnt(8)
v_add_f32 v88, v88, v92
v_add_f32 v89, v89, v93
v_add_f32 v90, v90, v94
v_add_f32 v91, v91, v95
v_add_f32 v80, v80, v88
v_add_f32 v81, v81, v89
v_add_f32 v82, v82, v90
v_add_f32 v83, v83, v91
v_fma_f32 v132, v5, v80, v164
v_fma_f32 v133, v5, v81, v165
v_fma_f32 v134, v5, v82, v166
v_fma_f32 v135, v5, v83, v167
v_cvt_pk_bf16_f32 v144, v128, v129
v_cvt_pk_bf16_f32 v145, v130, v131
v_cvt_pk_bf16_f32 v146, v132, v133
v_cvt_pk_bf16_f32 v147, v134, v135
buffer_store_dwordx4 v[144:147], v52, s[84:87], 0 offen offset:0
s_waitcnt lgkmcnt(6)
v_add_f32 v96, v96, v100
v_add_f32 v97, v97, v101
v_add_f32 v98, v98, v102
v_add_f32 v99, v99, v103
s_waitcnt lgkmcnt(4)
v_add_f32 v104, v104, v108
v_add_f32 v105, v105, v109
v_add_f32 v106, v106, v110
v_add_f32 v107, v107, v111
v_add_f32 v96, v96, v104
v_add_f32 v97, v97, v105
v_add_f32 v98, v98, v106
v_add_f32 v99, v99, v107
v_fma_f32 v136, v5, v96, v168
v_fma_f32 v137, v5, v97, v169
v_fma_f32 v138, v5, v98, v170
v_fma_f32 v139, v5, v99, v171
s_waitcnt lgkmcnt(2)
v_add_f32 v112, v112, v116
v_add_f32 v113, v113, v117
v_add_f32 v114, v114, v118
v_add_f32 v115, v115, v119
s_waitcnt lgkmcnt(0)
v_add_f32 v120, v120, v124
v_add_f32 v121, v121, v125
v_add_f32 v122, v122, v126
v_add_f32 v123, v123, v127
v_add_f32 v112, v112, v120
v_add_f32 v113, v113, v121
v_add_f32 v114, v114, v122
v_add_f32 v115, v115, v123
v_fma_f32 v140, v5, v112, v172
v_fma_f32 v141, v5, v113, v173
v_fma_f32 v142, v5, v114, v174
v_fma_f32 v143, v5, v115, v175
v_cvt_pk_bf16_f32 v148, v136, v137
v_cvt_pk_bf16_f32 v149, v138, v139
v_cvt_pk_bf16_f32 v150, v140, v141
v_cvt_pk_bf16_f32 v151, v142, v143
buffer_store_dwordx4 v[148:151], v52, s[84:87], 0 offen offset:64
s_mov_b32 s99, 0
FUSED_exit_%=:
s_mov_b32 %0, s99
s_mov_b32 %1, s98
FUSED_N_END:
)ASM"
      : "=s"(generic), "+s"(tag) : "s"(ka), "s"(b), "s"(w), "s"(ldsb), "s"(mode) : "memory", "vcc", "scc", "s20", "s21", "s22", "s23", "s24", "s25", "s26", "s27", "s28", "s29", "s30", "s31", "s36", "s37", "s38", "s39", "s40", "s41", "s42", "s43", "s44", "s45", "s46", "s47", "s48", "s49", "s50", "s51", "s52", "s53", "s54", "s55", "s56", "s57", "s58", "s59", "s60", "s61", "s62", "s63", "s64", "s65", "s66", "s67", "s68", "s69", "s70", "s71", "s72", "s73", "s74", "s75", "s76", "s77", "s78", "s79", "s80", "s81", "s82", "s83", "s84", "s85", "s86", "s87", "s88", "s89", "s90", "s91", "s92", "s93", "s94", "s95", "s96", "s97", "s98", "s99", "v0", "v1", "v2", "v3", "v4", "v5", "v6", "v7", "v8", "v9", "v10", "v11", "v12", "v13", "v14", "v15", "v16", "v17", "v18", "v19", "v20", "v21", "v22", "v23", "v24", "v25", "v26", "v27", "v28", "v29", "v30", "v31", "v32", "v33", "v34", "v35", "v36", "v37", "v38", "v39", "v40", "v41", "v42", "v43", "v44", "v45", "v46", "v47", "v48", "v49", "v50", "v51", "v52", "v53", "v54", "v55", "v56", "v57", "v58", "v59", "v60", "v61", "v62", "v63", "v64", "v65", "v66", "v67", "v68", "v69", "v70", "v71", "v72", "v73", "v74", "v75", "v76", "v77", "v78", "v79", "v80", "v81", "v82", "v83", "v84", "v85", "v86", "v87", "v88", "v89", "v90", "v91", "v92", "v93", "v94", "v95", "v96", "v97", "v98", "v99", "v100", "v101", "v102", "v103", "v104", "v105", "v106", "v107", "v108", "v109", "v110", "v111", "v112", "v113", "v114", "v115", "v116", "v117", "v118", "v119", "v120", "v121", "v122", "v123", "v124", "v125", "v126", "v127", "v128", "v129", "v130", "v131", "v132", "v133", "v134", "v135", "v136", "v137", "v138", "v139", "v140", "v141", "v142", "v143", "v144", "v145", "v146", "v147", "v148", "v149", "v150", "v151", "v152", "v153", "v154", "v155", "v156", "v157", "v158", "v159", "v160", "v161", "v162", "v163", "v164", "v165", "v166", "v167", "v168", "v169", "v170", "v171", "v172", "v173", "v174", "v175", "v176", "v177", "v178", "v179", "v180", "v181", "v182", "v183", "v184", "v185", "v186", "v187", "v188", "v189", "v190", "v191", "v192", "v193", "v194", "v195", "v196", "v197", "v198", "v199", "v200", "v201", "v202", "v203", "v204", "v205", "v206", "v207", "v208", "v209", "v210", "v211", "v212", "v213", "v214", "v215", "v216", "v217", "v218", "v219", "v220", "v221", "v222", "v223", "v224", "v225", "v226", "v227", "v228", "v229", "v230", "v231", "v232", "v233", "v234", "v235", "v236", "v237", "v238", "v239", "v240", "v241", "v242", "v243", "v244", "v245", "v246", "v247", "v248", "v249", "v250", "v251", "v252", "v253", "v254", "v255", "a0", "a1", "a2", "a3", "a4", "a5", "a6", "a7", "a8", "a9", "a10", "a11", "a12", "a13", "a14", "a15", "a16", "a17", "a18", "a19", "a20", "a21", "a22", "a23", "a24", "a25", "a26", "a27", "a28", "a29", "a30", "a31", "a32", "a33", "a34", "a35", "a36", "a37", "a38", "a39", "a40", "a41", "a42", "a43", "a44", "a45", "a46", "a47", "a48", "a49", "a50", "a51", "a52", "a53", "a54", "a55", "a56", "a57", "a58", "a59", "a60", "a61", "a62", "a63", "a64", "a65", "a66", "a67", "a68", "a69", "a70", "a71", "a72", "a73", "a74", "a75", "a76", "a77", "a78", "a79", "a80", "a81", "a82", "a83", "a84", "a85", "a86", "a87", "a88", "a89", "a90", "a91", "a92", "a93", "a94", "a95", "a96", "a97", "a98", "a99", "a100", "a101", "a102", "a103", "a104", "a105", "a106", "a107", "a108", "a109", "a110", "a111", "a112", "a113", "a114", "a115", "a116", "a117", "a118", "a119", "a120", "a121", "a122", "a123", "a124", "a125", "a126", "a127", "a128", "a129", "a130", "a131", "a132", "a133", "a134", "a135", "a136", "a137", "a138", "a139", "a140", "a141", "a142", "a143", "a144", "a145", "a146", "a147", "a148", "a149", "a150", "a151", "a152", "a153", "a154", "a155", "a156", "a157", "a158", "a159", "a160", "a161", "a162", "a163", "a164", "a165", "a166", "a167", "a168", "a169", "a170", "a171", "a172", "a173", "a174", "a175", "a176", "a177", "a178", "a179", "a180", "a181", "a182", "a183", "a184", "a185", "a186", "a187", "a188", "a189", "a190", "a191", "a192", "a193", "a194", "a195", "a196", "a197", "a198", "a199", "a200", "a201", "a202", "a203", "a204", "a205", "a206", "a207", "a208", "a209", "a210", "a211", "a212", "a213", "a214", "a215", "a216", "a217", "a218", "a219", "a220", "a221", "a222", "a223", "a224", "a225", "a226", "a227", "a228", "a229", "a230", "a231", "a232", "a233", "a234", "a235", "a236", "a237", "a238", "a239", "a240", "a241", "a242", "a243", "a244", "a245", "a246", "a247", "a248", "a249", "a250", "a251", "a252", "a253", "a254", "a255");
    if (!generic) break;
    asm volatile("" : "+s"(b), "+s"(w), "+s"(tag));
    ArgsP a = fresh_args(ka); const int tid = fresh_tid(w);
    char* ws = a->ws;
    const int h = 4 * (b & 7) + ((b >> 3) & 3), j = b >> 5;
    const int lane = tid & 63;
    for (int q4 = 0; q4 < 4; ++q4) {
      const int blk = q4 < 2 ? j : 15 - j;
      const int qt = blk * 8 + 2 * (int)w + (q4 & 1);
      bf16x8 qf[4];
#pragma unroll
      for (int s = 0; s < 4; ++s) qf[s] = ld16(ws, (unsigned)WS_Q + ((h * 128 + qt) * 4 + s) * 1024 + lane * 16);
      attn_qtile_generic(lane, h, qt, (unsigned)WS_KV + h * 32 * 32768, qf, ws);
    }
    mode = 1;
  }
  if (b == 0 && w == 0) {
    ArgsP a = fresh_args(ka);
    if (fresh_tid(0) == 0) __hip_atomic_store((gu32*)a->ws, tag, __ATOMIC_RELAXED, __HIP_MEMORY_SCOPE_AGENT);
  }
}

extern "C" void kernel_launch(void* const* d_in, const int* in_sizes, int n_in, void* d_out, int out_size, void* d_ws, size_t ws_size, hipStream_t stream) {
  const u16 *Aq = (const u16*)d_in[0], *Ak = (const u16*)d_in[1], *Av = (const u16*)d_in[2], *shk = (const u16*)d_in[3], *ssmk = (const u16*)d_in[4], *ssmD = (const u16*)d_in[5];
  const u16 *ckv = (const u16*)d_in[7], *wq = (const u16*)d_in[8], *wuk = (const u16*)d_in[9], *wuv = (const u16*)d_in[10], *wo = (const u16*)d_in[11];
  const u16* gw = (const u16*)d_in[12];
  (void)hipFuncSetAttribute((const void*)fused_kernel, hipFuncAttributeMaxDynamicSharedMemorySize, 163840);
  fused_kernel<<<256, 256, 163840, stream>>>(Aq, Ak, Av, shk, ssmk, ssmD, ckv, wq, wuk, wuv, wo, gw, (u16*)d_out, (char*)d_ws);
}
```
